```python
import torch

def _softmax(x, axis=-1):
    return torch.softmax(x, dim=axis)

def _gelu(x):
    return torch.nn.functional.gelu(x, approximate='tanh')

def _silu(x):
    return torch.nn.functional.silu(x)

def _sigmoid(x):
    return torch.sigmoid(x)

def _layernorm(x, axis=-1, eps=1e-05):
    m = x.mean(dim=axis, keepdim=True)
    v = x.var(dim=axis, keepdim=True, unbiased=False)
    return (x - m) / torch.sqrt(v + eps)

def _scatter_add(tgt, idx, src, axis=0):
    return tgt.clone().index_add_(axis, idx, src)

def _segment_sum(src, seg, n):
    out = torch.zeros((n,) + src.shape[1:], dtype=src.dtype)
    return out.index_add_(0, seg, src)

def _pad(x, pw):
    flat = tuple((v for pair in reversed(pw) for v in pair))
    return torch.nn.functional.pad(x, flat)
kernel_name = 'adaLN_cond__ssm_conv_h3__geglu_ffn__prompt'

def setup_inputs():

    _w = lambda a: (lambda t: t.to(torch.bfloat16) if t.is_floating_point() else t)(torch.as_tensor(a))

    def _A_setup():
        B, S, D = (1, 2048, 448)
        ks = 4
        return {'q': _w((lambda *s: torch.randn(*s, dtype=torch.bfloat16) * 0.05)(B, S, D)), 'k': _w((lambda *s: torch.randn(*s, dtype=torch.bfloat16) * 0.8)(B, S, D)), 'v': _w((lambda *s: torch.randn(*s, dtype=torch.bfloat16) * 0.8)(B, S, D)), 'shift_k': _w((lambda *s: torch.randn(*s, dtype=torch.bfloat16) * 0.8)(D, ks) * 0.5), 'ssm_k': _w((lambda *s: torch.randn(*s, dtype=torch.bfloat16) * 0.8)(D, S) * (1.0 / S ** 0.5)), 'ssm_D': _w((lambda *s: torch.randn(*s, dtype=torch.bfloat16) * 0.8)(D))}
    _A_in = _A_setup()

    def _B_setup():
        B, S, D, F = (1, 2048, 448, 1792)
        return {'x': _w((lambda *s: torch.randn(*s, dtype=torch.bfloat16) * 0.05)(B, S, D)), 'w_gate': _w((lambda *s: torch.randn(*s, dtype=torch.bfloat16) * 0.05)(D, F)), 'w_up': _w((lambda *s: torch.randn(*s, dtype=torch.bfloat16) * 0.05)(D, F)), 'w_down': _w((lambda *s: torch.randn(*s, dtype=torch.bfloat16) * 0.05)(F, D))}
    _B_in = _B_setup()
    return {'A_q': _A_in['q'], 'A_k': _A_in['k'], 'A_v': _A_in['v'], 'A_shift_k': _A_in['shift_k'], 'A_ssm_k': _A_in['ssm_k'], 'A_ssm_D': _A_in['ssm_D'], 'B_x': _B_in['x'], 'B_w_gate': _B_in['w_gate'], 'B_w_up': _B_in['w_up'], 'B_w_down': _B_in['w_down'], 'c': _w((lambda *s: torch.randn(*s, dtype=torch.bfloat16) * 0.1)(1, 448) * 8.0), 'w_mod': _w((lambda *s: torch.randn(*s, dtype=torch.bfloat16) * 0.1)(448, 6 * 448) * 448 ** (-0.5) * 8.0)}

def reference(A_q, A_k, A_v, A_shift_k, A_ssm_k, A_ssm_D, B_x, B_w_gate, B_w_up, B_w_down, c, w_mod):

    def _A_ref(q, k, v, shift_k, ssm_k, ssm_D):
        B, S, D = q.shape
        ks = shift_k.shape[-1]
        k_pad = _pad(k, ((0, 0), (ks - 1, 0), (0, 0)))
        k_shift = torch.zeros_like(k)
        for t in range(ks):
            k_shift = k_shift + k_pad[:, t:t + S, :] * shift_k[:, ks - 1 - t][None, None, :]
        kv = k_shift * v
        L = 2 * S
        kv_pad = _pad(kv, ((0, 0), (0, S), (0, 0)))
        kf_pad = _pad(ssm_k, ((0, 0), (0, S)))
        kv_f = (lambda x, axes=(-2, -1): torch.fft.rfft2(x.float(), dim=axes))(kv_pad[:, None, :, :], axes=(1, 2))
        kf_f = (lambda x, axes=(-2, -1): torch.fft.rfft2(x.float(), dim=axes))(kf_pad[:, None, :], axes=(1, 2))
        kf_f = (lambda x, *d: x.permute(*d))(kf_f, 1, 2, 0)
        y_f = kv_f * kf_f[None, :, :, :]
        y = (lambda x, s, axes=(-2, -1): torch.fft.irfft2(x, s=s, dim=axes).to(torch.bfloat16))(y_f, (1, L), axes=(1, 2))[:, 0, :S, :]
        y = y + kv * ssm_D[None, None, :]
        return q * y

    def _B_ref(x, w_gate, w_up, w_down):
        return _gelu(x @ w_gate) * (x @ w_up) @ w_down
    x = A_q
    mod = (_silu(c) @ w_mod).reshape(c.shape[0], 6, 1, -1)
    sa, ca, ga, sb, cb, gb = (mod[:, 0].clone(), mod[:, 1].clone(), mod[:, 2].clone(), mod[:, 3].clone(), mod[:, 4].clone(), mod[:, 5].clone())
    h = _layernorm(x, axis=-1) * (1 + ca) + sa
    x = x + ga * _A_ref(h, A_k, A_v, A_shift_k, A_ssm_k, A_ssm_D)
    h = _layernorm(x, axis=-1) * (1 + cb) + sb
    x = x + gb * _B_ref(h, B_w_gate, B_w_up, B_w_down)
    return x
```

```cpp
#include <hip/hip_runtime.h>
#include <hip/hip_bf16.h>
#include <stdint.h>
#include <type_traits>

typedef unsigned short u16;
typedef unsigned int u32;
typedef short bf16x8 __attribute__((ext_vector_type(8)));
typedef short s16x4 __attribute__((ext_vector_type(4)));
typedef float f32x2 __attribute__((ext_vector_type(2)));
typedef float f32x4 __attribute__((ext_vector_type(4)));
typedef float f32x16 __attribute__((ext_vector_type(16)));
typedef u32 u32x2 __attribute__((ext_vector_type(2)));
typedef u32 u32x4 __attribute__((ext_vector_type(4)));
typedef __bf16 bf16x2_t __attribute__((ext_vector_type(2)));
#define LAS __attribute__((address_space(3)))
#define DI __device__ __forceinline__

constexpr int S = 2048, D = 448, F = 1792;

constexpr size_t SZ_DS = (size_t)D * S * 2;
constexpr size_t OFF_RE = 0;
constexpr size_t OFF_RO = OFF_RE + SZ_DS;
constexpr size_t OFF_QT = OFF_RO + SZ_DS;
constexpr size_t OFF_X1T = OFF_QT + SZ_DS;
constexpr size_t OFF_MEAN = OFF_X1T + SZ_DS;
constexpr size_t OFF_RSTD = OFF_MEAN + S * 4;
constexpr size_t OFF_MOD = OFF_RSTD + S * 4;
constexpr size_t OFF_VP = OFF_MOD + 16384;
constexpr size_t OFF_WGU = OFF_VP + 8 * 2 * 3584 * 4;
constexpr size_t OFF_WD = OFF_WGU + (size_t)2 * D * F * 2;
constexpr size_t OFF_ACT = OFF_WD + (size_t)D * F * 2;

template <int I, int N, class Fn>
DI void static_for(Fn&& f) {
  if constexpr (I < N) {
    f(std::integral_constant<int, I>{});
    static_for<I + 1, N>(f);
  }
}

DI float lo_f(u32 x) { return __builtin_bit_cast(float, x << 16); }
DI float hi_f(u32 x) { return __builtin_bit_cast(float, x & 0xffff0000u); }
DI u32 pack_bf16(float a, float b) {
  f32x2 v = {a, b};
  return __builtin_bit_cast(u32, __builtin_convertvector(v, bf16x2_t));
}
DI float round_bf16(float a) { return lo_f(pack_bf16(a, 0.f)); }
DI float bf2f(u16 x) { return __builtin_bit_cast(float, (u32)x << 16); }

template <int N>
DI void wait_vm() { asm volatile("s_waitcnt vmcnt(%0)" ::"n"(N) : "memory"); }
DI void wait_lgkm0() { asm volatile("s_waitcnt lgkmcnt(0)" ::: "memory"); }
DI void bar() { __builtin_amdgcn_s_barrier(); }

DI void glds16(const void* g, char* lds) {
  __builtin_amdgcn_global_load_lds((const __attribute__((address_space(1))) void*)g, (LAS void*)lds, 16, 0, 0);
}

__global__ __launch_bounds__(256) void k0_prep(const u16* __restrict__ q, const u16* __restrict__ k,
                                               const u16* __restrict__ v, const u16* __restrict__ shift_k,
                                               const u16* __restrict__ c, const u16* __restrict__ w_mod,
                                               char* __restrict__ ws) {
  __shared__ float red[4][32];
  const int b = blockIdx.x, tid = threadIdx.x, lane = tid & 63, w = tid >> 6;
  if (b < 224) {
    const int xcd = b & 7, idx = b >> 3;
    const int sg = xcd * 2 + idx / 14, cg = idx % 14;
    const int cp = lane & 15, sq = lane >> 4;
    const int d0 = cg * 32 + cp * 2;
    const int a = sg * 16 + w * 4 + sq;
    const int s0 = a * 8;
    u32 kk[12], vv[9], qq[8];
#pragma unroll
    for (int i = 0; i < 12; ++i) {
      int r = s0 - 4 + i;
      int rc = r < 0 ? 0 : r;
      u32 t = *(const u32*)(k + (size_t)rc * D + d0);
      kk[i] = r < 0 ? 0u : t;
    }
#pragma unroll
    for (int i = 0; i < 9; ++i) {
      int r = s0 - 1 + i;
      int rc = r < 0 ? 0 : r;
      u32 t = *(const u32*)(v + (size_t)rc * D + d0);
      vv[i] = r < 0 ? 0u : t;
    }
#pragma unroll
    for (int i = 0; i < 8; ++i) qq[i] = *(const u32*)(q + (size_t)(s0 + i) * D + d0);
    const u32x4 sk = *(const u32x4*)(shift_k + d0 * 4);
    float w0[4] = {lo_f(sk[0]), hi_f(sk[0]), lo_f(sk[1]), hi_f(sk[1])};
    float w1[4] = {lo_f(sk[2]), hi_f(sk[2]), lo_f(sk[3]), hi_f(sk[3])};
    float u0[9], u1[9];
#pragma unroll
    for (int i = 0; i < 9; ++i) {
      float a0 = lo_f(kk[i + 3]) * w0[0] + lo_f(kk[i + 2]) * w0[1] + lo_f(kk[i + 1]) * w0[2] + lo_f(kk[i]) * w0[3];
      float a1 = hi_f(kk[i + 3]) * w1[0] + hi_f(kk[i + 2]) * w1[1] + hi_f(kk[i + 1]) * w1[2] + hi_f(kk[i]) * w1[3];
      u0[i] = a0 * lo_f(vv[i]);
      u1[i] = a1 * hi_f(vv[i]);
    }
    u16* RE = (u16*)(ws + OFF_RE);
    u16* RO = (u16*)(ws + OFF_RO);
    u16* QT = (u16*)(ws + OFF_QT);
    const int x0 = 2040 - s0;
    u32x4 o;
    o = u32x4{pack_bf16(u0[8], u0[7]), pack_bf16(u0[6], u0[5]), pack_bf16(u0[4], u0[3]), pack_bf16(u0[2], u0[1])};
    *(u32x4*)(RO + (size_t)d0 * S + x0) = o;
    o = u32x4{pack_bf16(u1[8], u1[7]), pack_bf16(u1[6], u1[5]), pack_bf16(u1[4], u1[3]), pack_bf16(u1[2], u1[1])};
    *(u32x4*)(RO + (size_t)(d0 + 1) * S + x0) = o;
    o = u32x4{pack_bf16(u0[7], u0[6]), pack_bf16(u0[5], u0[4]), pack_bf16(u0[3], u0[2]), pack_bf16(u0[1], u0[0])};
    *(u32x4*)(RE + (size_t)d0 * S + x0) = o;
    o = u32x4{pack_bf16(u1[7], u1[6]), pack_bf16(u1[5], u1[4]), pack_bf16(u1[3], u1[2]), pack_bf16(u1[1], u1[0])};
    *(u32x4*)(RE + (size_t)(d0 + 1) * S + x0) = o;
    o = u32x4{(qq[0] & 0xffffu) | (qq[2] << 16), (qq[4] & 0xffffu) | (qq[6] << 16), (qq[1] & 0xffffu) | (qq[3] << 16),
              (qq[5] & 0xffffu) | (qq[7] << 16)};
    *(u32x4*)(QT + (size_t)d0 * S + s0) = o;
    o = u32x4{(qq[0] >> 16) | (qq[2] & 0xffff0000u), (qq[4] >> 16) | (qq[6] & 0xffff0000u),
              (qq[1] >> 16) | (qq[3] & 0xffff0000u), (qq[5] >> 16) | (qq[7] & 0xffff0000u)};
    *(u32x4*)(QT + (size_t)(d0 + 1) * S + s0) = o;
  } else if (b < 288) {
    const int bb = b - 224, xcd = bb & 7;
    const int row = xcd * 256 + (bb >> 3) * 32 + w * 8 + (lane >> 3);
    const int sub = lane & 7;
    u32x4 x[7];
#pragma unroll
    for (int i = 0; i < 7; ++i) x[i] = *(const u32x4*)(q + (size_t)row * D + (i * 8 + sub) * 8);
    float s = 0.f;
#pragma unroll
    for (int i = 0; i < 7; ++i)
#pragma unroll
      for (int j = 0; j < 4; ++j) s += lo_f(x[i][j]) + hi_f(x[i][j]);
    s += __shfl_xor(s, 1);
    s += __shfl_xor(s, 2);
    s += __shfl_xor(s, 4);
    const float mean = s * (1.f / D);
    float vs = 0.f;
#pragma unroll
    for (int i = 0; i < 7; ++i)
#pragma unroll
      for (int j = 0; j < 4; ++j) {
        float a = lo_f(x[i][j]) - mean, bq = hi_f(x[i][j]) - mean;
        vs += a * a + bq * bq;
      }
    vs += __shfl_xor(vs, 1);
    vs += __shfl_xor(vs, 2);
    vs += __shfl_xor(vs, 4);
    const float rstd = 1.f / sqrtf(vs * (1.f / D) + 1e-5f);
    if (sub == 0) {
      const int p = (row & ~7) | ((row & 1) << 2) | ((row >> 1) & 3);
      ((float*)(ws + OFF_MEAN))[p] = mean;
      ((float*)(ws + OFF_RSTD))[p] = rstd;
    }
  } else {
    const int bb = b - 288;
    const int co = lane & 3, rs = lane >> 2;
    const int col0 = bb * 32 + co * 8;
    u32x4 x[7];
    float sc[7];
#pragma unroll
    for (int i = 0; i < 7; ++i) {
      const int r = i * 64 + w * 16 + rs;
      x[i] = *(const u32x4*)(w_mod + (size_t)r * (6 * D) + col0);
      sc[i] = bf2f(c[r]);
    }
    float acc[8] = {0.f, 0.f, 0.f, 0.f, 0.f, 0.f, 0.f, 0.f};
#pragma unroll
    for (int i = 0; i < 7; ++i) {
      const float cv = sc[i];
      const float sl = round_bf16(cv / (1.f + __expf(-cv)));
#pragma unroll
      for (int j = 0; j < 4; ++j) {
        acc[2 * j] += sl * lo_f(x[i][j]);
        acc[2 * j + 1] += sl * hi_f(x[i][j]);
      }
    }
#pragma unroll
    for (int j = 0; j < 8; ++j) {
      float t = acc[j];
      t += __shfl_xor(t, 4);
      t += __shfl_xor(t, 8);
      t += __shfl_xor(t, 16);
      t += __shfl_xor(t, 32);
      acc[j] = t;
    }
    if (lane < 4) {
#pragma unroll
      for (int j = 0; j < 8; ++j) red[w][co * 8 + j] = acc[j];
    }
    __syncthreads();
    if (tid < 32) {
      const float t = (red[0][tid] + red[1][tid]) + (red[2][tid] + red[3][tid]);
      ((float*)(ws + OFF_MOD))[bb * 32 + tid] = round_bf16(t);
    }
  }
}

constexpr int K1_KSTR = 260 * 16;
constexpr int K1_USTR = 286 * 16;
constexpr int K1_UOFF = 4 * K1_KSTR;
constexpr int K1_XOFF = K1_UOFF + 2 * K1_USTR;
constexpr int K1_CH = K1_XOFF + 8192;
constexpr int K1_LDS = 2 * K1_CH;

__global__ __launch_bounds__(448) void k1_conv(const u16* __restrict__ ssm_k, const u16* __restrict__ ssm_D,
                                               const u16* __restrict__ w_gate, const u16* __restrict__ w_up,
                                               const u16* __restrict__ w_down, char* __restrict__ ws) {
  extern __shared__ __attribute__((aligned(16))) char smem[];
  const int b = blockIdx.x, tid = threadIdx.x, lane = tid & 63;
  const int w = __builtin_amdgcn_readfirstlane(tid >> 6);
  const float* MOD = (const float*)(ws + OFF_MOD);
  if (w < 4) {
    const int ch = w >> 1, half = w & 1;
    const int d = 2 * b + ch;
    char* L = smem + ch * K1_CH;
    const u16* kf = ssm_k + (size_t)d * S;
#pragma unroll
    for (int cc2 = 0; cc2 < 2; ++cc2) {
      const int cc = half * 2 + cc2;
#pragma unroll
      for (int i = 0; i < 4; ++i) {
        int chunk = 4 + i * 64 + lane;
        if (chunk > 258) chunk = 258;
        glds16(kf + (8 * chunk + 2 * cc - 30), L + cc * K1_KSTR + (4 + i * 64) * 16);
      }
    }
    {
      const u16* U = (const u16*)(ws + (half ? OFF_RO : OFF_RE)) + (size_t)d * S;
#pragma unroll
      for (int i = 0; i < 4; ++i) glds16(U + (i * 64 + lane) * 8, L + K1_UOFF + half * K1_USTR + i * 1024);
    }
    if (half == 0) {
      const int cc = lane >> 4, wd = lane & 15;
      const int di = wd + cc - 15;
      u32 t = ((const u32*)kf)[di < 0 ? 0 : di];
      *(LAS u32*)(L + cc * K1_KSTR + wd * 4) = di < 0 ? 0u : t;
    } else {
      const int l5 = lane & 31;
      if (l5 < 28) *(LAS u32x4*)(L + K1_UOFF + (lane >> 5) * K1_USTR + (256 + l5) * 16) = u32x4{0u, 0u, 0u, 0u};
    }
    const int n = lane & 15, q4 = lane >> 4;
    const int pbase = 32 * (n >> 1) + 8 * q4 + 4 * (n & 1);
    u32x2 qv[4];
    f32x4 mu[4], rs[4];
#pragma unroll
    for (int j = 0; j < 4; ++j) {
      const int p = 256 * (4 * half + j) + pbase;
      qv[j] = *(const u32x2*)((const u16*)(ws + OFF_QT) + (size_t)d * S + p);
      mu[j] = *(const f32x4*)((const float*)(ws + OFF_MEAN) + p);
      rs[j] = *(const f32x4*)((const float*)(ws + OFF_RSTD) + p);
    }
    const float sa = MOD[d], ca1 = round_bf16(1.f + MOD[D + d]), ga = MOD[2 * D + d];
    const float Dd = bf2f(ssm_D[d]);
    wait_vm<0>();
    wait_lgkm0();
    bar();
    const char* Ab = L + (n & 3) * K1_KSTR + ((n >> 2) + q4 + 16 * half) * 16;
    const char* Bb = L + K1_UOFF + (n & 1) * K1_USTR + (252 - 4 * (n >> 1) + q4 + 16 * half) * 16;
    f32x4 acc[8];
#pragma unroll
    for (int j = 0; j < 8; ++j) acc[j] = f32x4{0.f, 0.f, 0.f, 0.f};
#pragma unroll
    for (int dl = 0; dl < 4; ++dl) {
      bf16x8 af[8], bfr[8];
#pragma unroll
      for (int g = 0; g < 8; ++g) af[g] = *(const LAS bf16x8*)(Ab + 64 * dl + 512 * g);
#pragma unroll
      for (int cI = 0; cI < 8; ++cI) bfr[cI] = *(const LAS bf16x8*)(Bb + 64 * dl - 512 * cI);
#pragma unroll
      for (int g = 0; g < 8; ++g)
#pragma unroll
        for (int J = g; J < 8; ++J)
          acc[J] = __builtin_amdgcn_mfma_f32_16x16x32_bf16(af[g], bfr[J - g], acc[J], 0, 0, 0);
    }
    LAS f32x4* X = (LAS f32x4*)(L + K1_XOFF);
    f32x4 own0, own1, own2, own3;
    if (half == 0) {
#pragma unroll
      for (int j = 0; j < 4; ++j) X[j * 64 + lane] = acc[4 + j];
      own0 = acc[0]; own1 = acc[1]; own2 = acc[2]; own3 = acc[3];
    } else {
#pragma unroll
      for (int j = 0; j < 4; ++j) X[256 + j * 64 + lane] = acc[j];
      own0 = acc[4]; own1 = acc[5]; own2 = acc[6]; own3 = acc[7];
    }
    const f32x4 own[4] = {own0, own1, own2, own3};
    wait_lgkm0();
    bar();
    const int sh = (n & 1) ? 16 : 0;
    u16* X1T = (u16*)(ws + OFF_X1T) + (size_t)d * S;
#pragma unroll
    for (int j = 0; j < 4; ++j) {
      const int J = 4 * half + j;
      f32x4 y = X[(half ? 0 : 256) + j * 64 + lane];
      y += own[j];
      const u32x4 uc = *(const LAS u32x4*)(L + K1_UOFF + K1_USTR + (255 - (32 * J + 4 * (n >> 1) + q4)) * 16);
      float o[4];
#pragma unroll
      for (int r = 0; r < 4; ++r) {
        const float kv = __builtin_bit_cast(float, (uc[3 - r] << sh) & 0xffff0000u);
        const float qf = (r & 1) ? hi_f(qv[j][r >> 1]) : lo_f(qv[j][r >> 1]);
        const float h = (qf - mu[j][r]) * rs[j][r] * ca1 + sa;
        o[r] = qf + ga * (h * (y[r] + kv * Dd));
      }
      *(u32x2*)(X1T + 256 * J + pbase) = u32x2{pack_bf16(o[0], o[1]), pack_bf16(o[2], o[3])};
    }
  } else {
    const int o8 = lane & 7, r7 = lane >> 3;
    const bool act = r7 < 7;
    const int r7c = act ? r7 : 0;
    if (w < 6) {
      const int isUp = w - 4;
      const u16* W = isUp ? w_up : w_gate;
      const int fg = b % 28, kg = b / 28;
      const int k0 = kg * 56 + r7c * 8, f0 = fg * 64 + o8 * 8;
      u32x4 x[8];
#pragma unroll
      for (int i = 0; i < 8; ++i) x[i] = *(const u32x4*)(W + (size_t)(k0 + i) * F + f0);
      const f32x4 sb0 = *(const f32x4*)(MOD + 3 * D + k0), sb1 = *(const f32x4*)(MOD + 3 * D + k0 + 4);
      const f32x4 cb0 = *(const f32x4*)(MOD + 4 * D + k0), cb1 = *(const f32x4*)(MOD + 4 * D + k0 + 4);
      bar();
      float sc[8], sbv[8];
#pragma unroll
      for (int i = 0; i < 4; ++i) {
        sc[i] = round_bf16(1.f + cb0[i]);
        sc[4 + i] = round_bf16(1.f + cb1[i]);
        sbv[i] = sb0[i];
        sbv[4 + i] = sb1[i];
      }
      const int t = f0 >> 4, hp = o8 & 1;
      const int ks = k0 >> 4, hh = (k0 >> 3) & 1;
      u32x4* dst = (u32x4*)(ws + OFF_WGU) + ((size_t)(t * 28 + ks) * 64 + hh * 32 + 4 * hp + 2 * isUp);
      float* vp = (float*)(ws + OFF_VP) + (size_t)kg * 2 * 3584 + t * 32 + 4 * hp + 2 * isUp;
#pragma unroll
      for (int e = 0; e < 8; ++e) {
        float wv[8];
#pragma unroll
        for (int i = 0; i < 8; ++i) wv[i] = (e & 1) ? hi_f(x[i][e >> 1]) : lo_f(x[i][e >> 1]);
        u32x4 o;
        float v1 = 0.f, v2 = 0.f;
#pragma unroll
        for (int i = 0; i < 4; ++i) {
          o[i] = pack_bf16(wv[2 * i] * sc[2 * i], wv[2 * i + 1] * sc[2 * i + 1]);
          v1 += lo_f(o[i]) + hi_f(o[i]);
          v2 += wv[2 * i] * sbv[2 * i] + wv[2 * i + 1] * sbv[2 * i + 1];
        }
        if (!act) { v1 = 0.f; v2 = 0.f; }
        v1 += __shfl_xor(v1, 8);
        v2 += __shfl_xor(v2, 8);
        v1 += __shfl_xor(v1, 16);
        v2 += __shfl_xor(v2, 16);
        v1 += __shfl_xor(v1, 32);
        v2 += __shfl_xor(v2, 32);
        const int rho = 8 * (e >> 1) + (e & 1);
        if (act) dst[rho] = o;
        if (lane < 8) {
          vp[rho] = v1;
          vp[3584 + rho] = v2;
        }
      }
    } else {
      const int dg = b % 7, fk = b / 7;
      const int f0 = fk * 56 + r7c * 8, d0 = dg * 64 + o8 * 8;
      u32x4 x[8];
#pragma unroll
      for (int i = 0; i < 8; ++i) x[i] = *(const u32x4*)(w_down + (size_t)(f0 + i) * D + d0);
      bar();
      u32x4* dst = (u32x4*)(ws + OFF_WD) + ((size_t)((d0 >> 5) * 112 + (f0 >> 4)) * 64 + ((f0 >> 3) & 1) * 32 + (d0 & 31));
#pragma unroll
      for (int e = 0; e < 8; ++e) {
        u32x4 o;
#pragma unroll
        for (int i = 0; i < 4; ++i) {
          const u32 a = x[2 * i][e >> 1], c2 = x[2 * i + 1][e >> 1];
          o[i] = (e & 1) ? ((a >> 16) | (c2 & 0xffff0000u)) : ((a & 0xffffu) | (c2 << 16));
        }
        if (act) dst[e] = o;
      }
    }
  }
}

constexpr int K2_STG = 22528;
constexpr int K2_NBUF = 7;
constexpr int K2_VS = K2_STG * K2_NBUF;
constexpr int K2_ST = K2_VS + 448 * 4;
constexpr int K2_LDS = K2_ST + 256 * 4;

DI float gelu_mul(float g, float u) {
  const float c1 = -2.302208198f;
  const float c2 = -0.1029432397f;
  const float z = g * (c1 + c2 * g * g);
  const float e = __builtin_amdgcn_exp2f(z);
  return g * u * __builtin_amdgcn_rcpf(1.f + e);
}

DI void glds16_asm(const void* sbase, u32 voff, u32 lds_addr) {
  u32 keep;
  asm volatile("s_mov_b32 %0, m0\n\ts_mov_b32 m0, %3\n\ts_nop 0\n\tglobal_load_lds_dwordx4 %1, %2\n\ts_mov_b32 m0, %0"
               : "=&s"(keep)
               : "v"(voff), "s"(sbase), "s"(lds_addr)
               : "memory");
}

template <int NT>
struct K2Frags {
  bf16x8 x[2];
  bf16x8 wf[2][NT];
};

template <int NT, int NL>
DI void k2_body(char* smem, const char* xbase, u32 xoff, const char* wbase, u32 woff, int w, int lane, int t0,
                u32x4* act_dst, const float (&vp)[8], int tid) {
  const int nt = w & 3;
  const u32 lds0 = (u32)(size_t)(LAS char*)smem;
  auto issue = [&](auto sI) {
    constexpr int s = decltype(sI)::value;
    const u32 buf = lds0 + (s % K2_NBUF) * K2_STG;
    glds16_asm(xbase + (size_t)s * 32 * S * 2, xoff, buf + 14336 + w * 1024);
    if constexpr (NL == 3) {
      glds16_asm(wbase + (2 * s) * 1024, woff, buf + w * 2048);
      glds16_asm(wbase + (2 * s + 1) * 1024, woff, buf + w * 2048 + 1024);
    }
  };
  static_for<0, 7>(issue);
  const int h = lane >> 5, blk = (lane >> 4) & 1, qq = (lane & 15) >> 2, p = lane & 3;
  int xo[2];
#pragma unroll
  for (int t = 0; t < 2; ++t) {
    const int row = 8 * h + 4 * t + qq;
    const int chn = 4 * nt + 2 * blk + (p >> 1);
    xo[t] = 14336 + 256 * row + 16 * (chn ^ ((qq << 2) | ((2 * h + t) & 3))) + 8 * (p & 1);
  }
  const int wo = t0 * 2048 + lane * 16;
  f32x16 acc[NT];
#pragma unroll
  for (int i = 0; i < NT; ++i)
#pragma unroll
    for (int r = 0; r < 16; ++r) acc[i][r] = 0.f;
  float ssum = 0.f, ssq = 0.f;
  const bf16x2_t ones = {(__bf16)1.0f, (__bf16)1.0f};
  auto rd = [&](auto sI, K2Frags<NT>& f) {
    constexpr int s = decltype(sI)::value;
    char* buf = smem + (s % K2_NBUF) * K2_STG;
#pragma unroll
    for (int ks = 0; ks < 2; ++ks) {
      s16x4 x0 = __builtin_amdgcn_ds_read_tr16_b64_v4i16((LAS s16x4*)(buf + xo[0] + ks * 4096));
      s16x4 x1 = __builtin_amdgcn_ds_read_tr16_b64_v4i16((LAS s16x4*)(buf + xo[1] + ks * 4096));
      f.x[ks] = __builtin_shufflevector(x0, x1, 0, 1, 2, 3, 4, 5, 6, 7);
#pragma unroll
      for (int i = 0; i < NT; ++i) f.wf[ks][i] = *(const LAS bf16x8*)(buf + wo + i * 2048 + ks * 1024);
    }
  };
  auto mm = [&](const K2Frags<NT>& f) {
#pragma unroll
    for (int ks = 0; ks < 2; ++ks) {
#pragma unroll
      for (int i = 0; i < NT; ++i)
        acc[i] = __builtin_amdgcn_mfma_f32_32x32x16_bf16(f.wf[ks][i], f.x[ks], acc[i], 0, 0, 0);
      if constexpr (NT == 3) {
        const u32x4 xu = __builtin_bit_cast(u32x4, f.x[ks]);
#pragma unroll
        for (int j = 0; j < 4; ++j) {
          const u32 xj = xu[j];
          const bf16x2_t xv = __builtin_bit_cast(bf16x2_t, xj);
          ssum = __builtin_amdgcn_fdot2_f32_bf16(xv, ones, ssum, false);
          ssq = __builtin_amdgcn_fdot2_f32_bf16(xv, xv, ssq, false);
        }
      }
    }
  };
  K2Frags<NT> fa, fb;
  wait_vm<NL * 6>();
  bar();
  rd(std::integral_constant<int, 0>{}, fa);
  static_for<0, 14>([&](auto sI) {
    constexpr int s = decltype(sI)::value;
    if constexpr (s < 13) {
      constexpr int last = (s + 6 < 13) ? s + 6 : 13;
      wait_vm<NL*(last - (s + 1))>();
      wait_lgkm0();
      __builtin_amdgcn_sched_barrier(0);
      bar();
      if constexpr (s + 7 <= 13) issue(std::integral_constant<int, s + 7>{});
      if constexpr (s & 1) rd(std::integral_constant<int, s + 1>{}, fa);
      else rd(std::integral_constant<int, s + 1>{}, fb);
    }
    if constexpr (s & 1) mm(fb);
    else mm(fa);
    if constexpr (NT == 3) asm volatile("" : "+v"(ssum), "+v"(ssq));
#pragma unroll
    for (int i = 0; i < 2 * NT; ++i) {
      __builtin_amdgcn_sched_group_barrier(0x008, 1, 0);
      __builtin_amdgcn_sched_group_barrier(0x100, 2, 0);
      __builtin_amdgcn_sched_group_barrier(0x002, 3, 0);
    }
    __builtin_amdgcn_sched_barrier(0);
  });
  LAS float* VS = (LAS float*)(smem + K2_VS);
  LAS float* ST = (LAS float*)(smem + K2_ST);
  if (tid < 448) VS[tid] = ((vp[0] + vp[1]) + (vp[2] + vp[3])) + ((vp[4] + vp[5]) + (vp[6] + vp[7]));
  if constexpr (NT == 3) {
    ssum += __shfl_xor(ssum, 32);
    ssq += __shfl_xor(ssq, 32);
    const float mean = ssum * (1.f / D);
    float var = ssq * (1.f / D) - mean * mean;
    var = var < 0.f ? 0.f : var;
    const float rstd = 1.f / sqrtf(var + 1e-5f);
    if (lane < 32) {
      ST[nt * 32 + lane] = mean;
      ST[128 + nt * 32 + lane] = rstd;
    }
  }
  wait_lgkm0();
  bar();
  const float mean = ST[nt * 32 + (lane & 31)], rstd = ST[128 + nt * 32 + (lane & 31)];
  const float nmr = -mean * rstd;
#pragma unroll
  for (int i = 0; i < NT; ++i) {
    u32x4 o;
#pragma unroll
    for (int g = 0; g < 4; ++g) {
      const f32x4 v1 = *(const LAS f32x4*)(VS + (t0 + i) * 32 + 8 * g + 4 * h);
      const f32x4 v2 = *(const LAS f32x4*)(VS + 224 + (t0 + i) * 32 + 8 * g + 4 * h);
      float z[4];
#pragma unroll
      for (int e = 0; e < 4; ++e) z[e] = acc[i][4 * g + e] * rstd + (nmr * v1[e] + v2[e]);
      o[g] = pack_bf16(gelu_mul(z[0], z[2]), gelu_mul(z[1], z[3]));
    }
    act_dst[(size_t)i * 64] = o;
  }
}

__global__ __launch_bounds__(512, 2) void k2_gemm1(char* __restrict__ ws) {
  extern __shared__ __attribute__((aligned(16))) char smem[];
  const int tid = threadIdx.x, lane = tid & 63;
  const int w = __builtin_amdgcn_readfirstlane(tid >> 6);
  const int xcd = blockIdx.x & 7, idx = blockIdx.x >> 3;
  const int mt = (xcd & 1) * 8 + (idx & 7);
  const int ft = (xcd >> 1) * 4 + (idx >> 3);
  float vp[8];
  {
    const int tc = tid < 448 ? tid : 0;
    const int which = tc >= 224, row = tc - which * 224;
    const float* src = (const float*)(ws + OFF_VP) + which * 3584 + ft * 224 + row;
#pragma unroll
    for (int i = 0; i < 8; ++i) vp[i] = src[(size_t)i * 2 * 3584];
  }
  const int xr = lane >> 4, slot = lane & 15;
  const int chn = slot ^ ((xr << 2) | (w & 3));
  const char* xbase = ws + OFF_X1T + ((size_t)(4 * w) * S + mt * 128) * 2;
  const u32 xoff = (u32)(xr * S + chn * 8) * 2;
  const char* wbase = ws + OFF_WGU + (size_t)((ft * 7 + (w < 7 ? w : 0)) * 28) * 1024;
  const u32 woff = lane * 16;
  const int t0 = (w >> 2) * 4;
  u32x4* act_dst = (u32x4*)(ws + OFF_ACT) + ((size_t)(mt * 4 + (w & 3)) * 112 + ft * 7 + t0) * 64 + lane;
  if (w < 4) k2_body<4, 3>(smem, xbase, xoff, wbase, woff, w, lane, 0, act_dst, vp, tid);
  else if (w < 7) k2_body<3, 3>(smem, xbase, xoff, wbase, woff, w, lane, 4, act_dst, vp, tid);
  else k2_body<3, 1>(smem, xbase, xoff, wbase, woff, w, lane, 4, act_dst, vp, tid);
}

constexpr int K3_WIN = 12;
constexpr int K3_LDS = 4 * 3 * 4 * 1024;

__global__ __launch_bounds__(256, 1) void k3_gemm2(const char* __restrict__ ws, u16* __restrict__ out) {
  extern __shared__ __attribute__((aligned(16))) char smem[];
  const int tid = threadIdx.x, lane = tid & 63;
  const int w = __builtin_amdgcn_readfirstlane(tid >> 6);
  const int xcd = blockIdx.x & 7, idx = blockIdx.x >> 3;
  const int st = xcd * 4 + (idx & 3), dt = idx >> 2;
  const u32x4* a0 = (const u32x4*)(ws + OFF_WD) + ((size_t)(2 * dt) * 112 + 28 * w) * 64 + lane;
  const u32x4* a1 = a0 + 112 * 64;
  const u32x4* b0 = (const u32x4*)(ws + OFF_ACT) + ((size_t)(2 * st) * 112 + 28 * w) * 64 + lane;
  const u32x4* b1 = b0 + 112 * 64;
  u32x4 ra0[K3_WIN], ra1[K3_WIN], rb0[K3_WIN], rb1[K3_WIN];
#pragma unroll
  for (int i = 0; i < K3_WIN; ++i) {
    ra0[i] = a0[i * 64];
    rb0[i] = b0[i * 64];
    ra1[i] = a1[i * 64];
    rb1[i] = b1[i * 64];
  }
  __builtin_amdgcn_sched_barrier(0);
  const int ti = w >> 1, tj = w & 1, n = lane & 31, h = lane >> 5;
  const int p = st * 64 + tj * 32 + n;
  const int dbase = dt * 64 + ti * 32 + 4 * h;
  u16 xr[16];
  f32x4 gb[4];
  {
    const u16* X1T = (const u16*)(ws + OFF_X1T);
#pragma unroll
    for (int r = 0; r < 16; ++r) xr[r] = X1T[(size_t)(dbase + 8 * (r >> 2) + (r & 3)) * S + p];
#pragma unroll
    for (int g = 0; g < 4; ++g) gb[g] = *(const f32x4*)((const float*)(ws + OFF_MOD) + 5 * D + dbase + 8 * g);
  }
  __builtin_amdgcn_sched_barrier(0);
  f32x16 acc[4];
#pragma unroll
  for (int i = 0; i < 4; ++i)
#pragma unroll
    for (int r = 0; r < 16; ++r) acc[i][r] = 0.f;
#pragma unroll
  for (int i = 0; i < 28; ++i) {
    const bf16x8 fa0 = __builtin_bit_cast(bf16x8, ra0[i % K3_WIN]), fa1 = __builtin_bit_cast(bf16x8, ra1[i % K3_WIN]);
    const bf16x8 fb0 = __builtin_bit_cast(bf16x8, rb0[i % K3_WIN]), fb1 = __builtin_bit_cast(bf16x8, rb1[i % K3_WIN]);
    acc[0] = __builtin_amdgcn_mfma_f32_32x32x16_bf16(fa0, fb0, acc[0], 0, 0, 0);
    acc[1] = __builtin_amdgcn_mfma_f32_32x32x16_bf16(fa0, fb1, acc[1], 0, 0, 0);
    acc[2] = __builtin_amdgcn_mfma_f32_32x32x16_bf16(fa1, fb0, acc[2], 0, 0, 0);
    acc[3] = __builtin_amdgcn_mfma_f32_32x32x16_bf16(fa1, fb1, acc[3], 0, 0, 0);
    if (i + K3_WIN < 28) {
      ra0[i % K3_WIN] = a0[(i + K3_WIN) * 64];
      rb0[i % K3_WIN] = b0[(i + K3_WIN) * 64];
      ra1[i % K3_WIN] = a1[(i + K3_WIN) * 64];
      rb1[i % K3_WIN] = b1[(i + K3_WIN) * 64];
    }
    __builtin_amdgcn_sched_barrier(0);
  }
  LAS f32x4* R = (LAS f32x4*)smem;
#pragma unroll
  for (int t = 0; t < 4; ++t) {
    if (t != w) {
      const int slot = (w - t - 1) & 3;
#pragma unroll
      for (int g = 0; g < 4; ++g)
        R[((t * 3 + slot) * 4 + g) * 64 + lane] = f32x4{acc[t][4 * g], acc[t][4 * g + 1], acc[t][4 * g + 2], acc[t][4 * g + 3]};
    }
  }
  __syncthreads();
  f32x16 own = w == 0 ? acc[0] : (w == 1 ? acc[1] : (w == 2 ? acc[2] : acc[3]));
  const int s = (p & ~7) | ((p & 3) << 1) | ((p >> 2) & 1);
#pragma unroll
  for (int g = 0; g < 4; ++g) {
    f32x4 vsum = f32x4{own[4 * g], own[4 * g + 1], own[4 * g + 2], own[4 * g + 3]};
#pragma unroll
    for (int sl = 0; sl < 3; ++sl) vsum += R[((w * 3 + sl) * 4 + g) * 64 + lane];
    float o[4];
#pragma unroll
    for (int e = 0; e < 4; ++e) o[e] = bf2f(xr[4 * g + e]) + gb[g][e] * vsum[e];
    *(u32x2*)(out + (size_t)s * D + dbase + 8 * g) = u32x2{pack_bf16(o[0], o[1]), pack_bf16(o[2], o[3])};
  }
}

extern "C" void kernel_launch(void* const* d_in, const int* in_sizes, int n_in, void* d_out, int out_size, void* d_ws,
                              size_t ws_size, hipStream_t stream) {
  const u16* q = (const u16*)d_in[0];
  const u16* k = (const u16*)d_in[1];
  const u16* v = (const u16*)d_in[2];
  const u16* shift_k = (const u16*)d_in[3];
  const u16* ssm_k = (const u16*)d_in[4];
  const u16* ssm_D = (const u16*)d_in[5];
  const u16* w_gate = (const u16*)d_in[7];
  const u16* w_up = (const u16*)d_in[8];
  const u16* w_down = (const u16*)d_in[9];
  const u16* c = (const u16*)d_in[10];
  const u16* w_mod = (const u16*)d_in[11];
  char* ws = (char*)d_ws;
  (void)hipFuncSetAttribute((const void*)k1_conv, hipFuncAttributeMaxDynamicSharedMemorySize, K1_LDS);
  (void)hipFuncSetAttribute((const void*)k2_gemm1, hipFuncAttributeMaxDynamicSharedMemorySize, K2_LDS);
  (void)hipFuncSetAttribute((const void*)k3_gemm2, hipFuncAttributeMaxDynamicSharedMemorySize, K3_LDS);
  k0_prep<<<372, 256, 0, stream>>>(q, k, v, shift_k, c, w_mod, ws);
  k1_conv<<<224, 448, K1_LDS, stream>>>(ssm_k, ssm_D, w_gate, w_up, w_down, ws);
  k2_gemm1<<<256, 512, K2_LDS, stream>>>(ws);
  k3_gemm2<<<224, 256, K3_LDS, stream>>>(ws, (u16*)d_out);
}
```

```cpp
#include <hip/hip_runtime.h>
#include <hip/hip_bf16.h>
#include <stdint.h>
#include <type_traits>

typedef unsigned short u16;
typedef unsigned int u32;
typedef short bf16x8 __attribute__((ext_vector_type(8)));
typedef short s16x4 __attribute__((ext_vector_type(4)));
typedef float f32x2 __attribute__((ext_vector_type(2)));
typedef float f32x4 __attribute__((ext_vector_type(4)));
typedef float f32x16 __attribute__((ext_vector_type(16)));
typedef u32 u32x2 __attribute__((ext_vector_type(2)));
typedef u32 u32x4 __attribute__((ext_vector_type(4)));
typedef __bf16 bf16x2_t __attribute__((ext_vector_type(2)));
#define LAS __attribute__((address_space(3)))
#define DI __device__ __forceinline__

constexpr int S = 2048, D = 448, F = 1792;

constexpr size_t SZ_DS = (size_t)D * S * 2;
constexpr size_t OFF_RE = 0;
constexpr size_t OFF_RO = OFF_RE + SZ_DS;
constexpr size_t OFF_QT = OFF_RO + SZ_DS;
constexpr size_t OFF_X1T = OFF_QT + SZ_DS;
constexpr size_t OFF_MEAN = OFF_X1T + SZ_DS;
constexpr size_t OFF_RSTD = OFF_MEAN + S * 4;
constexpr size_t OFF_MOD = OFF_RSTD + S * 4;
constexpr size_t OFF_SG = OFF_MOD + 16384;
constexpr size_t OFF_CB = OFF_MOD + 12288;
constexpr size_t OFF_XS = OFF_SG + 3 * S * 8;
constexpr size_t OFF_VP = OFF_XS + SZ_DS;
constexpr size_t OFF_WGU = OFF_VP + 8 * 2 * 3584 * 4;
constexpr size_t OFF_WD = OFF_WGU + (size_t)2 * D * F * 2;
constexpr size_t OFF_ACT = OFF_WD + (size_t)D * F * 2;

#define STAMP_DECL
#define STAMP(wsp, kid, i)
#define STAMP_FLUSH(wsp, kid)
#define HWID(wsp, kid)
#if defined(__HIP_DEVICE_COMPILE__)
#define NOPK __attribute__((target("no-packed-fp32-ops")))
#else
#define NOPK
#endif

template <int I, int N, class Fn>
DI void static_for(Fn&& f) {
  if constexpr (I < N) {
    f(std::integral_constant<int, I>{});
    static_for<I + 1, N>(f);
  }
}

DI float lo_f(u32 x) { return __builtin_bit_cast(float, x << 16); }
DI float hi_f(u32 x) { return __builtin_bit_cast(float, x & 0xffff0000u); }
DI u32 pack_bf16(float a, float b) {
  f32x2 v = {a, b};
  return __builtin_bit_cast(u32, __builtin_convertvector(v, bf16x2_t));
}
DI float round_bf16(float a) { return lo_f(pack_bf16(a, 0.f)); }
DI float bf2f(u16 x) { return __builtin_bit_cast(float, (u32)x << 16); }

template <int N>
DI void wait_vm() { asm volatile("s_waitcnt vmcnt(%0)" ::"n"(N) : "memory"); }
DI void wait_lgkm0() { asm volatile("s_waitcnt lgkmcnt(0)" ::: "memory"); }
DI void bar() { __builtin_amdgcn_s_barrier(); }

#define SNOP "s_nop 0\n\t"
#define SNOP2 ""
DI const void* uni(const void* p) {
  const unsigned long long v = (unsigned long long)p;
  const u32 lo = __builtin_amdgcn_readfirstlane((u32)v), hi = __builtin_amdgcn_readfirstlane((u32)(v >> 32));
  return (const void*)(((unsigned long long)hi << 32) | lo);
}
DI void glds16_asm(const void* sbase, u32 voff, u32 lds_addr) {
  u32 keep;
  sbase = uni(sbase);
  lds_addr = __builtin_amdgcn_readfirstlane(lds_addr);
  asm volatile("s_mov_b32 %0, m0\n\ts_mov_b32 m0, %3\n\t" SNOP "global_load_lds_dwordx4 %1, %2\n\ts_mov_b32 m0, %0"
               : "=&s"(keep)
               : "v"(voff), "s"(sbase), "s"(lds_addr)
               : "memory");
}
DI void glds16(const void* g, char* lds) {
  __builtin_amdgcn_global_load_lds((const __attribute__((address_space(1))) void*)g, (LAS void*)lds, 16, 0, 0);
}

template <int CTRL>
DI float dpp_add(float v) {
  const int x = __builtin_bit_cast(int, v);
  return v + __builtin_bit_cast(float, __builtin_amdgcn_update_dpp(x, x, CTRL, 0xf, 0xf, false));
}
DI float sum8(float v) {
  v = dpp_add<0xB1>(v);
  v = dpp_add<0x4E>(v);
  v = dpp_add<0x141>(v);
  return v;
}
DI float sum16(float v) { return dpp_add<0x140>(sum8(v)); }
DI float dot2(u32 a, u32 b, float c) {
  return __builtin_amdgcn_fdot2_f32_bf16(__builtin_bit_cast(bf16x2_t, a), __builtin_bit_cast(bf16x2_t, b), c, false);
}
typedef __amdgpu_buffer_rsrc_t rsrc_t;
DI rsrc_t mk_rsrc(const void* p, u32 bytes) { return __builtin_amdgcn_make_buffer_rsrc((void*)p, 0, bytes, 0x00020000); }
DI void st16_sc1(void* p, u32x4 v) { asm volatile("global_store_dwordx4 %0, %1, off sc1\n\ts_nop 1" ::"v"(p), "v"(v) : "memory"); }
DI void st8_sc1(void* p, u32x2 v) { asm volatile("global_store_dwordx2 %0, %1, off sc1" ::"v"(p), "v"(v) : "memory"); }
DI void st4_sc1(void* p, float v) { asm volatile("global_store_dword %0, %1, off sc1" ::"v"(p), "v"(v) : "memory"); }

constexpr int K0_GRID = 400;

__global__ __launch_bounds__(256) void k0_prep(const u16* __restrict__ q, const u16* __restrict__ k,
                                               const u16* __restrict__ v, const u16* __restrict__ shift_k,
                                               const u16* __restrict__ c, const u16* __restrict__ w_mod,
                                               const u16* __restrict__ ssm_k, char* __restrict__ ws) {
  __shared__ float red[4][32];
  const int b = blockIdx.x, tid = threadIdx.x, lane = tid & 63, w = tid >> 6;
  STAMP_DECL;
  STAMP(ws, 0, 0);
  const int xcd = b & 7, idx = b >> 3;
  int misc = -1;
  if (xcd == 7) misc = idx;
  else if (idx >= 32) misc = idx < 46 ? 50 + xcd * 14 + (idx - 32) : 1000;
  if (misc < 0) {
    const int rb = idx;
    const int sq = lane >> 5, cp = lane & 31;
    const int d0 = xcd * 64 + cp * 2;
    const int a = rb * 8 + w * 2 + sq;
    const int s0 = a * 8;
    const rsrc_t rk = mk_rsrc(k, S * D * 2), rv = mk_rsrc(v, S * D * 2), rq = mk_rsrc(q, S * D * 2);
    const int oB = (s0 * D + d0) * 2;
    const int oA = oB - 4 * D * 2, oC = oB + 4 * D * 2;
    u32 kk[12], vv[9], qq[8];
#pragma unroll
    for (int i = 0; i < 4; ++i) kk[i] = __builtin_amdgcn_raw_buffer_load_b32(rk, oA + i * D * 2, 0, 0);
#pragma unroll
    for (int i = 0; i < 4; ++i) kk[4 + i] = __builtin_amdgcn_raw_buffer_load_b32(rk, oB + i * D * 2, 0, 0);
#pragma unroll
    for (int i = 0; i < 4; ++i) kk[8 + i] = __builtin_amdgcn_raw_buffer_load_b32(rk, oC + i * D * 2, 0, 0);
    vv[0] = __builtin_amdgcn_raw_buffer_load_b32(rv, oA + 3 * D * 2, 0, 0);
#pragma unroll
    for (int i = 0; i < 4; ++i) vv[1 + i] = __builtin_amdgcn_raw_buffer_load_b32(rv, oB + i * D * 2, 0, 0);
#pragma unroll
    for (int i = 0; i < 4; ++i) vv[5 + i] = __builtin_amdgcn_raw_buffer_load_b32(rv, oC + i * D * 2, 0, 0);
#pragma unroll
    for (int i = 0; i < 4; ++i) qq[i] = __builtin_amdgcn_raw_buffer_load_b32(rq, oB + i * D * 2, 0, 0);
#pragma unroll
    for (int i = 0; i < 4; ++i) qq[4 + i] = __builtin_amdgcn_raw_buffer_load_b32(rq, oC + i * D * 2, 0, 0);
    const u32x4 sk = *(const u32x4*)(shift_k + d0 * 4);
    u32 warm = 0;
    if (w == 0) warm = *(const u32*)((const char*)ssm_k + ((size_t)(xcd * 32 + rb) * 64 + lane) * 128);
    f32x2 wt[4] = {{lo_f(sk[0]), lo_f(sk[2])}, {hi_f(sk[0]), hi_f(sk[2])}, {lo_f(sk[1]), lo_f(sk[3])}, {hi_f(sk[1]), hi_f(sk[3])}};
    f32x2 kf[12], u[9];
#pragma unroll
    for (int i = 0; i < 12; ++i) kf[i] = f32x2{lo_f(kk[i]), hi_f(kk[i])};
#pragma unroll
    for (int i = 0; i < 9; ++i) {
      f32x2 t = kf[i + 3] * wt[0];
      t = __builtin_elementwise_fma(kf[i + 2], wt[1], t);
      t = __builtin_elementwise_fma(kf[i + 1], wt[2], t);
      t = __builtin_elementwise_fma(kf[i], wt[3], t);
      u[i] = t * f32x2{lo_f(vv[i]), hi_f(vv[i])};
    }
    {
      char* dst = ws + (size_t)d0 * (S * 2) + (2040 - s0) * 2;
#pragma unroll
      for (int cI = 0; cI < 2; ++cI) {
        *(u32x4*)(dst + OFF_RO + cI * S * 2) = u32x4{pack_bf16(u[8][cI], u[7][cI]), pack_bf16(u[6][cI], u[5][cI]),
                                                     pack_bf16(u[4][cI], u[3][cI]), pack_bf16(u[2][cI], u[1][cI])};
        *(u32x4*)(dst + OFF_RE + cI * S * 2) = u32x4{pack_bf16(u[7][cI], u[6][cI]), pack_bf16(u[5][cI], u[4][cI]),
                                                     pack_bf16(u[3][cI], u[2][cI]), pack_bf16(u[1][cI], u[0][cI])};
      }
      char* dq = ws + OFF_QT + (size_t)d0 * (S * 2) + s0 * 2;
#pragma unroll
      for (int cI = 0; cI < 2; ++cI) {
        const u32 sel = cI ? 0x07060302u : 0x05040100u;
        *(u32x4*)(dq + cI * S * 2) = u32x4{__builtin_amdgcn_perm(qq[2], qq[0], sel), __builtin_amdgcn_perm(qq[6], qq[4], sel),
                                           __builtin_amdgcn_perm(qq[3], qq[1], sel), __builtin_amdgcn_perm(qq[7], qq[5], sel)};
      }
    }
    asm volatile("" ::"v"(warm));
  } else if (misc < 64) {
    const int bb = misc;
    const int row = bb * 32 + w * 8 + (lane >> 3);
    const int sub = lane & 7;
    u32x4 x[7];
#pragma unroll
    for (int i = 0; i < 7; ++i) x[i] = *(const u32x4*)(q + (size_t)row * D + (i * 8 + sub) * 8);
    if (sub < 3) ((unsigned long long*)(ws + OFF_SG))[sub * S + row] = 0ull;
    float s = 0.f, s2 = 0.f;
#pragma unroll
    for (int i = 0; i < 7; ++i)
#pragma unroll
      for (int j = 0; j < 4; ++j) {
        const u32 t = x[i][j];
        s = dot2(t, 0x3f803f80u, s);
        s2 = dot2(t, t, s2);
      }
    s = sum8(s);
    s2 = sum8(s2);
    const float mean = s * (1.f / D);
    float var = s2 * (1.f / D) - mean * mean;
    var = var < 0.f ? 0.f : var;
    const float rstd = __builtin_amdgcn_rsqf(var + 1e-5f);
    if (sub < 2) {
      const int p = (row & ~7) | ((row & 1) << 2) | ((row >> 1) & 3);
      ((float*)(ws + OFF_MEAN))[sub * S + p] = sub ? rstd : mean;
    }
  } else if (misc < 148) {
    const int bb = misc - 64;
    const int co = lane >> 4, rs = lane & 15;
    const int col0 = bb * 32 + co * 8;
    u32x4 x[7];
    float sc[7];
#pragma unroll
    for (int i = 0; i < 7; ++i) {
      const int r = i * 64 + w * 16 + rs;
      x[i] = *(const u32x4*)(w_mod + (size_t)r * (6 * D) + col0);
      sc[i] = bf2f(c[r]);
    }
    f32x2 acc[4] = {{0.f, 0.f}, {0.f, 0.f}, {0.f, 0.f}, {0.f, 0.f}};
#pragma unroll
    for (int i = 0; i < 7; ++i) {
      const float cv = sc[i];
      const float sl = round_bf16(cv * __builtin_amdgcn_rcpf(1.f + __builtin_amdgcn_exp2f(cv * -1.44269504f)));
#pragma unroll
      for (int j = 0; j < 4; ++j)
        acc[j] = __builtin_elementwise_fma(f32x2{lo_f(x[i][j]), hi_f(x[i][j])}, f32x2{sl, sl}, acc[j]);
    }
    float sel = 0.f;
#pragma unroll
    for (int j = 0; j < 8; ++j) {
      const float t = sum16(acc[j >> 1][j & 1]);
      sel = (rs == j) ? t : sel;
    }
    if (rs < 8) red[w][co * 8 + rs] = sel;
    __syncthreads();
    if (tid < 32) {
      const float t = (red[0][tid] + red[1][tid]) + (red[2][tid] + red[3][tid]);
      const float m = round_bf16(t);
      const int col = bb * 32 + tid;
      ((float*)(ws + OFF_MOD))[col] = m;
      if (col >= 3 * D && col < 5 * D) {
        const bool isc = col >= 4 * D;
        ((u16*)(ws + OFF_CB))[isc ? col - 4 * D : col - 2 * D] = (u16)pack_bf16(isc ? 1.f + m : m, 0.f);
      }
    }
  }
  STAMP(ws, 0, 1);
  STAMP_FLUSH(ws, misc < 0 ? 0 : (misc < 64 ? 4 : 5));
}

constexpr int K1_KSTR = 260 * 16;
constexpr int K1_USTR = 286 * 16;
constexpr int K1_UOFF = 4 * K1_KSTR;
constexpr int K1_XOFF = K1_UOFF + 2 * K1_USTR;
constexpr int K1_CH = K1_XOFF + 8192;
constexpr int K1_LDS = 2 * K1_CH;

DI void glds16x4_asm(const void* sbase, u32 voff, u32 voff_last, u32 lds_addr) {
  u32 keep;
  sbase = uni(sbase);
  lds_addr = __builtin_amdgcn_readfirstlane(lds_addr);
  asm volatile(
      "s_mov_b32 %0, m0\n\ts_mov_b32 m0, %4\n\t" SNOP
      "global_load_lds_dwordx4 %1, %3\n\t"
      "global_load_lds_dwordx4 %1, %3 offset:1024\n\t"
      "global_load_lds_dwordx4 %1, %3 offset:2048\n\t"
      "global_load_lds_dwordx4 %2, %3 offset:3072\n\t"
      "s_mov_b32 m0, %0"
      : "=&s"(keep)
      : "v"(voff), "v"(voff_last), "s"(sbase), "s"(lds_addr)
      : "memory");
}
#define CAS __attribute__((address_space(4)))
template <int half>
DI void k1_conv_wave(const u16* __restrict__ ssm_k, const u16* __restrict__ ssm_D, char* __restrict__ ws, char* smem,
                     int ch, int lane) {
  STAMP_DECL;
  STAMP(ws, 1, 0);
  const int cpair = (blockIdx.x & 7) * 32 + (blockIdx.x >> 3);
  const int d = 2 * cpair + ch;
  char* L = smem + ch * K1_CH;
  const u32 L0 = (u32)(size_t)(LAS char*)L;
  const u16* kf = ssm_k + (size_t)d * S;
  {
    const u32 vo = lane * 16, vl = (lane > 62 ? 62 : lane) * 16;
    glds16x4_asm(kf + (2 + 4 * half), vo, vl, L0 + (2 * half) * K1_KSTR + 64);
    glds16x4_asm(kf + (4 + 4 * half), vo, vl, L0 + (2 * half + 1) * K1_KSTR + 64);
    glds16x4_asm(ws + (half ? OFF_RO : OFF_RE) + (size_t)d * S * 2, vo, vo, L0 + K1_UOFF + half * K1_USTR);
  }
  const int n = lane & 15, q4 = lane >> 4;
  const int pbase = 32 * (n >> 1) + 8 * q4 + 4 * (n & 1) + 1024 * half;
  u32 head = 0;
  const int di = n + q4 - 15;
  if constexpr (half == 0) {
    asm volatile(SNOP2 "global_load_dword %0, %1, %2" : "=v"(head) : "v"((u32)(di < 0 ? 0 : di) * 4), "s"(uni(kf)) : "memory");
  }
  const CAS float* MODc = (const CAS float*)(size_t)(ws + OFF_MOD);
  const float sa = MODc[d], ca = MODc[D + d], ga = MODc[2 * D + d], cbv = MODc[4 * D + d];
  const u32 Draw = ((const CAS u32*)(size_t)ssm_D)[cpair];
  if constexpr (half == 1) {
    const int l5 = lane & 31;
    if (l5 < 28) *(LAS u32x4*)(L + K1_UOFF + (lane >> 5) * K1_USTR + (256 + l5) * 16) = u32x4{0u, 0u, 0u, 0u};
  }
  u32 Ab = L0 + (n & 3) * K1_KSTR + ((n >> 2) + q4 + 16 * half) * 16;
  u32 Bb = L0 + K1_UOFF + (n & 1) * K1_USTR + (28 - 4 * (n >> 1) + q4 + 16 * half) * 16;
  u32 Hd = L0 + q4 * K1_KSTR + n * 4;
  asm volatile("" : "+v"(Ab), "+v"(Bb), "+v"(Hd) : "s"(sa), "s"(ca), "s"(ga), "s"(Draw), "s"(cbv));
  asm volatile("s_waitcnt vmcnt(0)" : "+v"(head)::"memory");
  if constexpr (half == 0) *(LAS u32*)(size_t)Hd = di < 0 ? 0u : head;
  wait_lgkm0();
  STAMP(ws, 1, 1);
  bar();
  STAMP(ws, 1, 2);
  bf16x8 fa[2][8], fb[2][8];
  auto rd = [&](auto dI) {
    constexpr int dl = decltype(dI)::value;
#pragma unroll
    for (int g = 0; g < 8; ++g) {
      fa[dl & 1][g] = *(const LAS bf16x8*)(size_t)(Ab + 64 * dl + 512 * g);
      fb[dl & 1][g] = *(const LAS bf16x8*)(size_t)(Bb + 64 * dl + 512 * (7 - g));
    }
  };
  rd(std::integral_constant<int, 0>{});
  __builtin_amdgcn_sched_barrier(0);
  u32x2 qv[4];
  f32x4 mu[4], rs[4];
  asm volatile(SNOP2
               "global_load_dwordx2 %0, %12, %14\n\t"
               "global_load_dwordx2 %1, %12, %14 offset:512\n\t"
               "global_load_dwordx2 %2, %12, %14 offset:1024\n\t"
               "global_load_dwordx2 %3, %12, %14 offset:1536\n\t"
               "global_load_dwordx4 %4, %13, %15\n\t"
               "global_load_dwordx4 %5, %13, %15 offset:1024\n\t"
               "global_load_dwordx4 %6, %13, %15 offset:2048\n\t"
               "global_load_dwordx4 %7, %13, %15 offset:3072\n\t"
               "global_load_dwordx4 %8, %13, %16\n\t"
               "global_load_dwordx4 %9, %13, %16 offset:1024\n\t"
               "global_load_dwordx4 %10, %13, %16 offset:2048\n\t"
               "global_load_dwordx4 %11, %13, %16 offset:3072"
               : "=&v"(qv[0]), "=&v"(qv[1]), "=&v"(qv[2]), "=&v"(qv[3]), "=&v"(mu[0]), "=&v"(mu[1]), "=&v"(mu[2]),
                 "=&v"(mu[3]), "=&v"(rs[0]), "=&v"(rs[1]), "=&v"(rs[2]), "=&v"(rs[3])
               : "v"((u32)pbase * 2), "v"((u32)pbase * 4), "s"(uni(ws + OFF_QT + (size_t)d * S * 2)),
                 "s"(uni(ws + OFF_MEAN)), "s"(uni(ws + OFF_RSTD))
               : "memory");
  f32x4 acc[8];
#pragma unroll
  for (int j = 0; j < 8; ++j) acc[j] = f32x4{0.f, 0.f, 0.f, 0.f};
  f32x4 qf[4], hg[4], kd[4];
  u32x4 uc[4];
  static_for<0, 4>([&](auto dI) {
    constexpr int dl = decltype(dI)::value;
    if constexpr (dl < 3) rd(std::integral_constant<int, dl + 1>{});
    if constexpr (dl == 2) {
      asm volatile("s_waitcnt vmcnt(0)"
                   : "+v"(qv[0]), "+v"(qv[1]), "+v"(qv[2]), "+v"(qv[3]), "+v"(mu[0]), "+v"(mu[1]), "+v"(mu[2]), "+v"(mu[3]),
                     "+v"(rs[0]), "+v"(rs[1]), "+v"(rs[2]), "+v"(rs[3]));
    }
    if constexpr (dl == 1) {
#pragma unroll
      for (int j = 0; j < 4; ++j)
        uc[j] = *(const LAS u32x4*)(L + K1_UOFF + K1_USTR + (255 - (32 * (4 * half + j) + 4 * (n >> 1) + q4)) * 16);
    }
    if constexpr (dl >= 2) {
      const int sh = (n & 1) ? 16 : 0;
      const float gc = ga * round_bf16(1.f + ca), gs = ga * sa, Dd = ch ? hi_f(Draw) : lo_f(Draw);
#pragma unroll
      for (int j = 2 * (dl - 2); j < 2 * (dl - 2) + 2; ++j) {
#pragma unroll
        for (int r = 0; r < 4; ++r) {
          kd[j][r] = __builtin_bit_cast(float, (uc[j][3 - r] << sh) & 0xffff0000u) * Dd;
          qf[j][r] = (r & 1) ? hi_f(qv[j][r >> 1]) : lo_f(qv[j][r >> 1]);
          hg[j][r] = (qf[j][r] - mu[j][r]) * rs[j][r] * gc + gs;
        }
        asm volatile("" : "+v"(kd[j]), "+v"(qf[j]), "+v"(hg[j]));
      }
    }
#pragma unroll
    for (int sdiag = 0; sdiag < 8; ++sdiag)
#pragma unroll
      for (int g = 0; g <= sdiag; ++g)
        acc[sdiag] = __builtin_amdgcn_mfma_f32_16x16x32_bf16(fa[dl & 1][g], fb[dl & 1][sdiag - g], acc[sdiag], 0, 0, 0);
    if constexpr (dl == 0) {
#pragma unroll
      for (int i = 0; i < 16; ++i) {
        __builtin_amdgcn_sched_group_barrier(0x008, 2, 0);
        __builtin_amdgcn_sched_group_barrier(0x100, 1, 0);
      }
    } else if constexpr (dl == 1) {
#pragma unroll
      for (int i = 0; i < 16; ++i) {
        __builtin_amdgcn_sched_group_barrier(0x008, 2, 0);
        __builtin_amdgcn_sched_group_barrier(0x100, 1, 0);
      }
#pragma unroll
      for (int i = 0; i < 4; ++i) {
        __builtin_amdgcn_sched_group_barrier(0x008, 1, 0);
        __builtin_amdgcn_sched_group_barrier(0x100, 1, 0);
      }
    } else {
      __builtin_amdgcn_sched_group_barrier(0x008, 4, 0);
#pragma unroll
      for (int i = 0; i < 16; ++i) {
        __builtin_amdgcn_sched_group_barrier(0x008, 1, 0);
        __builtin_amdgcn_sched_group_barrier(0x002, 2, 0);
        __builtin_amdgcn_sched_group_barrier(0x008, 1, 0);
        __builtin_amdgcn_sched_group_barrier(0x002, 2, 0);
        __builtin_amdgcn_sched_group_barrier(0x100, 1, 0);
      }
    }
    __builtin_amdgcn_sched_barrier(0);
  });
  STAMP(ws, 1, 3);
  LAS f32x4* X = (LAS f32x4*)(L + K1_XOFF);
#pragma unroll
  for (int j = 0; j < 4; ++j) X[half * 256 + j * 64 + lane] = acc[4 * (1 - half) + j];
  wait_lgkm0();
  bar();
  STAMP(ws, 1, 4);
  u16* X1T = (u16*)(ws + OFF_X1T) + (size_t)d * S + pbase;
  u16* XS = (u16*)(ws + OFF_XS) + (size_t)d * S + pbase;
  const float c1 = round_bf16(1.f + cbv);
#pragma unroll
  for (int j = 0; j < 4; ++j) {
    const f32x4 y = X[(1 - half) * 256 + j * 64 + lane];
    const f32x4 o = qf[j] + hg[j] * ((acc[4 * half + j] + kd[j]) + y);
    const u32 p0 = pack_bf16(o[0], o[1]), p1 = pack_bf16(o[2], o[3]);
    st8_sc1(XS + 256 * j, u32x2{pack_bf16(lo_f(p0) * c1, hi_f(p0) * c1), pack_bf16(lo_f(p1) * c1, hi_f(p1) * c1)});
    st8_sc1(X1T + 256 * j, u32x2{p0, p1});
  }
  STAMP(ws, 1, 5);
  STAMP_FLUSH(ws, 1);
}

DI bf16x8 tr_frag(const char* smem, int o0, int o1) {
  s16x4 x0 = __builtin_amdgcn_ds_read_tr16_b64_v4i16((LAS s16x4*)(smem + o0));
  s16x4 x1 = __builtin_amdgcn_ds_read_tr16_b64_v4i16((LAS s16x4*)(smem + o1));
  return __builtin_shufflevector(x0, x1, 0, 1, 2, 3, 4, 5, 6, 7);
}
DI f32x16 mfma32(u32x4 a, bf16x8 b, f32x16 c) {
  return __builtin_amdgcn_mfma_f32_32x32x16_bf16(__builtin_bit_cast(bf16x8, a), b, c, 0, 0, 0);
}

DI void k1_helper(int task, const u16* __restrict__ w_gate, const u16* __restrict__ w_up, const u16* __restrict__ w_down,
                  char* __restrict__ ws, char* smem, int w, int lane) {
  const u32 lds0 = (u32)(size_t)(LAS char*)smem;
  const int xr = lane >> 4, slot = lane & 15;
  const int chn = slot ^ ((xr << 2) | (w & 3));
  const int h = lane >> 5, blk = (lane >> 4) & 1, qq = (lane & 15) >> 2, p = lane & 3, n = lane & 31;
  const bool gu = task < 112;
  int kq = 0, fg = 0, dg = 0, fk = 0;
  if (gu) {
    fg = task % 28, kq = task / 28;
    const u16* src = (chn < 8 ? w_gate : w_up) + (size_t)(kq * 112 + 4 * w + xr) * F + fg * 64 + (chn & 7) * 8;
#pragma unroll
    for (int i = 0; i < 7; ++i) glds16(src + (size_t)i * 16 * F, smem + (w + 4 * i) * 1024);
  } else {
    dg = (task - 112) % 7, fk = (task - 112) / 7;
    const u16* src = w_down + (size_t)(fk * 224 + (chn >> 3) * 112 + 4 * w + xr) * D + dg * 64 + (chn & 7) * 8;
#pragma unroll
    for (int i = 0; i < 7; ++i) glds16(src + (size_t)i * 16 * D, smem + (w + 4 * i) * 1024);
  }
  const int col = gu ? blk * 128 + w * 32 + (p & 1) * 16 + (p >> 1) * 8 : (w >> 1) * 128 + (w & 1) * 64 + blk * 32 + p * 8;
  int o[2];
#pragma unroll
  for (int t = 0; t < 2; ++t)
    o[t] = 256 * (8 * h + 4 * t + qq) + 16 * ((col >> 4) ^ ((qq << 2) | ((2 * h + t) & 3))) + (col & 8);
  u32x4 bv[7];
  if (gu) {
    const u16* cbp = (const u16*)(ws + OFF_CB) + (n & 1) * D + kq * 112 + 8 * h;
#pragma unroll
    for (int i = 0; i < 7; ++i) bv[i] = *(const u32x4*)(cbp + 16 * i);
  }
  __syncthreads();
  u32x4* dst = gu ? (u32x4*)(ws + OFF_WGU) + ((size_t)(fg * 4 + w) * 28 + kq * 7) * 64 + lane
                  : (u32x4*)(ws + OFF_WD) + ((size_t)(dg * 2 + (w & 1)) * 112 + fk * 14 + (w >> 1) * 7) * 64 + lane;
  f32x16 acc;
#pragma unroll
  for (int r = 0; r < 16; ++r) acc[r] = 0.f;
#pragma unroll
  for (int i = 0; i < 7; ++i) {
    const u32x4 f = __builtin_bit_cast(u32x4, tr_frag(smem, o[0] + i * 4096, o[1] + i * 4096));
    st16_sc1(dst + i * 64, f);
    if (gu) acc = mfma32(f, __builtin_bit_cast(bf16x8, bv[i]), acc);
  }
  if (gu && n < 2) {
    float* vd = (float*)(ws + OFF_VP) + (size_t)(kq * 2 + n) * 3584 + (fg * 4 + w) * 32 + 4 * h;
#pragma unroll
    for (int g = 0; g < 4; ++g) *(f32x4*)(vd + 8 * g) = f32x4{acc[4 * g], acc[4 * g + 1], acc[4 * g + 2], acc[4 * g + 3]};
  }
}

constexpr int K1_GRID = 400;
__global__ NOPK __launch_bounds__(256, 2) void k1_conv(const u16* __restrict__ ssm_k, const u16* __restrict__ ssm_D,
                                                       const u16* __restrict__ w_gate, const u16* __restrict__ w_up,
                                                       const u16* __restrict__ w_down, char* __restrict__ ws) {
  extern __shared__ __attribute__((aligned(16))) char smem[];
  const int tid = threadIdx.x, lane = tid & 63;
  const int w = __builtin_amdgcn_readfirstlane(tid >> 6);
  const int xcd = blockIdx.x & 7, idx = blockIdx.x >> 3;
  if (xcd < 7 && idx < 32) {
    if (w & 1) k1_conv_wave<1>(ssm_k, ssm_D, ws, smem, w >> 1, lane);
    else k1_conv_wave<0>(ssm_k, ssm_D, ws, smem, w >> 1, lane);
  } else {
    STAMP_DECL;
    STAMP(ws, 7, 0);
    if (xcd == 7 && idx >= 42) return;
    const int task = xcd == 7 ? idx : 42 + xcd * 18 + (idx - 32);
    k1_helper(task, w_gate, w_up, w_down, ws, smem, w, lane);
    STAMP(ws, 7, 1);
    STAMP_FLUSH(ws, 7);
  }
}

constexpr int K2_XB = D * 256;
constexpr int K2_ST = K2_XB;
constexpr int K2_LDS = K2_XB + 3072;

DI void gload16_asm(u32x4& dst, const void* sbase, u32 voff) {
  sbase = uni(sbase);
  asm volatile(SNOP2 "global_load_dwordx4 %0, %1, %2" : "=v"(dst) : "v"(voff), "s"(sbase) : "memory");
}
template <int N>
DI void wait_vm_pin(u32x4& a, u32x4& b) {
  asm volatile("s_waitcnt vmcnt(%2)" : "+v"(a), "+v"(b) : "n"(N) : "memory");
}
DI float xor32(float v) {
  const u32 u = __builtin_bit_cast(u32, v);
  auto r = __builtin_amdgcn_permlane32_swap(u, u, false, false);
  const u32 a = r[0], b = r[1];
  return __builtin_bit_cast(float, (threadIdx.x & 32) ? a : b);
}

typedef unsigned long long u64;
typedef __attribute__((address_space(1))) u64 gu64;
#define RLX_AGENT __ATOMIC_RELAXED, __HIP_MEMORY_SCOPE_AGENT
#define EXP_AHEAD 2
constexpr int K2_AHEAD = EXP_AHEAD;

DI void k2_body(char* __restrict__ ws, char* smem) {
  const int tid = threadIdx.x, lane = tid & 63;
  const int w = __builtin_amdgcn_readfirstlane(tid >> 6);
  const int n = lane & 31, h = lane >> 5;
  STAMP_DECL;
  STAMP(ws, 2, 0);
  if (blockIdx.x < 32) {
    const int p0 = blockIdx.x * 64;
    const u32* src = (const u32*)(ws + OFF_X1T) + ((size_t)(w * 56 + h) * S + p0) / 2 + n;
    u32 x[28];
#pragma unroll
    for (int i = 0; i < 28; ++i) x[i] = src[(size_t)i * S];
    float s0 = 0.f, s1 = 0.f, q0 = 0.f, q1 = 0.f;
#pragma unroll
    for (int i = 0; i < 28; ++i) {
      const float a = lo_f(x[i]), b = hi_f(x[i]);
      s0 += a;
      s1 += b;
      q0 = __builtin_fmaf(a, a, q0);
      q1 = __builtin_fmaf(b, b, q1);
    }
    *(LAS f32x4*)(smem + ((w * 2 + h) * 32 + n) * 16) = f32x4{s0, q0, s1, q1};
    __syncthreads();
    if (tid < 64) {
      float sm = 0.f, sq = 0.f;
#pragma unroll
      for (int i = 0; i < 16; ++i) {
        const f32x2 t = *(const LAS f32x2*)(smem + (i * 64 + tid) * 8);
        sm += t[0];
        sq += t[1];
      }
      const float mean = sm * (1.f / D);
      float var = sq * (1.f / D) - mean * mean;
      var = (var < 0.f ? 0.f : var) + 1e-5f;
      const float rstd = 1.f / sqrtf(var);
      const float sig = var * rstd;
      const float mh = round_bf16(mean), sh = round_bf16(sig);
      const u32 b0 = pack_bf16(-mh, mh - mean), b1 = pack_bf16(-mh, sh), b2 = pack_bf16(sig - sh, sh);
      const float c1 = -2.302208198f * rstd;
      const float c2 = -0.1029432397f * (rstd * rstd * rstd);
      gu64* G = (gu64*)(size_t)(ws + OFF_SG) + p0 + tid;
      __hip_atomic_store(G, ((u64)b1 << 32) | b0, RLX_AGENT);
      __hip_atomic_store(G + S, ((u64)__builtin_bit_cast(u32, var) << 32) | b2, RLX_AGENT);
      __hip_atomic_store(G + 2 * S, ((u64)__builtin_bit_cast(u32, c1) << 32) | __builtin_bit_cast(u32, c2), RLX_AGENT);
    }
    STAMP(ws, 6, 1);
    STAMP_FLUSH(ws, 6);
    return;
  }
  const int bid = blockIdx.x - 32;
  const int xcd = bid & 7, idx = bid >> 3;
  const int mt = (xcd >> 1) * 4 + (idx & 3);
  const int ft = (xcd & 1) * 7 + (idx >> 2);
  const int tile = ft * 8 + w;
  const u32 lds0 = (u32)(size_t)(LAS char*)smem;
  u32x4 wf[28];
  const int xr = lane >> 4, slot = lane & 15;
  const char* xbase = ws + OFF_XS + ((size_t)(4 * w) * S + mt * 128) * 2;
  const u32 xoff = (u32)(xr * S + (slot ^ ((xr << 2) | (w & 3))) * 8) * 2;
  const char* wbase = ws + OFF_WGU + (size_t)(tile * 28) * 1024;
  const u32 wa = lane * 16, wb = lane * 16 + 1024;
  auto issue1 = [&](auto qI) {
    constexpr int q = decltype(qI)::value, j = q / 3, r = q % 3;
    if constexpr (r == 0) glds16_asm(xbase + (size_t)j * 32 * S * 2, xoff, lds0 + (32 * j + 4 * w) * 256);
    if constexpr (r == 1) gload16_asm(wf[2 * j], wbase + j * 2048, wa);
    if constexpr (r == 2) gload16_asm(wf[2 * j + 1], wbase + j * 2048, wb);
  };
  static_for<0, 3>(issue1);
  float vp[8];
  {
    const char* vb = ws + OFF_VP + (size_t)tile * 128;
    static_for<0, 8>([&](auto iI) {
      constexpr int i = decltype(iI)::value;
      float t;
      asm volatile(SNOP2 "global_load_dword %0, %1, %2" : "=v"(t) : "v"((u32)n * 4), "s"(uni(vb + i * 3584 * 4)) : "memory");
      vp[i] = t;
    });
  }
  static_for<3, 3 * (K2_AHEAD + 1)>(issue1);
  int xo[4][2];
  {
    const int blk = (lane >> 4) & 1, qq = (lane & 15) >> 2, p = lane & 3;
#pragma unroll
    for (int i = 0; i < 4; ++i) {
      const int nt = (w + 1 + i) & 3;
#pragma unroll
      for (int t = 0; t < 2; ++t) {
        const int row = 8 * h + 4 * t + qq;
        const int chn = 4 * nt + 2 * blk + (p >> 1);
        xo[i][t] = 256 * row + 16 * (chn ^ ((qq << 2) | ((2 * h + t) & 3))) + 8 * (p & 1);
      }
    }
  }
  f32x16 acc[4];
#pragma unroll
  for (int i = 0; i < 4; ++i)
#pragma unroll
    for (int r = 0; r < 16; ++r) acc[i][r] = 0.f;
  bf16x8 xa[2][3], xb[2][3];
  auto rd = [&](auto jI, bf16x8(&f)[2][3]) {
    constexpr int j = decltype(jI)::value;
#pragma unroll
    for (int ks = 0; ks < 2; ++ks)
#pragma unroll
      for (int i = 0; i < 3; ++i) f[ks][i] = tr_frag(smem, xo[i][0] + ks * 4096 + j * 8192, xo[i][1] + ks * 4096 + j * 8192);
  };
  asm volatile("" : "+v"(xo[0][0]), "+v"(xo[0][1]), "+v"(xo[1][0]), "+v"(xo[1][1]), "+v"(xo[2][0]), "+v"(xo[2][1]),
               "+v"(xo[3][0]), "+v"(xo[3][1]));
  wait_vm<3 * K2_AHEAD + 2 + 8>();
  __builtin_amdgcn_sched_barrier(0);
  bar();
  STAMP(ws, 2, 1);
  rd(std::integral_constant<int, 0>{}, xa);
  __builtin_amdgcn_sched_barrier(0);
  u32x4 aext;
  auto mk_aext = [&]() {
    float t0 = vp[0], t1 = vp[1], t2 = vp[2], t3 = vp[3], t4 = vp[4], t5 = vp[5], t6 = vp[6], t7 = vp[7];
    asm volatile("" : "+v"(t0), "+v"(t1), "+v"(t2), "+v"(t3), "+v"(t4), "+v"(t5), "+v"(t6), "+v"(t7));
    const float v1 = (t0 + t2) + (t4 + t6);
    const float v2 = (t1 + t3) + (t5 + t7);
    const float v1h = round_bf16(v1), v2h = round_bf16(v2);
    u32x4 a;
    a[0] = pack_bf16(v1h, v1h);
    a[1] = pack_bf16(v1 - v1h, v2h);
    a[2] = pack_bf16(v2h, v2 - v2h);
    a[3] = 0u;
    if (h) a = u32x4{0u, 0u, 0u, 0u};
    asm volatile("" : "+v"(a));
    aext = a;
  };
  const int gsl = w < 6 ? w : w - 6;
  gu64* G = (gu64*)(size_t)(ws + OFF_SG) + (gsl >> 1) * S + mt * 128 + (gsl & 1) * 64 + lane;
  u64 gv;
  constexpr int JG = 13 - K2_AHEAD;
  auto pin = [](f32x16& a) { asm volatile("" : "+v"(a)); };
  auto chunk = [&](auto jI, const bf16x8(&cur)[2][3], bf16x8(&nxt)[2][3]) {
    constexpr int j = decltype(jI)::value;
    constexpr int last = (j + K2_AHEAD < 13) ? j + K2_AHEAD : 13;
    constexpr int ex = (j > JG && j <= 12) ? 1 : 0;
    if constexpr (j < 13) {
      wait_vm_pin<3 * (last - (j + 1)) + 2 + ex>(wf[2 * j], wf[2 * j + 1]);
      __builtin_amdgcn_sched_barrier(0);
      bar();
    } else {
      wait_vm_pin<0>(wf[2 * j], wf[2 * j + 1]);
    }
    __builtin_amdgcn_sched_barrier(0);
    static_for<0, 6>([&](auto iI) {
      constexpr int sl = decltype(iI)::value, ks = sl / 3, i = sl % 3;
      acc[i] = mfma32(wf[2 * j + ks], cur[ks][i], acc[i]);
      pin(acc[i]);
      __builtin_amdgcn_sched_barrier(0);
      if constexpr (j < 13)
        nxt[ks][i] = tr_frag(smem, xo[i][0] + ks * 4096 + (j + 1) * 8192, xo[i][1] + ks * 4096 + (j + 1) * 8192);
      if constexpr ((sl & 1) == 0 && j + K2_AHEAD + 1 <= 13)
        issue1(std::integral_constant<int, 3 * (j + K2_AHEAD + 1) + sl / 2>{});
      if constexpr (j == JG && sl == 0) gv = __hip_atomic_load(G, RLX_AGENT);
      if constexpr (j == 1 && sl == 1) mk_aext();
      __builtin_amdgcn_sched_barrier(0);
    });
  };
  static_for<0, 14>([&](auto jI) {
    if constexpr (decltype(jI)::value & 1) chunk(jI, xb, xa);
    else chunk(jI, xa, xb);
  });
  STAMP(ws, 2, 2);
  constexpr int PF = 4;
  bf16x8 xr3[PF];
  auto rd3 = [&](auto kI) {
    constexpr int k = decltype(kI)::value;
    xr3[k % PF] = tr_frag(smem, xo[3][0] + k * 4096, xo[3][1] + k * 4096);
  };
  static_for<0, PF - 1>(rd3);
  __builtin_amdgcn_sched_barrier(0);
  constexpr int PRE = 2;
  static_for<0, PRE>([&](auto kI) {
    constexpr int k = decltype(kI)::value;
    rd3(std::integral_constant<int, k + PF - 1>{});
    acc[3] = mfma32(wf[k], xr3[k % PF], acc[3]);
    asm volatile("" : "+v"(acc[3]));
    __builtin_amdgcn_sched_barrier(0);
  });
  for (int spin = 0; !__all((gv >> 32) != 0) && spin < (1 << 16); ++spin) {
    __builtin_amdgcn_s_sleep(2);
    gv = __hip_atomic_load(G, RLX_AGENT);
  }
  LAS u64* CS = (LAS u64*)(smem + K2_ST);
  CS[gsl * 64 + lane] = gv;
  wait_lgkm0();
  bar();
  u64 g[4][3];
  auto ldg = [&](auto iI) {
    constexpr int i = decltype(iI)::value;
#pragma unroll
    for (int k = 0; k < 3; ++k) g[i][k] = CS[k * 128 + ((w + 1 + i) & 3) * 32 + n];
  };
  u32x4* act_base = (u32x4*)(ws + OFF_ACT) + ((size_t)(mt * 4) * 112 + tile) * 64 + lane;
  u32x4 ov;
  auto corr = [&](auto iI) {
    constexpr int i = decltype(iI)::value;
    const u32x4 bext = {(u32)g[i][0], (u32)(g[i][0] >> 32), (u32)g[i][1], 0u};
    acc[i] = mfma32(aext, __builtin_bit_cast(bf16x8, bext), acc[i]);
  };
  auto gl = [&](auto iI, int r) {
    constexpr int i = decltype(iI)::value;
    const float c1 = __builtin_bit_cast(float, (u32)(g[i][2] >> 32)), c2 = __builtin_bit_cast(float, (u32)g[i][2]);
    const float var = __builtin_bit_cast(float, (u32)(g[i][1] >> 32));
    const float a = acc[i][r], u = acc[i][r + 8];
    const float z = __builtin_fmaf(a * a, c2, c1) * a;
    const float e = __builtin_amdgcn_exp2f(z);
    return (a * u) * __builtin_amdgcn_rcpf(__builtin_fmaf(e, var, var));
  };
  auto unitB = [&](auto iI, auto gI) {
    constexpr int gq = decltype(gI)::value;
    ov[gq] = pack_bf16(gl(iI, 2 * gq), gl(iI, 2 * gq + 1));
  };
  auto unitC = [&](auto iI) {
    constexpr int i = decltype(iI)::value;
    act_base[(size_t)((w + 1 + i) & 3) * 112 * 64] = ov;
  };
  ldg(std::integral_constant<int, 0>{});
  __builtin_amdgcn_sched_barrier(0);
  static_for<PRE, 28>([&](auto kI) {
    constexpr int k = decltype(kI)::value;
    if constexpr (k + PF - 1 < 28) rd3(std::integral_constant<int, k + PF - 1>{});
    acc[3] = mfma32(wf[k], xr3[k % PF], acc[3]);
    constexpr int ti = (k - PRE) / 8, u = (k - PRE) % 8;
    if constexpr (ti < 3) {
      using TI = std::integral_constant<int, ti>;
      if constexpr (u == 0) corr(TI{});
      if constexpr (u == 1) unitB(TI{}, std::integral_constant<int, 0>{});
      if constexpr (u == 3) unitB(TI{}, std::integral_constant<int, 1>{});
      if constexpr (u == 5) unitB(TI{}, std::integral_constant<int, 2>{});
      if constexpr (u == 6) ldg(std::integral_constant<int, ti + 1>{});
      if constexpr (u == 7) {
        unitB(TI{}, std::integral_constant<int, 3>{});
        unitC(TI{});
      }
    }
    __builtin_amdgcn_sched_barrier(0);
  });
  STAMP(ws, 2, 3);
  corr(std::integral_constant<int, 3>{});
  static_for<0, 4>([&](auto gI) { unitB(std::integral_constant<int, 3>{}, gI); });
  unitC(std::integral_constant<int, 3>{});
  STAMP(ws, 2, 4);
  STAMP_FLUSH(ws, 2);
}

__global__ NOPK __launch_bounds__(512, 2) void k2_gemm1(char* __restrict__ ws) {
  extern __shared__ __attribute__((aligned(16))) char smem[];
  k2_body(ws, smem);
}

constexpr int K3_WIN = 12;
constexpr int K3_LDS = 4 * 3 * 4 * 1024;

__global__ __launch_bounds__(256, 1) void k3_gemm2(const char* __restrict__ ws, u16* __restrict__ out) {
  extern __shared__ __attribute__((aligned(16))) char smem[];
  const int tid = threadIdx.x, lane = tid & 63;
  const int w = __builtin_amdgcn_readfirstlane(tid >> 6);
  const int xcd = blockIdx.x & 7, idx = blockIdx.x >> 3;
  const int st = xcd * 4 + (idx & 3), dt = idx >> 2;
  const u32x4* a0 = (const u32x4*)(ws + OFF_WD) + ((size_t)(2 * dt) * 112 + 28 * w) * 64 + lane;
  const u32x4* a1 = a0 + 112 * 64;
  const u32x4* b0 = (const u32x4*)(ws + OFF_ACT) + ((size_t)(2 * st) * 112 + 28 * w) * 64 + lane;
  const u32x4* b1 = b0 + 112 * 64;
  STAMP_DECL;
  STAMP(ws, 3, 0);
  u32x4 ra0[K3_WIN], ra1[K3_WIN], rb0[K3_WIN], rb1[K3_WIN];
#pragma unroll
  for (int i = 0; i < K3_WIN; ++i) {
    ra0[i] = a0[i * 64];
    rb0[i] = b0[i * 64];
    ra1[i] = a1[i * 64];
    rb1[i] = b1[i * 64];
  }
  __builtin_amdgcn_sched_barrier(0);
  const int ti = w >> 1, tj = w & 1, n = lane & 31, h = lane >> 5;
  const int p = st * 64 + tj * 32 + n;
  const int dbase = dt * 64 + ti * 32 + 4 * h;
  u16 xr[16];
  f32x4 gb[4];
  {
    const u16* X1T = (const u16*)(ws + OFF_X1T);
#pragma unroll
    for (int r = 0; r < 16; ++r) xr[r] = X1T[(size_t)(dbase + 8 * (r >> 2) + (r & 3)) * S + p];
#pragma unroll
    for (int g = 0; g < 4; ++g) gb[g] = *(const f32x4*)((const float*)(ws + OFF_MOD) + 5 * D + dbase + 8 * g);
  }
  __builtin_amdgcn_sched_barrier(0);
  f32x16 acc[4];
#pragma unroll
  for (int i = 0; i < 4; ++i)
#pragma unroll
    for (int r = 0; r < 16; ++r) acc[i][r] = 0.f;
#pragma unroll
  for (int i = 0; i < 28; ++i) {
    const bf16x8 fa0 = __builtin_bit_cast(bf16x8, ra0[i % K3_WIN]), fa1 = __builtin_bit_cast(bf16x8, ra1[i % K3_WIN]);
    const bf16x8 fb0 = __builtin_bit_cast(bf16x8, rb0[i % K3_WIN]), fb1 = __builtin_bit_cast(bf16x8, rb1[i % K3_WIN]);
    acc[0] = __builtin_amdgcn_mfma_f32_32x32x16_bf16(fa0, fb0, acc[0], 0, 0, 0);
    acc[1] = __builtin_amdgcn_mfma_f32_32x32x16_bf16(fa0, fb1, acc[1], 0, 0, 0);
    acc[2] = __builtin_amdgcn_mfma_f32_32x32x16_bf16(fa1, fb0, acc[2], 0, 0, 0);
    acc[3] = __builtin_amdgcn_mfma_f32_32x32x16_bf16(fa1, fb1, acc[3], 0, 0, 0);
    if (i + K3_WIN < 28) {
      ra0[i % K3_WIN] = a0[(i + K3_WIN) * 64];
      rb0[i % K3_WIN] = b0[(i + K3_WIN) * 64];
      ra1[i % K3_WIN] = a1[(i + K3_WIN) * 64];
      rb1[i % K3_WIN] = b1[(i + K3_WIN) * 64];
    }
    __builtin_amdgcn_sched_barrier(0);
  }
  STAMP(ws, 3, 1);
  LAS f32x4* R = (LAS f32x4*)smem;
#pragma unroll
  for (int t = 0; t < 4; ++t) {
    if (t != w) {
      const int slot = (w - t - 1) & 3;
#pragma unroll
      for (int g = 0; g < 4; ++g)
        R[((t * 3 + slot) * 4 + g) * 64 + lane] = f32x4{acc[t][4 * g], acc[t][4 * g + 1], acc[t][4 * g + 2], acc[t][4 * g + 3]};
    }
  }
  __syncthreads();
  STAMP(ws, 3, 2);
  f32x16 own = w == 0 ? acc[0] : (w == 1 ? acc[1] : (w == 2 ? acc[2] : acc[3]));
  const int s = (p & ~7) | ((p & 3) << 1) | ((p >> 2) & 1);
#pragma unroll
  for (int g = 0; g < 4; ++g) {
    f32x4 vsum = f32x4{own[4 * g], own[4 * g + 1], own[4 * g + 2], own[4 * g + 3]};
#pragma unroll
    for (int sl = 0; sl < 3; ++sl) vsum += R[((w * 3 + sl) * 4 + g) * 64 + lane];
    float o[4];
#pragma unroll
    for (int e = 0; e < 4; ++e) o[e] = bf2f(xr[4 * g + e]) + gb[g][e] * vsum[e];
    *(u32x2*)(out + (size_t)s * D + dbase + 8 * g) = u32x2{pack_bf16(o[0], o[1]), pack_bf16(o[2], o[3])};
  }
  STAMP(ws, 3, 3);
  STAMP_FLUSH(ws, 3);
}

extern "C" void kernel_launch(void* const* d_in, const int* in_sizes, int n_in, void* d_out, int out_size, void* d_ws,
                              size_t ws_size, hipStream_t stream) {
  const u16* q = (const u16*)d_in[0];
  const u16* k = (const u16*)d_in[1];
  const u16* v = (const u16*)d_in[2];
  const u16* shift_k = (const u16*)d_in[3];
  const u16* ssm_k = (const u16*)d_in[4];
  const u16* ssm_D = (const u16*)d_in[5];
  const u16* w_gate = (const u16*)d_in[7];
  const u16* w_up = (const u16*)d_in[8];
  const u16* w_down = (const u16*)d_in[9];
  const u16* c = (const u16*)d_in[10];
  const u16* w_mod = (const u16*)d_in[11];
  char* ws = (char*)d_ws;
  (void)hipFuncSetAttribute((const void*)k1_conv, hipFuncAttributeMaxDynamicSharedMemorySize, K1_LDS);
  (void)hipFuncSetAttribute((const void*)k2_gemm1, hipFuncAttributeMaxDynamicSharedMemorySize, K2_LDS);
  (void)hipFuncSetAttribute((const void*)k3_gemm2, hipFuncAttributeMaxDynamicSharedMemorySize, K3_LDS);
  k0_prep<<<K0_GRID, 256, 0, stream>>>(q, k, v, shift_k, c, w_mod, ssm_k, ws);
  k1_conv<<<K1_GRID, 256, K1_LDS, stream>>>(ssm_k, ssm_D, w_gate, w_up, w_down, ws);
  k2_gemm1<<<256, 512, K2_LDS, stream>>>(ws);
  k3_gemm2<<<224, 256, K3_LDS, stream>>>(ws, (u16*)d_out);
}
```

```cpp
#include <hip/hip_runtime.h>
#include <hip/hip_bf16.h>
#include <stdint.h>
#include <type_traits>

typedef unsigned short u16;
typedef unsigned int u32;
typedef short bf16x8 __attribute__((ext_vector_type(8)));
typedef short s16x4 __attribute__((ext_vector_type(4)));
typedef float f32x2 __attribute__((ext_vector_type(2)));
typedef float f32x4 __attribute__((ext_vector_type(4)));
typedef float f32x16 __attribute__((ext_vector_type(16)));
typedef u32 u32x2 __attribute__((ext_vector_type(2)));
typedef u32 u32x4 __attribute__((ext_vector_type(4)));
typedef __bf16 bf16x2_t __attribute__((ext_vector_type(2)));
#define LAS __attribute__((address_space(3)))
#define DI __device__ __forceinline__

constexpr int S = 2048, D = 448, F = 1792;

constexpr size_t SZ_DS = (size_t)D * S * 2;
constexpr size_t OFF_RE = 0;
constexpr size_t OFF_RO = OFF_RE + SZ_DS;
constexpr size_t OFF_QT = OFF_RO + SZ_DS;
constexpr size_t OFF_X1T = OFF_QT + SZ_DS;
constexpr size_t OFF_MEAN = OFF_X1T + SZ_DS;
constexpr size_t OFF_RSTD = OFF_MEAN + S * 4;
constexpr size_t OFF_MOD = OFF_RSTD + S * 4;
constexpr size_t OFF_SG = OFF_MOD + 16384;
constexpr size_t OFF_CB = OFF_MOD + 12288;
constexpr size_t OFF_FLAG = OFF_MOD + 14336;
constexpr size_t OFF_XS = OFF_SG + 3 * S * 8;
constexpr size_t OFF_VP = OFF_XS + SZ_DS;
constexpr size_t OFF_WGU = OFF_VP + 8 * 2 * 3584 * 4;
constexpr size_t OFF_WD = OFF_WGU + (size_t)2 * D * F * 2;
constexpr size_t OFF_ACT = OFF_WD + (size_t)D * F * 2;

#define STAMP_DECL
#define STAMP(wsp, kid, i)
#define STAMP_FLUSH(wsp, kid)
#define HWID(wsp, kid)
#if defined(__HIP_DEVICE_COMPILE__)
#define NOPK __attribute__((target("no-packed-fp32-ops")))
#else
#define NOPK
#endif

template <int I, int N, class Fn>
DI void static_for(Fn&& f) {
  if constexpr (I < N) {
    f(std::integral_constant<int, I>{});
    static_for<I + 1, N>(f);
  }
}

DI float lo_f(u32 x) { return __builtin_bit_cast(float, x << 16); }
DI float hi_f(u32 x) { return __builtin_bit_cast(float, x & 0xffff0000u); }
DI u32 pack_bf16(float a, float b) {
  f32x2 v = {a, b};
  return __builtin_bit_cast(u32, __builtin_convertvector(v, bf16x2_t));
}
DI float round_bf16(float a) { return lo_f(pack_bf16(a, 0.f)); }
DI float bf2f(u16 x) { return __builtin_bit_cast(float, (u32)x << 16); }

template <int N>
DI void wait_vm() { asm volatile("s_waitcnt vmcnt(%0)" ::"n"(N) : "memory"); }
DI void wait_lgkm0() { asm volatile("s_waitcnt lgkmcnt(0)" ::: "memory"); }
DI void bar() { __builtin_amdgcn_s_barrier(); }

#define SNOP "s_nop 0\n\t"
#define SNOP2 ""
DI const void* uni(const void* p) {
  const unsigned long long v = (unsigned long long)p;
  const u32 lo = __builtin_amdgcn_readfirstlane((u32)v), hi = __builtin_amdgcn_readfirstlane((u32)(v >> 32));
  return (const void*)(((unsigned long long)hi << 32) | lo);
}
DI void glds16_asm(const void* sbase, u32 voff, u32 lds_addr) {
  u32 keep;
  sbase = uni(sbase);
  lds_addr = __builtin_amdgcn_readfirstlane(lds_addr);
  asm volatile("s_mov_b32 %0, m0\n\ts_mov_b32 m0, %3\n\t" SNOP "global_load_lds_dwordx4 %1, %2\n\ts_mov_b32 m0, %0"
               : "=&s"(keep)
               : "v"(voff), "s"(sbase), "s"(lds_addr)
               : "memory");
}
DI void glds16(const void* g, char* lds) {
  __builtin_amdgcn_global_load_lds((const __attribute__((address_space(1))) void*)g, (LAS void*)lds, 16, 0, 0);
}

template <int CTRL>
DI float dpp_add(float v) {
  const int x = __builtin_bit_cast(int, v);
  return v + __builtin_bit_cast(float, __builtin_amdgcn_update_dpp(x, x, CTRL, 0xf, 0xf, false));
}
DI float sum8(float v) {
  v = dpp_add<0xB1>(v);
  v = dpp_add<0x4E>(v);
  v = dpp_add<0x141>(v);
  return v;
}
DI float sum16(float v) { return dpp_add<0x140>(sum8(v)); }
DI float dot2(u32 a, u32 b, float c) {
  return __builtin_amdgcn_fdot2_f32_bf16(__builtin_bit_cast(bf16x2_t, a), __builtin_bit_cast(bf16x2_t, b), c, false);
}
typedef __amdgpu_buffer_rsrc_t rsrc_t;
DI rsrc_t mk_rsrc(const void* p, u32 bytes) { return __builtin_amdgcn_make_buffer_rsrc((void*)p, 0, bytes, 0x00020000); }
DI void st16_sc1(void* p, u32x4 v) { asm volatile("global_store_dwordx4 %0, %1, off sc1\n\ts_nop 1" ::"v"(p), "v"(v) : "memory"); }
DI void st8_sc1(void* p, u32x2 v) { asm volatile("global_store_dwordx2 %0, %1, off sc1" ::"v"(p), "v"(v) : "memory"); }
DI void st4_sc1(void* p, float v) { asm volatile("global_store_dword %0, %1, off sc1" ::"v"(p), "v"(v) : "memory"); }

constexpr int CODE_LINES = 184;
constexpr int K0_GRID = 400;

extern "C" __global__ __launch_bounds__(256) void k0_prep(const u16* __restrict__ q, const u16* __restrict__ k,
                                               const u16* __restrict__ v, const u16* __restrict__ shift_k,
                                               const u16* __restrict__ c, const u16* __restrict__ w_mod,
                                               const u16* __restrict__ ssm_k, char* __restrict__ ws) {
  __shared__ float red[4][32];
  const int b = blockIdx.x, tid = threadIdx.x, lane = tid & 63, w = tid >> 6;
  STAMP_DECL;
  STAMP(ws, 0, 0);
  const int xcd = b & 7, idx = b >> 3;
  u32 cwarm = 0;
  if (idx == 0) {
    u32 lo, hi;
    asm volatile(
        "s_getpc_b64 s[40:41]\n\ts_add_u32 s40, s40, k1_conv@rel32@lo+4\n\ts_addc_u32 s41, s41, k1_conv@rel32@hi+12\n\t"
        "s_mov_b32 %0, s40\n\ts_mov_b32 %1, s41"
        : "=s"(lo), "=s"(hi)::"s40", "s41", "scc");
    const char* code = (const char*)(((unsigned long long)hi << 32) | lo);
    if (tid < CODE_LINES) cwarm = *(const u32*)(code + tid * 128);
  }
  int misc = -1;
  if (xcd == 7) misc = idx;
  else if (idx >= 32) misc = idx < 46 ? 50 + xcd * 14 + (idx - 32) : 1000;
  if (misc < 0) {
    const int rb = idx;
    const int sq = lane >> 5, cp = lane & 31;
    const int d0 = xcd * 64 + cp * 2;
    const int a = rb * 8 + w * 2 + sq;
    const int s0 = a * 8;
    const rsrc_t rk = mk_rsrc(k, S * D * 2), rv = mk_rsrc(v, S * D * 2), rq = mk_rsrc(q, S * D * 2);
    const int oB = (s0 * D + d0) * 2;
    const int oA = oB - 4 * D * 2, oC = oB + 4 * D * 2;
    u32 kk[12], vv[9], qq[8];
#pragma unroll
    for (int i = 0; i < 4; ++i) kk[i] = __builtin_amdgcn_raw_buffer_load_b32(rk, oA + i * D * 2, 0, 0);
#pragma unroll
    for (int i = 0; i < 4; ++i) kk[4 + i] = __builtin_amdgcn_raw_buffer_load_b32(rk, oB + i * D * 2, 0, 0);
#pragma unroll
    for (int i = 0; i < 4; ++i) kk[8 + i] = __builtin_amdgcn_raw_buffer_load_b32(rk, oC + i * D * 2, 0, 0);
    vv[0] = __builtin_amdgcn_raw_buffer_load_b32(rv, oA + 3 * D * 2, 0, 0);
#pragma unroll
    for (int i = 0; i < 4; ++i) vv[1 + i] = __builtin_amdgcn_raw_buffer_load_b32(rv, oB + i * D * 2, 0, 0);
#pragma unroll
    for (int i = 0; i < 4; ++i) vv[5 + i] = __builtin_amdgcn_raw_buffer_load_b32(rv, oC + i * D * 2, 0, 0);
#pragma unroll
    for (int i = 0; i < 4; ++i) qq[i] = __builtin_amdgcn_raw_buffer_load_b32(rq, oB + i * D * 2, 0, 0);
#pragma unroll
    for (int i = 0; i < 4; ++i) qq[4 + i] = __builtin_amdgcn_raw_buffer_load_b32(rq, oC + i * D * 2, 0, 0);
    const u32x4 sk = *(const u32x4*)(shift_k + d0 * 4);
    u32 warm = 0;
    if (w == 0) warm = *(const u32*)((const char*)ssm_k + ((size_t)(xcd * 32 + rb) * 64 + lane) * 128);
    f32x2 wt[4] = {{lo_f(sk[0]), lo_f(sk[2])}, {hi_f(sk[0]), hi_f(sk[2])}, {lo_f(sk[1]), lo_f(sk[3])}, {hi_f(sk[1]), hi_f(sk[3])}};
    f32x2 kf[12], u[9];
#pragma unroll
    for (int i = 0; i < 12; ++i) kf[i] = f32x2{lo_f(kk[i]), hi_f(kk[i])};
#pragma unroll
    for (int i = 0; i < 9; ++i) {
      f32x2 t = kf[i + 3] * wt[0];
      t = __builtin_elementwise_fma(kf[i + 2], wt[1], t);
      t = __builtin_elementwise_fma(kf[i + 1], wt[2], t);
      t = __builtin_elementwise_fma(kf[i], wt[3], t);
      u[i] = t * f32x2{lo_f(vv[i]), hi_f(vv[i])};
    }
    {
      char* dst = ws + (size_t)d0 * (S * 2) + (2040 - s0) * 2;
#pragma unroll
      for (int cI = 0; cI < 2; ++cI) {
        *(u32x4*)(dst + OFF_RO + cI * S * 2) = u32x4{pack_bf16(u[8][cI], u[7][cI]), pack_bf16(u[6][cI], u[5][cI]),
                                                     pack_bf16(u[4][cI], u[3][cI]), pack_bf16(u[2][cI], u[1][cI])};
        *(u32x4*)(dst + OFF_RE + cI * S * 2) = u32x4{pack_bf16(u[7][cI], u[6][cI]), pack_bf16(u[5][cI], u[4][cI]),
                                                     pack_bf16(u[3][cI], u[2][cI]), pack_bf16(u[1][cI], u[0][cI])};
      }
      char* dq = ws + OFF_QT + (size_t)d0 * (S * 2) + s0 * 2;
#pragma unroll
      for (int cI = 0; cI < 2; ++cI) {
        const u32 sel = cI ? 0x07060302u : 0x05040100u;
        *(u32x4*)(dq + cI * S * 2) = u32x4{__builtin_amdgcn_perm(qq[2], qq[0], sel), __builtin_amdgcn_perm(qq[6], qq[4], sel),
                                           __builtin_amdgcn_perm(qq[3], qq[1], sel), __builtin_amdgcn_perm(qq[7], qq[5], sel)};
      }
    }
    asm volatile("" ::"v"(warm));
  } else if (misc < 64) {
    const int bb = misc;
    const int row = bb * 32 + w * 8 + (lane >> 3);
    const int sub = lane & 7;
    u32x4 x[7];
#pragma unroll
    for (int i = 0; i < 7; ++i) x[i] = *(const u32x4*)(q + (size_t)row * D + (i * 8 + sub) * 8);
    if (sub < 3) ((unsigned long long*)(ws + OFF_SG))[sub * S + row] = 0ull;
    if (bb < 2) ((u32*)(ws + OFF_FLAG))[bb * 256 + tid] = 0u;
    float s = 0.f, s2 = 0.f;
#pragma unroll
    for (int i = 0; i < 7; ++i)
#pragma unroll
      for (int j = 0; j < 4; ++j) {
        const u32 t = x[i][j];
        s = dot2(t, 0x3f803f80u, s);
        s2 = dot2(t, t, s2);
      }
    s = sum8(s);
    s2 = sum8(s2);
    const float mean = s * (1.f / D);
    float var = s2 * (1.f / D) - mean * mean;
    var = var < 0.f ? 0.f : var;
    const float rstd = __builtin_amdgcn_rsqf(var + 1e-5f);
    if (sub < 2) {
      const int p = (row & ~7) | ((row & 1) << 2) | ((row >> 1) & 3);
      ((float*)(ws + OFF_MEAN))[sub * S + p] = sub ? rstd : mean;
    }
  } else if (misc < 148) {
    const int bb = misc - 64;
    const int co = lane >> 4, rs = lane & 15;
    const int col0 = bb * 32 + co * 8;
    u32x4 x[7];
    float sc[7];
#pragma unroll
    for (int i = 0; i < 7; ++i) {
      const int r = i * 64 + w * 16 + rs;
      x[i] = *(const u32x4*)(w_mod + (size_t)r * (6 * D) + col0);
      sc[i] = bf2f(c[r]);
    }
    f32x2 acc[4] = {{0.f, 0.f}, {0.f, 0.f}, {0.f, 0.f}, {0.f, 0.f}};
#pragma unroll
    for (int i = 0; i < 7; ++i) {
      const float cv = sc[i];
      const float sl = round_bf16(cv * __builtin_amdgcn_rcpf(1.f + __builtin_amdgcn_exp2f(cv * -1.44269504f)));
#pragma unroll
      for (int j = 0; j < 4; ++j)
        acc[j] = __builtin_elementwise_fma(f32x2{lo_f(x[i][j]), hi_f(x[i][j])}, f32x2{sl, sl}, acc[j]);
    }
    float sel = 0.f;
#pragma unroll
    for (int j = 0; j < 8; ++j) {
      const float t = sum16(acc[j >> 1][j & 1]);
      sel = (rs == j) ? t : sel;
    }
    if (rs < 8) red[w][co * 8 + rs] = sel;
    __syncthreads();
    if (tid < 32) {
      const float t = (red[0][tid] + red[1][tid]) + (red[2][tid] + red[3][tid]);
      const float m = round_bf16(t);
      const int col = bb * 32 + tid;
      ((float*)(ws + OFF_MOD))[col] = m;
      if (col >= 3 * D && col < 5 * D) {
        const bool isc = col >= 4 * D;
        ((u16*)(ws + OFF_CB))[isc ? col - 4 * D : col - 2 * D] = (u16)pack_bf16(isc ? 1.f + m : m, 0.f);
      }
    }
  }
  asm volatile("" ::"v"(cwarm));
  STAMP(ws, 0, 1);
  STAMP_FLUSH(ws, misc < 0 ? 0 : (misc < 64 ? 4 : 5));
}

constexpr int K1_KSTR = 260 * 16;
constexpr int K1_USTR = 286 * 16;
constexpr int K1_UOFF = 4 * K1_KSTR;
constexpr int K1_XOFF = K1_UOFF + 2 * K1_USTR;
constexpr int K1_CH = K1_XOFF + 8192;
constexpr int K1_LDS = 2 * K1_CH;

DI void glds16x4_asm(const void* sbase, u32 voff, u32 voff_last, u32 lds_addr) {
  u32 keep;
  sbase = uni(sbase);
  lds_addr = __builtin_amdgcn_readfirstlane(lds_addr);
  asm volatile(
      "s_mov_b32 %0, m0\n\ts_mov_b32 m0, %4\n\t" SNOP
      "global_load_lds_dwordx4 %1, %3\n\t"
      "global_load_lds_dwordx4 %1, %3 offset:1024\n\t"
      "global_load_lds_dwordx4 %1, %3 offset:2048\n\t"
      "global_load_lds_dwordx4 %2, %3 offset:3072\n\t"
      "s_mov_b32 m0, %0"
      : "=&s"(keep)
      : "v"(voff), "v"(voff_last), "s"(sbase), "s"(lds_addr)
      : "memory");
}
#define CAS __attribute__((address_space(4)))
template <int half>
DI void k1_conv_wave(const u16* __restrict__ ssm_k, const u16* __restrict__ ssm_D, char* __restrict__ ws, char* smem,
                     int ch, int lane) {
  STAMP_DECL;
  STAMP(ws, 1, 0);
  const int cpair = (blockIdx.x & 7) * 32 + (blockIdx.x >> 3);
  const int d = 2 * cpair + ch;
  char* L = smem + ch * K1_CH;
  const u32 L0 = (u32)(size_t)(LAS char*)L;
  const u16* kf = ssm_k + (size_t)d * S;
  {
    const u32 vo = lane * 16, vl = (lane > 62 ? 62 : lane) * 16;
    glds16x4_asm(kf + (2 + 4 * half), vo, vl, L0 + (2 * half) * K1_KSTR + 64);
    glds16x4_asm(kf + (4 + 4 * half), vo, vl, L0 + (2 * half + 1) * K1_KSTR + 64);
    glds16x4_asm(ws + (half ? OFF_RO : OFF_RE) + (size_t)d * S * 2, vo, vo, L0 + K1_UOFF + half * K1_USTR);
  }
  const int n = lane & 15, q4 = lane >> 4;
  const int pbase = 32 * (n >> 1) + 8 * q4 + 4 * (n & 1) + 1024 * half;
  u32 head = 0;
  const int di = n + q4 - 15;
  if constexpr (half == 0) {
    asm volatile(SNOP2 "global_load_dword %0, %1, %2" : "=v"(head) : "v"((u32)(di < 0 ? 0 : di) * 4), "s"(uni(kf)) : "memory");
  }
  const CAS float* MODc = (const CAS float*)(size_t)(ws + OFF_MOD);
  const float sa = MODc[d], ca = MODc[D + d], ga = MODc[2 * D + d], cbv = MODc[4 * D + d];
  const u32 Draw = ((const CAS u32*)(size_t)ssm_D)[cpair];
  if constexpr (half == 1) {
    const int l5 = lane & 31;
    if (l5 < 28) *(LAS u32x4*)(L + K1_UOFF + (lane >> 5) * K1_USTR + (256 + l5) * 16) = u32x4{0u, 0u, 0u, 0u};
  }
  u32 Ab = L0 + (n & 3) * K1_KSTR + ((n >> 2) + q4 + 16 * half) * 16;
  u32 Bb = L0 + K1_UOFF + (n & 1) * K1_USTR + (28 - 4 * (n >> 1) + q4 + 16 * half) * 16;
  u32 Hd = L0 + q4 * K1_KSTR + n * 4;
  asm volatile("" : "+v"(Ab), "+v"(Bb), "+v"(Hd) : "s"(sa), "s"(ca), "s"(ga), "s"(Draw), "s"(cbv));
  asm volatile("s_waitcnt vmcnt(0)" : "+v"(head)::"memory");
  if constexpr (half == 0) *(LAS u32*)(size_t)Hd = di < 0 ? 0u : head;
  wait_lgkm0();
  STAMP(ws, 1, 1);
  bar();
  STAMP(ws, 1, 2);
  bf16x8 fa[2][8], fb[2][8];
  auto rd = [&](auto dI) {
    constexpr int dl = decltype(dI)::value;
#pragma unroll
    for (int g = 0; g < 8; ++g) {
      fa[dl & 1][g] = *(const LAS bf16x8*)(size_t)(Ab + 64 * dl + 512 * g);
      fb[dl & 1][g] = *(const LAS bf16x8*)(size_t)(Bb + 64 * dl + 512 * (7 - g));
    }
  };
  rd(std::integral_constant<int, 0>{});
  __builtin_amdgcn_sched_barrier(0);
  u32x2 qv[4];
  f32x4 mu[4], rs[4];
  asm volatile(SNOP2
               "global_load_dwordx2 %0, %12, %14\n\t"
               "global_load_dwordx2 %1, %12, %14 offset:512\n\t"
               "global_load_dwordx2 %2, %12, %14 offset:1024\n\t"
               "global_load_dwordx2 %3, %12, %14 offset:1536\n\t"
               "global_load_dwordx4 %4, %13, %15\n\t"
               "global_load_dwordx4 %5, %13, %15 offset:1024\n\t"
               "global_load_dwordx4 %6, %13, %15 offset:2048\n\t"
               "global_load_dwordx4 %7, %13, %15 offset:3072\n\t"
               "global_load_dwordx4 %8, %13, %16\n\t"
               "global_load_dwordx4 %9, %13, %16 offset:1024\n\t"
               "global_load_dwordx4 %10, %13, %16 offset:2048\n\t"
               "global_load_dwordx4 %11, %13, %16 offset:3072"
               : "=&v"(qv[0]), "=&v"(qv[1]), "=&v"(qv[2]), "=&v"(qv[3]), "=&v"(mu[0]), "=&v"(mu[1]), "=&v"(mu[2]),
                 "=&v"(mu[3]), "=&v"(rs[0]), "=&v"(rs[1]), "=&v"(rs[2]), "=&v"(rs[3])
               : "v"((u32)pbase * 2), "v"((u32)pbase * 4), "s"(uni(ws + OFF_QT + (size_t)d * S * 2)),
                 "s"(uni(ws + OFF_MEAN)), "s"(uni(ws + OFF_RSTD))
               : "memory");
  f32x4 acc[8];
#pragma unroll
  for (int j = 0; j < 8; ++j) acc[j] = f32x4{0.f, 0.f, 0.f, 0.f};
  f32x4 qf[4], hg[4], kd[4];
  u32x4 uc[4];
  static_for<0, 4>([&](auto dI) {
    constexpr int dl = decltype(dI)::value;
    if constexpr (dl < 3) rd(std::integral_constant<int, dl + 1>{});
    if constexpr (dl == 2) {
      asm volatile("s_waitcnt vmcnt(0)"
                   : "+v"(qv[0]), "+v"(qv[1]), "+v"(qv[2]), "+v"(qv[3]), "+v"(mu[0]), "+v"(mu[1]), "+v"(mu[2]), "+v"(mu[3]),
                     "+v"(rs[0]), "+v"(rs[1]), "+v"(rs[2]), "+v"(rs[3]));
    }
    if constexpr (dl == 1) {
#pragma unroll
      for (int j = 0; j < 4; ++j)
        uc[j] = *(const LAS u32x4*)(L + K1_UOFF + K1_USTR + (255 - (32 * (4 * half + j) + 4 * (n >> 1) + q4)) * 16);
    }
    if constexpr (dl >= 2) {
      const int sh = (n & 1) ? 16 : 0;
      const float gc = ga * round_bf16(1.f + ca), gs = ga * sa, Dd = ch ? hi_f(Draw) : lo_f(Draw);
#pragma unroll
      for (int j = 2 * (dl - 2); j < 2 * (dl - 2) + 2; ++j) {
#pragma unroll
        for (int r = 0; r < 4; ++r) {
          kd[j][r] = __builtin_bit_cast(float, (uc[j][3 - r] << sh) & 0xffff0000u) * Dd;
          qf[j][r] = (r & 1) ? hi_f(qv[j][r >> 1]) : lo_f(qv[j][r >> 1]);
          hg[j][r] = (qf[j][r] - mu[j][r]) * rs[j][r] * gc + gs;
        }
        asm volatile("" : "+v"(kd[j]), "+v"(qf[j]), "+v"(hg[j]));
      }
    }
#pragma unroll
    for (int sdiag = 0; sdiag < 8; ++sdiag)
#pragma unroll
      for (int g = 0; g <= sdiag; ++g)
        acc[sdiag] = __builtin_amdgcn_mfma_f32_16x16x32_bf16(fa[dl & 1][g], fb[dl & 1][sdiag - g], acc[sdiag], 0, 0, 0);
    if constexpr (dl == 0) {
#pragma unroll
      for (int i = 0; i < 16; ++i) {
        __builtin_amdgcn_sched_group_barrier(0x008, 2, 0);
        __builtin_amdgcn_sched_group_barrier(0x100, 1, 0);
      }
    } else if constexpr (dl == 1) {
#pragma unroll
      for (int i = 0; i < 16; ++i) {
        __builtin_amdgcn_sched_group_barrier(0x008, 2, 0);
        __builtin_amdgcn_sched_group_barrier(0x100, 1, 0);
      }
#pragma unroll
      for (int i = 0; i < 4; ++i) {
        __builtin_amdgcn_sched_group_barrier(0x008, 1, 0);
        __builtin_amdgcn_sched_group_barrier(0x100, 1, 0);
      }
    } else {
      __builtin_amdgcn_sched_group_barrier(0x008, 4, 0);
#pragma unroll
      for (int i = 0; i < 16; ++i) {
        __builtin_amdgcn_sched_group_barrier(0x008, 1, 0);
        __builtin_amdgcn_sched_group_barrier(0x002, 2, 0);
        __builtin_amdgcn_sched_group_barrier(0x008, 1, 0);
        __builtin_amdgcn_sched_group_barrier(0x002, 2, 0);
        __builtin_amdgcn_sched_group_barrier(0x100, 1, 0);
      }
    }
    __builtin_amdgcn_sched_barrier(0);
  });
  STAMP(ws, 1, 3);
  LAS f32x4* X = (LAS f32x4*)(L + K1_XOFF);
#pragma unroll
  for (int j = 0; j < 4; ++j) X[half * 256 + j * 64 + lane] = acc[4 * (1 - half) + j];
  wait_lgkm0();
  bar();
  STAMP(ws, 1, 4);
  u16* X1T = (u16*)(ws + OFF_X1T) + (size_t)d * S + pbase;
  u16* XS = (u16*)(ws + OFF_XS) + (size_t)d * S + pbase;
  const float c1 = round_bf16(1.f + cbv);
#pragma unroll
  for (int j = 0; j < 4; ++j) {
    const f32x4 y = X[(1 - half) * 256 + j * 64 + lane];
    const f32x4 o = qf[j] + hg[j] * ((acc[4 * half + j] + kd[j]) + y);
    const u32 p0 = pack_bf16(o[0], o[1]), p1 = pack_bf16(o[2], o[3]);
    st8_sc1(XS + 256 * j, u32x2{pack_bf16(lo_f(p0) * c1, hi_f(p0) * c1), pack_bf16(lo_f(p1) * c1, hi_f(p1) * c1)});
    st8_sc1(X1T + 256 * j, u32x2{p0, p1});
  }
  STAMP(ws, 1, 5);
  STAMP_FLUSH(ws, 1);
}

DI bf16x8 tr_frag(const char* smem, int o0, int o1) {
  s16x4 x0 = __builtin_amdgcn_ds_read_tr16_b64_v4i16((LAS s16x4*)(smem + o0));
  s16x4 x1 = __builtin_amdgcn_ds_read_tr16_b64_v4i16((LAS s16x4*)(smem + o1));
  return __builtin_shufflevector(x0, x1, 0, 1, 2, 3, 4, 5, 6, 7);
}
DI f32x16 mfma32(u32x4 a, bf16x8 b, f32x16 c) {
  return __builtin_amdgcn_mfma_f32_32x32x16_bf16(__builtin_bit_cast(bf16x8, a), b, c, 0, 0, 0);
}

DI void k1_helper(int task, const u16* __restrict__ w_gate, const u16* __restrict__ w_up, const u16* __restrict__ w_down,
                  char* __restrict__ ws, char* smem, int w, int lane) {
  const u32 lds0 = (u32)(size_t)(LAS char*)smem;
  const int xr = lane >> 4, slot = lane & 15;
  const int chn = slot ^ ((xr << 2) | (w & 3));
  const int h = lane >> 5, blk = (lane >> 4) & 1, qq = (lane & 15) >> 2, p = lane & 3, n = lane & 31;
  const bool gu = task < 112;
  int kq = 0, fg = 0, dg = 0, fk = 0;
  if (gu) {
    fg = task % 28, kq = task / 28;
    const u16* src = (chn < 8 ? w_gate : w_up) + (size_t)(kq * 112 + 4 * w + xr) * F + fg * 64 + (chn & 7) * 8;
#pragma unroll
    for (int i = 0; i < 7; ++i) glds16(src + (size_t)i * 16 * F, smem + (w + 4 * i) * 1024);
  } else {
    dg = (task - 112) % 7, fk = (task - 112) / 7;
    const u16* src = w_down + (size_t)(fk * 224 + (chn >> 3) * 112 + 4 * w + xr) * D + dg * 64 + (chn & 7) * 8;
#pragma unroll
    for (int i = 0; i < 7; ++i) glds16(src + (size_t)i * 16 * D, smem + (w + 4 * i) * 1024);
  }
  const int col = gu ? blk * 128 + w * 32 + (p & 1) * 16 + (p >> 1) * 8 : (w >> 1) * 128 + (w & 1) * 64 + blk * 32 + p * 8;
  int o[2];
#pragma unroll
  for (int t = 0; t < 2; ++t)
    o[t] = 256 * (8 * h + 4 * t + qq) + 16 * ((col >> 4) ^ ((qq << 2) | ((2 * h + t) & 3))) + (col & 8);
  u32x4 bv[7];
  if (gu) {
    const u16* cbp = (const u16*)(ws + OFF_CB) + (n & 1) * D + kq * 112 + 8 * h;
#pragma unroll
    for (int i = 0; i < 7; ++i) bv[i] = *(const u32x4*)(cbp + 16 * i);
  }
  __syncthreads();
  u32x4* dst = gu ? (u32x4*)(ws + OFF_WGU) + ((size_t)(fg * 4 + w) * 28 + kq * 7) * 64 + lane
                  : (u32x4*)(ws + OFF_WD) + ((size_t)(dg * 2 + (w & 1)) * 112 + fk * 14 + (w >> 1) * 7) * 64 + lane;
  f32x16 acc;
#pragma unroll
  for (int r = 0; r < 16; ++r) acc[r] = 0.f;
#pragma unroll
  for (int i = 0; i < 7; ++i) {
    const u32x4 f = __builtin_bit_cast(u32x4, tr_frag(smem, o[0] + i * 4096, o[1] + i * 4096));
    st16_sc1(dst + i * 64, f);
    if (gu) acc = mfma32(f, __builtin_bit_cast(bf16x8, bv[i]), acc);
  }
  if (gu && n < 2) {
    float* vd = (float*)(ws + OFF_VP) + (size_t)(kq * 2 + n) * 3584 + (fg * 4 + w) * 32 + 4 * h;
#pragma unroll
    for (int g = 0; g < 4; ++g) *(f32x4*)(vd + 8 * g) = f32x4{acc[4 * g], acc[4 * g + 1], acc[4 * g + 2], acc[4 * g + 3]};
  }
}

constexpr int K1_GRID = 400;
extern "C" __global__ NOPK __launch_bounds__(256, 2) void k1_conv(const u16* __restrict__ ssm_k, const u16* __restrict__ ssm_D,
                                                       const u16* __restrict__ w_gate, const u16* __restrict__ w_up,
                                                       const u16* __restrict__ w_down, char* __restrict__ ws) {
  extern __shared__ __attribute__((aligned(16))) char smem[];
  const int tid = threadIdx.x, lane = tid & 63;
  const int w = __builtin_amdgcn_readfirstlane(tid >> 6);
  const int xcd = blockIdx.x & 7, idx = blockIdx.x >> 3;
  if (xcd < 7 && idx < 32) {
    if (w & 1) k1_conv_wave<1>(ssm_k, ssm_D, ws, smem, w >> 1, lane);
    else k1_conv_wave<0>(ssm_k, ssm_D, ws, smem, w >> 1, lane);
  } else {
    STAMP_DECL;
    STAMP(ws, 7, 0);
    if (xcd == 7 && idx >= 42) return;
    const int task = xcd == 7 ? idx : 42 + xcd * 18 + (idx - 32);
    k1_helper(task, w_gate, w_up, w_down, ws, smem, w, lane);
    STAMP(ws, 7, 1);
    STAMP_FLUSH(ws, 7);
  }
}

constexpr int K2_XB = D * 256;
constexpr int K2_ST = K2_XB;
constexpr int K2_LDS = 131072;

DI void gload16_asm(u32x4& dst, const void* sbase, u32 voff) {
  sbase = uni(sbase);
  asm volatile(SNOP2 "global_load_dwordx4 %0, %1, %2" : "=v"(dst) : "v"(voff), "s"(sbase) : "memory");
}
template <int N>
DI void wait_vm_pin(u32x4& a, u32x4& b) {
  asm volatile("s_waitcnt vmcnt(%2)" : "+v"(a), "+v"(b) : "n"(N) : "memory");
}
DI float xor32(float v) {
  const u32 u = __builtin_bit_cast(u32, v);
  auto r = __builtin_amdgcn_permlane32_swap(u, u, false, false);
  const u32 a = r[0], b = r[1];
  return __builtin_bit_cast(float, (threadIdx.x & 32) ? a : b);
}

typedef unsigned long long u64;
typedef __attribute__((address_space(1))) u64 gu64;
typedef __attribute__((address_space(1))) u32 gu32;
#define RLX_AGENT __ATOMIC_RELAXED, __HIP_MEMORY_SCOPE_AGENT
#define EXP_AHEAD 2
constexpr int K2_AHEAD = EXP_AHEAD;

DI void k2_body(char* __restrict__ ws, u16* __restrict__ out, char* smem) {
  const int tid = threadIdx.x, lane = tid & 63;
  const int w = __builtin_amdgcn_readfirstlane(tid >> 6);
  const int n = lane & 31, h = lane >> 5;
  STAMP_DECL;
  STAMP(ws, 2, 0);
  if (blockIdx.x < 32) {
    const int p0 = blockIdx.x * 64;
    const u32* src = (const u32*)(ws + OFF_X1T) + ((size_t)(w * 56 + h) * S + p0) / 2 + n;
    u32 x[28];
#pragma unroll
    for (int i = 0; i < 28; ++i) x[i] = src[(size_t)i * S];
    float s0 = 0.f, s1 = 0.f, q0 = 0.f, q1 = 0.f;
#pragma unroll
    for (int i = 0; i < 28; ++i) {
      const float a = lo_f(x[i]), b = hi_f(x[i]);
      s0 += a;
      s1 += b;
      q0 = __builtin_fmaf(a, a, q0);
      q1 = __builtin_fmaf(b, b, q1);
    }
    *(LAS f32x4*)(smem + ((w * 2 + h) * 32 + n) * 16) = f32x4{s0, q0, s1, q1};
    __syncthreads();
    if (tid < 64) {
      float sm = 0.f, sq = 0.f;
#pragma unroll
      for (int i = 0; i < 16; ++i) {
        const f32x2 t = *(const LAS f32x2*)(smem + (i * 64 + tid) * 8);
        sm += t[0];
        sq += t[1];
      }
      const float mean = sm * (1.f / D);
      float var = sq * (1.f / D) - mean * mean;
      var = (var < 0.f ? 0.f : var) + 1e-5f;
      const float rstd = 1.f / sqrtf(var);
      const float sig = var * rstd;
      const float mh = round_bf16(mean), sh = round_bf16(sig);
      const u32 b0 = pack_bf16(-mh, mh - mean), b1 = pack_bf16(-mh, sh), b2 = pack_bf16(sig - sh, sh);
      const float c1 = -2.302208198f * rstd;
      const float c2 = -0.1029432397f * (rstd * rstd * rstd);
      gu64* G = (gu64*)(size_t)(ws + OFF_SG) + p0 + tid;
      __hip_atomic_store(G, ((u64)b1 << 32) | b0, RLX_AGENT);
      __hip_atomic_store(G + S, ((u64)__builtin_bit_cast(u32, var) << 32) | b2, RLX_AGENT);
      __hip_atomic_store(G + 2 * S, ((u64)__builtin_bit_cast(u32, c1) << 32) | __builtin_bit_cast(u32, c2), RLX_AGENT);
    }
    STAMP(ws, 6, 1);
    STAMP_FLUSH(ws, 6);
    return;
  }
  const int bid = blockIdx.x - 32;
  const int xcd = bid & 7, idx = bid >> 3;
  const int mt = (xcd >> 1) * 4 + (idx & 3);
  const int ft = (xcd & 1) * 7 + (idx >> 2);
  const int tile = ft * 8 + w;
  const u32 lds0 = (u32)(size_t)(LAS char*)smem;
  u32x4 wf[28];
  const int xr = lane >> 4, slot = lane & 15;
  const char* xbase = ws + OFF_XS + ((size_t)(4 * w) * S + mt * 128) * 2;
  const u32 xoff = (u32)(xr * S + (slot ^ ((xr << 2) | (w & 3))) * 8) * 2;
  const char* wbase = ws + OFF_WGU + (size_t)(tile * 28) * 1024;
  const u32 wa = lane * 16, wb = lane * 16 + 1024;
  auto issue1 = [&](auto qI) {
    constexpr int q = decltype(qI)::value, j = q / 3, r = q % 3;
    if constexpr (r == 0) glds16_asm(xbase + (size_t)j * 32 * S * 2, xoff, lds0 + (32 * j + 4 * w) * 256);
    if constexpr (r == 1) gload16_asm(wf[2 * j], wbase + j * 2048, wa);
    if constexpr (r == 2) gload16_asm(wf[2 * j + 1], wbase + j * 2048, wb);
  };
  static_for<0, 3>(issue1);
  float vp[8];
  {
    const char* vb = ws + OFF_VP + (size_t)tile * 128;
    static_for<0, 8>([&](auto iI) {
      constexpr int i = decltype(iI)::value;
      float t;
      asm volatile(SNOP2 "global_load_dword %0, %1, %2" : "=v"(t) : "v"((u32)n * 4), "s"(uni(vb + i * 3584 * 4)) : "memory");
      vp[i] = t;
    });
  }
  static_for<3, 3 * (K2_AHEAD + 1)>(issue1);
  int xo[4][2];
  {
    const int blk = (lane >> 4) & 1, qq = (lane & 15) >> 2, p = lane & 3;
#pragma unroll
    for (int i = 0; i < 4; ++i) {
      const int nt = (w + 1 + i) & 3;
#pragma unroll
      for (int t = 0; t < 2; ++t) {
        const int row = 8 * h + 4 * t + qq;
        const int chn = 4 * nt + 2 * blk + (p >> 1);
        xo[i][t] = 256 * row + 16 * (chn ^ ((qq << 2) | ((2 * h + t) & 3))) + 8 * (p & 1);
      }
    }
  }
  f32x16 acc[4];
#pragma unroll
  for (int i = 0; i < 4; ++i)
#pragma unroll
    for (int r = 0; r < 16; ++r) acc[i][r] = 0.f;
  bf16x8 xa[2][3], xb[2][3];
  auto rd = [&](auto jI, bf16x8(&f)[2][3]) {
    constexpr int j = decltype(jI)::value;
#pragma unroll
    for (int ks = 0; ks < 2; ++ks)
#pragma unroll
      for (int i = 0; i < 3; ++i) f[ks][i] = tr_frag(smem, xo[i][0] + ks * 4096 + j * 8192, xo[i][1] + ks * 4096 + j * 8192);
  };
  asm volatile("" : "+v"(xo[0][0]), "+v"(xo[0][1]), "+v"(xo[1][0]), "+v"(xo[1][1]), "+v"(xo[2][0]), "+v"(xo[2][1]),
               "+v"(xo[3][0]), "+v"(xo[3][1]));
  wait_vm<3 * K2_AHEAD + 2 + 8>();
  __builtin_amdgcn_sched_barrier(0);
  bar();
  STAMP(ws, 2, 1);
  rd(std::integral_constant<int, 0>{}, xa);
  __builtin_amdgcn_sched_barrier(0);
  u32x4 aext;
  auto mk_aext = [&]() {
    float t0 = vp[0], t1 = vp[1], t2 = vp[2], t3 = vp[3], t4 = vp[4], t5 = vp[5], t6 = vp[6], t7 = vp[7];
    asm volatile("" : "+v"(t0), "+v"(t1), "+v"(t2), "+v"(t3), "+v"(t4), "+v"(t5), "+v"(t6), "+v"(t7));
    const float v1 = (t0 + t2) + (t4 + t6);
    const float v2 = (t1 + t3) + (t5 + t7);
    const float v1h = round_bf16(v1), v2h = round_bf16(v2);
    u32x4 a;
    a[0] = pack_bf16(v1h, v1h);
    a[1] = pack_bf16(v1 - v1h, v2h);
    a[2] = pack_bf16(v2h, v2 - v2h);
    a[3] = 0u;
    if (h) a = u32x4{0u, 0u, 0u, 0u};
    asm volatile("" : "+v"(a));
    aext = a;
  };
  const int gsl = w < 6 ? w : w - 6;
  gu64* G = (gu64*)(size_t)(ws + OFF_SG) + (gsl >> 1) * S + mt * 128 + (gsl & 1) * 64 + lane;
  u64 gv;
  constexpr int JG = 13 - K2_AHEAD;
  auto pin = [](f32x16& a) { asm volatile("" : "+v"(a)); };
  auto chunk = [&](auto jI, const bf16x8(&cur)[2][3], bf16x8(&nxt)[2][3]) {
    constexpr int j = decltype(jI)::value;
    constexpr int last = (j + K2_AHEAD < 13) ? j + K2_AHEAD : 13;
    constexpr int ex = (j > JG && j <= 12) ? 1 : 0;
    if constexpr (j < 13) {
      wait_vm_pin<3 * (last - (j + 1)) + 2 + ex>(wf[2 * j], wf[2 * j + 1]);
      __builtin_amdgcn_sched_barrier(0);
      bar();
    } else {
      wait_vm_pin<0>(wf[2 * j], wf[2 * j + 1]);
    }
    __builtin_amdgcn_sched_barrier(0);
    static_for<0, 6>([&](auto iI) {
      constexpr int sl = decltype(iI)::value, ks = sl / 3, i = sl % 3;
      acc[i] = mfma32(wf[2 * j + ks], cur[ks][i], acc[i]);
      pin(acc[i]);
      __builtin_amdgcn_sched_barrier(0);
      if constexpr (j == 6 && ks == 0) {
        int a0 = xo[i][0] + 7 * 8192, a1 = xo[i][1] + 7 * 8192;
        asm volatile("" : "+v"(a0), "+v"(a1));
        xo[i][0] = a0;
        xo[i][1] = a1;
      }
      if constexpr (j < 13)
        nxt[ks][i] = tr_frag(smem, xo[i][0] + ks * 4096 + ((j + 1) % 7) * 8192, xo[i][1] + ks * 4096 + ((j + 1) % 7) * 8192);
      if constexpr ((sl & 1) == 0 && j + K2_AHEAD + 1 <= 13)
        issue1(std::integral_constant<int, 3 * (j + K2_AHEAD + 1) + sl / 2>{});
      if constexpr (j == JG && sl == 0) gv = __hip_atomic_load(G, RLX_AGENT);
      if constexpr (j == 1 && sl == 1) mk_aext();
      __builtin_amdgcn_sched_barrier(0);
    });
  };
  static_for<0, 14>([&](auto jI) {
    if constexpr (decltype(jI)::value & 1) chunk(jI, xb, xa);
    else chunk(jI, xa, xb);
  });
  STAMP(ws, 2, 2);
  const int sh2 = ft / 7, dt = ft - 7 * sh2;
  u32x4 wd[28];
  const char* wdbase = ws + OFF_WD + ((size_t)(2 * dt) * 112 + 14 * w) * 1024;
  auto ldwd = [&](auto kI) {
    constexpr int k = decltype(kI)::value;
    gload16_asm(wd[k], wdbase + (k / 14) * 112 * 1024 + (k % 14) * 1024, wa);
  };
  constexpr int PF = 4;
  bf16x8 xr3[PF];
  auto rd3 = [&](auto kI) {
    constexpr int k = decltype(kI)::value;
    if constexpr (k == 14) {
      int a0 = xo[3][0] + 14 * 4096, a1 = xo[3][1] + 14 * 4096;
      asm volatile("" : "+v"(a0), "+v"(a1));
      xo[3][0] = a0;
      xo[3][1] = a1;
    }
    xr3[k % PF] = tr_frag(smem, xo[3][0] + (k % 14) * 4096, xo[3][1] + (k % 14) * 4096);
  };
  static_for<0, PF - 1>(rd3);
  __builtin_amdgcn_sched_barrier(0);
  constexpr int PRE = 2;
  static_for<0, PRE>([&](auto kI) {
    constexpr int k = decltype(kI)::value;
    rd3(std::integral_constant<int, k + PF - 1>{});
    acc[3] = mfma32(wf[k], xr3[k % PF], acc[3]);
    pin(acc[3]);
    __builtin_amdgcn_sched_barrier(0);
  });
  for (int spin = 0; !__all((gv >> 32) != 0) && spin < (1 << 16); ++spin) {
    __builtin_amdgcn_s_sleep(2);
    gv = __hip_atomic_load(G, RLX_AGENT);
  }
  __builtin_amdgcn_s_waitcnt(0x0F70);
  LAS u64* CS = (LAS u64*)(smem + K2_ST);
  CS[gsl * 64 + lane] = gv;
  wait_lgkm0();
  bar();
  u64 g[4][3];
  auto ldg = [&](auto iI) {
    constexpr int i = decltype(iI)::value;
#pragma unroll
    for (int k = 0; k < 3; ++k) g[i][k] = CS[k * 128 + ((w + 1 + i) & 3) * 32 + n];
  };
  u32x4* act_base = (u32x4*)(ws + OFF_ACT) + ((size_t)(mt * 4) * 112 + tile) * 64 + lane;
  u32x4 ov;
  auto corr = [&](auto iI) {
    constexpr int i = decltype(iI)::value;
    const u32x4 bext = {(u32)g[i][0], (u32)(g[i][0] >> 32), (u32)g[i][1], 0u};
    acc[i] = mfma32(aext, __builtin_bit_cast(bf16x8, bext), acc[i]);
  };
  auto gl = [&](auto iI, int r) {
    constexpr int i = decltype(iI)::value;
    const float c1 = __builtin_bit_cast(float, (u32)(g[i][2] >> 32)), c2 = __builtin_bit_cast(float, (u32)g[i][2]);
    const float var = __builtin_bit_cast(float, (u32)(g[i][1] >> 32));
    const float a = acc[i][r], u = acc[i][r + 8];
    const float z = __builtin_fmaf(a * a, c2, c1) * a;
    const float e = __builtin_amdgcn_exp2f(z);
    return (a * u) * __builtin_amdgcn_rcpf(__builtin_fmaf(e, var, var));
  };
  auto unitB = [&](auto iI, auto gI) {
    constexpr int gq = decltype(gI)::value;
    ov[gq] = pack_bf16(gl(iI, 2 * gq), gl(iI, 2 * gq + 1));
  };
  auto unitC = [&](auto iI) {
    constexpr int i = decltype(iI)::value;
    st16_sc1(act_base + (size_t)((w + 1 + i) & 3) * 112 * 64, ov);
  };
  ldg(std::integral_constant<int, 0>{});
  __builtin_amdgcn_sched_barrier(0);
  static_for<PRE, 28>([&](auto kI) {
    constexpr int k = decltype(kI)::value;
    acc[3] = mfma32(wf[k], xr3[k % PF], acc[3]);
    pin(acc[3]);
    __builtin_amdgcn_sched_barrier(0);
    if constexpr (k + PF - 1 < 28) rd3(std::integral_constant<int, k + PF - 1>{});
    ldwd(std::integral_constant<int, k - PRE>{});
    constexpr int ti = (k - PRE) / 8, u = (k - PRE) % 8;
    if constexpr (ti < 3) {
      using TI = std::integral_constant<int, ti>;
      if constexpr (u == 0) corr(TI{});
      if constexpr (u == 1) unitB(TI{}, std::integral_constant<int, 0>{});
      if constexpr (u == 3) unitB(TI{}, std::integral_constant<int, 1>{});
      if constexpr (u == 5) unitB(TI{}, std::integral_constant<int, 2>{});
      if constexpr (u == 6) ldg(std::integral_constant<int, ti + 1>{});
      if constexpr (u == 7) {
        unitB(TI{}, std::integral_constant<int, 3>{});
        unitC(TI{});
      }
    }
    __builtin_amdgcn_sched_barrier(0);
  });
  STAMP(ws, 2, 3);
  ldwd(std::integral_constant<int, 26>{});
  ldwd(std::integral_constant<int, 27>{});
  corr(std::integral_constant<int, 3>{});
  static_for<0, 4>([&](auto gI) { unitB(std::integral_constant<int, 3>{}, gI); });
  unitC(std::integral_constant<int, 3>{});
  asm volatile("s_waitcnt vmcnt(0)"
               : "+v"(wd[0]), "+v"(wd[1]), "+v"(wd[2]), "+v"(wd[3]), "+v"(wd[4]), "+v"(wd[5]), "+v"(wd[6]), "+v"(wd[7]),
                 "+v"(wd[8]), "+v"(wd[9]), "+v"(wd[10]), "+v"(wd[11]), "+v"(wd[12]), "+v"(wd[13])::"memory");
  asm volatile(""
               : "+v"(wd[14]), "+v"(wd[15]), "+v"(wd[16]), "+v"(wd[17]), "+v"(wd[18]), "+v"(wd[19]), "+v"(wd[20]),
                 "+v"(wd[21]), "+v"(wd[22]), "+v"(wd[23]), "+v"(wd[24]), "+v"(wd[25]), "+v"(wd[26]), "+v"(wd[27])::"memory");
  bar();
  gu32* FL = (gu32*)(size_t)(ws + OFF_FLAG) + mt * 32;
  if (tid == 0) __hip_atomic_store(FL + ft, 1u, RLX_AGENT);
  STAMP(ws, 2, 4);
  const int tt = w >> 1, hf = w & 1, ti2 = tt >> 1, tj2 = tt & 1;
  const int pcol = mt * 128 + sh2 * 64 + tj2 * 32 + n;
  const int dbase = dt * 64 + ti2 * 32 + 16 * hf + 4 * h;
  u16 xres[8];
  f32x4 gbv[2];
  {
    const u16* X1T = (const u16*)(ws + OFF_X1T);
#pragma unroll
    for (int r = 0; r < 8; ++r) xres[r] = X1T[(size_t)(dbase + 8 * (r >> 2) + (r & 3)) * S + pcol];
#pragma unroll
    for (int q = 0; q < 2; ++q) gbv[q] = *(const f32x4*)((const float*)(ws + OFF_MOD) + 5 * D + dbase + 8 * q);
  }
  {
    const int f0 = (14 * w) >> 3, f1 = (14 * w + 13) >> 3;
    const int fl = f0 + (lane & 3) > f1 ? f1 : f0 + (lane & 3);
    for (int spin = 0; spin < (1 << 16); ++spin) {
      const u32 v = __hip_atomic_load(FL + fl, RLX_AGENT);
      if (__all(v != 0)) break;
      __builtin_amdgcn_s_sleep(1);
    }
    __builtin_amdgcn_fence(__ATOMIC_ACQUIRE, "wavefront");
  }
  STAMP(ws, 2, 5);
  const rsrc_t ra = mk_rsrc(ws + OFF_ACT + ((size_t)(mt * 4 + sh2 * 2) * 112 + 14 * w) * 1024, 126 * 1024);
  u32x4 af[28];
  auto ldact = [&](auto kI) {
    constexpr int k = decltype(kI)::value;
    af[k] = __builtin_amdgcn_raw_buffer_load_b128(ra, lane * 16 + (k % 14) * 1024, (k / 14) * 112 * 1024, 16);
  };
  constexpr int AW = 14;
  static_for<0, AW>(ldact);
  __builtin_amdgcn_sched_barrier(0);
  LAS f32x4* R = (LAS f32x4*)smem;
  f32x16 o0, o1;
  static_for<0, 2>([&](auto tI) {
    constexpr int tj = decltype(tI)::value;
#pragma unroll
    for (int r = 0; r < 16; ++r) o0[r] = o1[r] = 0.f;
    static_for<0, 14>([&](auto kI) {
      constexpr int k = decltype(kI)::value;
      const bf16x8 b = __builtin_bit_cast(bf16x8, af[tj * 14 + k]);
      o0 = mfma32(wd[k], b, o0);
      o1 = mfma32(wd[14 + k], b, o1);
      pin(o0);
      pin(o1);
      if constexpr (tj * 14 + k + AW < 28) ldact(std::integral_constant<int, tj * 14 + k + AW>{});
      __builtin_amdgcn_sched_barrier(0);
    });
#pragma unroll
    for (int q = 0; q < 4; ++q) {
      R[(((0 * 2 + tj) * 8 + w) * 4 + q) * 64 + lane] = f32x4{o0[4 * q], o0[4 * q + 1], o0[4 * q + 2], o0[4 * q + 3]};
      R[(((1 * 2 + tj) * 8 + w) * 4 + q) * 64 + lane] = f32x4{o1[4 * q], o1[4 * q + 1], o1[4 * q + 2], o1[4 * q + 3]};
    }
    __builtin_amdgcn_sched_barrier(0);
  });
  wait_lgkm0();
  bar();
  STAMP(ws, 2, 6);
  const int srow = (pcol & ~7) | ((pcol & 3) << 1) | ((pcol >> 2) & 1);
#pragma unroll
  for (int q = 0; q < 2; ++q) {
    f32x4 v[8];
#pragma unroll
    for (int sw = 0; sw < 8; ++sw) v[sw] = R[((tt * 8 + sw) * 4 + 2 * hf + q) * 64 + lane];
    const f32x4 sum = ((v[0] + v[1]) + (v[2] + v[3])) + ((v[4] + v[5]) + (v[6] + v[7]));
    float o[4];
#pragma unroll
    for (int e = 0; e < 4; ++e) o[e] = __builtin_fmaf(gbv[q][e], sum[e], bf2f(xres[4 * q + e]));
    *(u32x2*)(out + (size_t)srow * D + dbase + 8 * q) = u32x2{pack_bf16(o[0], o[1]), pack_bf16(o[2], o[3])};
  }
  STAMP_FLUSH(ws, 2);
  STAMP(ws, 3, 0);
  STAMP_FLUSH(ws, 3);
}

extern "C" __global__ NOPK __launch_bounds__(512, 2) void k2_ffn(char* __restrict__ ws, u16* __restrict__ out) {
  extern __shared__ __attribute__((aligned(16))) char smem[];
  k2_body(ws, out, smem);
}

extern "C" void kernel_launch(void* const* d_in, const int* in_sizes, int n_in, void* d_out, int out_size, void* d_ws,
                              size_t ws_size, hipStream_t stream) {
  const u16* q = (const u16*)d_in[0];
  const u16* k = (const u16*)d_in[1];
  const u16* v = (const u16*)d_in[2];
  const u16* shift_k = (const u16*)d_in[3];
  const u16* ssm_k = (const u16*)d_in[4];
  const u16* ssm_D = (const u16*)d_in[5];
  const u16* w_gate = (const u16*)d_in[7];
  const u16* w_up = (const u16*)d_in[8];
  const u16* w_down = (const u16*)d_in[9];
  const u16* c = (const u16*)d_in[10];
  const u16* w_mod = (const u16*)d_in[11];
  char* ws = (char*)d_ws;
  (void)hipFuncSetAttribute((const void*)k1_conv, hipFuncAttributeMaxDynamicSharedMemorySize, K1_LDS);
  (void)hipFuncSetAttribute((const void*)k2_ffn, hipFuncAttributeMaxDynamicSharedMemorySize, K2_LDS);
  k0_prep<<<K0_GRID, 256, 0, stream>>>(q, k, v, shift_k, c, w_mod, ssm_k, ws);
  k1_conv<<<K1_GRID, 256, K1_LDS, stream>>>(ssm_k, ssm_D, w_gate, w_up, w_down, ws);
  k2_ffn<<<256, 512, K2_LDS, stream>>>(ws, (u16*)d_out);
}
```

```cpp
#include <hip/hip_runtime.h>
#include <hip/hip_bf16.h>
#include <stdint.h>
#include <type_traits>

typedef unsigned short u16;
typedef unsigned int u32;
typedef short bf16x8 __attribute__((ext_vector_type(8)));
typedef short s16x4 __attribute__((ext_vector_type(4)));
typedef float f32x2 __attribute__((ext_vector_type(2)));
typedef float f32x4 __attribute__((ext_vector_type(4)));
typedef float f32x16 __attribute__((ext_vector_type(16)));
typedef u32 u32x2 __attribute__((ext_vector_type(2)));
typedef u32 u32x4 __attribute__((ext_vector_type(4)));
typedef __bf16 bf16x2_t __attribute__((ext_vector_type(2)));
#define LAS __attribute__((address_space(3)))
#define DI __device__ __forceinline__

constexpr int S = 2048, D = 448, F = 1792;

constexpr size_t SZ_DS = (size_t)D * S * 2;
constexpr size_t OFF_RE = 0;
constexpr size_t OFF_RO = OFF_RE + SZ_DS;
constexpr size_t OFF_QT = OFF_RO + SZ_DS;
constexpr size_t OFF_X1T = OFF_QT + SZ_DS;
constexpr size_t OFF_MEAN = OFF_X1T + SZ_DS;
constexpr size_t OFF_RSTD = OFF_MEAN + S * 4;
constexpr size_t OFF_MOD = OFF_RSTD + S * 4;
constexpr size_t OFF_SG = OFF_MOD + 16384;
constexpr size_t OFF_CB = OFF_MOD + 12288;

constexpr size_t OFF_FLAG = OFF_SG + 3 * S * 8;
constexpr size_t OFF_XS = OFF_FLAG + 512 * 128;
constexpr size_t OFF_VP = OFF_XS + SZ_DS;
constexpr size_t OFF_WGU = OFF_VP + 8 * 2 * 3584 * 4;
constexpr size_t OFF_WD = OFF_WGU + (size_t)2 * D * F * 2;
constexpr size_t OFF_ACT = OFF_WD + (size_t)D * F * 2;

#define STAMP_DECL
#define STAMP(wsp, kid, i)
#define STAMP_FLUSH(wsp, kid)
#define HWID(wsp, kid)
#define XSTAMP(i)
#define YSTAMP(i) STAMP(ws, 2, i)
#if defined(__HIP_DEVICE_COMPILE__)
#define NOPK __attribute__((target("no-packed-fp32-ops")))
#else
#define NOPK
#endif

template <int I, int N, class Fn>
DI void static_for(Fn&& f) {
  if constexpr (I < N) {
    f(std::integral_constant<int, I>{});
    static_for<I + 1, N>(f);
  }
}

DI float lo_f(u32 x) { return __builtin_bit_cast(float, x << 16); }
DI float hi_f(u32 x) { return __builtin_bit_cast(float, x & 0xffff0000u); }
DI u32 pack_bf16(float a, float b) {
  f32x2 v = {a, b};
  return __builtin_bit_cast(u32, __builtin_convertvector(v, bf16x2_t));
}
DI float round_bf16(float a) { return lo_f(pack_bf16(a, 0.f)); }
DI float bf2f(u16 x) { return __builtin_bit_cast(float, (u32)x << 16); }

template <int N>
DI void wait_vm() { asm volatile("s_waitcnt vmcnt(%0)" ::"n"(N) : "memory"); }
DI void wait_lgkm0() { asm volatile("s_waitcnt lgkmcnt(0)" ::: "memory"); }
DI void bar() { __builtin_amdgcn_s_barrier(); }

#define SNOP "s_nop 0\n\t"
#define SNOP2 ""
DI const void* uni(const void* p) {
  const unsigned long long v = (unsigned long long)p;
  const u32 lo = __builtin_amdgcn_readfirstlane((u32)v), hi = __builtin_amdgcn_readfirstlane((u32)(v >> 32));
  return (const void*)(((unsigned long long)hi << 32) | lo);
}
DI void glds16_asm(const void* sbase, u32 voff, u32 lds_addr) {
  u32 keep;
  sbase = uni(sbase);
  lds_addr = __builtin_amdgcn_readfirstlane(lds_addr);
  asm volatile("s_mov_b32 %0, m0\n\ts_mov_b32 m0, %3\n\t" SNOP "global_load_lds_dwordx4 %1, %2\n\ts_mov_b32 m0, %0"
               : "=&s"(keep)
               : "v"(voff), "s"(sbase), "s"(lds_addr)
               : "memory");
}
DI void glds16(const void* g, char* lds) {
  __builtin_amdgcn_global_load_lds((const __attribute__((address_space(1))) void*)g, (LAS void*)lds, 16, 0, 2);
}

template <int CTRL>
DI float dpp_add(float v) {
  const int x = __builtin_bit_cast(int, v);
  return v + __builtin_bit_cast(float, __builtin_amdgcn_update_dpp(x, x, CTRL, 0xf, 0xf, false));
}
DI float sum8(float v) {
  v = dpp_add<0xB1>(v);
  v = dpp_add<0x4E>(v);
  v = dpp_add<0x141>(v);
  return v;
}
DI float sum16(float v) { return dpp_add<0x140>(sum8(v)); }
DI float dot2(u32 a, u32 b, float c) {
  return __builtin_amdgcn_fdot2_f32_bf16(__builtin_bit_cast(bf16x2_t, a), __builtin_bit_cast(bf16x2_t, b), c, false);
}
typedef __amdgpu_buffer_rsrc_t rsrc_t;
DI rsrc_t mk_rsrc(const void* p, u32 bytes) { return __builtin_amdgcn_make_buffer_rsrc((void*)p, 0, bytes, 0x00020000); }
DI void st16_sc1(void* p, u32x4 v) { asm volatile("global_store_dwordx4 %0, %1, off sc1\n\ts_nop 1" ::"v"(p), "v"(v) : "memory"); }
DI void st8_sc1(void* p, u32x2 v) { asm volatile("global_store_dwordx2 %0, %1, off sc1" ::"v"(p), "v"(v) : "memory"); }
DI void st4_sc1(void* p, float v) { asm volatile("global_store_dword %0, %1, off sc1" ::"v"(p), "v"(v) : "memory"); }

#define ACTAUX 16
#define NTAUX 2
constexpr int CODE_LINES = 250;
constexpr int K0_GRID = 400;

extern "C" __global__ __launch_bounds__(256) void k0_prep(const u16* __restrict__ q, const u16* __restrict__ k,
                                               const u16* __restrict__ v, const u16* __restrict__ shift_k,
                                               const u16* __restrict__ c, const u16* __restrict__ w_mod,
                                               const u16* __restrict__ ssm_k, char* __restrict__ ws) {
  __shared__ float red[4][32];
  const int b = blockIdx.x, tid = threadIdx.x, lane = tid & 63, w = tid >> 6;
  STAMP_DECL;
  STAMP(ws, 0, 0);
  const int xcd = b & 7, idx = b >> 3;
  u32 cwarm = 0;
  if (idx == 0) {
    u32 lo, hi;
    asm volatile(
        "s_getpc_b64 s[40:41]\n\ts_add_u32 s40, s40, k1_conv@rel32@lo+4\n\ts_addc_u32 s41, s41, k1_conv@rel32@hi+12\n\t"
        "s_mov_b32 %0, s40\n\ts_mov_b32 %1, s41"
        : "=s"(lo), "=s"(hi)::"s40", "s41", "scc");
    const char* code = (const char*)(((unsigned long long)hi << 32) | lo);
    if (tid < CODE_LINES) cwarm = *(const u32*)(code + tid * 128);
  }
  int misc = -1;
  if (xcd == 7) misc = idx;
  else if (idx >= 32) misc = idx < 46 ? 50 + xcd * 14 + (idx - 32) : 1000;
  if (misc < 0) {
    const int rb = idx;
    const int sq = lane >> 5, cp = lane & 31;
    const int d0 = xcd * 64 + cp * 2;
    const int a = rb * 8 + w * 2 + sq;
    const int s0 = a * 8;
    const rsrc_t rk = mk_rsrc(k, S * D * 2), rv = mk_rsrc(v, S * D * 2), rq = mk_rsrc(q, S * D * 2);
    const int oB = (s0 * D + d0) * 2;
    const int oA = oB - 4 * D * 2, oC = oB + 4 * D * 2;
    u32 kk[12], vv[9], qq[8];
#pragma unroll
    for (int i = 0; i < 4; ++i) kk[i] = __builtin_amdgcn_raw_buffer_load_b32(rk, oA + i * D * 2, 0, NTAUX);
#pragma unroll
    for (int i = 0; i < 4; ++i) kk[4 + i] = __builtin_amdgcn_raw_buffer_load_b32(rk, oB + i * D * 2, 0, NTAUX);
#pragma unroll
    for (int i = 0; i < 4; ++i) kk[8 + i] = __builtin_amdgcn_raw_buffer_load_b32(rk, oC + i * D * 2, 0, NTAUX);
    vv[0] = __builtin_amdgcn_raw_buffer_load_b32(rv, oA + 3 * D * 2, 0, NTAUX);
#pragma unroll
    for (int i = 0; i < 4; ++i) vv[1 + i] = __builtin_amdgcn_raw_buffer_load_b32(rv, oB + i * D * 2, 0, NTAUX);
#pragma unroll
    for (int i = 0; i < 4; ++i) vv[5 + i] = __builtin_amdgcn_raw_buffer_load_b32(rv, oC + i * D * 2, 0, NTAUX);
#pragma unroll
    for (int i = 0; i < 4; ++i) qq[i] = __builtin_amdgcn_raw_buffer_load_b32(rq, oB + i * D * 2, 0, NTAUX);
#pragma unroll
    for (int i = 0; i < 4; ++i) qq[4 + i] = __builtin_amdgcn_raw_buffer_load_b32(rq, oC + i * D * 2, 0, NTAUX);
    const u32x4 sk = *(const u32x4*)(shift_k + d0 * 4);
    u32 warm = 0;
    if (w == 0) warm = __builtin_nontemporal_load((const u32*)((const char*)ssm_k + ((size_t)(xcd * 32 + rb) * 64 + lane) * 128));
    f32x2 wt[4] = {{lo_f(sk[0]), lo_f(sk[2])}, {hi_f(sk[0]), hi_f(sk[2])}, {lo_f(sk[1]), lo_f(sk[3])}, {hi_f(sk[1]), hi_f(sk[3])}};
    f32x2 kf[12], u[9];
#pragma unroll
    for (int i = 0; i < 12; ++i) kf[i] = f32x2{lo_f(kk[i]), hi_f(kk[i])};
#pragma unroll
    for (int i = 0; i < 9; ++i) {
      f32x2 t = kf[i + 3] * wt[0];
      t = __builtin_elementwise_fma(kf[i + 2], wt[1], t);
      t = __builtin_elementwise_fma(kf[i + 1], wt[2], t);
      t = __builtin_elementwise_fma(kf[i], wt[3], t);
      u[i] = t * f32x2{lo_f(vv[i]), hi_f(vv[i])};
    }
    {
      char* dst = ws + (size_t)d0 * (S * 2) + (2040 - s0) * 2;
#pragma unroll
      for (int cI = 0; cI < 2; ++cI) {
        *(u32x4*)(dst + OFF_RO + cI * S * 2) = u32x4{pack_bf16(u[8][cI], u[7][cI]), pack_bf16(u[6][cI], u[5][cI]),
                                                     pack_bf16(u[4][cI], u[3][cI]), pack_bf16(u[2][cI], u[1][cI])};
        *(u32x4*)(dst + OFF_RE + cI * S * 2) = u32x4{pack_bf16(u[7][cI], u[6][cI]), pack_bf16(u[5][cI], u[4][cI]),
                                                     pack_bf16(u[3][cI], u[2][cI]), pack_bf16(u[1][cI], u[0][cI])};
      }
      char* dq = ws + OFF_QT + (size_t)d0 * (S * 2) + s0 * 2;
#pragma unroll
      for (int cI = 0; cI < 2; ++cI) {
        const u32 sel = cI ? 0x07060302u : 0x05040100u;
        *(u32x4*)(dq + cI * S * 2) = u32x4{__builtin_amdgcn_perm(qq[2], qq[0], sel), __builtin_amdgcn_perm(qq[6], qq[4], sel),
                                           __builtin_amdgcn_perm(qq[3], qq[1], sel), __builtin_amdgcn_perm(qq[7], qq[5], sel)};
      }
    }
    asm volatile("" ::"v"(warm));
  } else if (misc < 64) {
    const int bb = misc;
    const int row = bb * 32 + w * 8 + (lane >> 3);
    const int sub = lane & 7;
    u32x4 x[7];
#pragma unroll
    for (int i = 0; i < 7; ++i) x[i] = __builtin_nontemporal_load((const u32x4*)(q + (size_t)row * D + (i * 8 + sub) * 8));
    if (sub < 3) ((unsigned long long*)(ws + OFF_SG))[sub * S + row] = 0ull;
    if (bb < 2) ((u32*)(ws + OFF_FLAG))[(bb * 256 + tid) * 32] = 0u;
    float s = 0.f, s2 = 0.f;
#pragma unroll
    for (int i = 0; i < 7; ++i)
#pragma unroll
      for (int j = 0; j < 4; ++j) {
        const u32 t = x[i][j];
        s = dot2(t, 0x3f803f80u, s);
        s2 = dot2(t, t, s2);
      }
    s = sum8(s);
    s2 = sum8(s2);
    const float mean = s * (1.f / D);
    float var = s2 * (1.f / D) - mean * mean;
    var = var < 0.f ? 0.f : var;
    const float rstd = __builtin_amdgcn_rsqf(var + 1e-5f);
    if (sub < 2) {
      const int p = (row & ~7) | ((row & 1) << 2) | ((row >> 1) & 3);
      ((float*)(ws + OFF_MEAN))[sub * S + p] = sub ? rstd : mean;
    }
  } else if (misc < 148) {
    const int bb = misc - 64;
    const int co = lane >> 4, rs = lane & 15;
    const int col0 = bb * 32 + co * 8;
    u32x4 x[7];
    float sc[7];
#pragma unroll
    for (int i = 0; i < 7; ++i) {
      const int r = i * 64 + w * 16 + rs;
      x[i] = __builtin_nontemporal_load((const u32x4*)(w_mod + (size_t)r * (6 * D) + col0));
      sc[i] = bf2f(c[r]);
    }
    f32x2 acc[4] = {{0.f, 0.f}, {0.f, 0.f}, {0.f, 0.f}, {0.f, 0.f}};
#pragma unroll
    for (int i = 0; i < 7; ++i) {
      const float cv = sc[i];
      const float sl = round_bf16(cv * __builtin_amdgcn_rcpf(1.f + __builtin_amdgcn_exp2f(cv * -1.44269504f)));
#pragma unroll
      for (int j = 0; j < 4; ++j)
        acc[j] = __builtin_elementwise_fma(f32x2{lo_f(x[i][j]), hi_f(x[i][j])}, f32x2{sl, sl}, acc[j]);
    }
    float sel = 0.f;
#pragma unroll
    for (int j = 0; j < 8; ++j) {
      const float t = sum16(acc[j >> 1][j & 1]);
      sel = (rs == j) ? t : sel;
    }
    if (rs < 8) red[w][co * 8 + rs] = sel;
    __syncthreads();
    if (tid < 32) {
      const float t = (red[0][tid] + red[1][tid]) + (red[2][tid] + red[3][tid]);
      const float m = round_bf16(t);
      const int col = bb * 32 + tid;
      ((float*)(ws + OFF_MOD))[col] = m;
      if (col >= 3 * D && col < 5 * D) {
        const bool isc = col >= 4 * D;
        ((u16*)(ws + OFF_CB))[isc ? col - 4 * D : col - 2 * D] = (u16)pack_bf16(isc ? 1.f + m : m, 0.f);
      }
    }
  }
  asm volatile("" ::"v"(cwarm));
  STAMP(ws, 0, 1);
  STAMP_FLUSH(ws, misc < 0 ? 0 : (misc < 64 ? 4 : 5));
}

constexpr int K1_KSTR = 260 * 16;
constexpr int K1_USTR = 286 * 16;
constexpr int K1_UOFF = 4 * K1_KSTR;
constexpr int K1_XOFF = K1_UOFF + 2 * K1_USTR;
constexpr int K1_CH = K1_XOFF + 8192;
constexpr int K1_LDS = 2 * K1_CH;

DI void glds16x4_asm(const void* sbase, u32 voff, u32 voff_last, u32 lds_addr) {
  u32 keep;
  sbase = uni(sbase);
  lds_addr = __builtin_amdgcn_readfirstlane(lds_addr);
  asm volatile(
      "s_mov_b32 %0, m0\n\ts_mov_b32 m0, %4\n\t" SNOP
      "global_load_lds_dwordx4 %1, %3\n\t"
      "global_load_lds_dwordx4 %1, %3 offset:1024\n\t"
      "global_load_lds_dwordx4 %1, %3 offset:2048\n\t"
      "global_load_lds_dwordx4 %2, %3 offset:3072\n\t"
      "s_mov_b32 m0, %0"
      : "=&s"(keep)
      : "v"(voff), "v"(voff_last), "s"(sbase), "s"(lds_addr)
      : "memory");
}
#define CAS __attribute__((address_space(4)))
template <int half>
DI void k1_conv_wave(const u16* __restrict__ ssm_k, const u16* __restrict__ ssm_D, char* __restrict__ ws, char* smem,
                     int ch, int lane) {
  STAMP_DECL;
  STAMP(ws, 1, 0);
  const int cpair = (blockIdx.x & 7) * 32 + (blockIdx.x >> 3);
  const int d = 2 * cpair + ch;
  char* L = smem + ch * K1_CH;
  const u32 L0 = (u32)(size_t)(LAS char*)L;
  const u16* kf = ssm_k + (size_t)d * S;
  {
    const u32 vo = lane * 16, vl = (lane > 62 ? 62 : lane) * 16;
    glds16x4_asm(kf + (2 + 4 * half), vo, vl, L0 + (2 * half) * K1_KSTR + 64);
    glds16x4_asm(kf + (4 + 4 * half), vo, vl, L0 + (2 * half + 1) * K1_KSTR + 64);
    glds16x4_asm(ws + (half ? OFF_RO : OFF_RE) + (size_t)d * S * 2, vo, vo, L0 + K1_UOFF + half * K1_USTR);
  }
  const int n = lane & 15, q4 = lane >> 4;
  const int pbase = 32 * (n >> 1) + 8 * q4 + 4 * (n & 1) + 1024 * half;
  u32 head = 0;
  const int di = n + q4 - 15;
  if constexpr (half == 0) {
    asm volatile(SNOP2 "global_load_dword %0, %1, %2" : "=v"(head) : "v"((u32)(di < 0 ? 0 : di) * 4), "s"(uni(kf)) : "memory");
  }
  const CAS float* MODc = (const CAS float*)(size_t)(ws + OFF_MOD);
  const float sa = MODc[d], ca = MODc[D + d], ga = MODc[2 * D + d], cbv = MODc[4 * D + d];
  const u32 Draw = ((const CAS u32*)(size_t)ssm_D)[cpair];
  if constexpr (half == 1) {
    const int l5 = lane & 31;
    if (l5 < 28) *(LAS u32x4*)(L + K1_UOFF + (lane >> 5) * K1_USTR + (256 + l5) * 16) = u32x4{0u, 0u, 0u, 0u};
  }
  u32 Ab = L0 + (n & 3) * K1_KSTR + ((n >> 2) + q4 + 16 * half) * 16;
  u32 Bb = L0 + K1_UOFF + (n & 1) * K1_USTR + (28 - 4 * (n >> 1) + q4 + 16 * half) * 16;
  u32 Hd = L0 + q4 * K1_KSTR + n * 4;
  asm volatile("" : "+v"(Ab), "+v"(Bb), "+v"(Hd) : "s"(sa), "s"(ca), "s"(ga), "s"(Draw), "s"(cbv));
  asm volatile("s_waitcnt vmcnt(0)" : "+v"(head)::"memory");
  if constexpr (half == 0) *(LAS u32*)(size_t)Hd = di < 0 ? 0u : head;
  wait_lgkm0();
  STAMP(ws, 1, 1);
  bar();
  STAMP(ws, 1, 2);
  bf16x8 fa[2][8], fb[2][8];
  auto rd = [&](auto dI) {
    constexpr int dl = decltype(dI)::value;
#pragma unroll
    for (int g = 0; g < 8; ++g) {
      fa[dl & 1][g] = *(const LAS bf16x8*)(size_t)(Ab + 64 * dl + 512 * g);
      fb[dl & 1][g] = *(const LAS bf16x8*)(size_t)(Bb + 64 * dl + 512 * (7 - g));
    }
  };
  rd(std::integral_constant<int, 0>{});
  __builtin_amdgcn_sched_barrier(0);
  u32x2 qv[4];
  f32x4 mu[4], rs[4];
  asm volatile(SNOP2
               "global_load_dwordx2 %0, %12, %14\n\t"
               "global_load_dwordx2 %1, %12, %14 offset:512\n\t"
               "global_load_dwordx2 %2, %12, %14 offset:1024\n\t"
               "global_load_dwordx2 %3, %12, %14 offset:1536\n\t"
               "global_load_dwordx4 %4, %13, %15\n\t"
               "global_load_dwordx4 %5, %13, %15 offset:1024\n\t"
               "global_load_dwordx4 %6, %13, %15 offset:2048\n\t"
               "global_load_dwordx4 %7, %13, %15 offset:3072\n\t"
               "global_load_dwordx4 %8, %13, %16\n\t"
               "global_load_dwordx4 %9, %13, %16 offset:1024\n\t"
               "global_load_dwordx4 %10, %13, %16 offset:2048\n\t"
               "global_load_dwordx4 %11, %13, %16 offset:3072"
               : "=&v"(qv[0]), "=&v"(qv[1]), "=&v"(qv[2]), "=&v"(qv[3]), "=&v"(mu[0]), "=&v"(mu[1]), "=&v"(mu[2]),
                 "=&v"(mu[3]), "=&v"(rs[0]), "=&v"(rs[1]), "=&v"(rs[2]), "=&v"(rs[3])
               : "v"((u32)pbase * 2), "v"((u32)pbase * 4), "s"(uni(ws + OFF_QT + (size_t)d * S * 2)),
                 "s"(uni(ws + OFF_MEAN)), "s"(uni(ws + OFF_RSTD))
               : "memory");
  f32x4 acc[8];
#pragma unroll
  for (int j = 0; j < 8; ++j) acc[j] = f32x4{0.f, 0.f, 0.f, 0.f};
  f32x4 qf[4], hg[4], kd[4];
  u32x4 uc[4];
  static_for<0, 4>([&](auto dI) {
    constexpr int dl = decltype(dI)::value;
    if constexpr (dl < 3) rd(std::integral_constant<int, dl + 1>{});
    if constexpr (dl == 2) {
      asm volatile("s_waitcnt vmcnt(0)"
                   : "+v"(qv[0]), "+v"(qv[1]), "+v"(qv[2]), "+v"(qv[3]), "+v"(mu[0]), "+v"(mu[1]), "+v"(mu[2]), "+v"(mu[3]),
                     "+v"(rs[0]), "+v"(rs[1]), "+v"(rs[2]), "+v"(rs[3]));
    }
    if constexpr (dl == 1) {
#pragma unroll
      for (int j = 0; j < 4; ++j)
        uc[j] = *(const LAS u32x4*)(L + K1_UOFF + K1_USTR + (255 - (32 * (4 * half + j) + 4 * (n >> 1) + q4)) * 16);
    }
    if constexpr (dl >= 2) {
      const int sh = (n & 1) ? 16 : 0;
      const float gc = ga * round_bf16(1.f + ca), gs = ga * sa, Dd = ch ? hi_f(Draw) : lo_f(Draw);
#pragma unroll
      for (int j = 2 * (dl - 2); j < 2 * (dl - 2) + 2; ++j) {
#pragma unroll
        for (int r = 0; r < 4; ++r) {
          kd[j][r] = __builtin_bit_cast(float, (uc[j][3 - r] << sh) & 0xffff0000u) * Dd;
          qf[j][r] = (r & 1) ? hi_f(qv[j][r >> 1]) : lo_f(qv[j][r >> 1]);
          hg[j][r] = (qf[j][r] - mu[j][r]) * rs[j][r] * gc + gs;
        }
        asm volatile("" : "+v"(kd[j]), "+v"(qf[j]), "+v"(hg[j]));
      }
    }
#pragma unroll
    for (int sdiag = 0; sdiag < 8; ++sdiag)
#pragma unroll
      for (int g = 0; g <= sdiag; ++g)
        acc[sdiag] = __builtin_amdgcn_mfma_f32_16x16x32_bf16(fa[dl & 1][g], fb[dl & 1][sdiag - g], acc[sdiag], 0, 0, 0);
    if constexpr (dl == 0) {
#pragma unroll
      for (int i = 0; i < 16; ++i) {
        __builtin_amdgcn_sched_group_barrier(0x008, 2, 0);
        __builtin_amdgcn_sched_group_barrier(0x100, 1, 0);
      }
    } else if constexpr (dl == 1) {
#pragma unroll
      for (int i = 0; i < 16; ++i) {
        __builtin_amdgcn_sched_group_barrier(0x008, 2, 0);
        __builtin_amdgcn_sched_group_barrier(0x100, 1, 0);
      }
#pragma unroll
      for (int i = 0; i < 4; ++i) {
        __builtin_amdgcn_sched_group_barrier(0x008, 1, 0);
        __builtin_amdgcn_sched_group_barrier(0x100, 1, 0);
      }
    } else {
      __builtin_amdgcn_sched_group_barrier(0x008, 4, 0);
#pragma unroll
      for (int i = 0; i < 16; ++i) {
        __builtin_amdgcn_sched_group_barrier(0x008, 1, 0);
        __builtin_amdgcn_sched_group_barrier(0x002, 2, 0);
        __builtin_amdgcn_sched_group_barrier(0x008, 1, 0);
        __builtin_amdgcn_sched_group_barrier(0x002, 2, 0);
        __builtin_amdgcn_sched_group_barrier(0x100, 1, 0);
      }
    }
    __builtin_amdgcn_sched_barrier(0);
  });
  STAMP(ws, 1, 3);
  LAS f32x4* X = (LAS f32x4*)(L + K1_XOFF);
#pragma unroll
  for (int j = 0; j < 4; ++j) X[half * 256 + j * 64 + lane] = acc[4 * (1 - half) + j];
  wait_lgkm0();
  bar();
  STAMP(ws, 1, 4);
  u16* X1T = (u16*)(ws + OFF_X1T) + (size_t)d * S + pbase;
  u16* XS = (u16*)(ws + OFF_XS) + (size_t)d * S + pbase;
  const float c1 = round_bf16(1.f + cbv);
#pragma unroll
  for (int j = 0; j < 4; ++j) {
    const f32x4 y = X[(1 - half) * 256 + j * 64 + lane];
    const f32x4 o = qf[j] + hg[j] * ((acc[4 * half + j] + kd[j]) + y);
    const u32 p0 = pack_bf16(o[0], o[1]), p1 = pack_bf16(o[2], o[3]);
    st8_sc1(XS + 256 * j, u32x2{pack_bf16(lo_f(p0) * c1, hi_f(p0) * c1), pack_bf16(lo_f(p1) * c1, hi_f(p1) * c1)});
    st8_sc1(X1T + 256 * j, u32x2{p0, p1});
  }
  STAMP(ws, 1, 5);
  STAMP_FLUSH(ws, 1);
}

DI bf16x8 tr_frag(const char* smem, int o0, int o1) {
  s16x4 x0 = __builtin_amdgcn_ds_read_tr16_b64_v4i16((LAS s16x4*)(smem + o0));
  s16x4 x1 = __builtin_amdgcn_ds_read_tr16_b64_v4i16((LAS s16x4*)(smem + o1));
  return __builtin_shufflevector(x0, x1, 0, 1, 2, 3, 4, 5, 6, 7);
}
DI f32x16 mfma32(u32x4 a, bf16x8 b, f32x16 c) {
  return __builtin_amdgcn_mfma_f32_32x32x16_bf16(__builtin_bit_cast(bf16x8, a), b, c, 0, 0, 0);
}

DI void k1_helper(int task, const u16* __restrict__ w_gate, const u16* __restrict__ w_up, const u16* __restrict__ w_down,
                  char* __restrict__ ws, char* smem, int w, int lane) {
  const u32 lds0 = (u32)(size_t)(LAS char*)smem;
  const int xr = lane >> 4, slot = lane & 15;
  const int chn = slot ^ ((xr << 2) | (w & 3));
  const int h = lane >> 5, blk = (lane >> 4) & 1, qq = (lane & 15) >> 2, p = lane & 3, n = lane & 31;
  const bool gu = task < 112;
  int kq = 0, fg = 0, dg = 0, fk = 0;
  if (gu) {
    fg = task % 28, kq = task / 28;
    const u16* src = (chn < 8 ? w_gate : w_up) + (size_t)(kq * 112 + 4 * w + xr) * F + fg * 64 + (chn & 7) * 8;
#pragma unroll
    for (int i = 0; i < 7; ++i) glds16(src + (size_t)i * 16 * F, smem + (w + 4 * i) * 1024);
  } else {
    dg = (task - 112) % 7, fk = (task - 112) / 7;
    const u16* src = w_down + (size_t)(fk * 224 + (chn >> 3) * 112 + 4 * w + xr) * D + dg * 64 + (chn & 7) * 8;
#pragma unroll
    for (int i = 0; i < 7; ++i) glds16(src + (size_t)i * 16 * D, smem + (w + 4 * i) * 1024);
  }
  const int col = gu ? blk * 128 + w * 32 + (p & 1) * 16 + (p >> 1) * 8 : (w >> 1) * 128 + (w & 1) * 64 + blk * 32 + p * 8;
  int o[2];
#pragma unroll
  for (int t = 0; t < 2; ++t)
    o[t] = 256 * (8 * h + 4 * t + qq) + 16 * ((col >> 4) ^ ((qq << 2) | ((2 * h + t) & 3))) + (col & 8);
  u32x4 bv[7];
  if (gu) {
    const u16* cbp = (const u16*)(ws + OFF_CB) + (n & 1) * D + kq * 112 + 8 * h;
#pragma unroll
    for (int i = 0; i < 7; ++i) bv[i] = *(const u32x4*)(cbp + 16 * i);
  }
  __syncthreads();
  u32x4* dst = gu ? (u32x4*)(ws + OFF_WGU) + ((size_t)(fg * 4 + w) * 28 + kq * 7) * 64 + lane
                  : (u32x4*)(ws + OFF_WD) + ((size_t)(dg * 2 + (w & 1)) * 112 + fk * 14 + (w >> 1) * 7) * 64 + lane;
  f32x16 acc;
#pragma unroll
  for (int r = 0; r < 16; ++r) acc[r] = 0.f;
#pragma unroll
  for (int i = 0; i < 7; ++i) {
    const u32x4 f = __builtin_bit_cast(u32x4, tr_frag(smem, o[0] + i * 4096, o[1] + i * 4096));
    st16_sc1(dst + i * 64, f);
    if (gu) acc = mfma32(f, __builtin_bit_cast(bf16x8, bv[i]), acc);
  }
  if (gu && n < 2) {
    float* vd = (float*)(ws + OFF_VP) + (size_t)(kq * 2 + n) * 3584 + (fg * 4 + w) * 32 + 4 * h;
#pragma unroll
    for (int g = 0; g < 4; ++g) *(f32x4*)(vd + 8 * g) = f32x4{acc[4 * g], acc[4 * g + 1], acc[4 * g + 2], acc[4 * g + 3]};
  }
}

constexpr int K1_GRID = 400;
extern "C" __global__ NOPK __launch_bounds__(256, 2) void k1_conv(const u16* __restrict__ ssm_k, const u16* __restrict__ ssm_D,
                                                       const u16* __restrict__ w_gate, const u16* __restrict__ w_up,
                                                       const u16* __restrict__ w_down, char* __restrict__ ws) {
  extern __shared__ __attribute__((aligned(16))) char smem[];
  const int tid = threadIdx.x, lane = tid & 63;
  const int w = __builtin_amdgcn_readfirstlane(tid >> 6);
  const int xcd = blockIdx.x & 7, idx = blockIdx.x >> 3;
  if (xcd < 7 && idx < 32) {
    if (w & 1) k1_conv_wave<1>(ssm_k, ssm_D, ws, smem, w >> 1, lane);
    else k1_conv_wave<0>(ssm_k, ssm_D, ws, smem, w >> 1, lane);
  } else {
    STAMP_DECL;
    STAMP(ws, 7, 0);
    if (xcd == 7 && idx >= 42) return;
    const int task = xcd == 7 ? idx : 42 + xcd * 18 + (idx - 32);
    k1_helper(task, w_gate, w_up, w_down, ws, smem, w, lane);
    STAMP(ws, 7, 1);
    STAMP_FLUSH(ws, 7);
  }
}

constexpr int K2_XB = D * 256;
constexpr int K2_ST = K2_XB;
constexpr int K2_LDS = 131072;

DI void gload16_asm(u32x4& dst, const void* sbase, u32 voff) {
  sbase = uni(sbase);
  asm volatile(SNOP2 "global_load_dwordx4 %0, %1, %2" : "=v"(dst) : "v"(voff), "s"(sbase) : "memory");
}
template <int OFF>
DI void gload16_off(u32x4& dst, const void* sbase, u32 voff) {
  sbase = uni(sbase);
  asm volatile(SNOP2 "global_load_dwordx4 %0, %1, %2 offset:%3" : "=v"(dst) : "v"(voff), "s"(sbase), "n"(OFF) : "memory");
}
DI void glds16_nom0(const void* sbase, u32 voff, u32 lds_addr) {
  sbase = uni(sbase);
  lds_addr = __builtin_amdgcn_readfirstlane(lds_addr);
  asm volatile("s_mov_b32 m0, %2\n\t" SNOP "global_load_lds_dwordx4 %0, %1" ::"v"(voff), "s"(sbase), "s"(lds_addr) : "memory");
}
template <int N>
DI void wait_vm_pin(u32x4& a, u32x4& b) {
  asm volatile("s_waitcnt vmcnt(%2)" : "+v"(a), "+v"(b) : "n"(N) : "memory");
}
DI float xor32(float v) {
  const u32 u = __builtin_bit_cast(u32, v);
  auto r = __builtin_amdgcn_permlane32_swap(u, u, false, false);
  const u32 a = r[0], b = r[1];
  return __builtin_bit_cast(float, (threadIdx.x & 32) ? a : b);
}

typedef unsigned long long u64;
typedef __attribute__((address_space(1))) u64 gu64;
typedef __attribute__((address_space(1))) u32 gu32;
#define RLX_AGENT __ATOMIC_RELAXED, __HIP_MEMORY_SCOPE_AGENT
#define EXP_AHEAD 2
constexpr int K2_AHEAD = EXP_AHEAD;

DI void k2_stats(char* __restrict__ ws, char* smem) {
  const int tid = threadIdx.x, lane = tid & 63;
  const int w = __builtin_amdgcn_readfirstlane(tid >> 6);
  const int n = lane & 31, h = lane >> 5;
  STAMP_DECL;
  STAMP(ws, 2, 0);
  {
    const int p0 = blockIdx.x * 64;
    const u32* src = (const u32*)(ws + OFF_X1T) + ((size_t)(w * 56 + h) * S + p0) / 2 + n;
    u32 x[28];
#pragma unroll
    for (int i = 0; i < 28; ++i) x[i] = src[(size_t)i * S];
    float s0 = 0.f, s1 = 0.f, q0 = 0.f, q1 = 0.f;
#pragma unroll
    for (int i = 0; i < 28; ++i) {
      const float a = lo_f(x[i]), b = hi_f(x[i]);
      s0 += a;
      s1 += b;
      q0 = __builtin_fmaf(a, a, q0);
      q1 = __builtin_fmaf(b, b, q1);
    }
    *(LAS f32x4*)(smem + ((w * 2 + h) * 32 + n) * 16) = f32x4{s0, q0, s1, q1};
    __syncthreads();
    if (tid < 64) {
      float sm = 0.f, sq = 0.f;
#pragma unroll
      for (int i = 0; i < 16; ++i) {
        const f32x2 t = *(const LAS f32x2*)(smem + (i * 64 + tid) * 8);
        sm += t[0];
        sq += t[1];
      }
      const float mean = sm * (1.f / D);
      float var = sq * (1.f / D) - mean * mean;
      var = (var < 0.f ? 0.f : var) + 1e-5f;
      const float rstd = 1.f / sqrtf(var);
      const float sig = var * rstd;
      const float mh = round_bf16(mean), sh = round_bf16(sig);
      const u32 b0 = pack_bf16(-mh, mh - mean), b1 = pack_bf16(-mh, sh), b2 = pack_bf16(sig - sh, sh);
      const float c1 = -2.302208198f * rstd;
      const float c2 = -0.1029432397f * (rstd * rstd * rstd);
      gu64* G = (gu64*)(size_t)(ws + OFF_SG) + p0 + tid;
      __hip_atomic_store(G, ((u64)b1 << 32) | b0, RLX_AGENT);
      __hip_atomic_store(G + S, ((u64)__builtin_bit_cast(u32, var) << 32) | b2, RLX_AGENT);
      __hip_atomic_store(G + 2 * S, ((u64)__builtin_bit_cast(u32, c1) << 32) | __builtin_bit_cast(u32, c2), RLX_AGENT);
    }
    STAMP(ws, 6, 1);
    u32 wm[7];
    asm volatile("s_waitcnt vmcnt(0)" ::"v"(wm[0]), "v"(wm[1]), "v"(wm[2]), "v"(wm[3]), "v"(wm[4]), "v"(wm[5]), "v"(wm[6]) : "memory");
    STAMP(ws, 6, 2);
    STAMP_FLUSH(ws, 6);
  }
}

template <int GRP>
DI void k2_gemm(char* __restrict__ ws, u16* __restrict__ out, char* smem, const int w) {
  const int tid = threadIdx.x, lane = tid & 63;
  const int n = lane & 31, h = lane >> 5;
  STAMP_DECL;
  STAMP(ws, 2, 0);
  const int bid = blockIdx.x - 32;
  const int xcd = bid & 7, idx = bid >> 3;
  const int mt = (xcd >> 1) * 4 + (idx & 3);
  const int dt = idx >> 2, sh2 = xcd & 1;
  const int ft = sh2 * 7 + dt;
  const int tile = ft * 8 + w;
  const u32 lds0 = (u32)(size_t)(LAS char*)smem;
  u32x4 wf[28];
  const int xr = lane >> 4, slot = lane & 15;
  const char* xbase = ws + OFF_XS + ((size_t)(4 * w) * S + mt * 128) * 2;
  const u32 xoff = (u32)(xr * S + (slot ^ ((xr << 2) | (w & 3))) * 8) * 2;
  const char* wbase = ws + OFF_WGU + (size_t)(tile * 28) * 1024 + 4096;
  const u32 wa = lane * 16;
  auto issue1 = [&](auto qI) {
    constexpr int q = decltype(qI)::value, j = q / 3, r = q % 3;
    if constexpr (r == 0) glds16_nom0(xbase, xoff + j * (32 * S * 2), lds0 + (32 * j + 4 * w) * 256);
    if constexpr (r != 0) {
      constexpr int k = 2 * j + r - 1;
      gload16_off<(k % 8) * 1024 - 4096>(wf[k], wbase + (k / 8) * 8192, wa);
    }
  };
  static_for<0, 3>(issue1);
  float vp[8];
  {
    const char* vb = ws + OFF_VP + (size_t)tile * 128;
    static_for<0, 8>([&](auto iI) {
      constexpr int i = decltype(iI)::value;
      float t;
      asm volatile(SNOP2 "global_load_dword %0, %1, %2" : "=v"(t) : "v"((u32)n * 4), "s"(uni(vb + i * 3584 * 4)) : "memory");
      vp[i] = t;
    });
  }
  static_for<3, 3 * (K2_AHEAD + 1)>(issue1);
  constexpr int NTL[4] = {0, 2, 1, 3};
  int xo[4][2];
  {
    const int blk = (lane >> 4) & 1, qq = (lane & 15) >> 2, p = lane & 3;
#pragma unroll
    for (int i = 0; i < 4; ++i) {
#pragma unroll
      for (int t = 0; t < 2; ++t) {
        const int row = 8 * h + 4 * t + qq;
        const int chn = 4 * NTL[i] + 2 * blk + (p >> 1);
        xo[i][t] = 256 * row + 16 * (chn ^ ((qq << 2) | ((2 * h + t) & 3))) + 8 * (p & 1);
      }
    }
  }
  f32x16 acc[4];
#pragma unroll
  for (int i = 0; i < 4; ++i)
#pragma unroll
    for (int r = 0; r < 16; ++r) acc[i][r] = 0.f;
  bf16x8 xa[2][3], xb[2][3];
  asm volatile("" : "+v"(xo[0][0]), "+v"(xo[0][1]), "+v"(xo[1][0]), "+v"(xo[1][1]), "+v"(xo[2][0]), "+v"(xo[2][1]),
               "+v"(xo[3][0]), "+v"(xo[3][1]));
  wait_vm<3 * K2_AHEAD + 2 + 8>();
  __builtin_amdgcn_sched_barrier(0);
  bar();
  YSTAMP(1);
#pragma unroll
  for (int ks = 0; ks < 2; ++ks)
#pragma unroll
    for (int i = 0; i < 3; ++i) xa[ks][i] = tr_frag(smem, xo[i][0] + ks * 4096, xo[i][1] + ks * 4096);
  __builtin_amdgcn_sched_barrier(0);
  u32x4 aext;
  auto mk_aext = [&]() {
    float t0 = vp[0], t1 = vp[1], t2 = vp[2], t3 = vp[3], t4 = vp[4], t5 = vp[5], t6 = vp[6], t7 = vp[7];
    asm volatile("" : "+v"(t0), "+v"(t1), "+v"(t2), "+v"(t3), "+v"(t4), "+v"(t5), "+v"(t6), "+v"(t7));
    const float v1 = (t0 + t2) + (t4 + t6);
    const float v2 = (t1 + t3) + (t5 + t7);
    const float v1h = round_bf16(v1), v2h = round_bf16(v2);
    u32x4 a;
    a[0] = pack_bf16(v1h, v1h);
    a[1] = pack_bf16(v1 - v1h, v2h);
    a[2] = pack_bf16(v2h, v2 - v2h);
    a[3] = 0u;
    if (h) a = u32x4{0u, 0u, 0u, 0u};
    asm volatile("" : "+v"(a));
    aext = a;
  };
  const int gsl = w < 6 ? w : w - 6;
  const char* gbase = ws + OFF_SG + (size_t)((gsl >> 1) * S + mt * 128 + (gsl & 1) * 64) * 8;
  LAS u64* CS = (LAS u64*)(smem + K2_ST);
  u64 gv;
  constexpr int JG = 9;
  auto pin = [](f32x16& a) { asm volatile("" : "+v"(a)); };
  auto chunk = [&](auto jI, const bf16x8(&cur)[2][3], bf16x8(&nxt)[2][3]) {
    constexpr int j = decltype(jI)::value;
    constexpr int last = (j + K2_AHEAD < 13) ? j + K2_AHEAD : 13;
    constexpr int ex = (j > JG && j + 1 <= JG + K2_AHEAD) ? 1 : 0;
    if constexpr (j < 13) {
      wait_vm_pin<3 * (last - (j + 1)) + 2 + ex>(wf[2 * j], wf[2 * j + 1]);
      if constexpr (j == JG + K2_AHEAD + 1) wait_lgkm0();
      __builtin_amdgcn_sched_barrier(0);
      bar();
    } else {
      wait_vm_pin<0>(wf[2 * j], wf[2 * j + 1]);
    }
    __builtin_amdgcn_sched_barrier(0);
    static_for<0, 6>([&](auto iI) {
      constexpr int sl = decltype(iI)::value, ks = sl / 3, i = sl % 3;
      acc[i] = mfma32(wf[2 * j + ks], cur[ks][i], acc[i]);
      pin(acc[i]);
      __builtin_amdgcn_sched_barrier(0);
      if constexpr (j == 6 && ks == 0) {
        int a0 = xo[i][0] + 7 * 8192, a1 = xo[i][1] + 7 * 8192;
        asm volatile("" : "+v"(a0), "+v"(a1));
        xo[i][0] = a0;
        xo[i][1] = a1;
      }
      if constexpr (j < 13)
        nxt[ks][i] = tr_frag(smem, xo[i][0] + ks * 4096 + ((j + 1) % 7) * 8192, xo[i][1] + ks * 4096 + ((j + 1) % 7) * 8192);
      if constexpr (j == JG && sl == 0) {
        u64 t;
        asm volatile(SNOP2 "global_load_dwordx2 %0, %1, %2 sc1" : "=v"(t) : "v"((u32)lane * 8), "s"(uni(gbase)) : "memory");
        gv = t;
      }
      if constexpr ((sl & 1) == 0 && j + K2_AHEAD + 1 <= 13)
        issue1(std::integral_constant<int, 3 * (j + K2_AHEAD + 1) + sl / 2>{});
      if constexpr (j == 1 && sl == 1) mk_aext();
      if constexpr (j == JG + K2_AHEAD && sl == 3) {
        u64 t = gv;
        asm volatile("" : "+v"(t));
        if (!__all((t >> 32) != 0)) {
          for (int spin = 0; spin < (1 << 16); ++spin) {
            __builtin_amdgcn_s_sleep(2);
            t = __hip_atomic_load((gu64*)(size_t)gbase + lane, RLX_AGENT);
            if (__all((t >> 32) != 0)) break;
          }
          __builtin_amdgcn_s_waitcnt(0x0F70);
        }
        CS[gsl * 64 + lane] = t;
      }
      __builtin_amdgcn_sched_barrier(0);
    });
  };
  static_for<0, 14>([&](auto jI) {
    if constexpr (decltype(jI)::value & 1) chunk(jI, xb, xa);
    else chunk(jI, xa, xb);
  });
  STAMP(ws, 2, 2);
  u32x4 wd[28];
  const char* wdbase = ws + OFF_WD + ((size_t)(2 * dt) * 112 + 14 * w) * 1024 + 4096;
  auto ldwd = [&](auto kI) {
    constexpr int k = decltype(kI)::value, kk = k % 14;
    gload16_off<(kk % 8) * 1024 - 4096>(wd[k], wdbase + (k / 14) * 112 * 1024 + (kk / 8) * 8192, wa);
  };
  u32x4* act_base = (u32x4*)(ws + OFF_ACT) + ((size_t)(mt * 4) * 112 + tile) * 64 + lane;
  auto passM = [&]() {
    constexpr int PF = 4;
    bf16x8 xr3[PF];
    auto rd3 = [&](auto kI) {
      constexpr int k = decltype(kI)::value;
      if constexpr (k == 14) {
        int a0 = xo[3][0] + 14 * 4096, a1 = xo[3][1] + 14 * 4096;
        asm volatile("" : "+v"(a0), "+v"(a1));
        xo[3][0] = a0;
        xo[3][1] = a1;
      }
      xr3[k % PF] = tr_frag(smem, xo[3][0] + (k % 14) * 4096, xo[3][1] + (k % 14) * 4096);
    };
    static_for<0, PF - 1>(rd3);
    __builtin_amdgcn_sched_barrier(0);
    static_for<0, 28>([&](auto kI) {
      constexpr int k = decltype(kI)::value;
      acc[3] = mfma32(wf[k], xr3[k % PF], acc[3]);
      pin(acc[3]);
      __builtin_amdgcn_sched_barrier(0);
      if constexpr (k + PF - 1 < 28) rd3(std::integral_constant<int, k + PF - 1>{});
      if constexpr (k & 1) ldwd(std::integral_constant<int, k / 2>{});
      __builtin_amdgcn_sched_barrier(0);
    });
  };
  auto epi = [&](auto iI, auto wI) {
    constexpr int i = decltype(iI)::value, W0 = decltype(wI)::value;
    u64 g[3];
#pragma unroll
    for (int k = 0; k < 3; ++k) g[k] = CS[k * 128 + NTL[i] * 32 + n];
    const u32x4 bext = {(u32)g[0], (u32)(g[0] >> 32), (u32)g[1], 0u};
    const f32x16 a = mfma32(aext, __builtin_bit_cast(bf16x8, bext), acc[i]);
    if constexpr (W0 >= 0) static_for<W0, W0 + 4>(ldwd);
    const float c1 = __builtin_bit_cast(float, (u32)(g[2] >> 32)), c2 = __builtin_bit_cast(float, (u32)g[2]);
    const float var = __builtin_bit_cast(float, (u32)(g[1] >> 32));
    float e[8], m[8];
#pragma unroll
    for (int r = 0; r < 8; ++r) e[r] = __builtin_amdgcn_exp2f(__builtin_fmaf(a[r] * a[r], c2, c1) * a[r]);
#pragma unroll
    for (int r = 0; r < 8; ++r) m[r] = a[r] * a[r + 8];
    if constexpr (W0 >= 0) {
      asm volatile("" : "+v"(e[0]), "+v"(e[7]));
      static_for<W0 + 4, W0 + 7>(ldwd);
    }
#pragma unroll
    for (int r = 0; r < 8; ++r) m[r] *= __builtin_amdgcn_rcpf(__builtin_fmaf(e[r], var, var));
    const u32x4 ov = {pack_bf16(m[0], m[1]), pack_bf16(m[2], m[3]), pack_bf16(m[4], m[5]), pack_bf16(m[6], m[7])};
    st16_sc1(act_base + (size_t)NTL[i] * 112 * 64, ov);
    __builtin_amdgcn_sched_barrier(0);
  };
  using I0 = std::integral_constant<int, 0>;
  using I1 = std::integral_constant<int, 1>;
  using I2 = std::integral_constant<int, 2>;
  using I3 = std::integral_constant<int, 3>;
  using WN = std::integral_constant<int, -1>;
  using W14 = std::integral_constant<int, 14>;
  using W21 = std::integral_constant<int, 21>;
  gu32* FL = (gu32*)(size_t)(ws + OFF_FLAG) + mt * 32 * 32;
  if constexpr (GRP == 0) {
    passM();
    XSTAMP(0);
    epi(I0{}, W14{});
    epi(I1{}, W21{});
    XSTAMP(1);
    epi(I2{}, WN{});
    XSTAMP(5);
    wait_vm<1>();
    XSTAMP(6);
  } else {
    epi(I0{}, WN{});
    epi(I1{}, WN{});
    XSTAMP(0);
    passM();
    XSTAMP(1);
    epi(I2{}, W14{});
    XSTAMP(5);
    wait_vm<14 + 7 + 1>();
    XSTAMP(6);
  }
  bar();
  if (tid == 0) __hip_atomic_store(FL + ft * 32, 1u, RLX_AGENT);
  STAMP(ws, 2, 3);
  if constexpr (GRP == 0) epi(I3{}, WN{});
  else epi(I3{}, W21{});
  const int f0 = (14 * w) >> 3, f1 = (14 * w + 13) >> 3;
  const int fl = (f0 + (lane & 3) > f1 ? f1 : f0 + (lane & 3)) * 32;
  u32 pv;
  asm volatile(SNOP2 "global_load_dword %0, %1, %2 sc1" : "=v"(pv) : "v"((u32)fl * 4), "s"(uni((const void*)FL)) : "memory");
  asm volatile("s_waitcnt vmcnt(0)"
               : "+v"(wd[0]), "+v"(wd[1]), "+v"(wd[2]), "+v"(wd[3]), "+v"(wd[4]), "+v"(wd[5]), "+v"(wd[6]), "+v"(wd[7]),
                 "+v"(wd[8]), "+v"(wd[9]), "+v"(wd[10]), "+v"(wd[11]), "+v"(wd[12]), "+v"(wd[13]), "+v"(pv)::"memory");
  asm volatile(""
               : "+v"(wd[14]), "+v"(wd[15]), "+v"(wd[16]), "+v"(wd[17]), "+v"(wd[18]), "+v"(wd[19]), "+v"(wd[20]),
                 "+v"(wd[21]), "+v"(wd[22]), "+v"(wd[23]), "+v"(wd[24]), "+v"(wd[25]), "+v"(wd[26]), "+v"(wd[27])::"memory");
  bar();
  if (tid == 0) __hip_atomic_store(FL + (16 + ft) * 32, 1u, RLX_AGENT);
  STAMP(ws, 2, 4);
  for (int spin = 0; !__all(pv != 0) && spin < (1 << 16); ++spin) {
    __builtin_amdgcn_s_sleep(1);
    pv = __hip_atomic_load(FL + fl, RLX_AGENT);
  }
  __builtin_amdgcn_fence(__ATOMIC_ACQUIRE, "wavefront");
  YSTAMP(5);
  const rsrc_t ra = mk_rsrc(ws + OFF_ACT + ((size_t)(mt * 4 + sh2 * 2) * 112 + 14 * w) * 1024, 126 * 1024);
  u32x4 af[28];
  auto ldact = [&](auto kI) {
    constexpr int k = decltype(kI)::value;
    af[k] = __builtin_amdgcn_raw_buffer_load_b128(ra, lane * 16 + (k % 14) * 1024, (k / 14) * 112 * 1024, ACTAUX);
  };
  static_for<0, 8>(ldact);
  pv = __hip_atomic_load(FL + 512 + fl, RLX_AGENT);
  static_for<8, 14>(ldact);
  const int tt = w >> 1, hf = w & 1, ti2 = tt >> 1, tj2 = tt & 1;
  const int pcol = mt * 128 + sh2 * 64 + tj2 * 32 + n;
  const int dbase = dt * 64 + ti2 * 32 + 16 * hf + 4 * h;
  __builtin_amdgcn_sched_barrier(0);
  LAS f32x4* R = (LAS f32x4*)smem;
  f32x16 o0, o1;
#pragma unroll
  for (int r = 0; r < 16; ++r) o0[r] = o1[r] = 0.f;
  auto step = [&](auto kI) {
    constexpr int k = decltype(kI)::value;
    const bf16x8 b = __builtin_bit_cast(bf16x8, af[k]);
    o0 = mfma32(wd[k % 14], b, o0);
    o1 = mfma32(wd[14 + k % 14], b, o1);
    pin(o0);
    pin(o1);
    __builtin_amdgcn_sched_barrier(0);
  };
  auto dump = [&](int tj) {
#pragma unroll
    for (int q = 0; q < 4; ++q) {
      R[(((0 * 2 + tj) * 8 + w) * 4 + q) * 64 + lane] = f32x4{o0[4 * q], o0[4 * q + 1], o0[4 * q + 2], o0[4 * q + 3]};
      R[(((1 * 2 + tj) * 8 + w) * 4 + q) * 64 + lane] = f32x4{o1[4 * q], o1[4 * q + 1], o1[4 * q + 2], o1[4 * q + 3]};
    }
    __builtin_amdgcn_sched_barrier(0);
  };
  static_for<0, 8>(step);
  for (int spin = 0; !__all(pv != 0) && spin < (1 << 16); ++spin) {
    __builtin_amdgcn_s_sleep(1);
    pv = __hip_atomic_load(FL + 512 + fl, RLX_AGENT);
  }
  __builtin_amdgcn_fence(__ATOMIC_ACQUIRE, "wavefront");
  static_for<14, 22>(ldact);
  __builtin_amdgcn_sched_barrier(0);
  static_for<8, 14>([&](auto kI) {
    step(kI);
    ldact(std::integral_constant<int, decltype(kI)::value + 14>{});
    __builtin_amdgcn_sched_barrier(0);
  });
  u16 xres[8];
  f32x4 gbv[2];
  {
    const u16* X1T = (const u16*)(ws + OFF_X1T) + (size_t)dbase * S + pcol;
#pragma unroll
    for (int r = 0; r < 8; ++r) xres[r] = X1T[(size_t)(8 * (r >> 2) + (r & 3)) * S];
#pragma unroll
    for (int q = 0; q < 2; ++q) gbv[q] = *(const f32x4*)((const float*)(ws + OFF_MOD) + 5 * D + dbase + 8 * q);
  }
  dump(0);
#pragma unroll
  for (int r = 0; r < 16; ++r) o0[r] = o1[r] = 0.f;
  static_for<14, 28>(step);
  dump(1);
  wait_lgkm0();
  bar();
  YSTAMP(6);
  const int srow = (pcol & ~7) | ((pcol & 3) << 1) | ((pcol >> 2) & 1);
#pragma unroll
  for (int q = 0; q < 2; ++q) {
    f32x4 v[8];
#pragma unroll
    for (int sw = 0; sw < 8; ++sw) v[sw] = R[((tt * 8 + sw) * 4 + 2 * hf + q) * 64 + lane];
    const f32x4 sum = ((v[0] + v[1]) + (v[2] + v[3])) + ((v[4] + v[5]) + (v[6] + v[7]));
    float o[4];
#pragma unroll
    for (int e = 0; e < 4; ++e) o[e] = __builtin_fmaf(gbv[q][e], sum[e], bf2f(xres[4 * q + e]));
    *(u32x2*)(out + (size_t)srow * D + dbase + 8 * q) = u32x2{pack_bf16(o[0], o[1]), pack_bf16(o[2], o[3])};
  }
}

extern "C" __global__ NOPK __launch_bounds__(512, 2) void k2_ffn(char* __restrict__ ws, u16* __restrict__ out) {
  extern __shared__ __attribute__((aligned(16))) char smem[];
  const int w = __builtin_amdgcn_readfirstlane(threadIdx.x >> 6);
  if (blockIdx.x < 32) k2_stats(ws, smem);
  else if (w < 4) k2_gemm<0>(ws, out, smem, w);
  else k2_gemm<1>(ws, out, smem, w);
}

extern "C" void kernel_launch(void* const* d_in, const int* in_sizes, int n_in, void* d_out, int out_size, void* d_ws,
                              size_t ws_size, hipStream_t stream) {
  const u16* q = (const u16*)d_in[0];
  const u16* k = (const u16*)d_in[1];
  const u16* v = (const u16*)d_in[2];
  const u16* shift_k = (const u16*)d_in[3];
  const u16* ssm_k = (const u16*)d_in[4];
  const u16* ssm_D = (const u16*)d_in[5];
  const u16* w_gate = (const u16*)d_in[7];
  const u16* w_up = (const u16*)d_in[8];
  const u16* w_down = (const u16*)d_in[9];
  const u16* c = (const u16*)d_in[10];
  const u16* w_mod = (const u16*)d_in[11];
  char* ws = (char*)d_ws;
  (void)hipFuncSetAttribute((const void*)k1_conv, hipFuncAttributeMaxDynamicSharedMemorySize, K1_LDS);
  (void)hipFuncSetAttribute((const void*)k2_ffn, hipFuncAttributeMaxDynamicSharedMemorySize, K2_LDS);
  k0_prep<<<K0_GRID, 256, 0, stream>>>(q, k, v, shift_k, c, w_mod, ssm_k, ws);
  k1_conv<<<K1_GRID, 256, K1_LDS, stream>>>(ssm_k, ssm_D, w_gate, w_up, w_down, ws);
  k2_ffn<<<256, 512, K2_LDS, stream>>>(ws, (u16*)d_out);
}
```

```cpp
#include <hip/hip_runtime.h>
#include <hip/hip_bf16.h>
#include <stdint.h>
#include <type_traits>

typedef unsigned short u16;
typedef unsigned int u32;
typedef short bf16x8 __attribute__((ext_vector_type(8)));
typedef short s16x4 __attribute__((ext_vector_type(4)));
typedef float f32x2 __attribute__((ext_vector_type(2)));
typedef float f32x4 __attribute__((ext_vector_type(4)));
typedef float f32x16 __attribute__((ext_vector_type(16)));
typedef u32 u32x2 __attribute__((ext_vector_type(2)));
typedef u32 u32x4 __attribute__((ext_vector_type(4)));
typedef __bf16 bf16x2_t __attribute__((ext_vector_type(2)));
#define LAS __attribute__((address_space(3)))
#define DI __device__ __forceinline__

constexpr int S = 2048, D = 448, F = 1792;

constexpr size_t SZ_DS = (size_t)D * S * 2;
constexpr size_t OFF_RE = 0;
constexpr size_t OFF_RO = OFF_RE + SZ_DS;
constexpr size_t OFF_QT = OFF_RO + SZ_DS;
constexpr size_t OFF_X1T = OFF_QT + SZ_DS;
constexpr size_t OFF_MEAN = OFF_X1T + SZ_DS;
constexpr size_t OFF_RSTD = OFF_MEAN + S * 4;
constexpr size_t OFF_MOD = OFF_RSTD + S * 4;
constexpr size_t OFF_SG = OFF_MOD + 16384;
constexpr size_t OFF_CB = OFF_MOD + 12288;

constexpr size_t OFF_VG = OFF_SG + 3 * S * 8;
constexpr size_t OFF_XS = OFF_VG + 4 * S * 8;
constexpr size_t OFF_VP = OFF_XS + SZ_DS;
constexpr size_t OFF_WGU = OFF_VP + 8 * 2 * 3584 * 4;
constexpr size_t OFF_WD = OFF_WGU + (size_t)2 * D * F * 2;
constexpr size_t OFF_ACT = OFF_WD + (size_t)D * F * 2;
constexpr size_t WS_END = OFF_ACT + (size_t)S * F * 2;

#define STAMP_DECL
#define STAMP(wsp, kid, i)
#define STAMP_FLUSH(wsp, kid)
#define HWID(wsp, kid)
#define XSTAMP(i)
#define YSTAMP(i) STAMP(ws, 2, i)
#if defined(__HIP_DEVICE_COMPILE__)
#define NOPK __attribute__((target("no-packed-fp32-ops")))
#else
#define NOPK
#endif

template <int I, int N, class Fn>
DI void static_for(Fn&& f) {
  if constexpr (I < N) {
    f(std::integral_constant<int, I>{});
    static_for<I + 1, N>(f);
  }
}

DI float lo_f(u32 x) { return __builtin_bit_cast(float, x << 16); }
DI float hi_f(u32 x) { return __builtin_bit_cast(float, x & 0xffff0000u); }
DI u32 pack_bf16(float a, float b) {
  f32x2 v = {a, b};
  return __builtin_bit_cast(u32, __builtin_convertvector(v, bf16x2_t));
}
DI float round_bf16(float a) { return lo_f(pack_bf16(a, 0.f)); }
DI float bf2f(u16 x) { return __builtin_bit_cast(float, (u32)x << 16); }

template <int N>
DI void wait_vm() { asm volatile("s_waitcnt vmcnt(%0)" ::"n"(N) : "memory"); }
DI void wait_lgkm0() { asm volatile("s_waitcnt lgkmcnt(0)" ::: "memory"); }
DI void bar() { __builtin_amdgcn_s_barrier(); }

#define SNOP "s_nop 0\n\t"
#define SNOP2 ""
DI const void* uni(const void* p) {
  const unsigned long long v = (unsigned long long)p;
  const u32 lo = __builtin_amdgcn_readfirstlane((u32)v), hi = __builtin_amdgcn_readfirstlane((u32)(v >> 32));
  return (const void*)(((unsigned long long)hi << 32) | lo);
}
DI void glds16_asm(const void* sbase, u32 voff, u32 lds_addr) {
  u32 keep;
  sbase = uni(sbase);
  lds_addr = __builtin_amdgcn_readfirstlane(lds_addr);
  asm volatile("s_mov_b32 %0, m0\n\ts_mov_b32 m0, %3\n\t" SNOP "global_load_lds_dwordx4 %1, %2\n\ts_mov_b32 m0, %0"
               : "=&s"(keep)
               : "v"(voff), "s"(sbase), "s"(lds_addr)
               : "memory");
}
DI void glds16(const void* g, char* lds) {
  __builtin_amdgcn_global_load_lds((const __attribute__((address_space(1))) void*)g, (LAS void*)lds, 16, 0, 2);
}

template <int CTRL>
DI float dpp_add(float v) {
  const int x = __builtin_bit_cast(int, v);
  return v + __builtin_bit_cast(float, __builtin_amdgcn_update_dpp(x, x, CTRL, 0xf, 0xf, false));
}
DI float sum8(float v) {
  v = dpp_add<0xB1>(v);
  v = dpp_add<0x4E>(v);
  v = dpp_add<0x141>(v);
  return v;
}
DI float sum16(float v) { return dpp_add<0x140>(sum8(v)); }
DI float dot2(u32 a, u32 b, float c) {
  return __builtin_amdgcn_fdot2_f32_bf16(__builtin_bit_cast(bf16x2_t, a), __builtin_bit_cast(bf16x2_t, b), c, false);
}
typedef __amdgpu_buffer_rsrc_t rsrc_t;
DI rsrc_t mk_rsrc(const void* p, u32 bytes) { return __builtin_amdgcn_make_buffer_rsrc((void*)p, 0, bytes, 0x00020000); }
DI void st16_sc1(void* p, u32x4 v) { asm volatile("global_store_dwordx4 %0, %1, off sc1\n\ts_nop 1" ::"v"(p), "v"(v) : "memory"); }
DI void st8_sc1(void* p, u32x2 v) { asm volatile("global_store_dwordx2 %0, %1, off sc1" ::"v"(p), "v"(v) : "memory"); }
DI void st4_sc1(void* p, float v) { asm volatile("global_store_dword %0, %1, off sc1" ::"v"(p), "v"(v) : "memory"); }

#define EXP_E12 2
#define EXP_E13 6
#define EXP_LOCK 4
#define EXP_NW2 14
DI bf16x8 tr_frag(const char* smem, int o0, int o1) {
  s16x4 x0 = __builtin_amdgcn_ds_read_tr16_b64_v4i16((LAS s16x4*)(smem + o0));
  s16x4 x1 = __builtin_amdgcn_ds_read_tr16_b64_v4i16((LAS s16x4*)(smem + o1));
  return __builtin_shufflevector(x0, x1, 0, 1, 2, 3, 4, 5, 6, 7);
}
DI f32x16 mfma32(u32x4 a, bf16x8 b, f32x16 c) {
  return __builtin_amdgcn_mfma_f32_32x32x16_bf16(__builtin_bit_cast(bf16x8, a), b, c, 0, 0, 0);
}

DI void retile(int task, const u16* __restrict__ w_gate, const u16* __restrict__ w_up, const u16* __restrict__ w_down,
               char* __restrict__ ws, char* smem, int w, int lane) {
  const int xr = lane >> 4, slot = lane & 15;
  const int chn = slot ^ ((xr << 2) | (w & 3));
  const int h = lane >> 5, blk = (lane >> 4) & 1, qq = (lane & 15) >> 2, p = lane & 3;
  const bool gu = task < 112;
  int kq = 0, fg = 0, dg = 0, fk = 0;
  if (gu) {
    fg = task % 28, kq = task / 28;
    const u16* src = (chn < 8 ? w_gate : w_up) + (size_t)(kq * 112 + 4 * w + xr) * F + fg * 64 + (chn & 7) * 8;
#pragma unroll
    for (int i = 0; i < 7; ++i) glds16(src + (size_t)i * 16 * F, smem + (w + 4 * i) * 1024);
  } else {
    dg = (task - 112) % 7, fk = (task - 112) / 7;
    const u16* src = w_down + (size_t)(fk * 224 + (chn >> 3) * 112 + 4 * w + xr) * D + dg * 64 + (chn & 7) * 8;
#pragma unroll
    for (int i = 0; i < 7; ++i) glds16(src + (size_t)i * 16 * D, smem + (w + 4 * i) * 1024);
  }
  const int col = gu ? blk * 128 + w * 32 + (p & 1) * 16 + (p >> 1) * 8 : (w >> 1) * 128 + (w & 1) * 64 + blk * 32 + p * 8;
  int o[2];
#pragma unroll
  for (int t = 0; t < 2; ++t)
    o[t] = 256 * (8 * h + 4 * t + qq) + 16 * ((col >> 4) ^ ((qq << 2) | ((2 * h + t) & 3))) + (col & 8);
  u32x4* dst = gu ? (u32x4*)(ws + OFF_WGU) + ((size_t)(fg * 4 + w) * 28 + kq * 7) * 64 + lane
                  : (u32x4*)(ws + OFF_WD) + ((size_t)(dg * 2 + (w & 1)) * 112 + fk * 14 + (w >> 1) * 7) * 64 + lane;
  u32x4 bv[7];
  if (gu) {
    const u16* cbp = (const u16*)(ws + OFF_CB) + (lane & 1) * D + kq * 112 + 8 * h;
#pragma unroll
    for (int i = 0; i < 7; ++i) bv[i] = *(const u32x4*)(cbp + 16 * i);
  }
  __syncthreads();
  f32x16 acc;
#pragma unroll
  for (int r = 0; r < 16; ++r) acc[r] = 0.f;
#pragma unroll
  for (int i = 0; i < 7; ++i) {
    const bf16x8 f = tr_frag(smem, o[0] + i * 4096, o[1] + i * 4096);
    st16_sc1(dst + i * 64, __builtin_bit_cast(u32x4, f));
    if (gu) acc = mfma32(bv[i], f, acc);
  }
  if (gu && h == 0) ((f32x2*)(ws + OFF_VP))[kq * 3584 + (fg * 4 + w) * 32 + lane] = f32x2{acc[0], acc[1]};
}

#define EXP_T1 7
#define ACTAUX 16
#define NTAUX 2
constexpr int K0_GRID = 336;

extern "C" __global__ __launch_bounds__(256) void k0_prep(const u16* __restrict__ q, const u16* __restrict__ k,
                                               const u16* __restrict__ v, const u16* __restrict__ shift_k,
                                               const u16* __restrict__ c, const u16* __restrict__ w_mod,
                                               const u16* __restrict__ ssm_k, const u16* __restrict__ w_gate,
                                               const u16* __restrict__ w_up, const u16* __restrict__ w_down,
                                               const u16* __restrict__ outp, char* __restrict__ ws) {
  __shared__ float red[4][64];
  const int b = blockIdx.x, tid = threadIdx.x, lane = tid & 63, w = __builtin_amdgcn_readfirstlane(tid >> 6);
  STAMP_DECL;
  STAMP(ws, 0, 0);
  const int xcd = b & 7, idx = b >> 3;
  u32 cwarm = 0;
  if (idx == 40 || idx == 41) {
    u32 lo, hi, len;
    asm volatile(
        "s_getpc_b64 s[40:41]\n\ts_add_u32 s40, s40, k1_conv@rel32@lo+4\n\ts_addc_u32 s41, s41, k1_conv@rel32@hi+12\n\t"
        "s_mov_b32 %0, s40\n\ts_mov_b32 %1, s41\n\ts_mov_b32 %2, k_code_end-k1_conv"
        : "=s"(lo), "=s"(hi), "=s"(len)::"s40", "s41", "scc");
    const char* code = (const char*)(((unsigned long long)hi << 32) | lo);
    if ((u32)((idx - 40) * 256 + tid) * 128 < len) cwarm = *(const u32*)(code + ((idx - 40) * 256 + tid) * 128);
  }
  u32 twarm = 0;
  if (idx == 38 || idx == 39) {
    const int t = (idx - 38) * 256 + tid;
    const char* p = nullptr;
    if (t < 25) p = (const char*)w_gate + t * 65536;
    else if (t < 50) p = (const char*)w_up + (t - 25) * 65536;
    else if (t < 75) p = (const char*)w_down + (t - 50) * 65536;
    else if (t < 103) p = (const char*)outp + (t - 75) * 65536;
    else if ((size_t)(t - 103) * 65536 < WS_END) p = ws + (size_t)(t - 103) * 65536;
    if (p) twarm = __builtin_nontemporal_load((const u32*)p);
  }
  cwarm += twarm;
  int misc = -1;
  if (xcd == 7) misc = idx < 14 ? 64 + idx : (idx < 36 ? idx - 14 : 1000);
  else if (idx < 4) misc = 64 + 14 + xcd * 4 + idx;
  else if (idx >= 36) misc = 22 + xcd * 6 + (idx - 36);
  if (misc < 0) {
    const int rb = idx - 4;
    const int sq = lane >> 5, cp = lane & 31;
    const int d0 = xcd * 64 + cp * 2;
    const int a = rb * 8 + w * 2 + sq;
    const int s0 = a * 8;
    const rsrc_t rk = mk_rsrc(k, S * D * 2), rv = mk_rsrc(v, S * D * 2), rq = mk_rsrc(q, S * D * 2);
    const int oB = (s0 * D + d0) * 2;
    const int oA = oB - 4 * D * 2, oC = oB + 4 * D * 2;
    u32 kk[12], vv[9], qq[8];
#pragma unroll
    for (int i = 0; i < 4; ++i) kk[i] = __builtin_amdgcn_raw_buffer_load_b32(rk, oA + i * D * 2, 0, NTAUX);
#pragma unroll
    for (int i = 0; i < 4; ++i) kk[4 + i] = __builtin_amdgcn_raw_buffer_load_b32(rk, oB + i * D * 2, 0, NTAUX);
#pragma unroll
    for (int i = 0; i < 4; ++i) kk[8 + i] = __builtin_amdgcn_raw_buffer_load_b32(rk, oC + i * D * 2, 0, NTAUX);
    vv[0] = __builtin_amdgcn_raw_buffer_load_b32(rv, oA + 3 * D * 2, 0, NTAUX);
#pragma unroll
    for (int i = 0; i < 4; ++i) vv[1 + i] = __builtin_amdgcn_raw_buffer_load_b32(rv, oB + i * D * 2, 0, NTAUX);
#pragma unroll
    for (int i = 0; i < 4; ++i) vv[5 + i] = __builtin_amdgcn_raw_buffer_load_b32(rv, oC + i * D * 2, 0, NTAUX);
#pragma unroll
    for (int i = 0; i < 4; ++i) qq[i] = __builtin_amdgcn_raw_buffer_load_b32(rq, oB + i * D * 2, 0, NTAUX);
#pragma unroll
    for (int i = 0; i < 4; ++i) qq[4 + i] = __builtin_amdgcn_raw_buffer_load_b32(rq, oC + i * D * 2, 0, NTAUX);
    const u32x4 sk = *(const u32x4*)(shift_k + d0 * 4);
    u32 warm = 0;
    if (w == 0) warm = __builtin_nontemporal_load((const u32*)((const char*)ssm_k + ((size_t)(xcd * 32 + rb) * 64 + lane) * 128));
    f32x2 wt[4] = {{lo_f(sk[0]), lo_f(sk[2])}, {hi_f(sk[0]), hi_f(sk[2])}, {lo_f(sk[1]), lo_f(sk[3])}, {hi_f(sk[1]), hi_f(sk[3])}};
    f32x2 kf[12], u[9];
#pragma unroll
    for (int i = 0; i < 12; ++i) kf[i] = f32x2{lo_f(kk[i]), hi_f(kk[i])};
#pragma unroll
    for (int i = 0; i < 9; ++i) {
      f32x2 t = kf[i + 3] * wt[0];
      t = __builtin_elementwise_fma(kf[i + 2], wt[1], t);
      t = __builtin_elementwise_fma(kf[i + 1], wt[2], t);
      t = __builtin_elementwise_fma(kf[i], wt[3], t);
      u[i] = t * f32x2{lo_f(vv[i]), hi_f(vv[i])};
    }
    {
      char* dst = ws + (size_t)d0 * (S * 2) + (2040 - s0) * 2;
#pragma unroll
      for (int cI = 0; cI < 2; ++cI) {
        *(u32x4*)(dst + OFF_RO + cI * S * 2) = u32x4{pack_bf16(u[8][cI], u[7][cI]), pack_bf16(u[6][cI], u[5][cI]),
                                                     pack_bf16(u[4][cI], u[3][cI]), pack_bf16(u[2][cI], u[1][cI])};
        *(u32x4*)(dst + OFF_RE + cI * S * 2) = u32x4{pack_bf16(u[7][cI], u[6][cI]), pack_bf16(u[5][cI], u[4][cI]),
                                                     pack_bf16(u[3][cI], u[2][cI]), pack_bf16(u[1][cI], u[0][cI])};
      }
      char* dq = ws + OFF_QT + (size_t)d0 * (S * 2) + s0 * 2;
#pragma unroll
      for (int cI = 0; cI < 2; ++cI) {
        const u32 sel = cI ? 0x07060302u : 0x05040100u;
        *(u32x4*)(dq + cI * S * 2) = u32x4{__builtin_amdgcn_perm(qq[2], qq[0], sel), __builtin_amdgcn_perm(qq[6], qq[4], sel),
                                           __builtin_amdgcn_perm(qq[3], qq[1], sel), __builtin_amdgcn_perm(qq[7], qq[5], sel)};
      }
    }
    asm volatile("" ::"v"(warm));
  } else if (misc < 64) {
    const int bb = misc;
    const int row = bb * 32 + w * 8 + (lane >> 3);
    const int sub = lane & 7;
    u32x4 x[7];
#pragma unroll
    for (int i = 0; i < 7; ++i) x[i] = __builtin_nontemporal_load((const u32x4*)(q + (size_t)row * D + (i * 8 + sub) * 8));
    if (sub < 3) ((unsigned long long*)(ws + OFF_SG))[sub * S + row] = 0ull;
    float s = 0.f, s2 = 0.f;
#pragma unroll
    for (int i = 0; i < 7; ++i)
#pragma unroll
      for (int j = 0; j < 4; ++j) {
        const u32 t = x[i][j];
        s = dot2(t, 0x3f803f80u, s);
        s2 = dot2(t, t, s2);
      }
    s = sum8(s);
    s2 = sum8(s2);
    const float mean = s * (1.f / D);
    float var = s2 * (1.f / D) - mean * mean;
    var = var < 0.f ? 0.f : var;
    const float rstd = __builtin_amdgcn_rsqf(var + 1e-5f);
    if (sub < 2) {
      const int p = (row & ~7) | ((row & 1) << 2) | ((row >> 1) & 3);
      ((float*)(ws + OFF_MEAN))[sub * S + p] = sub ? rstd : mean;
    }
  } else if (misc < 106) {
    const int bb = misc - 64;
    const int co = lane >> 3, rs = lane & 7;
    const int col0 = bb * 64 + co * 8;
    u32x4 x[14];
#pragma unroll
    for (int i = 0; i < 14; ++i)
      x[i] = __builtin_nontemporal_load((const u32x4*)(w_mod + (size_t)(w * 112 + rs * 14 + i) * (6 * D) + col0));
    u32 cv[7];
#pragma unroll
    for (int i = 0; i < 7; ++i) cv[i] = *(const u32*)(c + w * 112 + rs * 14 + 2 * i);
    __builtin_amdgcn_sched_barrier(0);
    f32x2 acc[4] = {{0.f, 0.f}, {0.f, 0.f}, {0.f, 0.f}, {0.f, 0.f}};
#pragma unroll
    for (int i = 0; i < 14; ++i) {
      const float cf = (i & 1) ? hi_f(cv[i >> 1]) : lo_f(cv[i >> 1]);
      const float sl = round_bf16(cf * __builtin_amdgcn_rcpf(1.f + __builtin_amdgcn_exp2f(cf * -1.44269504f)));
#pragma unroll
      for (int j = 0; j < 4; ++j)
        acc[j] = __builtin_elementwise_fma(f32x2{lo_f(x[i][j]), hi_f(x[i][j])}, f32x2{sl, sl}, acc[j]);
    }
    float sel = 0.f;
#pragma unroll
    for (int j = 0; j < 8; ++j) {
      const float t = sum8(acc[j >> 1][j & 1]);
      sel = (rs == j) ? t : sel;
    }
    red[w][lane] = sel;
    __syncthreads();
    if (tid < 64) {
      const float t = (red[0][tid] + red[1][tid]) + (red[2][tid] + red[3][tid]);
      const float m = round_bf16(t);
      const int col = bb * 64 + tid;
      ((float*)(ws + OFF_MOD))[col] = m;
      if (col >= 3 * D && col < 5 * D) {
        const bool isc = col >= 4 * D;
        ((u16*)(ws + OFF_CB))[isc ? col - 4 * D : col - 2 * D] = (u16)pack_bf16(isc ? 1.f + m : m, 0.f);
      }
    }
  }
  asm volatile("" ::"v"(cwarm));
  STAMP(ws, 0, 1);
  STAMP_FLUSH(ws, misc < 0 ? 0 : (misc < 64 ? 4 : 5));
}

constexpr int K1_KSTR = 260 * 16;
constexpr int K1_USTR = 286 * 16;
constexpr int K1_UOFF = 4 * K1_KSTR;
constexpr int K1_XOFF = K1_UOFF + 2 * K1_USTR;
constexpr int K1_CH = K1_XOFF + 8192;
constexpr int K1_LDS = 2 * K1_CH;

DI void glds16x4_asm(const void* sbase, u32 voff, u32 voff_last, u32 lds_addr) {
  u32 keep;
  sbase = uni(sbase);
  lds_addr = __builtin_amdgcn_readfirstlane(lds_addr);
  asm volatile(
      "s_mov_b32 %0, m0\n\ts_mov_b32 m0, %4\n\t" SNOP
      "global_load_lds_dwordx4 %1, %3\n\t"
      "global_load_lds_dwordx4 %1, %3 offset:1024\n\t"
      "global_load_lds_dwordx4 %1, %3 offset:2048\n\t"
      "global_load_lds_dwordx4 %2, %3 offset:3072\n\t"
      "s_mov_b32 m0, %0"
      : "=&s"(keep)
      : "v"(voff), "v"(voff_last), "s"(sbase), "s"(lds_addr)
      : "memory");
}
#define CAS __attribute__((address_space(4)))
template <int half>
DI void k1_conv_wave(const u16* __restrict__ ssm_k, const u16* __restrict__ ssm_D, char* __restrict__ ws, char* smem,
                     int ch, int lane) {
  STAMP_DECL;
  STAMP(ws, 1, 0);
  const int cpair = (blockIdx.x & 7) * 32 + (blockIdx.x >> 3);
  const int d = 2 * cpair + ch;
  char* L = smem + ch * K1_CH;
  const u32 L0 = (u32)(size_t)(LAS char*)L;
  const u16* kf = ssm_k + (size_t)d * S;
  {
    const u32 vo = lane * 16, vl = (lane > 62 ? 62 : lane) * 16;
    glds16x4_asm(kf + (2 + 4 * half), vo, vl, L0 + (2 * half) * K1_KSTR + 64);
    glds16x4_asm(kf + (4 + 4 * half), vo, vl, L0 + (2 * half + 1) * K1_KSTR + 64);
    glds16x4_asm(ws + (half ? OFF_RO : OFF_RE) + (size_t)d * S * 2, vo, vo, L0 + K1_UOFF + half * K1_USTR);
  }
  const int n = lane & 15, q4 = lane >> 4;
  const int pbase = 32 * (n >> 1) + 8 * q4 + 4 * (n & 1) + 1024 * half;
  u32 head = 0;
  const int di = n + q4 - 15;
  if constexpr (half == 0) {
    asm volatile(SNOP2 "global_load_dword %0, %1, %2" : "=v"(head) : "v"((u32)(di < 0 ? 0 : di) * 4), "s"(uni(kf)) : "memory");
  }
  const CAS float* MODc = (const CAS float*)(size_t)(ws + OFF_MOD);
  const float sa = MODc[d], ca = MODc[D + d], ga = MODc[2 * D + d], cbv = MODc[4 * D + d];
  const u32 Draw = ((const CAS u32*)(size_t)ssm_D)[cpair];
  if constexpr (half == 1) {
    const int l5 = lane & 31;
    if (l5 < 28) *(LAS u32x4*)(L + K1_UOFF + (lane >> 5) * K1_USTR + (256 + l5) * 16) = u32x4{0u, 0u, 0u, 0u};
  }
  u32 Ab = L0 + (n & 3) * K1_KSTR + ((n >> 2) + q4 + 16 * half) * 16;
  u32 Bb = L0 + K1_UOFF + (n & 1) * K1_USTR + (28 - 4 * (n >> 1) + q4 + 16 * half) * 16;
  u32 Hd = L0 + q4 * K1_KSTR + n * 4;
  asm volatile("" : "+v"(Ab), "+v"(Bb), "+v"(Hd) : "s"(sa), "s"(ca), "s"(ga), "s"(Draw), "s"(cbv));
  asm volatile("s_waitcnt vmcnt(0)" : "+v"(head)::"memory");
  if constexpr (half == 0) *(LAS u32*)(size_t)Hd = di < 0 ? 0u : head;
  wait_lgkm0();
  STAMP(ws, 1, 1);
  bar();
  STAMP(ws, 1, 2);
  bf16x8 fa[2][8], fb[2][8];
  auto rd = [&](auto dI) {
    constexpr int dl = decltype(dI)::value;
#pragma unroll
    for (int g = 0; g < 8; ++g) {
      fa[dl & 1][g] = *(const LAS bf16x8*)(size_t)(Ab + 64 * dl + 512 * g);
      fb[dl & 1][g] = *(const LAS bf16x8*)(size_t)(Bb + 64 * dl + 512 * (7 - g));
    }
  };
  rd(std::integral_constant<int, 0>{});
  __builtin_amdgcn_sched_barrier(0);
  u32x2 qv[4];
  f32x4 mu[4], rs[4];
  asm volatile(SNOP2
               "global_load_dwordx2 %0, %12, %14\n\t"
               "global_load_dwordx2 %1, %12, %14 offset:512\n\t"
               "global_load_dwordx2 %2, %12, %14 offset:1024\n\t"
               "global_load_dwordx2 %3, %12, %14 offset:1536\n\t"
               "global_load_dwordx4 %4, %13, %15\n\t"
               "global_load_dwordx4 %5, %13, %15 offset:1024\n\t"
               "global_load_dwordx4 %6, %13, %15 offset:2048\n\t"
               "global_load_dwordx4 %7, %13, %15 offset:3072\n\t"
               "global_load_dwordx4 %8, %13, %16\n\t"
               "global_load_dwordx4 %9, %13, %16 offset:1024\n\t"
               "global_load_dwordx4 %10, %13, %16 offset:2048\n\t"
               "global_load_dwordx4 %11, %13, %16 offset:3072"
               : "=&v"(qv[0]), "=&v"(qv[1]), "=&v"(qv[2]), "=&v"(qv[3]), "=&v"(mu[0]), "=&v"(mu[1]), "=&v"(mu[2]),
                 "=&v"(mu[3]), "=&v"(rs[0]), "=&v"(rs[1]), "=&v"(rs[2]), "=&v"(rs[3])
               : "v"((u32)pbase * 2), "v"((u32)pbase * 4), "s"(uni(ws + OFF_QT + (size_t)d * S * 2)),
                 "s"(uni(ws + OFF_MEAN)), "s"(uni(ws + OFF_RSTD))
               : "memory");
  f32x4 acc[8];
#pragma unroll
  for (int j = 0; j < 8; ++j) acc[j] = f32x4{0.f, 0.f, 0.f, 0.f};
  f32x4 qf[4], hg[4], kd[4];
  u32x4 uc[4];
  static_for<0, 4>([&](auto dI) {
    constexpr int dl = decltype(dI)::value;
    if constexpr (dl < 3) rd(std::integral_constant<int, dl + 1>{});
    if constexpr (dl == 2) {
      asm volatile("s_waitcnt vmcnt(0)"
                   : "+v"(qv[0]), "+v"(qv[1]), "+v"(qv[2]), "+v"(qv[3]), "+v"(mu[0]), "+v"(mu[1]), "+v"(mu[2]), "+v"(mu[3]),
                     "+v"(rs[0]), "+v"(rs[1]), "+v"(rs[2]), "+v"(rs[3]));
    }
    if constexpr (dl == 1) {
#pragma unroll
      for (int j = 0; j < 4; ++j)
        uc[j] = *(const LAS u32x4*)(L + K1_UOFF + K1_USTR + (255 - (32 * (4 * half + j) + 4 * (n >> 1) + q4)) * 16);
    }
    if constexpr (dl >= 2) {
      const int sh = (n & 1) ? 16 : 0;
      const float gc = ga * round_bf16(1.f + ca), gs = ga * sa, Dd = ch ? hi_f(Draw) : lo_f(Draw);
#pragma unroll
      for (int j = 2 * (dl - 2); j < 2 * (dl - 2) + 2; ++j) {
#pragma unroll
        for (int r = 0; r < 4; ++r) {
          kd[j][r] = __builtin_bit_cast(float, (uc[j][3 - r] << sh) & 0xffff0000u) * Dd;
          qf[j][r] = (r & 1) ? hi_f(qv[j][r >> 1]) : lo_f(qv[j][r >> 1]);
          hg[j][r] = (qf[j][r] - mu[j][r]) * rs[j][r] * gc + gs;
        }
        asm volatile("" : "+v"(kd[j]), "+v"(qf[j]), "+v"(hg[j]));
      }
    }
#pragma unroll
    for (int sdiag = 0; sdiag < 8; ++sdiag)
#pragma unroll
      for (int g = 0; g <= sdiag; ++g)
        acc[sdiag] = __builtin_amdgcn_mfma_f32_16x16x32_bf16(fa[dl & 1][g], fb[dl & 1][sdiag - g], acc[sdiag], 0, 0, 0);
    if constexpr (dl == 0) {
#pragma unroll
      for (int i = 0; i < 16; ++i) {
        __builtin_amdgcn_sched_group_barrier(0x008, 2, 0);
        __builtin_amdgcn_sched_group_barrier(0x100, 1, 0);
      }
    } else if constexpr (dl == 1) {
#pragma unroll
      for (int i = 0; i < 16; ++i) {
        __builtin_amdgcn_sched_group_barrier(0x008, 2, 0);
        __builtin_amdgcn_sched_group_barrier(0x100, 1, 0);
      }
#pragma unroll
      for (int i = 0; i < 4; ++i) {
        __builtin_amdgcn_sched_group_barrier(0x008, 1, 0);
        __builtin_amdgcn_sched_group_barrier(0x100, 1, 0);
      }
    } else {
      __builtin_amdgcn_sched_group_barrier(0x008, 4, 0);
#pragma unroll
      for (int i = 0; i < 16; ++i) {
        __builtin_amdgcn_sched_group_barrier(0x008, 1, 0);
        __builtin_amdgcn_sched_group_barrier(0x002, 2, 0);
        __builtin_amdgcn_sched_group_barrier(0x008, 1, 0);
        __builtin_amdgcn_sched_group_barrier(0x002, 2, 0);
        __builtin_amdgcn_sched_group_barrier(0x100, 1, 0);
      }
    }
    __builtin_amdgcn_sched_barrier(0);
  });
  STAMP(ws, 1, 3);
  LAS f32x4* X = (LAS f32x4*)(L + K1_XOFF);
#pragma unroll
  for (int j = 0; j < 4; ++j) X[half * 256 + j * 64 + lane] = acc[4 * (1 - half) + j];
  wait_lgkm0();
  bar();
  STAMP(ws, 1, 4);
  u16* X1T = (u16*)(ws + OFF_X1T) + (size_t)d * S + pbase;
  u16* XS = (u16*)(ws + OFF_XS) + (size_t)d * S + pbase;
  const float c1 = round_bf16(1.f + cbv);
#pragma unroll
  for (int j = 0; j < 4; ++j) {
    const f32x4 y = X[(1 - half) * 256 + j * 64 + lane];
    const f32x4 o = qf[j] + hg[j] * ((acc[4 * half + j] + kd[j]) + y);
    const u32 p0 = pack_bf16(o[0], o[1]), p1 = pack_bf16(o[2], o[3]);
    st8_sc1(XS + 256 * j, u32x2{pack_bf16(lo_f(p0) * c1, hi_f(p0) * c1), pack_bf16(lo_f(p1) * c1, hi_f(p1) * c1)});
    st8_sc1(X1T + 256 * j, u32x2{p0, p1});
  }
  STAMP(ws, 1, 5);
  STAMP_FLUSH(ws, 1);
}

#define EXP_DELAY 40
constexpr int K1_GRID = 456;
extern "C" __global__ NOPK __launch_bounds__(256, 2) void k1_conv(const u16* __restrict__ ssm_k,
                                                                  const u16* __restrict__ ssm_D,
                                                                  const u16* __restrict__ w_gate,
                                                                  const u16* __restrict__ w_up,
                                                                  const u16* __restrict__ w_down, char* __restrict__ ws) {
  extern __shared__ __attribute__((aligned(16))) char smem[];
  const int tid = threadIdx.x, lane = tid & 63;
  const int w = __builtin_amdgcn_readfirstlane(tid >> 6);
  const int xcd = blockIdx.x & 7, idx = blockIdx.x >> 3;
  if (xcd < 7 && idx < 32) {
    if (w & 1) k1_conv_wave<1>(ssm_k, ssm_D, ws, smem, w >> 1, lane);
    else k1_conv_wave<0>(ssm_k, ssm_D, ws, smem, w >> 1, lane);
  } else {
    STAMP_DECL;
    STAMP(ws, 7, 0);
    if (xcd == 7 && idx >= 42 && idx < 50) return;
    if (xcd < 7) __builtin_amdgcn_s_sleep(EXP_DELAY);
    if (idx >= 50) {
      u32x4* ap = (u32x4*)(ws + OFF_ACT) + (size_t)(blockIdx.x - 400) * 8192 + tid;
      const u32x4 sent = {~0u, ~0u, ~0u, ~0u};
#pragma unroll 8
      for (int i = 0; i < 32; ++i) st16_sc1(ap + i * 256, sent);
      wait_vm<0>();
    } else {
      retile(xcd == 7 ? idx : 42 + xcd * 18 + (idx - 32), w_gate, w_up, w_down, ws, smem, w, lane);
    }
    STAMP(ws, 7, 1);
    STAMP_FLUSH(ws, 7);
  }
}

constexpr int K2_XB = D * 256;
constexpr int K2_ST = K2_XB;
constexpr int K2_LDS = 131072;

DI void gload16_asm(u32x4& dst, const void* sbase, u32 voff) {
  sbase = uni(sbase);
  asm volatile(SNOP2 "global_load_dwordx4 %0, %1, %2" : "=v"(dst) : "v"(voff), "s"(sbase) : "memory");
}
DI u32 umin32(u32 v) { return v < 0xFFFFFFFEu ? v : 0xFFFFFFFEu; }
DI u32 umax3(u32 a, u32 b, u32 c) { const u32 t = a > b ? a : b; return t > c ? t : c; }
template <int OFF>
DI void gload16_off(u32x4& dst, const void* sbase, u32 voff) {
  sbase = uni(sbase);
  asm volatile(SNOP2 "global_load_dwordx4 %0, %1, %2 offset:%3" : "=v"(dst) : "v"(voff), "s"(sbase), "n"(OFF) : "memory");
}
DI void glds16_nom0(const void* sbase, u32 voff, u32 lds_addr) {
  sbase = uni(sbase);
  lds_addr = __builtin_amdgcn_readfirstlane(lds_addr);
  asm volatile("s_mov_b32 m0, %2\n\t" SNOP "global_load_lds_dwordx4 %0, %1" ::"v"(voff), "s"(sbase), "s"(lds_addr) : "memory");
}
template <int N>
DI void wait_vm_pin(u32x4& a, u32x4& b) {
  asm volatile("s_waitcnt vmcnt(%2)" : "+v"(a), "+v"(b) : "n"(N) : "memory");
}
DI float xor32(float v) {
  const u32 u = __builtin_bit_cast(u32, v);
  auto r = __builtin_amdgcn_permlane32_swap(u, u, false, false);
  const u32 a = r[0], b = r[1];
  return __builtin_bit_cast(float, (threadIdx.x & 32) ? a : b);
}

typedef unsigned long long u64;
typedef __attribute__((address_space(1))) u64 gu64;
typedef __attribute__((address_space(1))) u32 gu32;
#define RLX_AGENT __ATOMIC_RELAXED, __HIP_MEMORY_SCOPE_AGENT
#define EXP_AHEAD 2
constexpr int K2_AHEAD = EXP_AHEAD;

DI void k2_stats(char* __restrict__ ws, char* smem) {
  const int tid = threadIdx.x, lane = tid & 63;
  const int w = __builtin_amdgcn_readfirstlane(tid >> 6);
  const int n = lane & 31, h = lane >> 5;
  STAMP_DECL;
  STAMP(ws, 2, 0);
  {
    const int p0 = blockIdx.x * 64;
    const u32* src = (const u32*)(ws + OFF_X1T) + ((size_t)(w * 56 + h) * S + p0) / 2 + n;
    u32 x[28];
#pragma unroll
    for (int i = 0; i < 28; ++i) x[i] = src[(size_t)i * S];
    float s0 = 0.f, s1 = 0.f, q0 = 0.f, q1 = 0.f;
#pragma unroll
    for (int i = 0; i < 28; ++i) {
      const float a = lo_f(x[i]), b = hi_f(x[i]);
      s0 += a;
      s1 += b;
      q0 = __builtin_fmaf(a, a, q0);
      q1 = __builtin_fmaf(b, b, q1);
    }
    *(LAS f32x4*)(smem + ((w * 2 + h) * 32 + n) * 16) = f32x4{s0, q0, s1, q1};
    __syncthreads();
    if (tid < 64) {
      float sm = 0.f, sq = 0.f;
#pragma unroll
      for (int i = 0; i < 16; ++i) {
        const f32x2 t = *(const LAS f32x2*)(smem + (i * 64 + tid) * 8);
        sm += t[0];
        sq += t[1];
      }
      const float mean = sm * (1.f / D);
      float var = sq * (1.f / D) - mean * mean;
      var = (var < 0.f ? 0.f : var) + 1e-5f;
      const float rstd = 1.f / sqrtf(var);
      const float sig = var * rstd;
      const float mh = round_bf16(mean), sh = round_bf16(sig);
      const u32 b0 = pack_bf16(-mh, mh - mean), b1 = pack_bf16(-mh, sh), b2 = pack_bf16(sig - sh, sh);
      const float c1 = -2.302208198f * rstd;
      const float c2 = -0.1029432397f * (rstd * rstd * rstd);
      gu64* G = (gu64*)(size_t)(ws + OFF_SG) + p0 + tid;
      __hip_atomic_store(G, ((u64)b1 << 32) | b0, RLX_AGENT);
      __hip_atomic_store(G + S, ((u64)__builtin_bit_cast(u32, var) << 32) | b2, RLX_AGENT);
      __hip_atomic_store(G + 2 * S, ((u64)__builtin_bit_cast(u32, c1) << 32) | __builtin_bit_cast(u32, c2), RLX_AGENT);
    }
    STAMP(ws, 6, 1);
    STAMP(ws, 6, 2);
    STAMP_FLUSH(ws, 6);
  }
}

DI void k2_gemm(char* __restrict__ ws, u16* __restrict__ out, char* smem, const int w) {
  const int tid = threadIdx.x, lane = tid & 63;
  const int n = lane & 31, h = lane >> 5;
  STAMP_DECL;
  STAMP(ws, 2, 0);
  const int bid = blockIdx.x - 32;
  const int xcd = bid & 7, idx = bid >> 3;
  const int mt = (xcd >> 1) * 4 + (idx & 3);
  const int dt = idx >> 2, sh2 = xcd & 1;
  const int ft = sh2 * 7 + dt;
  const int tile = ft * 8 + w;
  const u32 lds0 = (u32)(size_t)(LAS char*)smem;
  u32x4 wf[28];
  const int xr = lane >> 4, slot = lane & 15;
  const char* xbase = ws + OFF_XS + ((size_t)(4 * w) * S + mt * 128) * 2;
  const u32 xoff = (u32)(xr * S + (slot ^ ((xr << 2) | (w & 3))) * 8) * 2;
  const char* wbase = ws + OFF_WGU + (size_t)(tile * 28) * 1024 + 4096;
  const u32 wa = lane * 16;
  auto issue1 = [&](auto qI) {
    constexpr int q = decltype(qI)::value, j = q / 3, r = q % 3;
    if constexpr (r == 0) glds16_nom0(xbase, xoff + j * (32 * S * 2), lds0 + (32 * j + 4 * w) * 256);
    if constexpr (r != 0) {
      constexpr int k = 2 * j + r - 1;
      gload16_off<(k % 8) * 1024 - 4096>(wf[k], wbase + (k / 8) * 8192, wa);
    }
  };
  static_for<0, 3>(issue1);
  static_for<3, 3 * (K2_AHEAD + 1)>(issue1);
  constexpr int NTL[4] = {0, 2, 1, 3};
  int xo[4][2];
  {
    const int blk = (lane >> 4) & 1, qq = (lane & 15) >> 2, p = lane & 3;
#pragma unroll
    for (int i = 0; i < 4; ++i) {
#pragma unroll
      for (int t = 0; t < 2; ++t) {
        const int row = 8 * h + 4 * t + qq;
        const int chn = 4 * NTL[i] + 2 * blk + (p >> 1);
        xo[i][t] = 256 * row + 16 * (chn ^ ((qq << 2) | ((2 * h + t) & 3))) + 8 * (p & 1);
      }
    }
  }
  f32x16 acc[4];
#pragma unroll
  for (int i = 0; i < 4; ++i)
#pragma unroll
    for (int r = 0; r < 16; ++r) acc[i][r] = 0.f;
  bf16x8 xa[2][3], xb[2][3];
  asm volatile("" : "+v"(xo[0][0]), "+v"(xo[0][1]), "+v"(xo[1][0]), "+v"(xo[1][1]), "+v"(xo[2][0]), "+v"(xo[2][1]),
               "+v"(xo[3][0]), "+v"(xo[3][1]));
  wait_vm<3 * K2_AHEAD + 2>();
  __builtin_amdgcn_sched_barrier(0);
  bar();
  YSTAMP(1);
#pragma unroll
  for (int ks = 0; ks < 2; ++ks)
#pragma unroll
    for (int i = 0; i < 3; ++i) xa[ks][i] = tr_frag(smem, xo[i][0] + ks * 4096, xo[i][1] + ks * 4096);
  __builtin_amdgcn_sched_barrier(0);
  u32x4 aext;
  auto mk_aext = [&](f32x2 p0, f32x2 p1, f32x2 p2, f32x2 p3) {
    const float v1 = (p0[0] + p1[0]) + (p2[0] + p3[0]);
    const float v2 = (p0[1] + p1[1]) + (p2[1] + p3[1]);
    const float v1h = round_bf16(v1), v2h = round_bf16(v2);
    u32x4 a;
    a[0] = pack_bf16(v1h, v1h);
    a[1] = pack_bf16(v1 - v1h, v2h);
    a[2] = pack_bf16(v2h, v2 - v2h);
    a[3] = 0u;
    if (h) a = u32x4{0u, 0u, 0u, 0u};
    asm volatile("" : "+v"(a));
    aext = a;
  };
  const char* vbase = ws + OFF_VP + (size_t)tile * 256;
  const int gsl = w < 6 ? w : w - 6;
  const char* gbase = ws + OFF_SG + (size_t)((gsl >> 1) * S + mt * 128 + (gsl & 1) * 64) * 8;
  LAS u64* CS = (LAS u64*)(smem + K2_ST);
  u64 gv;
  f32x2 gp[4];
  constexpr int JG = 9;
  auto pin = [](f32x16& a) { asm volatile("" : "+v"(a)); };
  u32x4 wd[28];
  const char* wdbase = ws + OFF_WD + ((size_t)(2 * dt) * 112 + 14 * w) * 1024 + 4096;
  auto ldwd = [&](auto kI) {
    constexpr int k = decltype(kI)::value, kk = k % 14;
    gload16_off<(kk % 8) * 1024 - 4096>(wd[k], wdbase + (k / 14) * 112 * 1024 + (kk / 8) * 8192, wa);
  };
  constexpr int E12 = EXP_E12, E13 = EXP_E13;
  auto chunk = [&](auto jI, const bf16x8(&cur)[2][3], bf16x8(&nxt)[2][3]) {
    constexpr int j = decltype(jI)::value;
    constexpr int last = (j + K2_AHEAD < 13) ? j + K2_AHEAD : 13;
    constexpr int ex = (j > JG && j + 1 <= JG + K2_AHEAD) ? 5 : 0;
    if constexpr (j < 13) {
      wait_vm_pin<3 * (last - (j + 1)) + 2 + ex>(wf[2 * j], wf[2 * j + 1]);
      if constexpr (j == JG + K2_AHEAD + 1) wait_lgkm0();
      __builtin_amdgcn_sched_barrier(0);
      bar();
    } else {
      wait_vm_pin<E12>(wf[2 * j], wf[2 * j + 1]);
    }
    __builtin_amdgcn_sched_barrier(0);
    static_for<0, 6>([&](auto iI) {
      constexpr int sl = decltype(iI)::value, ks = sl / 3, i = sl % 3;
      acc[i] = mfma32(wf[2 * j + ks], cur[ks][i], acc[i]);
      pin(acc[i]);
      __builtin_amdgcn_sched_barrier(0);
      if constexpr (j == 6 && ks == 0) {
        int a0 = xo[i][0] + 7 * 8192, a1 = xo[i][1] + 7 * 8192;
        asm volatile("" : "+v"(a0), "+v"(a1));
        xo[i][0] = a0;
        xo[i][1] = a1;
      }
      if constexpr (j < 13)
        nxt[ks][i] = tr_frag(smem, xo[i][0] + ks * 4096 + ((j + 1) % 7) * 8192, xo[i][1] + ks * 4096 + ((j + 1) % 7) * 8192);
      if constexpr (j == JG && sl == 0) {
        u64 t;
        asm volatile(SNOP2 "global_load_dwordx2 %0, %1, %2 sc1" : "=v"(t) : "v"((u32)lane * 8), "s"(uni(gbase)) : "memory");
        gv = t;
        static_for<0, 4>([&](auto qI) {
          f32x2 tp;
          asm volatile(SNOP2 "global_load_dwordx2 %0, %1, %2"
                       : "=v"(tp)
                       : "v"((u32)n * 8), "s"(uni(vbase + decltype(qI)::value * 3584 * 8))
                       : "memory");
          gp[decltype(qI)::value] = tp;
        });
      }
      if constexpr ((sl & 1) == 0 && j + K2_AHEAD + 1 <= 13)
        issue1(std::integral_constant<int, 3 * (j + K2_AHEAD + 1) + sl / 2>{});
      if constexpr (j == 12 && sl < E12) ldwd(std::integral_constant<int, sl>{});
      if constexpr (j == 13 && sl < E13) ldwd(std::integral_constant<int, E12 + sl>{});
      if constexpr (j == JG + K2_AHEAD && sl == 3) {
        u64 t = gv;
        f32x2 p0 = gp[0], p1 = gp[1], p2 = gp[2], p3 = gp[3];
        asm volatile("" : "+v"(t), "+v"(p0), "+v"(p1), "+v"(p2), "+v"(p3));
        mk_aext(p0, p1, p2, p3);
        if (!__all((t >> 32) != 0)) {
          for (int spin = 0; spin < (1 << 16); ++spin) {
            __builtin_amdgcn_s_sleep(2);
            t = __hip_atomic_load((gu64*)(size_t)gbase + lane, RLX_AGENT);
            if (__all((t >> 32) != 0)) break;
          }
          __builtin_amdgcn_s_waitcnt(0x0F70);
        }
        CS[gsl * 64 + lane] = t;
      }
      __builtin_amdgcn_sched_barrier(0);
    });
  };
  static_for<0, 14>([&](auto jI) {
    if constexpr (decltype(jI)::value & 1) chunk(jI, xb, xa);
    else chunk(jI, xa, xb);
  });
  YSTAMP(2);
  const rsrc_t rw = mk_rsrc(ws + OFF_ACT + ((size_t)(mt * 4) * 112 + tile) * 1024, 337 * 1024);
  constexpr int PF = 4;
  bf16x8 xr3[PF];
  auto rd3 = [&](auto kI) {
    constexpr int k = decltype(kI)::value;
    if constexpr (k == 14) {
      int a0 = xo[3][0] + 14 * 4096, a1 = xo[3][1] + 14 * 4096;
      asm volatile("" : "+v"(a0), "+v"(a1));
      xo[3][0] = a0;
      xo[3][1] = a1;
    }
    xr3[k % PF] = tr_frag(smem, xo[3][0] + (k % 14) * 4096, xo[3][1] + (k % 14) * 4096);
  };
  u64 g[3];
  auto ldg = [&](auto iI) {
#pragma unroll
    for (int k = 0; k < 3; ++k) g[k] = CS[k * 128 + NTL[decltype(iI)::value] * 32 + n];
  };
  float c1, c2, var;
  u32x4 ov;
  auto corr = [&](auto iI) {
    constexpr int i = decltype(iI)::value;
    const u32x4 bext = {(u32)g[0], (u32)(g[0] >> 32), (u32)g[1], 0u};
    acc[i] = mfma32(aext, __builtin_bit_cast(bf16x8, bext), acc[i]);
    c1 = __builtin_bit_cast(float, (u32)(g[2] >> 32));
    c2 = __builtin_bit_cast(float, (u32)g[2]);
    var = __builtin_bit_cast(float, (u32)(g[1] >> 32));
  };
  auto half_epi = [&](auto iI, auto hI) {
    constexpr int i = decltype(iI)::value, r0 = 4 * decltype(hI)::value;
    float e[4], m[4];
#pragma unroll
    for (int r = 0; r < 4; ++r) {
      const float a = acc[i][r0 + r];
      e[r] = __builtin_amdgcn_exp2f(__builtin_fmaf(a * a, c2, c1) * a);
      m[r] = a * acc[i][r0 + r + 8];
    }
#pragma unroll
    for (int r = 0; r < 4; ++r) m[r] *= __builtin_amdgcn_rcpf(__builtin_fmaf(e[r], var, var));
    ov[r0 / 2] = umin32(pack_bf16(m[0], m[1]));
    ov[r0 / 2 + 1] = umin32(pack_bf16(m[2], m[3]));
  };
  auto put = [&](auto iI) {
    __builtin_amdgcn_raw_buffer_store_b128(ov, rw, lane * 16, NTL[decltype(iI)::value] * 112 * 1024, 16);
  };
  using I0 = std::integral_constant<int, 0>;
  using I1 = std::integral_constant<int, 1>;
  using I2 = std::integral_constant<int, 2>;
  using I3 = std::integral_constant<int, 3>;
  const rsrc_t ra = mk_rsrc(ws + OFF_ACT + ((size_t)(mt * 4 + sh2 * 2) * 112 + 14 * w) * 1024, 126 * 1024);
  u32x4 af[28];
  auto ldact = [&](auto kI) {
    constexpr int k = decltype(kI)::value;
    af[k] = __builtin_amdgcn_raw_buffer_load_b128(ra, lane * 16 + (k % 14) * 1024, (k / 14) * 112 * 1024, ACTAUX);
  };
  float pe, pm, pr;
  auto ep_a = [&](auto iI, auto rI) {
    constexpr int i = decltype(iI)::value, r = decltype(rI)::value;
    const float a = acc[i][r];
    float e = __builtin_amdgcn_exp2f(__builtin_fmaf(a * a, c2, c1) * a), m = a * acc[i][r + 8];
    asm volatile("" : "+v"(e), "+v"(m));
    pe = e;
    pm = m;
  };
  auto ep_b = [&](auto rI) {
    constexpr int r = decltype(rI)::value;
    float o = pm * __builtin_amdgcn_rcpf(__builtin_fmaf(pe, var, var));
    if constexpr (r & 1) {
      u32 t = umin32(pack_bf16(pr, o));
      asm volatile("" : "+v"(t));
      ov[r / 2] = t;
    } else {
      asm volatile("" : "+v"(o));
      pr = o;
    }
  };
  static_for<0, PF - 1>(rd3);
  ldg(I0{});
  corr(I0{});
  ldg(I1{});
  __builtin_amdgcn_sched_barrier(0);
  static_for<0, 28>([&](auto kI) {
    constexpr int k = decltype(kI)::value;
    acc[3] = mfma32(wf[k], xr3[k % PF], acc[3]);
    pin(acc[3]);
    __builtin_amdgcn_sched_barrier(0);
    if constexpr (k % EXP_LOCK == EXP_LOCK - 1) bar();
    if constexpr (k + PF - 1 < 28) rd3(std::integral_constant<int, k + PF - 1>{});
    constexpr int ti = k / 9, u = k % 9;
    if constexpr (ti < 3) {
      using TI = std::integral_constant<int, ti>;
      if constexpr (u >= 1) ep_b(std::integral_constant<int, u - 1>{});
      if constexpr (u < 8) ep_a(TI{}, std::integral_constant<int, u>{});
      if constexpr (u == 8) {
        put(TI{});
        if constexpr (ti < 2) {
          corr(std::integral_constant<int, ti + 1>{});
          ldg(std::integral_constant<int, ti + 2>{});
        }
      }
    }
    if constexpr (k >= 1 && E12 + E13 + k - 1 < 28) ldwd(std::integral_constant<int, E12 + E13 + k - 1>{});
    if constexpr (k >= 21) ldact(std::integral_constant<int, k - 21>{});
    __builtin_amdgcn_sched_barrier(0);
  });
  YSTAMP(3);
  corr(I3{});
  static_for<7, 11>(ldact);
  __builtin_amdgcn_sched_barrier(0);
  half_epi(I3{}, I0{});
  __builtin_amdgcn_sched_barrier(0);
  static_for<11, 14>(ldact);
  __builtin_amdgcn_sched_barrier(0);
  half_epi(I3{}, I1{});
  put(I3{});
  bar();
  YSTAMP(4);
  auto chk = [&](auto gI) {
    constexpr int g = decltype(gI)::value;
    u32 m = 0;
#pragma unroll
    for (int k = 7 * g; k < 7 * g + 7; ++k) m = umax3(umax3(m, af[k][0], af[k][1]), af[k][2], af[k][3]);
    return (bool)__all(m != ~0u);
  };
  auto valid = [&](auto gI) {
    constexpr int g = decltype(gI)::value;
    if (__builtin_expect(!chk(gI), 0)) {
#pragma nounroll
      for (int spin = 0; spin < (1 << 14); ++spin) {
        __builtin_amdgcn_s_sleep(8);
        static_for<7 * g, 7 * g + 7>(ldact);
        if (chk(gI)) break;
      }
      __builtin_amdgcn_s_waitcnt(0x0F70);
    }
    __builtin_amdgcn_sched_barrier(0);
  };
  const int pcol = mt * 128 + sh2 * 64 + n;
  const int dbase = dt * 64 + w * 8 + 4 * h;
  __builtin_amdgcn_sched_barrier(0);
  LAS f32x4* R = (LAS f32x4*)smem;
  f32x16 o[4];
#pragma unroll
  for (int i = 0; i < 4; ++i)
#pragma unroll
    for (int r = 0; r < 16; ++r) o[i][r] = 0.f;
  auto dump1 = [&](auto tjI, auto cI) {
    constexpr int tj = decltype(tjI)::value, c = decltype(cI)::value, i = tj * 2 + (c >> 2), q = c & 3;
    R[((tj * 8 + c) * 8 + w) * 64 + lane] = f32x4{o[i][4 * q], o[i][4 * q + 1], o[i][4 * q + 2], o[i][4 * q + 3]};
  };
  auto step = [&](auto kI) {
    constexpr int k = decltype(kI)::value, tj = k / 14;
    const bf16x8 b = __builtin_bit_cast(bf16x8, af[k]);
    o[2 * tj] = mfma32(wd[k % 14], b, o[2 * tj]);
    pin(o[2 * tj]);
    __builtin_amdgcn_sched_barrier(0);
    if constexpr (k >= 15 && k < 19) dump1(I0{}, std::integral_constant<int, 2 * (k - 15)>{});
    o[2 * tj + 1] = mfma32(wd[14 + k % 14], b, o[2 * tj + 1]);
    pin(o[2 * tj + 1]);
    __builtin_amdgcn_sched_barrier(0);
    if constexpr (k >= 15 && k < 19) dump1(I0{}, std::integral_constant<int, 2 * (k - 15) + 1>{});
    if constexpr (k >= EXP_T1 && k < 14) {
      constexpr int n0 = ((k - EXP_T1) * 14) / (14 - EXP_T1), n1 = ((k + 1 - EXP_T1) * 14) / (14 - EXP_T1);
      static_for<14 + n0, 14 + n1>(ldact);
    }
    __builtin_amdgcn_sched_barrier(0);
  };
  u16 xres[2][4];
  f32x4 gbv;
  auto fin = [&](auto tjI) {
    constexpr int tj = decltype(tjI)::value;
    f32x4 v[8];
#pragma unroll
    for (int sw = 0; sw < 8; ++sw) v[sw] = R[((tj * 8 + w) * 8 + sw) * 64 + lane];
    const f32x4 sum = ((v[0] + v[1]) + (v[2] + v[3])) + ((v[4] + v[5]) + (v[6] + v[7]));
    float r[4];
#pragma unroll
    for (int e = 0; e < 4; ++e) r[e] = __builtin_fmaf(gbv[e], sum[e], bf2f(xres[tj][e]));
    const int pc = pcol + 32 * tj;
    const int srow = (pc & ~7) | ((pc & 3) << 1) | ((pc >> 2) & 1);
    *(u32x2*)(out + (size_t)srow * D + dbase) = u32x2{pack_bf16(r[0], r[1]), pack_bf16(r[2], r[3])};
  };
  valid(I0{});
  asm volatile(""
               : "+v"(wd[0]), "+v"(wd[1]), "+v"(wd[2]), "+v"(wd[3]), "+v"(wd[4]), "+v"(wd[5]), "+v"(wd[6]), "+v"(wd[7]),
                 "+v"(wd[8]), "+v"(wd[9]), "+v"(wd[10]), "+v"(wd[11]), "+v"(wd[12]), "+v"(wd[13])::"memory");
  asm volatile(""
               : "+v"(wd[14]), "+v"(wd[15]), "+v"(wd[16]), "+v"(wd[17]), "+v"(wd[18]), "+v"(wd[19]), "+v"(wd[20]),
                 "+v"(wd[21]), "+v"(wd[22]), "+v"(wd[23]), "+v"(wd[24]), "+v"(wd[25]), "+v"(wd[26]), "+v"(wd[27])::"memory");
  YSTAMP(5);
  XSTAMP(0);
  static_for<0, 3>(step);
  {
    const u16* X1T = (const u16*)(ws + OFF_X1T) + (size_t)dbase * S + pcol;
#pragma unroll
    for (int r = 0; r < 8; ++r) xres[r >> 2][r & 3] = X1T[(size_t)(r & 3) * S + (r >> 2) * 32];
    gbv = *(const f32x4*)((const float*)(ws + OFF_MOD) + 5 * D + dbase);
    __builtin_amdgcn_sched_barrier(0);
  }
  static_for<3, 7>(step);
  XSTAMP(1);
  valid(I1{});
  static_for<7, 14>(step);
  XSTAMP(2);
  valid(I2{});
  XSTAMP(3);
  static_for<14, 21>(step);
  wait_lgkm0();
  bar();
  XSTAMP(4);
  valid(I3{});
  static_for<21, 24>(step);
  fin(I0{});
  __builtin_amdgcn_sched_barrier(0);
  static_for<24, 28>(step);
  XSTAMP(5);
  static_for<0, 8>([&](auto cI) { dump1(I1{}, cI); });
  wait_lgkm0();
  bar();
  STAMP(ws, 2, 6);
  fin(I1{});
}

extern "C" __global__ NOPK __launch_bounds__(512, 2) void k2_ffn(char* __restrict__ ws, u16* __restrict__ out) {
  extern __shared__ __attribute__((aligned(16))) char smem[];
  const int w = __builtin_amdgcn_readfirstlane(threadIdx.x >> 6);
  if (blockIdx.x < 32) k2_stats(ws, smem);
  else k2_gemm(ws, out, smem, w);
}

extern "C" __device__ __attribute__((used, noinline)) void k_code_end() {}

extern "C" void kernel_launch(void* const* d_in, const int* in_sizes, int n_in, void* d_out, int out_size, void* d_ws,
                              size_t ws_size, hipStream_t stream) {
  const u16* q = (const u16*)d_in[0];
  const u16* k = (const u16*)d_in[1];
  const u16* v = (const u16*)d_in[2];
  const u16* shift_k = (const u16*)d_in[3];
  const u16* ssm_k = (const u16*)d_in[4];
  const u16* ssm_D = (const u16*)d_in[5];
  const u16* w_gate = (const u16*)d_in[7];
  const u16* w_up = (const u16*)d_in[8];
  const u16* w_down = (const u16*)d_in[9];
  const u16* c = (const u16*)d_in[10];
  const u16* w_mod = (const u16*)d_in[11];
  char* ws = (char*)d_ws;
  (void)hipFuncSetAttribute((const void*)k1_conv, hipFuncAttributeMaxDynamicSharedMemorySize, K1_LDS);
  (void)hipFuncSetAttribute((const void*)k2_ffn, hipFuncAttributeMaxDynamicSharedMemorySize, K2_LDS);
  k0_prep<<<K0_GRID, 256, 0, stream>>>(q, k, v, shift_k, c, w_mod, ssm_k, w_gate, w_up, w_down, (const u16*)d_out, ws);
  k1_conv<<<K1_GRID, 256, K1_LDS, stream>>>(ssm_k, ssm_D, w_gate, w_up, w_down, ws);
  k2_ffn<<<256, 512, K2_LDS, stream>>>(ws, (u16*)d_out);
}
```

```cpp
#include <hip/hip_runtime.h>
#include <hip/hip_bf16.h>
#include <stdint.h>
#include <type_traits>

typedef unsigned short u16;
typedef unsigned int u32;
typedef short bf16x8 __attribute__((ext_vector_type(8)));
typedef short s16x4 __attribute__((ext_vector_type(4)));
typedef float f32x2 __attribute__((ext_vector_type(2)));
typedef float f32x4 __attribute__((ext_vector_type(4)));
typedef float f32x16 __attribute__((ext_vector_type(16)));
typedef u32 u32x2 __attribute__((ext_vector_type(2)));
typedef u32 u32x4 __attribute__((ext_vector_type(4)));
typedef __bf16 bf16x2_t __attribute__((ext_vector_type(2)));
#define LAS __attribute__((address_space(3)))
#define DI __device__ __forceinline__

constexpr int S = 2048, D = 448, F = 1792;

constexpr size_t SZ_DS = (size_t)D * S * 2;
constexpr size_t OFF_RE = 0;
constexpr size_t OFF_RO = OFF_RE + SZ_DS;
constexpr size_t OFF_QT = OFF_RO + SZ_DS;
constexpr size_t OFF_X1T = OFF_QT + SZ_DS;
constexpr size_t OFF_MEAN = OFF_X1T + SZ_DS;
constexpr size_t OFF_RSTD = OFF_MEAN + S * 4;
constexpr size_t OFF_MOD = OFF_RSTD + S * 4;
constexpr size_t OFF_SG = OFF_MOD + 16384;
constexpr size_t OFF_CB = OFF_MOD + 12288;

constexpr size_t OFF_VG = OFF_SG + 3 * S * 8;
constexpr size_t OFF_XS = OFF_VG + 4 * S * 8;
constexpr size_t OFF_VP = OFF_XS + SZ_DS;
constexpr size_t OFF_WGU = OFF_VP + 8 * 2 * 3584 * 4;
constexpr size_t OFF_WD = OFF_WGU + (size_t)2 * D * F * 2;
constexpr size_t OFF_ACT = OFF_WD + (size_t)D * F * 2;

#define STAMP_DECL
#define STAMP(wsp, kid, i)
#define STAMP_FLUSH(wsp, kid)
#define HWID(wsp, kid)
#define XSTAMP(i)
#define YSTAMP(i) STAMP(ws, 2, i)
#if defined(__HIP_DEVICE_COMPILE__)
#define NOPK __attribute__((target("no-packed-fp32-ops")))
#else
#define NOPK
#endif

template <int I, int N, class Fn>
DI void static_for(Fn&& f) {
  if constexpr (I < N) {
    f(std::integral_constant<int, I>{});
    static_for<I + 1, N>(f);
  }
}

DI float lo_f(u32 x) { return __builtin_bit_cast(float, x << 16); }
DI float hi_f(u32 x) { return __builtin_bit_cast(float, x & 0xffff0000u); }
DI u32 pack_bf16(float a, float b) {
  f32x2 v = {a, b};
  return __builtin_bit_cast(u32, __builtin_convertvector(v, bf16x2_t));
}
DI float round_bf16(float a) { return lo_f(pack_bf16(a, 0.f)); }
DI float bf2f(u16 x) { return __builtin_bit_cast(float, (u32)x << 16); }

template <int N>
DI void wait_vm() { asm volatile("s_waitcnt vmcnt(%0)" ::"n"(N) : "memory"); }
DI void wait_lgkm0() { asm volatile("s_waitcnt lgkmcnt(0)" ::: "memory"); }
DI void bar() { __builtin_amdgcn_s_barrier(); }

#define SNOP "s_nop 0\n\t"
#define SNOP2 ""
DI const void* uni(const void* p) {
  const unsigned long long v = (unsigned long long)p;
  const u32 lo = __builtin_amdgcn_readfirstlane((u32)v), hi = __builtin_amdgcn_readfirstlane((u32)(v >> 32));
  return (const void*)(((unsigned long long)hi << 32) | lo);
}
DI void glds16_asm(const void* sbase, u32 voff, u32 lds_addr) {
  u32 keep;
  sbase = uni(sbase);
  lds_addr = __builtin_amdgcn_readfirstlane(lds_addr);
  asm volatile("s_mov_b32 %0, m0\n\ts_mov_b32 m0, %3\n\t" SNOP "global_load_lds_dwordx4 %1, %2\n\ts_mov_b32 m0, %0"
               : "=&s"(keep)
               : "v"(voff), "s"(sbase), "s"(lds_addr)
               : "memory");
}
DI void glds16(const void* g, char* lds) {
  __builtin_amdgcn_global_load_lds((const __attribute__((address_space(1))) void*)g, (LAS void*)lds, 16, 0, 2);
}

template <int CTRL>
DI float dpp_add(float v) {
  const int x = __builtin_bit_cast(int, v);
  return v + __builtin_bit_cast(float, __builtin_amdgcn_update_dpp(x, x, CTRL, 0xf, 0xf, false));
}
DI float sum8(float v) {
  v = dpp_add<0xB1>(v);
  v = dpp_add<0x4E>(v);
  v = dpp_add<0x141>(v);
  return v;
}
DI float sum16(float v) { return dpp_add<0x140>(sum8(v)); }
DI float dot2(u32 a, u32 b, float c) {
  return __builtin_amdgcn_fdot2_f32_bf16(__builtin_bit_cast(bf16x2_t, a), __builtin_bit_cast(bf16x2_t, b), c, false);
}
typedef __amdgpu_buffer_rsrc_t rsrc_t;
DI rsrc_t mk_rsrc(const void* p, u32 bytes) { return __builtin_amdgcn_make_buffer_rsrc((void*)p, 0, bytes, 0x00020000); }
DI void st16_sc1(void* p, u32x4 v) { asm volatile("global_store_dwordx4 %0, %1, off sc1\n\ts_nop 1" ::"v"(p), "v"(v) : "memory"); }
DI void st8_sc1(void* p, u32x2 v) { asm volatile("global_store_dwordx2 %0, %1, off sc1" ::"v"(p), "v"(v) : "memory"); }
DI void st4_sc1(void* p, float v) { asm volatile("global_store_dword %0, %1, off sc1" ::"v"(p), "v"(v) : "memory"); }

#define EXP_E12 2
#define EXP_E13 6
#define EXP_LOCK 4
#define EXP_NW2 14
DI bf16x8 tr_frag(const char* smem, int o0, int o1) {
  s16x4 x0 = __builtin_amdgcn_ds_read_tr16_b64_v4i16((LAS s16x4*)(smem + o0));
  s16x4 x1 = __builtin_amdgcn_ds_read_tr16_b64_v4i16((LAS s16x4*)(smem + o1));
  return __builtin_shufflevector(x0, x1, 0, 1, 2, 3, 4, 5, 6, 7);
}
DI f32x16 mfma32(u32x4 a, bf16x8 b, f32x16 c) {
  return __builtin_amdgcn_mfma_f32_32x32x16_bf16(__builtin_bit_cast(bf16x8, a), b, c, 0, 0, 0);
}

DI void retile(int task, const u16* __restrict__ w_gate, const u16* __restrict__ w_up, const u16* __restrict__ w_down,
               char* __restrict__ ws, char* smem, int w, int lane) {
  const int xr = lane >> 4, slot = lane & 15;
  const int chn = slot ^ ((xr << 2) | (w & 3));
  const int h = lane >> 5, blk = (lane >> 4) & 1, qq = (lane & 15) >> 2, p = lane & 3;
  const bool gu = task < 112;
  int kq = 0, fg = 0, dg = 0, fk = 0;
  if (gu) {
    fg = task % 28, kq = task / 28;
    const u16* src = (chn < 8 ? w_gate : w_up) + (size_t)(kq * 112 + 4 * w + xr) * F + fg * 64 + (chn & 7) * 8;
#pragma unroll
    for (int i = 0; i < 7; ++i) glds16(src + (size_t)i * 16 * F, smem + (w + 4 * i) * 1024);
  } else {
    dg = (task - 112) % 7, fk = (task - 112) / 7;
    const u16* src = w_down + (size_t)(fk * 224 + (chn >> 3) * 112 + 4 * w + xr) * D + dg * 64 + (chn & 7) * 8;
#pragma unroll
    for (int i = 0; i < 7; ++i) glds16(src + (size_t)i * 16 * D, smem + (w + 4 * i) * 1024);
  }
  const int col = gu ? blk * 128 + w * 32 + (p & 1) * 16 + (p >> 1) * 8 : (w >> 1) * 128 + (w & 1) * 64 + blk * 32 + p * 8;
  int o[2];
#pragma unroll
  for (int t = 0; t < 2; ++t)
    o[t] = 256 * (8 * h + 4 * t + qq) + 16 * ((col >> 4) ^ ((qq << 2) | ((2 * h + t) & 3))) + (col & 8);
  u32x4* dst = gu ? (u32x4*)(ws + OFF_WGU) + ((size_t)(fg * 4 + w) * 28 + kq * 7) * 64 + lane
                  : (u32x4*)(ws + OFF_WD) + ((size_t)(dg * 2 + (w & 1)) * 112 + fk * 14 + (w >> 1) * 7) * 64 + lane;
  u32x4 bv[7];
  if (gu) {
    const u16* cbp = (const u16*)(ws + OFF_CB) + (lane & 1) * D + kq * 112 + 8 * h;
#pragma unroll
    for (int i = 0; i < 7; ++i) bv[i] = *(const u32x4*)(cbp + 16 * i);
  }
  __syncthreads();
  f32x16 acc;
#pragma unroll
  for (int r = 0; r < 16; ++r) acc[r] = 0.f;
#pragma unroll
  for (int i = 0; i < 7; ++i) {
    const bf16x8 f = tr_frag(smem, o[0] + i * 4096, o[1] + i * 4096);
    st16_sc1(dst + i * 64, __builtin_bit_cast(u32x4, f));
    if (gu) acc = mfma32(bv[i], f, acc);
  }
  if (gu && h == 0) ((f32x2*)(ws + OFF_VP))[kq * 3584 + (fg * 4 + w) * 32 + lane] = f32x2{acc[0], acc[1]};
}

#define EXP_T1 7
#define ACTAUX 16
#define NTAUX 2
constexpr int K0_GRID = 336;

extern "C" __global__ __launch_bounds__(256) void k0_prep(const u16* __restrict__ q, const u16* __restrict__ k,
                                               const u16* __restrict__ v, const u16* __restrict__ shift_k,
                                               const u16* __restrict__ c, const u16* __restrict__ w_mod,
                                               const u16* __restrict__ ssm_k, char* __restrict__ ws) {
  __shared__ float red[4][64];
  const int b = blockIdx.x, tid = threadIdx.x, lane = tid & 63, w = __builtin_amdgcn_readfirstlane(tid >> 6);
  STAMP_DECL;
  STAMP(ws, 0, 0);
  const int xcd = b & 7, idx = b >> 3;
  u32 cwarm = 0;
  if (idx == 40 || idx == 41) {
    u32 lo, hi, len;
    asm volatile(
        "s_getpc_b64 s[40:41]\n\ts_add_u32 s40, s40, k1_conv@rel32@lo+4\n\ts_addc_u32 s41, s41, k1_conv@rel32@hi+12\n\t"
        "s_mov_b32 %0, s40\n\ts_mov_b32 %1, s41\n\ts_mov_b32 %2, k_code_end-k1_conv"
        : "=s"(lo), "=s"(hi), "=s"(len)::"s40", "s41", "scc");
    const char* code = (const char*)(((unsigned long long)hi << 32) | lo);
    if ((u32)((idx - 40) * 256 + tid) * 128 < len) cwarm = *(const u32*)(code + ((idx - 40) * 256 + tid) * 128);
  }
  int misc = -1;
  if (xcd == 7) misc = idx < 14 ? 64 + idx : (idx < 36 ? idx - 14 : 1000);
  else if (idx < 4) misc = 64 + 14 + xcd * 4 + idx;
  else if (idx >= 36) misc = 22 + xcd * 6 + (idx - 36);
  if (misc < 0) {
    const int rb = idx - 4;
    const int sq = lane >> 5, cp = lane & 31;
    const int d0 = xcd * 64 + cp * 2;
    const int a = rb * 8 + w * 2 + sq;
    const int s0 = a * 8;
    const rsrc_t rk = mk_rsrc(k, S * D * 2), rv = mk_rsrc(v, S * D * 2), rq = mk_rsrc(q, S * D * 2);
    const int oB = (s0 * D + d0) * 2;
    const int oA = oB - 4 * D * 2, oC = oB + 4 * D * 2;
    u32 kk[12], vv[9], qq[8];
#pragma unroll
    for (int i = 0; i < 4; ++i) kk[i] = __builtin_amdgcn_raw_buffer_load_b32(rk, oA + i * D * 2, 0, NTAUX);
#pragma unroll
    for (int i = 0; i < 4; ++i) kk[4 + i] = __builtin_amdgcn_raw_buffer_load_b32(rk, oB + i * D * 2, 0, NTAUX);
#pragma unroll
    for (int i = 0; i < 4; ++i) kk[8 + i] = __builtin_amdgcn_raw_buffer_load_b32(rk, oC + i * D * 2, 0, NTAUX);
    vv[0] = __builtin_amdgcn_raw_buffer_load_b32(rv, oA + 3 * D * 2, 0, NTAUX);
#pragma unroll
    for (int i = 0; i < 4; ++i) vv[1 + i] = __builtin_amdgcn_raw_buffer_load_b32(rv, oB + i * D * 2, 0, NTAUX);
#pragma unroll
    for (int i = 0; i < 4; ++i) vv[5 + i] = __builtin_amdgcn_raw_buffer_load_b32(rv, oC + i * D * 2, 0, NTAUX);
#pragma unroll
    for (int i = 0; i < 4; ++i) qq[i] = __builtin_amdgcn_raw_buffer_load_b32(rq, oB + i * D * 2, 0, NTAUX);
#pragma unroll
    for (int i = 0; i < 4; ++i) qq[4 + i] = __builtin_amdgcn_raw_buffer_load_b32(rq, oC + i * D * 2, 0, NTAUX);
    const u32x4 sk = *(const u32x4*)(shift_k + d0 * 4);
    u32 warm = 0;
    if (w == 0) warm = __builtin_nontemporal_load((const u32*)((const char*)ssm_k + ((size_t)(xcd * 32 + rb) * 64 + lane) * 128));
    f32x2 wt[4] = {{lo_f(sk[0]), lo_f(sk[2])}, {hi_f(sk[0]), hi_f(sk[2])}, {lo_f(sk[1]), lo_f(sk[3])}, {hi_f(sk[1]), hi_f(sk[3])}};
    f32x2 kf[12], u[9];
#pragma unroll
    for (int i = 0; i < 12; ++i) kf[i] = f32x2{lo_f(kk[i]), hi_f(kk[i])};
#pragma unroll
    for (int i = 0; i < 9; ++i) {
      f32x2 t = kf[i + 3] * wt[0];
      t = __builtin_elementwise_fma(kf[i + 2], wt[1], t);
      t = __builtin_elementwise_fma(kf[i + 1], wt[2], t);
      t = __builtin_elementwise_fma(kf[i], wt[3], t);
      u[i] = t * f32x2{lo_f(vv[i]), hi_f(vv[i])};
    }
    {
      char* dst = ws + (size_t)d0 * (S * 2) + (2040 - s0) * 2;
#pragma unroll
      for (int cI = 0; cI < 2; ++cI) {
        *(u32x4*)(dst + OFF_RO + cI * S * 2) = u32x4{pack_bf16(u[8][cI], u[7][cI]), pack_bf16(u[6][cI], u[5][cI]),
                                                     pack_bf16(u[4][cI], u[3][cI]), pack_bf16(u[2][cI], u[1][cI])};
        *(u32x4*)(dst + OFF_RE + cI * S * 2) = u32x4{pack_bf16(u[7][cI], u[6][cI]), pack_bf16(u[5][cI], u[4][cI]),
                                                     pack_bf16(u[3][cI], u[2][cI]), pack_bf16(u[1][cI], u[0][cI])};
      }
      char* dq = ws + OFF_QT + (size_t)d0 * (S * 2) + s0 * 2;
#pragma unroll
      for (int cI = 0; cI < 2; ++cI) {
        const u32 sel = cI ? 0x07060302u : 0x05040100u;
        *(u32x4*)(dq + cI * S * 2) = u32x4{__builtin_amdgcn_perm(qq[2], qq[0], sel), __builtin_amdgcn_perm(qq[6], qq[4], sel),
                                           __builtin_amdgcn_perm(qq[3], qq[1], sel), __builtin_amdgcn_perm(qq[7], qq[5], sel)};
      }
    }
    asm volatile("" ::"v"(warm));
  } else if (misc < 64) {
    const int bb = misc;
    const int row = bb * 32 + w * 8 + (lane >> 3);
    const int sub = lane & 7;
    u32x4 x[7];
#pragma unroll
    for (int i = 0; i < 7; ++i) x[i] = __builtin_nontemporal_load((const u32x4*)(q + (size_t)row * D + (i * 8 + sub) * 8));
    if (sub < 3) ((unsigned long long*)(ws + OFF_SG))[sub * S + row] = 0ull;
    float s = 0.f, s2 = 0.f;
#pragma unroll
    for (int i = 0; i < 7; ++i)
#pragma unroll
      for (int j = 0; j < 4; ++j) {
        const u32 t = x[i][j];
        s = dot2(t, 0x3f803f80u, s);
        s2 = dot2(t, t, s2);
      }
    s = sum8(s);
    s2 = sum8(s2);
    const float mean = s * (1.f / D);
    float var = s2 * (1.f / D) - mean * mean;
    var = var < 0.f ? 0.f : var;
    const float rstd = __builtin_amdgcn_rsqf(var + 1e-5f);
    if (sub < 2) {
      const int p = (row & ~7) | ((row & 1) << 2) | ((row >> 1) & 3);
      ((float*)(ws + OFF_MEAN))[sub * S + p] = sub ? rstd : mean;
    }
  } else if (misc < 106) {
    const int bb = misc - 64;
    const int co = lane >> 3, rs = lane & 7;
    const int col0 = bb * 64 + co * 8;
    u32x4 x[14];
#pragma unroll
    for (int i = 0; i < 14; ++i)
      x[i] = __builtin_nontemporal_load((const u32x4*)(w_mod + (size_t)(w * 112 + rs * 14 + i) * (6 * D) + col0));
    u32 cv[7];
#pragma unroll
    for (int i = 0; i < 7; ++i) cv[i] = *(const u32*)(c + w * 112 + rs * 14 + 2 * i);
    __builtin_amdgcn_sched_barrier(0);
    f32x2 acc[4] = {{0.f, 0.f}, {0.f, 0.f}, {0.f, 0.f}, {0.f, 0.f}};
#pragma unroll
    for (int i = 0; i < 14; ++i) {
      const float cf = (i & 1) ? hi_f(cv[i >> 1]) : lo_f(cv[i >> 1]);
      const float sl = round_bf16(cf * __builtin_amdgcn_rcpf(1.f + __builtin_amdgcn_exp2f(cf * -1.44269504f)));
#pragma unroll
      for (int j = 0; j < 4; ++j)
        acc[j] = __builtin_elementwise_fma(f32x2{lo_f(x[i][j]), hi_f(x[i][j])}, f32x2{sl, sl}, acc[j]);
    }
    float sel = 0.f;
#pragma unroll
    for (int j = 0; j < 8; ++j) {
      const float t = sum8(acc[j >> 1][j & 1]);
      sel = (rs == j) ? t : sel;
    }
    red[w][lane] = sel;
    __syncthreads();
    if (tid < 64) {
      const float t = (red[0][tid] + red[1][tid]) + (red[2][tid] + red[3][tid]);
      const float m = round_bf16(t);
      const int col = bb * 64 + tid;
      ((float*)(ws + OFF_MOD))[col] = m;
      if (col >= 3 * D && col < 5 * D) {
        const bool isc = col >= 4 * D;
        ((u16*)(ws + OFF_CB))[isc ? col - 4 * D : col - 2 * D] = (u16)pack_bf16(isc ? 1.f + m : m, 0.f);
      }
    }
  }
  asm volatile("" ::"v"(cwarm));
  STAMP(ws, 0, 1);
  STAMP_FLUSH(ws, misc < 0 ? 0 : (misc < 64 ? 4 : 5));
}

constexpr int K1_KSTR = 260 * 16;
constexpr int K1_USTR = 286 * 16;
constexpr int K1_UOFF = 4 * K1_KSTR;
constexpr int K1_XOFF = K1_UOFF + 2 * K1_USTR;
constexpr int K1_CH = K1_XOFF + 8192;
constexpr int K1_LDS = 2 * K1_CH;

DI void glds16x4_asm(const void* sbase, u32 voff, u32 voff_last, u32 lds_addr) {
  u32 keep;
  sbase = uni(sbase);
  lds_addr = __builtin_amdgcn_readfirstlane(lds_addr);
  asm volatile(
      "s_mov_b32 %0, m0\n\ts_mov_b32 m0, %4\n\t" SNOP
      "global_load_lds_dwordx4 %1, %3\n\t"
      "global_load_lds_dwordx4 %1, %3 offset:1024\n\t"
      "global_load_lds_dwordx4 %1, %3 offset:2048\n\t"
      "global_load_lds_dwordx4 %2, %3 offset:3072\n\t"
      "s_mov_b32 m0, %0"
      : "=&s"(keep)
      : "v"(voff), "v"(voff_last), "s"(sbase), "s"(lds_addr)
      : "memory");
}
#define CAS __attribute__((address_space(4)))
template <int half>
DI void k1_conv_wave(const u16* __restrict__ ssm_k, const u16* __restrict__ ssm_D, char* __restrict__ ws, char* smem,
                     int ch, int lane) {
  STAMP_DECL;
  STAMP(ws, 1, 0);
  const int cpair = (blockIdx.x & 7) * 32 + (blockIdx.x >> 3);
  const int d = 2 * cpair + ch;
  char* L = smem + ch * K1_CH;
  const u32 L0 = (u32)(size_t)(LAS char*)L;
  const u16* kf = ssm_k + (size_t)d * S;
  {
    const u32 vo = lane * 16, vl = (lane > 62 ? 62 : lane) * 16;
    glds16x4_asm(kf + (2 + 4 * half), vo, vl, L0 + (2 * half) * K1_KSTR + 64);
    glds16x4_asm(kf + (4 + 4 * half), vo, vl, L0 + (2 * half + 1) * K1_KSTR + 64);
    glds16x4_asm(ws + (half ? OFF_RO : OFF_RE) + (size_t)d * S * 2, vo, vo, L0 + K1_UOFF + half * K1_USTR);
  }
  const int n = lane & 15, q4 = lane >> 4;
  const int pbase = 32 * (n >> 1) + 8 * q4 + 4 * (n & 1) + 1024 * half;
  u32 head = 0;
  const int di = n + q4 - 15;
  if constexpr (half == 0) {
    asm volatile(SNOP2 "global_load_dword %0, %1, %2" : "=v"(head) : "v"((u32)(di < 0 ? 0 : di) * 4), "s"(uni(kf)) : "memory");
  }
  const CAS float* MODc = (const CAS float*)(size_t)(ws + OFF_MOD);
  const float sa = MODc[d], ca = MODc[D + d], ga = MODc[2 * D + d], cbv = MODc[4 * D + d];
  const u32 Draw = ((const CAS u32*)(size_t)ssm_D)[cpair];
  if constexpr (half == 1) {
    const int l5 = lane & 31;
    if (l5 < 28) *(LAS u32x4*)(L + K1_UOFF + (lane >> 5) * K1_USTR + (256 + l5) * 16) = u32x4{0u, 0u, 0u, 0u};
  }
  u32 Ab = L0 + (n & 3) * K1_KSTR + ((n >> 2) + q4 + 16 * half) * 16;
  u32 Bb = L0 + K1_UOFF + (n & 1) * K1_USTR + (28 - 4 * (n >> 1) + q4 + 16 * half) * 16;
  u32 Hd = L0 + q4 * K1_KSTR + n * 4;
  asm volatile("" : "+v"(Ab), "+v"(Bb), "+v"(Hd) : "s"(sa), "s"(ca), "s"(ga), "s"(Draw), "s"(cbv));
  asm volatile("s_waitcnt vmcnt(0)" : "+v"(head)::"memory");
  if constexpr (half == 0) *(LAS u32*)(size_t)Hd = di < 0 ? 0u : head;
  wait_lgkm0();
  STAMP(ws, 1, 1);
  bar();
  STAMP(ws, 1, 2);
  bf16x8 fa[2][8], fb[2][8];
  auto rd = [&](auto dI) {
    constexpr int dl = decltype(dI)::value;
#pragma unroll
    for (int g = 0; g < 8; ++g) {
      fa[dl & 1][g] = *(const LAS bf16x8*)(size_t)(Ab + 64 * dl + 512 * g);
      fb[dl & 1][g] = *(const LAS bf16x8*)(size_t)(Bb + 64 * dl + 512 * (7 - g));
    }
  };
  rd(std::integral_constant<int, 0>{});
  __builtin_amdgcn_sched_barrier(0);
  u32x2 qv[4];
  f32x4 mu[4], rs[4];
  asm volatile(SNOP2
               "global_load_dwordx2 %0, %12, %14\n\t"
               "global_load_dwordx2 %1, %12, %14 offset:512\n\t"
               "global_load_dwordx2 %2, %12, %14 offset:1024\n\t"
               "global_load_dwordx2 %3, %12, %14 offset:1536\n\t"
               "global_load_dwordx4 %4, %13, %15\n\t"
               "global_load_dwordx4 %5, %13, %15 offset:1024\n\t"
               "global_load_dwordx4 %6, %13, %15 offset:2048\n\t"
               "global_load_dwordx4 %7, %13, %15 offset:3072\n\t"
               "global_load_dwordx4 %8, %13, %16\n\t"
               "global_load_dwordx4 %9, %13, %16 offset:1024\n\t"
               "global_load_dwordx4 %10, %13, %16 offset:2048\n\t"
               "global_load_dwordx4 %11, %13, %16 offset:3072"
               : "=&v"(qv[0]), "=&v"(qv[1]), "=&v"(qv[2]), "=&v"(qv[3]), "=&v"(mu[0]), "=&v"(mu[1]), "=&v"(mu[2]),
                 "=&v"(mu[3]), "=&v"(rs[0]), "=&v"(rs[1]), "=&v"(rs[2]), "=&v"(rs[3])
               : "v"((u32)pbase * 2), "v"((u32)pbase * 4), "s"(uni(ws + OFF_QT + (size_t)d * S * 2)),
                 "s"(uni(ws + OFF_MEAN)), "s"(uni(ws + OFF_RSTD))
               : "memory");
  f32x4 acc[8];
#pragma unroll
  for (int j = 0; j < 8; ++j) acc[j] = f32x4{0.f, 0.f, 0.f, 0.f};
  f32x4 qf[4], hg[4], kd[4];
  u32x4 uc[4];
  static_for<0, 4>([&](auto dI) {
    constexpr int dl = decltype(dI)::value;
    if constexpr (dl < 3) rd(std::integral_constant<int, dl + 1>{});
    if constexpr (dl == 2) {
      asm volatile("s_waitcnt vmcnt(0)"
                   : "+v"(qv[0]), "+v"(qv[1]), "+v"(qv[2]), "+v"(qv[3]), "+v"(mu[0]), "+v"(mu[1]), "+v"(mu[2]), "+v"(mu[3]),
                     "+v"(rs[0]), "+v"(rs[1]), "+v"(rs[2]), "+v"(rs[3]));
    }
    if constexpr (dl == 1) {
#pragma unroll
      for (int j = 0; j < 4; ++j)
        uc[j] = *(const LAS u32x4*)(L + K1_UOFF + K1_USTR + (255 - (32 * (4 * half + j) + 4 * (n >> 1) + q4)) * 16);
    }
    if constexpr (dl >= 2) {
      const int sh = (n & 1) ? 16 : 0;
      const float gc = ga * round_bf16(1.f + ca), gs = ga * sa, Dd = ch ? hi_f(Draw) : lo_f(Draw);
#pragma unroll
      for (int j = 2 * (dl - 2); j < 2 * (dl - 2) + 2; ++j) {
#pragma unroll
        for (int r = 0; r < 4; ++r) {
          kd[j][r] = __builtin_bit_cast(float, (uc[j][3 - r] << sh) & 0xffff0000u) * Dd;
          qf[j][r] = (r & 1) ? hi_f(qv[j][r >> 1]) : lo_f(qv[j][r >> 1]);
          hg[j][r] = (qf[j][r] - mu[j][r]) * rs[j][r] * gc + gs;
        }
        asm volatile("" : "+v"(kd[j]), "+v"(qf[j]), "+v"(hg[j]));
      }
    }
#pragma unroll
    for (int sdiag = 0; sdiag < 8; ++sdiag)
#pragma unroll
      for (int g = 0; g <= sdiag; ++g)
        acc[sdiag] = __builtin_amdgcn_mfma_f32_16x16x32_bf16(fa[dl & 1][g], fb[dl & 1][sdiag - g], acc[sdiag], 0, 0, 0);
    if constexpr (dl == 0) {
#pragma unroll
      for (int i = 0; i < 16; ++i) {
        __builtin_amdgcn_sched_group_barrier(0x008, 2, 0);
        __builtin_amdgcn_sched_group_barrier(0x100, 1, 0);
      }
    } else if constexpr (dl == 1) {
#pragma unroll
      for (int i = 0; i < 16; ++i) {
        __builtin_amdgcn_sched_group_barrier(0x008, 2, 0);
        __builtin_amdgcn_sched_group_barrier(0x100, 1, 0);
      }
#pragma unroll
      for (int i = 0; i < 4; ++i) {
        __builtin_amdgcn_sched_group_barrier(0x008, 1, 0);
        __builtin_amdgcn_sched_group_barrier(0x100, 1, 0);
      }
    } else {
      __builtin_amdgcn_sched_group_barrier(0x008, 4, 0);
#pragma unroll
      for (int i = 0; i < 16; ++i) {
        __builtin_amdgcn_sched_group_barrier(0x008, 1, 0);
        __builtin_amdgcn_sched_group_barrier(0x002, 2, 0);
        __builtin_amdgcn_sched_group_barrier(0x008, 1, 0);
        __builtin_amdgcn_sched_group_barrier(0x002, 2, 0);
        __builtin_amdgcn_sched_group_barrier(0x100, 1, 0);
      }
    }
    __builtin_amdgcn_sched_barrier(0);
  });
  STAMP(ws, 1, 3);
  LAS f32x4* X = (LAS f32x4*)(L + K1_XOFF);
#pragma unroll
  for (int j = 0; j < 4; ++j) X[half * 256 + j * 64 + lane] = acc[4 * (1 - half) + j];
  wait_lgkm0();
  bar();
  STAMP(ws, 1, 4);
  u16* X1T = (u16*)(ws + OFF_X1T) + (size_t)d * S + pbase;
  u16* XS = (u16*)(ws + OFF_XS) + (size_t)d * S + pbase;
  const float c1 = round_bf16(1.f + cbv);
#pragma unroll
  for (int j = 0; j < 4; ++j) {
    const f32x4 y = X[(1 - half) * 256 + j * 64 + lane];
    const f32x4 o = qf[j] + hg[j] * ((acc[4 * half + j] + kd[j]) + y);
    const u32 p0 = pack_bf16(o[0], o[1]), p1 = pack_bf16(o[2], o[3]);
    st8_sc1(XS + 256 * j, u32x2{pack_bf16(lo_f(p0) * c1, hi_f(p0) * c1), pack_bf16(lo_f(p1) * c1, hi_f(p1) * c1)});
    st8_sc1(X1T + 256 * j, u32x2{p0, p1});
  }
  STAMP(ws, 1, 5);
  STAMP_FLUSH(ws, 1);
}

#define EXP_DELAY 40
constexpr int K1_GRID = 456;
extern "C" __global__ NOPK __launch_bounds__(256, 2) void k1_conv(const u16* __restrict__ ssm_k,
                                                                  const u16* __restrict__ ssm_D,
                                                                  const u16* __restrict__ w_gate,
                                                                  const u16* __restrict__ w_up,
                                                                  const u16* __restrict__ w_down, char* __restrict__ ws) {
  extern __shared__ __attribute__((aligned(16))) char smem[];
  const int tid = threadIdx.x, lane = tid & 63;
  const int w = __builtin_amdgcn_readfirstlane(tid >> 6);
  const int xcd = blockIdx.x & 7, idx = blockIdx.x >> 3;
  if (xcd < 7 && idx < 32) {
    if (w & 1) k1_conv_wave<1>(ssm_k, ssm_D, ws, smem, w >> 1, lane);
    else k1_conv_wave<0>(ssm_k, ssm_D, ws, smem, w >> 1, lane);
  } else {
    STAMP_DECL;
    STAMP(ws, 7, 0);
    if (xcd == 7 && idx >= 42 && idx < 50) return;
    if (xcd < 7) __builtin_amdgcn_s_sleep(EXP_DELAY);
    if (idx >= 50) {
      u32x4* ap = (u32x4*)(ws + OFF_ACT) + (size_t)(blockIdx.x - 400) * 8192 + tid;
      const u32x4 sent = {~0u, ~0u, ~0u, ~0u};
#pragma unroll 8
      for (int i = 0; i < 32; ++i) st16_sc1(ap + i * 256, sent);
      wait_vm<0>();
    } else {
      retile(xcd == 7 ? idx : 42 + xcd * 18 + (idx - 32), w_gate, w_up, w_down, ws, smem, w, lane);
    }
    STAMP(ws, 7, 1);
    STAMP_FLUSH(ws, 7);
  }
}

constexpr int K2_XB = D * 256;
constexpr int K2_ST = K2_XB;
constexpr int K2_LDS = 131072;

DI void gload16_asm(u32x4& dst, const void* sbase, u32 voff) {
  sbase = uni(sbase);
  asm volatile(SNOP2 "global_load_dwordx4 %0, %1, %2" : "=v"(dst) : "v"(voff), "s"(sbase) : "memory");
}
DI u32 umin32(u32 v) { return v < 0xFFFFFFFEu ? v : 0xFFFFFFFEu; }
DI u32 umax3(u32 a, u32 b, u32 c) { const u32 t = a > b ? a : b; return t > c ? t : c; }
template <int OFF>
DI void gload16_off(u32x4& dst, const void* sbase, u32 voff) {
  sbase = uni(sbase);
  asm volatile(SNOP2 "global_load_dwordx4 %0, %1, %2 offset:%3" : "=v"(dst) : "v"(voff), "s"(sbase), "n"(OFF) : "memory");
}
DI void glds16_nom0(const void* sbase, u32 voff, u32 lds_addr) {
  sbase = uni(sbase);
  lds_addr = __builtin_amdgcn_readfirstlane(lds_addr);
  asm volatile("s_mov_b32 m0, %2\n\t" SNOP "global_load_lds_dwordx4 %0, %1" ::"v"(voff), "s"(sbase), "s"(lds_addr) : "memory");
}
template <int N>
DI void wait_vm_pin(u32x4& a, u32x4& b) {
  asm volatile("s_waitcnt vmcnt(%2)" : "+v"(a), "+v"(b) : "n"(N) : "memory");
}
DI float xor32(float v) {
  const u32 u = __builtin_bit_cast(u32, v);
  auto r = __builtin_amdgcn_permlane32_swap(u, u, false, false);
  const u32 a = r[0], b = r[1];
  return __builtin_bit_cast(float, (threadIdx.x & 32) ? a : b);
}

typedef unsigned long long u64;
typedef __attribute__((address_space(1))) u64 gu64;
typedef __attribute__((address_space(1))) u32 gu32;
#define RLX_AGENT __ATOMIC_RELAXED, __HIP_MEMORY_SCOPE_AGENT
#define EXP_AHEAD 2
constexpr int K2_AHEAD = EXP_AHEAD;

DI void k2_stats(char* __restrict__ ws, char* smem) {
  const int tid = threadIdx.x, lane = tid & 63;
  const int w = __builtin_amdgcn_readfirstlane(tid >> 6);
  const int n = lane & 31, h = lane >> 5;
  STAMP_DECL;
  STAMP(ws, 2, 0);
  {
    const int p0 = blockIdx.x * 64;
    const u32* src = (const u32*)(ws + OFF_X1T) + ((size_t)(w * 56 + h) * S + p0) / 2 + n;
    u32 x[28];
#pragma unroll
    for (int i = 0; i < 28; ++i) x[i] = src[(size_t)i * S];
    float s0 = 0.f, s1 = 0.f, q0 = 0.f, q1 = 0.f;
#pragma unroll
    for (int i = 0; i < 28; ++i) {
      const float a = lo_f(x[i]), b = hi_f(x[i]);
      s0 += a;
      s1 += b;
      q0 = __builtin_fmaf(a, a, q0);
      q1 = __builtin_fmaf(b, b, q1);
    }
    *(LAS f32x4*)(smem + ((w * 2 + h) * 32 + n) * 16) = f32x4{s0, q0, s1, q1};
    __syncthreads();
    if (tid < 64) {
      float sm = 0.f, sq = 0.f;
#pragma unroll
      for (int i = 0; i < 16; ++i) {
        const f32x2 t = *(const LAS f32x2*)(smem + (i * 64 + tid) * 8);
        sm += t[0];
        sq += t[1];
      }
      const float mean = sm * (1.f / D);
      float var = sq * (1.f / D) - mean * mean;
      var = (var < 0.f ? 0.f : var) + 1e-5f;
      const float rstd = 1.f / sqrtf(var);
      const float sig = var * rstd;
      const float mh = round_bf16(mean), sh = round_bf16(sig);
      const u32 b0 = pack_bf16(-mh, mh - mean), b1 = pack_bf16(-mh, sh), b2 = pack_bf16(sig - sh, sh);
      const float c1 = -2.302208198f * rstd;
      const float c2 = -0.1029432397f * (rstd * rstd * rstd);
      gu64* G = (gu64*)(size_t)(ws + OFF_SG) + p0 + tid;
      __hip_atomic_store(G, ((u64)b1 << 32) | b0, RLX_AGENT);
      __hip_atomic_store(G + S, ((u64)__builtin_bit_cast(u32, var) << 32) | b2, RLX_AGENT);
      __hip_atomic_store(G + 2 * S, ((u64)__builtin_bit_cast(u32, c1) << 32) | __builtin_bit_cast(u32, c2), RLX_AGENT);
    }
    STAMP(ws, 6, 1);
    STAMP(ws, 6, 2);
    STAMP_FLUSH(ws, 6);
  }
}

DI void k2_gemm(char* __restrict__ ws, u16* __restrict__ out, char* smem, const int w) {
  const int tid = threadIdx.x, lane = tid & 63;
  const int n = lane & 31, h = lane >> 5;
  STAMP_DECL;
  STAMP(ws, 2, 0);
  const int bid = blockIdx.x - 32;
  const int xcd = bid & 7, idx = bid >> 3;
  const int mt = (xcd >> 1) * 4 + (idx & 3);
  const int dt = idx >> 2, sh2 = xcd & 1;
  const int ft = sh2 * 7 + dt;
  const int tile = ft * 8 + w;
  const u32 lds0 = (u32)(size_t)(LAS char*)smem;
  u32x4 wf[28];
  const int xr = lane >> 4, slot = lane & 15;
  const char* xbase = ws + OFF_XS + ((size_t)(4 * w) * S + mt * 128) * 2;
  const u32 xoff = (u32)(xr * S + (slot ^ ((xr << 2) | (w & 3))) * 8) * 2;
  const char* wbase = ws + OFF_WGU + (size_t)(tile * 28) * 1024 + 4096;
  const u32 wa = lane * 16;
  auto issue1 = [&](auto qI) {
    constexpr int q = decltype(qI)::value, j = q / 3, r = q % 3;
    if constexpr (r == 0) glds16_nom0(xbase, xoff + j * (32 * S * 2), lds0 + (32 * j + 4 * w) * 256);
    if constexpr (r != 0) {
      constexpr int k = 2 * j + r - 1;
      gload16_off<(k % 8) * 1024 - 4096>(wf[k], wbase + (k / 8) * 8192, wa);
    }
  };
  static_for<0, 3>(issue1);
  static_for<3, 3 * (K2_AHEAD + 1)>(issue1);
  constexpr int NTL[4] = {0, 2, 1, 3};
  int xo[4][2];
  {
    const int blk = (lane >> 4) & 1, qq = (lane & 15) >> 2, p = lane & 3;
#pragma unroll
    for (int i = 0; i < 4; ++i) {
#pragma unroll
      for (int t = 0; t < 2; ++t) {
        const int row = 8 * h + 4 * t + qq;
        const int chn = 4 * NTL[i] + 2 * blk + (p >> 1);
        xo[i][t] = 256 * row + 16 * (chn ^ ((qq << 2) | ((2 * h + t) & 3))) + 8 * (p & 1);
      }
    }
  }
  f32x16 acc[4];
#pragma unroll
  for (int i = 0; i < 4; ++i)
#pragma unroll
    for (int r = 0; r < 16; ++r) acc[i][r] = 0.f;
  bf16x8 xa[2][3], xb[2][3];
  asm volatile("" : "+v"(xo[0][0]), "+v"(xo[0][1]), "+v"(xo[1][0]), "+v"(xo[1][1]), "+v"(xo[2][0]), "+v"(xo[2][1]),
               "+v"(xo[3][0]), "+v"(xo[3][1]));
  wait_vm<3 * K2_AHEAD + 2>();
  __builtin_amdgcn_sched_barrier(0);
  bar();
  YSTAMP(1);
#pragma unroll
  for (int ks = 0; ks < 2; ++ks)
#pragma unroll
    for (int i = 0; i < 3; ++i) xa[ks][i] = tr_frag(smem, xo[i][0] + ks * 4096, xo[i][1] + ks * 4096);
  __builtin_amdgcn_sched_barrier(0);
  u32x4 aext;
  auto mk_aext = [&](f32x2 p0, f32x2 p1, f32x2 p2, f32x2 p3) {
    const float v1 = (p0[0] + p1[0]) + (p2[0] + p3[0]);
    const float v2 = (p0[1] + p1[1]) + (p2[1] + p3[1]);
    const float v1h = round_bf16(v1), v2h = round_bf16(v2);
    u32x4 a;
    a[0] = pack_bf16(v1h, v1h);
    a[1] = pack_bf16(v1 - v1h, v2h);
    a[2] = pack_bf16(v2h, v2 - v2h);
    a[3] = 0u;
    if (h) a = u32x4{0u, 0u, 0u, 0u};
    asm volatile("" : "+v"(a));
    aext = a;
  };
  const char* vbase = ws + OFF_VP + (size_t)tile * 256;
  const int gsl = w < 6 ? w : w - 6;
  const char* gbase = ws + OFF_SG + (size_t)((gsl >> 1) * S + mt * 128 + (gsl & 1) * 64) * 8;
  LAS u64* CS = (LAS u64*)(smem + K2_ST);
  u64 gv;
  f32x2 gp[4];
  constexpr int JG = 9;
  auto pin = [](f32x16& a) { asm volatile("" : "+v"(a)); };
  u32x4 wd[28];
  const char* wdbase = ws + OFF_WD + ((size_t)(2 * dt) * 112 + 14 * w) * 1024 + 4096;
  auto ldwd = [&](auto kI) {
    constexpr int k = decltype(kI)::value, kk = k % 14;
    gload16_off<(kk % 8) * 1024 - 4096>(wd[k], wdbase + (k / 14) * 112 * 1024 + (kk / 8) * 8192, wa);
  };
  constexpr int E12 = EXP_E12, E13 = EXP_E13;
  auto chunk = [&](auto jI, const bf16x8(&cur)[2][3], bf16x8(&nxt)[2][3]) {
    constexpr int j = decltype(jI)::value;
    constexpr int last = (j + K2_AHEAD < 13) ? j + K2_AHEAD : 13;
    constexpr int ex = (j > JG && j + 1 <= JG + K2_AHEAD) ? 5 : 0;
    if constexpr (j < 13) {
      wait_vm_pin<3 * (last - (j + 1)) + 2 + ex>(wf[2 * j], wf[2 * j + 1]);
      if constexpr (j == JG + K2_AHEAD + 1) wait_lgkm0();
      __builtin_amdgcn_sched_barrier(0);
      bar();
    } else {
      wait_vm_pin<E12>(wf[2 * j], wf[2 * j + 1]);
    }
    __builtin_amdgcn_sched_barrier(0);
    static_for<0, 6>([&](auto iI) {
      constexpr int sl = decltype(iI)::value, ks = sl / 3, i = sl % 3;
      acc[i] = mfma32(wf[2 * j + ks], cur[ks][i], acc[i]);
      pin(acc[i]);
      __builtin_amdgcn_sched_barrier(0);
      if constexpr (j == 6 && ks == 0) {
        int a0 = xo[i][0] + 7 * 8192, a1 = xo[i][1] + 7 * 8192;
        asm volatile("" : "+v"(a0), "+v"(a1));
        xo[i][0] = a0;
        xo[i][1] = a1;
      }
      if constexpr (j < 13)
        nxt[ks][i] = tr_frag(smem, xo[i][0] + ks * 4096 + ((j + 1) % 7) * 8192, xo[i][1] + ks * 4096 + ((j + 1) % 7) * 8192);
      if constexpr (j == JG && sl == 0) {
        u64 t;
        asm volatile(SNOP2 "global_load_dwordx2 %0, %1, %2 sc1" : "=v"(t) : "v"((u32)lane * 8), "s"(uni(gbase)) : "memory");
        gv = t;
        static_for<0, 4>([&](auto qI) {
          f32x2 tp;
          asm volatile(SNOP2 "global_load_dwordx2 %0, %1, %2"
                       : "=v"(tp)
                       : "v"((u32)n * 8), "s"(uni(vbase + decltype(qI)::value * 3584 * 8))
                       : "memory");
          gp[decltype(qI)::value] = tp;
        });
      }
      if constexpr ((sl & 1) == 0 && j + K2_AHEAD + 1 <= 13)
        issue1(std::integral_constant<int, 3 * (j + K2_AHEAD + 1) + sl / 2>{});
      if constexpr (j == 12 && sl < E12) ldwd(std::integral_constant<int, sl>{});
      if constexpr (j == 13 && sl < E13) ldwd(std::integral_constant<int, E12 + sl>{});
      if constexpr (j == JG + K2_AHEAD && sl == 3) {
        u64 t = gv;
        f32x2 p0 = gp[0], p1 = gp[1], p2 = gp[2], p3 = gp[3];
        asm volatile("" : "+v"(t), "+v"(p0), "+v"(p1), "+v"(p2), "+v"(p3));
        mk_aext(p0, p1, p2, p3);
        if (!__all((t >> 32) != 0)) {
          for (int spin = 0; spin < (1 << 16); ++spin) {
            __builtin_amdgcn_s_sleep(2);
            t = __hip_atomic_load((gu64*)(size_t)gbase + lane, RLX_AGENT);
            if (__all((t >> 32) != 0)) break;
          }
          __builtin_amdgcn_s_waitcnt(0x0F70);
        }
        CS[gsl * 64 + lane] = t;
      }
      __builtin_amdgcn_sched_barrier(0);
    });
  };
  static_for<0, 14>([&](auto jI) {
    if constexpr (decltype(jI)::value & 1) chunk(jI, xb, xa);
    else chunk(jI, xa, xb);
  });
  YSTAMP(2);
  const rsrc_t rw = mk_rsrc(ws + OFF_ACT + ((size_t)(mt * 4) * 112 + tile) * 1024, 337 * 1024);
  constexpr int PF = 4;
  bf16x8 xr3[PF];
  auto rd3 = [&](auto kI) {
    constexpr int k = decltype(kI)::value;
    if constexpr (k == 14) {
      int a0 = xo[3][0] + 14 * 4096, a1 = xo[3][1] + 14 * 4096;
      asm volatile("" : "+v"(a0), "+v"(a1));
      xo[3][0] = a0;
      xo[3][1] = a1;
    }
    xr3[k % PF] = tr_frag(smem, xo[3][0] + (k % 14) * 4096, xo[3][1] + (k % 14) * 4096);
  };
  u64 g[3];
  auto ldg = [&](auto iI) {
#pragma unroll
    for (int k = 0; k < 3; ++k) g[k] = CS[k * 128 + NTL[decltype(iI)::value] * 32 + n];
  };
  float c1, c2, var;
  u32x4 ov;
  auto corr = [&](auto iI) {
    constexpr int i = decltype(iI)::value;
    const u32x4 bext = {(u32)g[0], (u32)(g[0] >> 32), (u32)g[1], 0u};
    acc[i] = mfma32(aext, __builtin_bit_cast(bf16x8, bext), acc[i]);
    c1 = __builtin_bit_cast(float, (u32)(g[2] >> 32));
    c2 = __builtin_bit_cast(float, (u32)g[2]);
    var = __builtin_bit_cast(float, (u32)(g[1] >> 32));
  };
  auto half_epi = [&](auto iI, auto hI) {
    constexpr int i = decltype(iI)::value, r0 = 4 * decltype(hI)::value;
    float e[4], m[4];
#pragma unroll
    for (int r = 0; r < 4; ++r) {
      const float a = acc[i][r0 + r];
      e[r] = __builtin_amdgcn_exp2f(__builtin_fmaf(a * a, c2, c1) * a);
      m[r] = a * acc[i][r0 + r + 8];
    }
#pragma unroll
    for (int r = 0; r < 4; ++r) m[r] *= __builtin_amdgcn_rcpf(__builtin_fmaf(e[r], var, var));
    ov[r0 / 2] = umin32(pack_bf16(m[0], m[1]));
    ov[r0 / 2 + 1] = umin32(pack_bf16(m[2], m[3]));
  };
  auto put = [&](auto iI) {
    __builtin_amdgcn_raw_buffer_store_b128(ov, rw, lane * 16, NTL[decltype(iI)::value] * 112 * 1024, 16);
  };
  using I0 = std::integral_constant<int, 0>;
  using I1 = std::integral_constant<int, 1>;
  using I2 = std::integral_constant<int, 2>;
  using I3 = std::integral_constant<int, 3>;
  const rsrc_t ra = mk_rsrc(ws + OFF_ACT + ((size_t)(mt * 4 + sh2 * 2) * 112 + 14 * w) * 1024, 126 * 1024);
  u32x4 af[28];
  auto ldact = [&](auto kI) {
    constexpr int k = decltype(kI)::value;
    af[k] = __builtin_amdgcn_raw_buffer_load_b128(ra, lane * 16 + (k % 14) * 1024, (k / 14) * 112 * 1024, ACTAUX);
  };
  float pe, pm, pr;
  auto ep_a = [&](auto iI, auto rI) {
    constexpr int i = decltype(iI)::value, r = decltype(rI)::value;
    const float a = acc[i][r];
    float e = __builtin_amdgcn_exp2f(__builtin_fmaf(a * a, c2, c1) * a), m = a * acc[i][r + 8];
    asm volatile("" : "+v"(e), "+v"(m));
    pe = e;
    pm = m;
  };
  auto ep_b = [&](auto rI) {
    constexpr int r = decltype(rI)::value;
    float o = pm * __builtin_amdgcn_rcpf(__builtin_fmaf(pe, var, var));
    if constexpr (r & 1) {
      u32 t = umin32(pack_bf16(pr, o));
      asm volatile("" : "+v"(t));
      ov[r / 2] = t;
    } else {
      asm volatile("" : "+v"(o));
      pr = o;
    }
  };
  static_for<0, PF - 1>(rd3);
  ldg(I0{});
  corr(I0{});
  ldg(I1{});
  __builtin_amdgcn_sched_barrier(0);
  static_for<0, 28>([&](auto kI) {
    constexpr int k = decltype(kI)::value;
    acc[3] = mfma32(wf[k], xr3[k % PF], acc[3]);
    pin(acc[3]);
    __builtin_amdgcn_sched_barrier(0);
    if constexpr (k % EXP_LOCK == EXP_LOCK - 1) bar();
    if constexpr (k + PF - 1 < 28) rd3(std::integral_constant<int, k + PF - 1>{});
    constexpr int ti = k / 9, u = k % 9;
    if constexpr (ti < 3) {
      using TI = std::integral_constant<int, ti>;
      if constexpr (u >= 1) ep_b(std::integral_constant<int, u - 1>{});
      if constexpr (u < 8) ep_a(TI{}, std::integral_constant<int, u>{});
      if constexpr (u == 8) {
        put(TI{});
        if constexpr (ti < 2) {
          corr(std::integral_constant<int, ti + 1>{});
          ldg(std::integral_constant<int, ti + 2>{});
        }
      }
    }
    if constexpr (k >= 1 && E12 + E13 + k - 1 < 28) ldwd(std::integral_constant<int, E12 + E13 + k - 1>{});
    if constexpr (k >= 21) ldact(std::integral_constant<int, k - 21>{});
    __builtin_amdgcn_sched_barrier(0);
  });
  YSTAMP(3);
  corr(I3{});
  static_for<7, 11>(ldact);
  __builtin_amdgcn_sched_barrier(0);
  half_epi(I3{}, I0{});
  __builtin_amdgcn_sched_barrier(0);
  static_for<11, 14>(ldact);
  __builtin_amdgcn_sched_barrier(0);
  half_epi(I3{}, I1{});
  put(I3{});
  bar();
  YSTAMP(4);
  auto chk = [&](auto gI) {
    constexpr int g = decltype(gI)::value;
    u32 m = 0;
#pragma unroll
    for (int k = 7 * g; k < 7 * g + 7; ++k) m = umax3(umax3(m, af[k][0], af[k][1]), af[k][2], af[k][3]);
    return (bool)__all(m != ~0u);
  };
  auto valid = [&](auto gI) {
    constexpr int g = decltype(gI)::value;
    if (__builtin_expect(!chk(gI), 0)) {
#pragma nounroll
      for (int spin = 0; spin < (1 << 14); ++spin) {
        __builtin_amdgcn_s_sleep(8);
        static_for<7 * g, 7 * g + 7>(ldact);
        if (chk(gI)) break;
      }
      __builtin_amdgcn_s_waitcnt(0x0F70);
    }
    __builtin_amdgcn_sched_barrier(0);
  };
  const int pcol = mt * 128 + sh2 * 64 + n;
  const int dbase = dt * 64 + w * 8 + 4 * h;
  __builtin_amdgcn_sched_barrier(0);
  LAS f32x4* R = (LAS f32x4*)smem;
  f32x16 o[4];
#pragma unroll
  for (int i = 0; i < 4; ++i)
#pragma unroll
    for (int r = 0; r < 16; ++r) o[i][r] = 0.f;
  auto dump1 = [&](auto tjI, auto cI) {
    constexpr int tj = decltype(tjI)::value, c = decltype(cI)::value, i = tj * 2 + (c >> 2), q = c & 3;
    R[((tj * 8 + c) * 8 + w) * 64 + lane] = f32x4{o[i][4 * q], o[i][4 * q + 1], o[i][4 * q + 2], o[i][4 * q + 3]};
  };
  auto step = [&](auto kI) {
    constexpr int k = decltype(kI)::value, tj = k / 14;
    const bf16x8 b = __builtin_bit_cast(bf16x8, af[k]);
    o[2 * tj] = mfma32(wd[k % 14], b, o[2 * tj]);
    pin(o[2 * tj]);
    __builtin_amdgcn_sched_barrier(0);
    if constexpr (k >= 15 && k < 19) dump1(I0{}, std::integral_constant<int, 2 * (k - 15)>{});
    o[2 * tj + 1] = mfma32(wd[14 + k % 14], b, o[2 * tj + 1]);
    pin(o[2 * tj + 1]);
    __builtin_amdgcn_sched_barrier(0);
    if constexpr (k >= 15 && k < 19) dump1(I0{}, std::integral_constant<int, 2 * (k - 15) + 1>{});
    if constexpr (k >= EXP_T1 && k < 14) {
      constexpr int n0 = ((k - EXP_T1) * 14) / (14 - EXP_T1), n1 = ((k + 1 - EXP_T1) * 14) / (14 - EXP_T1);
      static_for<14 + n0, 14 + n1>(ldact);
    }
    __builtin_amdgcn_sched_barrier(0);
  };
  u16 xres[2][4];
  f32x4 gbv;
  auto fin = [&](auto tjI) {
    constexpr int tj = decltype(tjI)::value;
    f32x4 v[8];
#pragma unroll
    for (int sw = 0; sw < 8; ++sw) v[sw] = R[((tj * 8 + w) * 8 + sw) * 64 + lane];
    const f32x4 sum = ((v[0] + v[1]) + (v[2] + v[3])) + ((v[4] + v[5]) + (v[6] + v[7]));
    float r[4];
#pragma unroll
    for (int e = 0; e < 4; ++e) r[e] = __builtin_fmaf(gbv[e], sum[e], bf2f(xres[tj][e]));
    const int pc = pcol + 32 * tj;
    const int srow = (pc & ~7) | ((pc & 3) << 1) | ((pc >> 2) & 1);
    *(u32x2*)(out + (size_t)srow * D + dbase) = u32x2{pack_bf16(r[0], r[1]), pack_bf16(r[2], r[3])};
  };
  valid(I0{});
  asm volatile(""
               : "+v"(wd[0]), "+v"(wd[1]), "+v"(wd[2]), "+v"(wd[3]), "+v"(wd[4]), "+v"(wd[5]), "+v"(wd[6]), "+v"(wd[7]),
                 "+v"(wd[8]), "+v"(wd[9]), "+v"(wd[10]), "+v"(wd[11]), "+v"(wd[12]), "+v"(wd[13])::"memory");
  asm volatile(""
               : "+v"(wd[14]), "+v"(wd[15]), "+v"(wd[16]), "+v"(wd[17]), "+v"(wd[18]), "+v"(wd[19]), "+v"(wd[20]),
                 "+v"(wd[21]), "+v"(wd[22]), "+v"(wd[23]), "+v"(wd[24]), "+v"(wd[25]), "+v"(wd[26]), "+v"(wd[27])::"memory");
  YSTAMP(5);
  XSTAMP(0);
  static_for<0, 3>(step);
  {
    const u16* X1T = (const u16*)(ws + OFF_X1T) + (size_t)dbase * S + pcol;
#pragma unroll
    for (int r = 0; r < 8; ++r) xres[r >> 2][r & 3] = X1T[(size_t)(r & 3) * S + (r >> 2) * 32];
    gbv = *(const f32x4*)((const float*)(ws + OFF_MOD) + 5 * D + dbase);
    __builtin_amdgcn_sched_barrier(0);
  }
  static_for<3, 7>(step);
  XSTAMP(1);
  valid(I1{});
  static_for<7, 14>(step);
  XSTAMP(2);
  valid(I2{});
  XSTAMP(3);
  static_for<14, 21>(step);
  wait_lgkm0();
  bar();
  XSTAMP(4);
  valid(I3{});
  static_for<21, 24>(step);
  fin(I0{});
  __builtin_amdgcn_sched_barrier(0);
  static_for<24, 28>(step);
  XSTAMP(5);
  static_for<0, 8>([&](auto cI) { dump1(I1{}, cI); });
  wait_lgkm0();
  bar();
  STAMP(ws, 2, 6);
  fin(I1{});
}

extern "C" __global__ NOPK __launch_bounds__(512, 2) void k2_ffn(char* __restrict__ ws, u16* __restrict__ out) {
  extern __shared__ __attribute__((aligned(16))) char smem[];
  const int w = __builtin_amdgcn_readfirstlane(threadIdx.x >> 6);
  if (blockIdx.x < 32) k2_stats(ws, smem);
  else k2_gemm(ws, out, smem, w);
}

extern "C" __device__ __attribute__((used, noinline)) void k_code_end() {}

extern "C" void kernel_launch(void* const* d_in, const int* in_sizes, int n_in, void* d_out, int out_size, void* d_ws,
                              size_t ws_size, hipStream_t stream) {
  const u16* q = (const u16*)d_in[0];
  const u16* k = (const u16*)d_in[1];
  const u16* v = (const u16*)d_in[2];
  const u16* shift_k = (const u16*)d_in[3];
  const u16* ssm_k = (const u16*)d_in[4];
  const u16* ssm_D = (const u16*)d_in[5];
  const u16* w_gate = (const u16*)d_in[7];
  const u16* w_up = (const u16*)d_in[8];
  const u16* w_down = (const u16*)d_in[9];
  const u16* c = (const u16*)d_in[10];
  const u16* w_mod = (const u16*)d_in[11];
  char* ws = (char*)d_ws;
  (void)hipFuncSetAttribute((const void*)k1_conv, hipFuncAttributeMaxDynamicSharedMemorySize, K1_LDS);
  (void)hipFuncSetAttribute((const void*)k2_ffn, hipFuncAttributeMaxDynamicSharedMemorySize, K2_LDS);
  k0_prep<<<K0_GRID, 256, 0, stream>>>(q, k, v, shift_k, c, w_mod, ssm_k, ws);
  k1_conv<<<K1_GRID, 256, K1_LDS, stream>>>(ssm_k, ssm_D, w_gate, w_up, w_down, ws);
  k2_ffn<<<256, 512, K2_LDS, stream>>>(ws, (u16*)d_out);
}
```

```cpp
#include <hip/hip_runtime.h>
#include <hip/hip_bf16.h>
#include <stdint.h>
#include <type_traits>

#define NTAUX 0
#define NTLOAD(p) (*(p))
typedef unsigned short u16;
typedef unsigned int u32;
typedef short bf16x8 __attribute__((ext_vector_type(8)));
typedef short s16x4 __attribute__((ext_vector_type(4)));
typedef float f32x2 __attribute__((ext_vector_type(2)));
typedef float f32x4 __attribute__((ext_vector_type(4)));
typedef float f32x16 __attribute__((ext_vector_type(16)));
typedef u32 u32x2 __attribute__((ext_vector_type(2)));
typedef u32 u32x4 __attribute__((ext_vector_type(4)));
typedef __bf16 bf16x2_t __attribute__((ext_vector_type(2)));
#define LAS __attribute__((address_space(3)))
#define DI __device__ __forceinline__

constexpr int S = 2048, D = 448, F = 1792;

constexpr size_t SZ_DS = (size_t)D * S * 2;
constexpr size_t OFF_RE = 0;
constexpr size_t OFF_RO = OFF_RE + SZ_DS;
constexpr size_t OFF_QT = OFF_RO + SZ_DS;
constexpr size_t OFF_X1T = OFF_QT + SZ_DS;
constexpr size_t OFF_MEAN = OFF_X1T + SZ_DS;
constexpr size_t OFF_RSTD = OFF_MEAN + S * 4;
constexpr size_t OFF_MOD = OFF_RSTD + S * 4;
constexpr size_t OFF_SG = OFF_MOD + 16384;
constexpr size_t OFF_CB = OFF_MOD + 12288;

constexpr size_t OFF_VG = OFF_SG + 3 * S * 8;
constexpr size_t OFF_XS = OFF_VG + 4 * S * 8;
constexpr size_t OFF_VP = OFF_XS + SZ_DS;
constexpr size_t OFF_WGU = OFF_VP + 8 * 2 * 3584 * 4;
constexpr size_t OFF_WD = OFF_WGU + (size_t)2 * D * F * 2;
constexpr size_t OFF_ACT = OFF_WD + (size_t)D * F * 2;
constexpr size_t WS_END = OFF_ACT + (size_t)S * F * 2;

#define STAMP_DECL
#define STAMP(wsp, kid, i)
#define STAMP_FLUSH(wsp, kid)
#define HWID(wsp, kid)
#define XSTAMP(i)
#define YSTAMP(i) STAMP(ws, 2, i)
#if defined(__HIP_DEVICE_COMPILE__)
#define NOPK __attribute__((target("no-packed-fp32-ops")))
#else
#define NOPK
#endif

template <int I, int N, class Fn>
DI void static_for(Fn&& f) {
  if constexpr (I < N) {
    f(std::integral_constant<int, I>{});
    static_for<I + 1, N>(f);
  }
}

DI float lo_f(u32 x) { return __builtin_bit_cast(float, x << 16); }
DI float hi_f(u32 x) { return __builtin_bit_cast(float, x & 0xffff0000u); }
DI u32 pack_bf16(float a, float b) {
  f32x2 v = {a, b};
  return __builtin_bit_cast(u32, __builtin_convertvector(v, bf16x2_t));
}
DI float round_bf16(float a) { return lo_f(pack_bf16(a, 0.f)); }
DI float bf2f(u16 x) { return __builtin_bit_cast(float, (u32)x << 16); }

template <int N>
DI void wait_vm() { asm volatile("s_waitcnt vmcnt(%0)" ::"n"(N) : "memory"); }
DI void wait_lgkm0() { asm volatile("s_waitcnt lgkmcnt(0)" ::: "memory"); }
DI void bar() { __builtin_amdgcn_s_barrier(); }

#define SNOP "s_nop 0\n\t"
#define SNOP2 ""
DI const void* uni(const void* p) {
  const unsigned long long v = (unsigned long long)p;
  const u32 lo = __builtin_amdgcn_readfirstlane((u32)v), hi = __builtin_amdgcn_readfirstlane((u32)(v >> 32));
  return (const void*)(((unsigned long long)hi << 32) | lo);
}
DI void glds16_asm(const void* sbase, u32 voff, u32 lds_addr) {
  u32 keep;
  sbase = uni(sbase);
  lds_addr = __builtin_amdgcn_readfirstlane(lds_addr);
  asm volatile("s_mov_b32 %0, m0\n\ts_mov_b32 m0, %3\n\t" SNOP "global_load_lds_dwordx4 %1, %2\n\ts_mov_b32 m0, %0"
               : "=&s"(keep)
               : "v"(voff), "s"(sbase), "s"(lds_addr)
               : "memory");
}
DI void glds16(const void* g, char* lds) {
  __builtin_amdgcn_global_load_lds((const __attribute__((address_space(1))) void*)g, (LAS void*)lds, 16, 0, NTAUX);
}

template <int CTRL>
DI float dpp_add(float v) {
  const int x = __builtin_bit_cast(int, v);
  return v + __builtin_bit_cast(float, __builtin_amdgcn_update_dpp(x, x, CTRL, 0xf, 0xf, false));
}
DI float sum8(float v) {
  v = dpp_add<0xB1>(v);
  v = dpp_add<0x4E>(v);
  v = dpp_add<0x141>(v);
  return v;
}
DI float sum16(float v) { return dpp_add<0x140>(sum8(v)); }
DI float dot2(u32 a, u32 b, float c) {
  return __builtin_amdgcn_fdot2_f32_bf16(__builtin_bit_cast(bf16x2_t, a), __builtin_bit_cast(bf16x2_t, b), c, false);
}
typedef __amdgpu_buffer_rsrc_t rsrc_t;
DI rsrc_t mk_rsrc(const void* p, u32 bytes) { return __builtin_amdgcn_make_buffer_rsrc((void*)p, 0, bytes, 0x00020000); }
DI void st16_sc1(void* p, u32x4 v) { asm volatile("global_store_dwordx4 %0, %1, off sc1\n\ts_nop 1" ::"v"(p), "v"(v) : "memory"); }
DI void st8_sc1(void* p, u32x2 v) { asm volatile("global_store_dwordx2 %0, %1, off sc1" ::"v"(p), "v"(v) : "memory"); }
DI void st4_sc1(void* p, float v) { asm volatile("global_store_dword %0, %1, off sc1" ::"v"(p), "v"(v) : "memory"); }

#define EXP_E12 2
#define EXP_E13 6
#define EXP_LOCK 4
#define EXP_NW2 14
DI bf16x8 tr_frag(const char* smem, int o0, int o1) {
  s16x4 x0 = __builtin_amdgcn_ds_read_tr16_b64_v4i16((LAS s16x4*)(smem + o0));
  s16x4 x1 = __builtin_amdgcn_ds_read_tr16_b64_v4i16((LAS s16x4*)(smem + o1));
  return __builtin_shufflevector(x0, x1, 0, 1, 2, 3, 4, 5, 6, 7);
}
DI f32x16 mfma32(u32x4 a, bf16x8 b, f32x16 c) {
  return __builtin_amdgcn_mfma_f32_32x32x16_bf16(__builtin_bit_cast(bf16x8, a), b, c, 0, 0, 0);
}

DI void retile(int task, const u16* __restrict__ w_gate, const u16* __restrict__ w_up, const u16* __restrict__ w_down,
               char* __restrict__ ws, char* smem, int w, int lane) {
  const int xr = lane >> 4, slot = lane & 15;
  const int chn = slot ^ ((xr << 2) | (w & 3));
  const int h = lane >> 5, blk = (lane >> 4) & 1, qq = (lane & 15) >> 2, p = lane & 3;
  const bool gu = task < 112;
  int kq = 0, fg = 0, dg = 0, fk = 0;
  if (gu) {
    fg = task % 28, kq = task / 28;
    const u16* src = (chn < 8 ? w_gate : w_up) + (size_t)(kq * 112 + 4 * w + xr) * F + fg * 64 + (chn & 7) * 8;
#pragma unroll
    for (int i = 0; i < 7; ++i) glds16(src + (size_t)i * 16 * F, smem + (w + 4 * i) * 1024);
  } else {
    dg = (task - 112) % 7, fk = (task - 112) / 7;
    const u16* src = w_down + (size_t)(fk * 224 + (chn >> 3) * 112 + 4 * w + xr) * D + dg * 64 + (chn & 7) * 8;
#pragma unroll
    for (int i = 0; i < 7; ++i) glds16(src + (size_t)i * 16 * D, smem + (w + 4 * i) * 1024);
  }
  const int col = gu ? blk * 128 + w * 32 + (p & 1) * 16 + (p >> 1) * 8 : (w >> 1) * 128 + (w & 1) * 64 + blk * 32 + p * 8;
  int o[2];
#pragma unroll
  for (int t = 0; t < 2; ++t)
    o[t] = 256 * (8 * h + 4 * t + qq) + 16 * ((col >> 4) ^ ((qq << 2) | ((2 * h + t) & 3))) + (col & 8);
  u32x4* dst = gu ? (u32x4*)(ws + OFF_WGU) + ((size_t)(fg * 4 + w) * 28 + kq * 7) * 64 + lane
                  : (u32x4*)(ws + OFF_WD) + ((size_t)(dg * 2 + (w & 1)) * 112 + fk * 14 + (w >> 1) * 7) * 64 + lane;
  u32x4 bv[7];
  if (gu) {
    const u16* cbp = (const u16*)(ws + OFF_CB) + (lane & 1) * D + kq * 112 + 8 * h;
#pragma unroll
    for (int i = 0; i < 7; ++i) bv[i] = *(const u32x4*)(cbp + 16 * i);
  }
  __syncthreads();
  f32x16 acc;
#pragma unroll
  for (int r = 0; r < 16; ++r) acc[r] = 0.f;
#pragma unroll
  for (int i = 0; i < 7; ++i) {
    const bf16x8 f = tr_frag(smem, o[0] + i * 4096, o[1] + i * 4096);
    st16_sc1(dst + i * 64, __builtin_bit_cast(u32x4, f));
    if (gu) acc = mfma32(bv[i], f, acc);
  }
  if (gu && h == 0) ((f32x2*)(ws + OFF_VP))[kq * 3584 + (fg * 4 + w) * 32 + lane] = f32x2{acc[0], acc[1]};
}

#define EXP_T1 7
#define ACTAUX 16
constexpr int K0_GRID = 336;

extern "C" __global__ __launch_bounds__(256) void k0_prep(const u16* __restrict__ q, const u16* __restrict__ k,
                                               const u16* __restrict__ v, const u16* __restrict__ shift_k,
                                               const u16* __restrict__ c, const u16* __restrict__ w_mod,
                                               const u16* __restrict__ ssm_k, const u16* __restrict__ w_gate,
                                               const u16* __restrict__ w_up, const u16* __restrict__ w_down,
                                               const u16* __restrict__ outp, char* __restrict__ ws) {
  __shared__ float red[4][64];
  const int b = blockIdx.x, tid = threadIdx.x, lane = tid & 63, w = __builtin_amdgcn_readfirstlane(tid >> 6);
  STAMP_DECL;
  STAMP(ws, 0, 0);
  const int xcd = b & 7, idx = b >> 3;
  u32 cwarm = 0;
  if (idx == 40 || idx == 41) {
    u32 lo, hi, len;
    asm volatile(
        "s_getpc_b64 s[40:41]\n\ts_add_u32 s40, s40, k1_conv@rel32@lo+4\n\ts_addc_u32 s41, s41, k1_conv@rel32@hi+12\n\t"
        "s_mov_b32 %0, s40\n\ts_mov_b32 %1, s41\n\ts_mov_b32 %2, k_code_end-k1_conv"
        : "=s"(lo), "=s"(hi), "=s"(len)::"s40", "s41", "scc");
    const char* code = (const char*)(((unsigned long long)hi << 32) | lo);
    if ((u32)((idx - 40) * 256 + tid) * 128 < len) cwarm = *(const u32*)(code + ((idx - 40) * 256 + tid) * 128);
  }
  u32 twarm = 0;
  if (idx == 38 || idx == 39) {
    const int t = (idx - 38) * 256 + tid;
    const char* p = nullptr;
    if (t < 25) p = (const char*)w_gate + t * 65536;
    else if (t < 50) p = (const char*)w_up + (t - 25) * 65536;
    else if (t < 75) p = (const char*)w_down + (t - 50) * 65536;
    else if (t < 103) p = (const char*)outp + (t - 75) * 65536;
    else if ((size_t)(t - 103) * 65536 < WS_END) p = ws + (size_t)(t - 103) * 65536;
    if (p) twarm = NTLOAD((const u32*)p);
  }
  cwarm += twarm;
  int misc = -1;
  if (xcd == 7) misc = idx < 14 ? 64 + idx : (idx < 36 ? idx - 14 : 1000);
  else if (idx < 4) misc = 64 + 14 + xcd * 4 + idx;
  else if (idx >= 36) misc = 22 + xcd * 6 + (idx - 36);
  if (misc < 0) {
    const int rb = idx - 4;
    const int sq = lane >> 5, cp = lane & 31;
    const int d0 = xcd * 64 + cp * 2;
    const int a = rb * 8 + w * 2 + sq;
    const int s0 = a * 8;
    const rsrc_t rk = mk_rsrc(k, S * D * 2), rv = mk_rsrc(v, S * D * 2), rq = mk_rsrc(q, S * D * 2);
    const int oB = (s0 * D + d0) * 2;
    const int oA = oB - 4 * D * 2, oC = oB + 4 * D * 2;
    u32 kk[12], vv[9], qq[8];
#pragma unroll
    for (int i = 0; i < 4; ++i) kk[i] = __builtin_amdgcn_raw_buffer_load_b32(rk, oA + i * D * 2, 0, NTAUX);
#pragma unroll
    for (int i = 0; i < 4; ++i) kk[4 + i] = __builtin_amdgcn_raw_buffer_load_b32(rk, oB + i * D * 2, 0, NTAUX);
#pragma unroll
    for (int i = 0; i < 4; ++i) kk[8 + i] = __builtin_amdgcn_raw_buffer_load_b32(rk, oC + i * D * 2, 0, NTAUX);
    vv[0] = __builtin_amdgcn_raw_buffer_load_b32(rv, oA + 3 * D * 2, 0, NTAUX);
#pragma unroll
    for (int i = 0; i < 4; ++i) vv[1 + i] = __builtin_amdgcn_raw_buffer_load_b32(rv, oB + i * D * 2, 0, NTAUX);
#pragma unroll
    for (int i = 0; i < 4; ++i) vv[5 + i] = __builtin_amdgcn_raw_buffer_load_b32(rv, oC + i * D * 2, 0, NTAUX);
#pragma unroll
    for (int i = 0; i < 4; ++i) qq[i] = __builtin_amdgcn_raw_buffer_load_b32(rq, oB + i * D * 2, 0, NTAUX);
#pragma unroll
    for (int i = 0; i < 4; ++i) qq[4 + i] = __builtin_amdgcn_raw_buffer_load_b32(rq, oC + i * D * 2, 0, NTAUX);
    const u32x4 sk = *(const u32x4*)(shift_k + d0 * 4);
    u32 warm = 0;
    if (w == 0) warm = NTLOAD((const u32*)((const char*)ssm_k + ((size_t)(xcd * 32 + rb) * 64 + lane) * 128));
    f32x2 wt[4] = {{lo_f(sk[0]), lo_f(sk[2])}, {hi_f(sk[0]), hi_f(sk[2])}, {lo_f(sk[1]), lo_f(sk[3])}, {hi_f(sk[1]), hi_f(sk[3])}};
    f32x2 kf[12], u[9];
#pragma unroll
    for (int i = 0; i < 12; ++i) kf[i] = f32x2{lo_f(kk[i]), hi_f(kk[i])};
#pragma unroll
    for (int i = 0; i < 9; ++i) {
      f32x2 t = kf[i + 3] * wt[0];
      t = __builtin_elementwise_fma(kf[i + 2], wt[1], t);
      t = __builtin_elementwise_fma(kf[i + 1], wt[2], t);
      t = __builtin_elementwise_fma(kf[i], wt[3], t);
      u[i] = t * f32x2{lo_f(vv[i]), hi_f(vv[i])};
    }
    {
      char* dst = ws + (size_t)d0 * (S * 2) + (2040 - s0) * 2;
#pragma unroll
      for (int cI = 0; cI < 2; ++cI) {
        *(u32x4*)(dst + OFF_RO + cI * S * 2) = u32x4{pack_bf16(u[8][cI], u[7][cI]), pack_bf16(u[6][cI], u[5][cI]),
                                                     pack_bf16(u[4][cI], u[3][cI]), pack_bf16(u[2][cI], u[1][cI])};
        *(u32x4*)(dst + OFF_RE + cI * S * 2) = u32x4{pack_bf16(u[7][cI], u[6][cI]), pack_bf16(u[5][cI], u[4][cI]),
                                                     pack_bf16(u[3][cI], u[2][cI]), pack_bf16(u[1][cI], u[0][cI])};
      }
      char* dq = ws + OFF_QT + (size_t)d0 * (S * 2) + s0 * 2;
#pragma unroll
      for (int cI = 0; cI < 2; ++cI) {
        const u32 sel = cI ? 0x07060302u : 0x05040100u;
        *(u32x4*)(dq + cI * S * 2) = u32x4{__builtin_amdgcn_perm(qq[2], qq[0], sel), __builtin_amdgcn_perm(qq[6], qq[4], sel),
                                           __builtin_amdgcn_perm(qq[3], qq[1], sel), __builtin_amdgcn_perm(qq[7], qq[5], sel)};
      }
    }
    asm volatile("" ::"v"(warm));
  } else if (misc < 64) {
    const int bb = misc;
    const int row = bb * 32 + w * 8 + (lane >> 3);
    const int sub = lane & 7;
    u32x4 x[7];
#pragma unroll
    for (int i = 0; i < 7; ++i) x[i] = NTLOAD((const u32x4*)(q + (size_t)row * D + (i * 8 + sub) * 8));
    if (sub < 3) ((unsigned long long*)(ws + OFF_SG))[sub * S + row] = 0ull;
    float s = 0.f, s2 = 0.f;
#pragma unroll
    for (int i = 0; i < 7; ++i)
#pragma unroll
      for (int j = 0; j < 4; ++j) {
        const u32 t = x[i][j];
        s = dot2(t, 0x3f803f80u, s);
        s2 = dot2(t, t, s2);
      }
    s = sum8(s);
    s2 = sum8(s2);
    const float mean = s * (1.f / D);
    float var = s2 * (1.f / D) - mean * mean;
    var = var < 0.f ? 0.f : var;
    const float rstd = __builtin_amdgcn_rsqf(var + 1e-5f);
    if (sub < 2) {
      const int p = (row & ~7) | ((row & 1) << 2) | ((row >> 1) & 3);
      ((float*)(ws + OFF_MEAN))[sub * S + p] = sub ? rstd : mean;
    }
  } else if (misc < 106) {
    const int bb = misc - 64;
    const int co = lane >> 3, rs = lane & 7;
    const int col0 = bb * 64 + co * 8;
    u32x4 x[14];
#pragma unroll
    for (int i = 0; i < 14; ++i)
      x[i] = NTLOAD((const u32x4*)(w_mod + (size_t)(w * 112 + rs * 14 + i) * (6 * D) + col0));
    u32 cv[7];
#pragma unroll
    for (int i = 0; i < 7; ++i) cv[i] = *(const u32*)(c + w * 112 + rs * 14 + 2 * i);
    __builtin_amdgcn_sched_barrier(0);
    f32x2 acc[4] = {{0.f, 0.f}, {0.f, 0.f}, {0.f, 0.f}, {0.f, 0.f}};
#pragma unroll
    for (int i = 0; i < 14; ++i) {
      const float cf = (i & 1) ? hi_f(cv[i >> 1]) : lo_f(cv[i >> 1]);
      const float sl = round_bf16(cf * __builtin_amdgcn_rcpf(1.f + __builtin_amdgcn_exp2f(cf * -1.44269504f)));
#pragma unroll
      for (int j = 0; j < 4; ++j)
        acc[j] = __builtin_elementwise_fma(f32x2{lo_f(x[i][j]), hi_f(x[i][j])}, f32x2{sl, sl}, acc[j]);
    }
    float sel = 0.f;
#pragma unroll
    for (int j = 0; j < 8; ++j) {
      const float t = sum8(acc[j >> 1][j & 1]);
      sel = (rs == j) ? t : sel;
    }
    red[w][lane] = sel;
    __syncthreads();
    if (tid < 64) {
      const float t = (red[0][tid] + red[1][tid]) + (red[2][tid] + red[3][tid]);
      const float m = round_bf16(t);
      const int col = bb * 64 + tid;
      ((float*)(ws + OFF_MOD))[col] = m;
      if (col >= 3 * D && col < 5 * D) {
        const bool isc = col >= 4 * D;
        ((u16*)(ws + OFF_CB))[isc ? col - 4 * D : col - 2 * D] = (u16)pack_bf16(isc ? 1.f + m : m, 0.f);
      }
    }
  }
  asm volatile("" ::"v"(cwarm));
  STAMP(ws, 0, 1);
  STAMP_FLUSH(ws, misc < 0 ? 0 : (misc < 64 ? 4 : 5));
}

constexpr int K1_KSTR = 260 * 16;
constexpr int K1_USTR = 286 * 16;
constexpr int K1_UOFF = 4 * K1_KSTR;
constexpr int K1_XOFF = K1_UOFF + 2 * K1_USTR;
constexpr int K1_CH = K1_XOFF + 8192;
constexpr int K1_LDS = 2 * K1_CH;

DI void glds16x4_asm(const void* sbase, u32 voff, u32 voff_last, u32 lds_addr) {
  u32 keep;
  sbase = uni(sbase);
  lds_addr = __builtin_amdgcn_readfirstlane(lds_addr);
  asm volatile(
      "s_mov_b32 %0, m0\n\ts_mov_b32 m0, %4\n\t" SNOP
      "global_load_lds_dwordx4 %1, %3\n\t"
      "global_load_lds_dwordx4 %1, %3 offset:1024\n\t"
      "global_load_lds_dwordx4 %1, %3 offset:2048\n\t"
      "global_load_lds_dwordx4 %2, %3 offset:3072\n\t"
      "s_mov_b32 m0, %0"
      : "=&s"(keep)
      : "v"(voff), "v"(voff_last), "s"(sbase), "s"(lds_addr)
      : "memory");
}
#define CAS __attribute__((address_space(4)))
template <int half>
DI void k1_conv_wave(const u16* __restrict__ ssm_k, const u16* __restrict__ ssm_D, char* __restrict__ ws, char* smem,
                     int ch, int lane) {
  STAMP_DECL;
  STAMP(ws, 1, 0);
  const int cpair = (blockIdx.x & 7) * 32 + (blockIdx.x >> 3);
  const int d = 2 * cpair + ch;
  char* L = smem + ch * K1_CH;
  const u32 L0 = (u32)(size_t)(LAS char*)L;
  const u16* kf = ssm_k + (size_t)d * S;
  {
    const u32 vo = lane * 16, vl = (lane > 62 ? 62 : lane) * 16;
    glds16x4_asm(kf + (2 + 4 * half), vo, vl, L0 + (2 * half) * K1_KSTR + 64);
    glds16x4_asm(kf + (4 + 4 * half), vo, vl, L0 + (2 * half + 1) * K1_KSTR + 64);
    glds16x4_asm(ws + (half ? OFF_RO : OFF_RE) + (size_t)d * S * 2, vo, vo, L0 + K1_UOFF + half * K1_USTR);
  }
  const int n = lane & 15, q4 = lane >> 4;
  const int pbase = 32 * (n >> 1) + 8 * q4 + 4 * (n & 1) + 1024 * half;
  u32 head = 0;
  const int di = n + q4 - 15;
  if constexpr (half == 0) {
    asm volatile(SNOP2 "global_load_dword %0, %1, %2" : "=v"(head) : "v"((u32)(di < 0 ? 0 : di) * 4), "s"(uni(kf)) : "memory");
  }
  const CAS float* MODc = (const CAS float*)(size_t)(ws + OFF_MOD);
  const float sa = MODc[d], ca = MODc[D + d], ga = MODc[2 * D + d], cbv = MODc[4 * D + d];
  const u32 Draw = ((const CAS u32*)(size_t)ssm_D)[cpair];
  if constexpr (half == 1) {
    const int l5 = lane & 31;
    if (l5 < 28) *(LAS u32x4*)(L + K1_UOFF + (lane >> 5) * K1_USTR + (256 + l5) * 16) = u32x4{0u, 0u, 0u, 0u};
  }
  u32 Ab = L0 + (n & 3) * K1_KSTR + ((n >> 2) + q4 + 16 * half) * 16;
  u32 Bb = L0 + K1_UOFF + (n & 1) * K1_USTR + (28 - 4 * (n >> 1) + q4 + 16 * half) * 16;
  u32 Hd = L0 + q4 * K1_KSTR + n * 4;
  asm volatile("" : "+v"(Ab), "+v"(Bb), "+v"(Hd) : "s"(sa), "s"(ca), "s"(ga), "s"(Draw), "s"(cbv));
  asm volatile("s_waitcnt vmcnt(0)" : "+v"(head)::"memory");
  if constexpr (half == 0) *(LAS u32*)(size_t)Hd = di < 0 ? 0u : head;
  wait_lgkm0();
  STAMP(ws, 1, 1);
  bar();
  STAMP(ws, 1, 2);
  bf16x8 fa[2][8], fb[2][8];
  auto rd = [&](auto dI) {
    constexpr int dl = decltype(dI)::value;
#pragma unroll
    for (int g = 0; g < 8; ++g) {
      fa[dl & 1][g] = *(const LAS bf16x8*)(size_t)(Ab + 64 * dl + 512 * g);
      fb[dl & 1][g] = *(const LAS bf16x8*)(size_t)(Bb + 64 * dl + 512 * (7 - g));
    }
  };
  rd(std::integral_constant<int, 0>{});
  __builtin_amdgcn_sched_barrier(0);
  u32x2 qv[4];
  f32x4 mu[4], rs[4];
  asm volatile(SNOP2
               "global_load_dwordx2 %0, %12, %14\n\t"
               "global_load_dwordx2 %1, %12, %14 offset:512\n\t"
               "global_load_dwordx2 %2, %12, %14 offset:1024\n\t"
               "global_load_dwordx2 %3, %12, %14 offset:1536\n\t"
               "global_load_dwordx4 %4, %13, %15\n\t"
               "global_load_dwordx4 %5, %13, %15 offset:1024\n\t"
               "global_load_dwordx4 %6, %13, %15 offset:2048\n\t"
               "global_load_dwordx4 %7, %13, %15 offset:3072\n\t"
               "global_load_dwordx4 %8, %13, %16\n\t"
               "global_load_dwordx4 %9, %13, %16 offset:1024\n\t"
               "global_load_dwordx4 %10, %13, %16 offset:2048\n\t"
               "global_load_dwordx4 %11, %13, %16 offset:3072"
               : "=&v"(qv[0]), "=&v"(qv[1]), "=&v"(qv[2]), "=&v"(qv[3]), "=&v"(mu[0]), "=&v"(mu[1]), "=&v"(mu[2]),
                 "=&v"(mu[3]), "=&v"(rs[0]), "=&v"(rs[1]), "=&v"(rs[2]), "=&v"(rs[3])
               : "v"((u32)pbase * 2), "v"((u32)pbase * 4), "s"(uni(ws + OFF_QT + (size_t)d * S * 2)),
                 "s"(uni(ws + OFF_MEAN)), "s"(uni(ws + OFF_RSTD))
               : "memory");
  f32x4 acc[8];
#pragma unroll
  for (int j = 0; j < 8; ++j) acc[j] = f32x4{0.f, 0.f, 0.f, 0.f};
  f32x4 qf[4], hg[4], kd[4];
  u32x4 uc[4];
  static_for<0, 4>([&](auto dI) {
    constexpr int dl = decltype(dI)::value;
    if constexpr (dl < 3) rd(std::integral_constant<int, dl + 1>{});
    if constexpr (dl == 2) {
      asm volatile("s_waitcnt vmcnt(0)"
                   : "+v"(qv[0]), "+v"(qv[1]), "+v"(qv[2]), "+v"(qv[3]), "+v"(mu[0]), "+v"(mu[1]), "+v"(mu[2]), "+v"(mu[3]),
                     "+v"(rs[0]), "+v"(rs[1]), "+v"(rs[2]), "+v"(rs[3]));
    }
    if constexpr (dl == 1) {
#pragma unroll
      for (int j = 0; j < 4; ++j)
        uc[j] = *(const LAS u32x4*)(L + K1_UOFF + K1_USTR + (255 - (32 * (4 * half + j) + 4 * (n >> 1) + q4)) * 16);
    }
    if constexpr (dl >= 2) {
      const int sh = (n & 1) ? 16 : 0;
      const float gc = ga * round_bf16(1.f + ca), gs = ga * sa, Dd = ch ? hi_f(Draw) : lo_f(Draw);
#pragma unroll
      for (int j = 2 * (dl - 2); j < 2 * (dl - 2) + 2; ++j) {
#pragma unroll
        for (int r = 0; r < 4; ++r) {
          kd[j][r] = __builtin_bit_cast(float, (uc[j][3 - r] << sh) & 0xffff0000u) * Dd;
          qf[j][r] = (r & 1) ? hi_f(qv[j][r >> 1]) : lo_f(qv[j][r >> 1]);
          hg[j][r] = (qf[j][r] - mu[j][r]) * rs[j][r] * gc + gs;
        }
        asm volatile("" : "+v"(kd[j]), "+v"(qf[j]), "+v"(hg[j]));
      }
    }
#pragma unroll
    for (int sdiag = 0; sdiag < 8; ++sdiag)
#pragma unroll
      for (int g = 0; g <= sdiag; ++g)
        acc[sdiag] = __builtin_amdgcn_mfma_f32_16x16x32_bf16(fa[dl & 1][g], fb[dl & 1][sdiag - g], acc[sdiag], 0, 0, 0);
    if constexpr (dl == 0) {
#pragma unroll
      for (int i = 0; i < 16; ++i) {
        __builtin_amdgcn_sched_group_barrier(0x008, 2, 0);
        __builtin_amdgcn_sched_group_barrier(0x100, 1, 0);
      }
    } else if constexpr (dl == 1) {
#pragma unroll
      for (int i = 0; i < 16; ++i) {
        __builtin_amdgcn_sched_group_barrier(0x008, 2, 0);
        __builtin_amdgcn_sched_group_barrier(0x100, 1, 0);
      }
#pragma unroll
      for (int i = 0; i < 4; ++i) {
        __builtin_amdgcn_sched_group_barrier(0x008, 1, 0);
        __builtin_amdgcn_sched_group_barrier(0x100, 1, 0);
      }
    } else {
      __builtin_amdgcn_sched_group_barrier(0x008, 4, 0);
#pragma unroll
      for (int i = 0; i < 16; ++i) {
        __builtin_amdgcn_sched_group_barrier(0x008, 1, 0);
        __builtin_amdgcn_sched_group_barrier(0x002, 2, 0);
        __builtin_amdgcn_sched_group_barrier(0x008, 1, 0);
        __builtin_amdgcn_sched_group_barrier(0x002, 2, 0);
        __builtin_amdgcn_sched_group_barrier(0x100, 1, 0);
      }
    }
    __builtin_amdgcn_sched_barrier(0);
  });
  STAMP(ws, 1, 3);
  LAS f32x4* X = (LAS f32x4*)(L + K1_XOFF);
#pragma unroll
  for (int j = 0; j < 4; ++j) X[half * 256 + j * 64 + lane] = acc[4 * (1 - half) + j];
  wait_lgkm0();
  bar();
  STAMP(ws, 1, 4);
  u16* X1T = (u16*)(ws + OFF_X1T) + (size_t)d * S + pbase;
  u16* XS = (u16*)(ws + OFF_XS) + (size_t)d * S + pbase;
  const float c1 = round_bf16(1.f + cbv);
#pragma unroll
  for (int j = 0; j < 4; ++j) {
    const f32x4 y = X[(1 - half) * 256 + j * 64 + lane];
    const f32x4 o = qf[j] + hg[j] * ((acc[4 * half + j] + kd[j]) + y);
    const u32 p0 = pack_bf16(o[0], o[1]), p1 = pack_bf16(o[2], o[3]);
    st8_sc1(XS + 256 * j, u32x2{pack_bf16(lo_f(p0) * c1, hi_f(p0) * c1), pack_bf16(lo_f(p1) * c1, hi_f(p1) * c1)});
    st8_sc1(X1T + 256 * j, u32x2{p0, p1});
  }
  STAMP(ws, 1, 5);
  STAMP_FLUSH(ws, 1);
}

#define EXP_DELAY 40
constexpr int K1_GRID = 456;
extern "C" __global__ NOPK __launch_bounds__(256, 2) void k1_conv(const u16* __restrict__ ssm_k,
                                                                  const u16* __restrict__ ssm_D,
                                                                  const u16* __restrict__ w_gate,
                                                                  const u16* __restrict__ w_up,
                                                                  const u16* __restrict__ w_down, char* __restrict__ ws) {
  extern __shared__ __attribute__((aligned(16))) char smem[];
  const int tid = threadIdx.x, lane = tid & 63;
  const int w = __builtin_amdgcn_readfirstlane(tid >> 6);
  const int xcd = blockIdx.x & 7, idx = blockIdx.x >> 3;
  if (xcd < 7 && idx < 32) {
    if (w & 1) k1_conv_wave<1>(ssm_k, ssm_D, ws, smem, w >> 1, lane);
    else k1_conv_wave<0>(ssm_k, ssm_D, ws, smem, w >> 1, lane);
  } else {
    STAMP_DECL;
    STAMP(ws, 7, 0);
    if (xcd == 7 && idx >= 42 && idx < 50) return;
    if (xcd < 7) __builtin_amdgcn_s_sleep(EXP_DELAY);
    if (idx >= 50) {
      u32x4* ap = (u32x4*)(ws + OFF_ACT) + (size_t)(blockIdx.x - 400) * 8192 + tid;
      const u32x4 sent = {~0u, ~0u, ~0u, ~0u};
#pragma unroll 8
      for (int i = 0; i < 32; ++i) st16_sc1(ap + i * 256, sent);
      wait_vm<0>();
    } else {
      retile(xcd == 7 ? idx : 42 + xcd * 18 + (idx - 32), w_gate, w_up, w_down, ws, smem, w, lane);
    }
    STAMP(ws, 7, 1);
    STAMP_FLUSH(ws, 7);
  }
}

constexpr int K2_XB = D * 256;
constexpr int K2_ST = K2_XB;
constexpr int K2_LDS = 131072;

DI void gload16_asm(u32x4& dst, const void* sbase, u32 voff) {
  sbase = uni(sbase);
  asm volatile(SNOP2 "global_load_dwordx4 %0, %1, %2" : "=v"(dst) : "v"(voff), "s"(sbase) : "memory");
}
DI u32 umin32(u32 v) { return v < 0xFFFFFFFEu ? v : 0xFFFFFFFEu; }
DI u32 umax3(u32 a, u32 b, u32 c) { const u32 t = a > b ? a : b; return t > c ? t : c; }
template <int OFF>
DI void gload16_off(u32x4& dst, const void* sbase, u32 voff) {
  sbase = uni(sbase);
  asm volatile(SNOP2 "global_load_dwordx4 %0, %1, %2 offset:%3" : "=v"(dst) : "v"(voff), "s"(sbase), "n"(OFF) : "memory");
}
DI void glds16_nom0(const void* sbase, u32 voff, u32 lds_addr) {
  sbase = uni(sbase);
  lds_addr = __builtin_amdgcn_readfirstlane(lds_addr);
  asm volatile("s_mov_b32 m0, %2\n\t" SNOP "global_load_lds_dwordx4 %0, %1" ::"v"(voff), "s"(sbase), "s"(lds_addr) : "memory");
}
template <int N>
DI void wait_vm_pin(u32x4& a, u32x4& b) {
  asm volatile("s_waitcnt vmcnt(%2)" : "+v"(a), "+v"(b) : "n"(N) : "memory");
}
DI float xor32(float v) {
  const u32 u = __builtin_bit_cast(u32, v);
  auto r = __builtin_amdgcn_permlane32_swap(u, u, false, false);
  const u32 a = r[0], b = r[1];
  return __builtin_bit_cast(float, (threadIdx.x & 32) ? a : b);
}

typedef unsigned long long u64;
typedef __attribute__((address_space(1))) u64 gu64;
typedef __attribute__((address_space(1))) u32 gu32;
#define RLX_AGENT __ATOMIC_RELAXED, __HIP_MEMORY_SCOPE_AGENT
#define EXP_AHEAD 2
constexpr int K2_AHEAD = EXP_AHEAD;

DI void k2_stats(char* __restrict__ ws, char* smem) {
  const int tid = threadIdx.x, lane = tid & 63;
  const int w = __builtin_amdgcn_readfirstlane(tid >> 6);
  const int n = lane & 31, h = lane >> 5;
  STAMP_DECL;
  STAMP(ws, 2, 0);
  {
    const int p0 = blockIdx.x * 64;
    const u32* src = (const u32*)(ws + OFF_X1T) + ((size_t)(w * 56 + h) * S + p0) / 2 + n;
    u32 x[28];
#pragma unroll
    for (int i = 0; i < 28; ++i) x[i] = src[(size_t)i * S];
    float s0 = 0.f, s1 = 0.f, q0 = 0.f, q1 = 0.f;
#pragma unroll
    for (int i = 0; i < 28; ++i) {
      const float a = lo_f(x[i]), b = hi_f(x[i]);
      s0 += a;
      s1 += b;
      q0 = __builtin_fmaf(a, a, q0);
      q1 = __builtin_fmaf(b, b, q1);
    }
    *(LAS f32x4*)(smem + ((w * 2 + h) * 32 + n) * 16) = f32x4{s0, q0, s1, q1};
    __syncthreads();
    if (tid < 64) {
      float sm = 0.f, sq = 0.f;
#pragma unroll
      for (int i = 0; i < 16; ++i) {
        const f32x2 t = *(const LAS f32x2*)(smem + (i * 64 + tid) * 8);
        sm += t[0];
        sq += t[1];
      }
      const float mean = sm * (1.f / D);
      float var = sq * (1.f / D) - mean * mean;
      var = (var < 0.f ? 0.f : var) + 1e-5f;
      const float rstd = 1.f / sqrtf(var);
      const float sig = var * rstd;
      const float mh = round_bf16(mean), sh = round_bf16(sig);
      const u32 b0 = pack_bf16(-mh, mh - mean), b1 = pack_bf16(-mh, sh), b2 = pack_bf16(sig - sh, sh);
      const float c1 = -2.302208198f * rstd;
      const float c2 = -0.1029432397f * (rstd * rstd * rstd);
      gu64* G = (gu64*)(size_t)(ws + OFF_SG) + p0 + tid;
      __hip_atomic_store(G, ((u64)b1 << 32) | b0, RLX_AGENT);
      __hip_atomic_store(G + S, ((u64)__builtin_bit_cast(u32, var) << 32) | b2, RLX_AGENT);
      __hip_atomic_store(G + 2 * S, ((u64)__builtin_bit_cast(u32, c1) << 32) | __builtin_bit_cast(u32, c2), RLX_AGENT);
    }
    STAMP(ws, 6, 1);
    STAMP(ws, 6, 2);
    STAMP_FLUSH(ws, 6);
  }
}

DI void k2_gemm(char* __restrict__ ws, u16* __restrict__ out, char* smem, const int w) {
  const int tid = threadIdx.x, lane = tid & 63;
  const int n = lane & 31, h = lane >> 5;
  STAMP_DECL;
  STAMP(ws, 2, 0);
  const int bid = blockIdx.x - 32;
  const int xcd = bid & 7, idx = bid >> 3;
  const int mt = (xcd >> 1) * 4 + (idx & 3);
  const int dt = idx >> 2, sh2 = xcd & 1;
  const int ft = sh2 * 7 + dt;
  const int tile = ft * 8 + w;
  const u32 lds0 = (u32)(size_t)(LAS char*)smem;
  u32x4 wf[28];
  const int xr = lane >> 4, slot = lane & 15;
  const char* xbase = ws + OFF_XS + ((size_t)(4 * w) * S + mt * 128) * 2;
  const u32 xoff = (u32)(xr * S + (slot ^ ((xr << 2) | (w & 3))) * 8) * 2;
  const char* wbase = ws + OFF_WGU + (size_t)(tile * 28) * 1024 + 4096;
  const u32 wa = lane * 16;
  auto issue1 = [&](auto qI) {
    constexpr int q = decltype(qI)::value, j = q / 3, r = q % 3;
    if constexpr (r == 0) glds16_nom0(xbase, xoff + j * (32 * S * 2), lds0 + (32 * j + 4 * w) * 256);
    if constexpr (r != 0) {
      constexpr int k = 2 * j + r - 1;
      gload16_off<(k % 8) * 1024 - 4096>(wf[k], wbase + (k / 8) * 8192, wa);
    }
  };
  static_for<0, 3>(issue1);
  static_for<3, 3 * (K2_AHEAD + 1)>(issue1);
  constexpr int NTL[4] = {0, 2, 1, 3};
  int xo[4][2];
  {
    const int blk = (lane >> 4) & 1, qq = (lane & 15) >> 2, p = lane & 3;
#pragma unroll
    for (int i = 0; i < 4; ++i) {
#pragma unroll
      for (int t = 0; t < 2; ++t) {
        const int row = 8 * h + 4 * t + qq;
        const int chn = 4 * NTL[i] + 2 * blk + (p >> 1);
        xo[i][t] = 256 * row + 16 * (chn ^ ((qq << 2) | ((2 * h + t) & 3))) + 8 * (p & 1);
      }
    }
  }
  f32x16 acc[4];
#pragma unroll
  for (int i = 0; i < 4; ++i)
#pragma unroll
    for (int r = 0; r < 16; ++r) acc[i][r] = 0.f;
  bf16x8 xa[2][3], xb[2][3];
  asm volatile("" : "+v"(xo[0][0]), "+v"(xo[0][1]), "+v"(xo[1][0]), "+v"(xo[1][1]), "+v"(xo[2][0]), "+v"(xo[2][1]),
               "+v"(xo[3][0]), "+v"(xo[3][1]));
  wait_vm<3 * K2_AHEAD + 2>();
  __builtin_amdgcn_sched_barrier(0);
  bar();
  YSTAMP(1);
#pragma unroll
  for (int ks = 0; ks < 2; ++ks)
#pragma unroll
    for (int i = 0; i < 3; ++i) xa[ks][i] = tr_frag(smem, xo[i][0] + ks * 4096, xo[i][1] + ks * 4096);
  __builtin_amdgcn_sched_barrier(0);
  u32x4 aext;
  auto mk_aext = [&](f32x2 p0, f32x2 p1, f32x2 p2, f32x2 p3) {
    const float v1 = (p0[0] + p1[0]) + (p2[0] + p3[0]);
    const float v2 = (p0[1] + p1[1]) + (p2[1] + p3[1]);
    const float v1h = round_bf16(v1), v2h = round_bf16(v2);
    u32x4 a;
    a[0] = pack_bf16(v1h, v1h);
    a[1] = pack_bf16(v1 - v1h, v2h);
    a[2] = pack_bf16(v2h, v2 - v2h);
    a[3] = 0u;
    if (h) a = u32x4{0u, 0u, 0u, 0u};
    asm volatile("" : "+v"(a));
    aext = a;
  };
  const char* vbase = ws + OFF_VP + (size_t)tile * 256;
  const int gsl = w < 6 ? w : w - 6;
  const char* gbase = ws + OFF_SG + (size_t)((gsl >> 1) * S + mt * 128 + (gsl & 1) * 64) * 8;
  LAS u64* CS = (LAS u64*)(smem + K2_ST);
  u64 gv;
  f32x2 gp[4];
  constexpr int JG = 9;
  auto pin = [](f32x16& a) { asm volatile("" : "+v"(a)); };
  u32x4 wd[28];
  const char* wdbase = ws + OFF_WD + ((size_t)(2 * dt) * 112 + 14 * w) * 1024 + 4096;
  auto ldwd = [&](auto kI) {
    constexpr int k = decltype(kI)::value, kk = k % 14;
    gload16_off<(kk % 8) * 1024 - 4096>(wd[k], wdbase + (k / 14) * 112 * 1024 + (kk / 8) * 8192, wa);
  };
  constexpr int E12 = EXP_E12, E13 = EXP_E13;
  auto chunk = [&](auto jI, const bf16x8(&cur)[2][3], bf16x8(&nxt)[2][3]) {
    constexpr int j = decltype(jI)::value;
    constexpr int last = (j + K2_AHEAD < 13) ? j + K2_AHEAD : 13;
    constexpr int ex = (j > JG && j + 1 <= JG + K2_AHEAD) ? 5 : 0;
    if constexpr (j < 13) {
      wait_vm_pin<3 * (last - (j + 1)) + 2 + ex>(wf[2 * j], wf[2 * j + 1]);
      if constexpr (j == JG + K2_AHEAD + 1) wait_lgkm0();
      __builtin_amdgcn_sched_barrier(0);
      bar();
    } else {
      wait_vm_pin<E12>(wf[2 * j], wf[2 * j + 1]);
    }
    __builtin_amdgcn_sched_barrier(0);
    static_for<0, 6>([&](auto iI) {
      constexpr int sl = decltype(iI)::value, ks = sl / 3, i = sl % 3;
      acc[i] = mfma32(wf[2 * j + ks], cur[ks][i], acc[i]);
      pin(acc[i]);
      __builtin_amdgcn_sched_barrier(0);
      if constexpr (j == 6 && ks == 0) {
        int a0 = xo[i][0] + 7 * 8192, a1 = xo[i][1] + 7 * 8192;
        asm volatile("" : "+v"(a0), "+v"(a1));
        xo[i][0] = a0;
        xo[i][1] = a1;
      }
      if constexpr (j < 13)
        nxt[ks][i] = tr_frag(smem, xo[i][0] + ks * 4096 + ((j + 1) % 7) * 8192, xo[i][1] + ks * 4096 + ((j + 1) % 7) * 8192);
      if constexpr (j == JG && sl == 0) {
        u64 t;
        asm volatile(SNOP2 "global_load_dwordx2 %0, %1, %2 sc1" : "=v"(t) : "v"((u32)lane * 8), "s"(uni(gbase)) : "memory");
        gv = t;
        static_for<0, 4>([&](auto qI) {
          f32x2 tp;
          asm volatile(SNOP2 "global_load_dwordx2 %0, %1, %2"
                       : "=v"(tp)
                       : "v"((u32)n * 8), "s"(uni(vbase + decltype(qI)::value * 3584 * 8))
                       : "memory");
          gp[decltype(qI)::value] = tp;
        });
      }
      if constexpr ((sl & 1) == 0 && j + K2_AHEAD + 1 <= 13)
        issue1(std::integral_constant<int, 3 * (j + K2_AHEAD + 1) + sl / 2>{});
      if constexpr (j == 12 && sl < E12) ldwd(std::integral_constant<int, sl>{});
      if constexpr (j == 13 && sl < E13) ldwd(std::integral_constant<int, E12 + sl>{});
      if constexpr (j == JG + K2_AHEAD && sl == 3) {
        u64 t = gv;
        f32x2 p0 = gp[0], p1 = gp[1], p2 = gp[2], p3 = gp[3];
        asm volatile("" : "+v"(t), "+v"(p0), "+v"(p1), "+v"(p2), "+v"(p3));
        mk_aext(p0, p1, p2, p3);
        if (!__all((t >> 32) != 0)) {
          for (int spin = 0; spin < (1 << 16); ++spin) {
            __builtin_amdgcn_s_sleep(2);
            t = __hip_atomic_load((gu64*)(size_t)gbase + lane, RLX_AGENT);
            if (__all((t >> 32) != 0)) break;
          }
          __builtin_amdgcn_s_waitcnt(0x0F70);
        }
        CS[gsl * 64 + lane] = t;
      }
      __builtin_amdgcn_sched_barrier(0);
    });
  };
  static_for<0, 14>([&](auto jI) {
    if constexpr (decltype(jI)::value & 1) chunk(jI, xb, xa);
    else chunk(jI, xa, xb);
  });
  YSTAMP(2);
  const rsrc_t rw = mk_rsrc(ws + OFF_ACT + ((size_t)(mt * 4) * 112 + tile) * 1024, 337 * 1024);
  constexpr int PF = 4;
  bf16x8 xr3[PF];
  auto rd3 = [&](auto kI) {
    constexpr int k = decltype(kI)::value;
    if constexpr (k == 14) {
      int a0 = xo[3][0] + 14 * 4096, a1 = xo[3][1] + 14 * 4096;
      asm volatile("" : "+v"(a0), "+v"(a1));
      xo[3][0] = a0;
      xo[3][1] = a1;
    }
    xr3[k % PF] = tr_frag(smem, xo[3][0] + (k % 14) * 4096, xo[3][1] + (k % 14) * 4096);
  };
  u64 g[3];
  auto ldg = [&](auto iI) {
#pragma unroll
    for (int k = 0; k < 3; ++k) g[k] = CS[k * 128 + NTL[decltype(iI)::value] * 32 + n];
  };
  float c1, c2, var;
  u32x4 ov;
  auto corr = [&](auto iI) {
    constexpr int i = decltype(iI)::value;
    const u32x4 bext = {(u32)g[0], (u32)(g[0] >> 32), (u32)g[1], 0u};
    acc[i] = mfma32(aext, __builtin_bit_cast(bf16x8, bext), acc[i]);
    c1 = __builtin_bit_cast(float, (u32)(g[2] >> 32));
    c2 = __builtin_bit_cast(float, (u32)g[2]);
    var = __builtin_bit_cast(float, (u32)(g[1] >> 32));
  };
  auto half_epi = [&](auto iI, auto hI) {
    constexpr int i = decltype(iI)::value, r0 = 4 * decltype(hI)::value;
    float e[4], m[4];
#pragma unroll
    for (int r = 0; r < 4; ++r) {
      const float a = acc[i][r0 + r];
      e[r] = __builtin_amdgcn_exp2f(__builtin_fmaf(a * a, c2, c1) * a);
      m[r] = a * acc[i][r0 + r + 8];
    }
#pragma unroll
    for (int r = 0; r < 4; ++r) m[r] *= __builtin_amdgcn_rcpf(__builtin_fmaf(e[r], var, var));
    ov[r0 / 2] = umin32(pack_bf16(m[0], m[1]));
    ov[r0 / 2 + 1] = umin32(pack_bf16(m[2], m[3]));
  };
  auto put = [&](auto iI) {
    __builtin_amdgcn_raw_buffer_store_b128(ov, rw, lane * 16 + NTL[decltype(iI)::value] * 112 * 1024, 0, 16);
    asm volatile("s_nop 1");
  };
  using I0 = std::integral_constant<int, 0>;
  using I1 = std::integral_constant<int, 1>;
  using I2 = std::integral_constant<int, 2>;
  using I3 = std::integral_constant<int, 3>;
  const rsrc_t ra = mk_rsrc(ws + OFF_ACT + ((size_t)(mt * 4 + sh2 * 2) * 112 + 14 * w) * 1024, 126 * 1024);
  u32x4 af[28];
  auto ldact = [&](auto kI) {
    constexpr int k = decltype(kI)::value;
    af[k] = __builtin_amdgcn_raw_buffer_load_b128(ra, lane * 16 + (k % 14) * 1024, (k / 14) * 112 * 1024, ACTAUX);
  };
  float pe, pm, pr;
  auto ep_a = [&](auto iI, auto rI) {
    constexpr int i = decltype(iI)::value, r = decltype(rI)::value;
    const float a = acc[i][r];
    float e = __builtin_amdgcn_exp2f(__builtin_fmaf(a * a, c2, c1) * a), m = a * acc[i][r + 8];
    asm volatile("" : "+v"(e), "+v"(m));
    pe = e;
    pm = m;
  };
  auto ep_b = [&](auto rI) {
    constexpr int r = decltype(rI)::value;
    float o = pm * __builtin_amdgcn_rcpf(__builtin_fmaf(pe, var, var));
    if constexpr (r & 1) {
      u32 t = umin32(pack_bf16(pr, o));
      asm volatile("" : "+v"(t));
      ov[r / 2] = t;
    } else {
      asm volatile("" : "+v"(o));
      pr = o;
    }
  };
  static_for<0, PF - 1>(rd3);
  ldg(I0{});
  corr(I0{});
  ldg(I1{});
  __builtin_amdgcn_sched_barrier(0);
  static_for<0, 28>([&](auto kI) {
    constexpr int k = decltype(kI)::value;
    acc[3] = mfma32(wf[k], xr3[k % PF], acc[3]);
    pin(acc[3]);
    __builtin_amdgcn_sched_barrier(0);
    if constexpr (k % EXP_LOCK == EXP_LOCK - 1) bar();
    if constexpr (k + PF - 1 < 28) rd3(std::integral_constant<int, k + PF - 1>{});
    constexpr int ti = k / 9, u = k % 9;
    if constexpr (ti < 3) {
      using TI = std::integral_constant<int, ti>;
      if constexpr (u >= 1) ep_b(std::integral_constant<int, u - 1>{});
      if constexpr (u < 8) ep_a(TI{}, std::integral_constant<int, u>{});
      if constexpr (u == 8) {
        put(TI{});
        if constexpr (ti < 2) {
          corr(std::integral_constant<int, ti + 1>{});
          ldg(std::integral_constant<int, ti + 2>{});
        }
      }
    }
    if constexpr (k >= 1 && E12 + E13 + k - 1 < 28) ldwd(std::integral_constant<int, E12 + E13 + k - 1>{});
    if constexpr (k >= 21) ldact(std::integral_constant<int, k - 21>{});
    __builtin_amdgcn_sched_barrier(0);
  });
  YSTAMP(3);
  corr(I3{});
  static_for<7, 11>(ldact);
  __builtin_amdgcn_sched_barrier(0);
  half_epi(I3{}, I0{});
  __builtin_amdgcn_sched_barrier(0);
  static_for<11, 14>(ldact);
  __builtin_amdgcn_sched_barrier(0);
  half_epi(I3{}, I1{});
  put(I3{});
  bar();
  YSTAMP(4);
  auto chk = [&](auto gI) {
    constexpr int g = decltype(gI)::value;
    u32 m = 0;
#pragma unroll
    for (int k = 7 * g; k < 7 * g + 7; ++k) m = umax3(umax3(m, af[k][0], af[k][1]), af[k][2], af[k][3]);
    return (bool)__all(m != ~0u);
  };
  auto valid = [&](auto gI) {
    constexpr int g = decltype(gI)::value;
    if (__builtin_expect(!chk(gI), 0)) {
#pragma nounroll
      for (int spin = 0; spin < (1 << 14); ++spin) {
        __builtin_amdgcn_s_sleep(8);
        static_for<7 * g, 7 * g + 7>(ldact);
        if (chk(gI)) break;
      }
      __builtin_amdgcn_s_waitcnt(0x0F70);
    }
    __builtin_amdgcn_sched_barrier(0);
  };
  const int pcol = mt * 128 + sh2 * 64 + n;
  const int dbase = dt * 64 + w * 8 + 4 * h;
  __builtin_amdgcn_sched_barrier(0);
  LAS f32x4* R = (LAS f32x4*)smem;
  f32x16 o[4];
#pragma unroll
  for (int i = 0; i < 4; ++i)
#pragma unroll
    for (int r = 0; r < 16; ++r) o[i][r] = 0.f;
  auto dump1 = [&](auto tjI, auto cI) {
    constexpr int tj = decltype(tjI)::value, c = decltype(cI)::value, i = tj * 2 + (c >> 2), q = c & 3;
    R[((tj * 8 + c) * 8 + w) * 64 + lane] = f32x4{o[i][4 * q], o[i][4 * q + 1], o[i][4 * q + 2], o[i][4 * q + 3]};
  };
  auto step = [&](auto kI) {
    constexpr int k = decltype(kI)::value, tj = k / 14;
    const bf16x8 b = __builtin_bit_cast(bf16x8, af[k]);
    o[2 * tj] = mfma32(wd[k % 14], b, o[2 * tj]);
    pin(o[2 * tj]);
    __builtin_amdgcn_sched_barrier(0);
    if constexpr (k >= 15 && k < 19) dump1(I0{}, std::integral_constant<int, 2 * (k - 15)>{});
    o[2 * tj + 1] = mfma32(wd[14 + k % 14], b, o[2 * tj + 1]);
    pin(o[2 * tj + 1]);
    __builtin_amdgcn_sched_barrier(0);
    if constexpr (k >= 15 && k < 19) dump1(I0{}, std::integral_constant<int, 2 * (k - 15) + 1>{});
    if constexpr (k >= EXP_T1 && k < 14) {
      constexpr int n0 = ((k - EXP_T1) * 14) / (14 - EXP_T1), n1 = ((k + 1 - EXP_T1) * 14) / (14 - EXP_T1);
      static_for<14 + n0, 14 + n1>(ldact);
    }
    __builtin_amdgcn_sched_barrier(0);
  };
  u16 xres[2][4];
  f32x4 gbv;
  auto fin = [&](auto tjI) {
    constexpr int tj = decltype(tjI)::value;
    f32x4 v[8];
#pragma unroll
    for (int sw = 0; sw < 8; ++sw) v[sw] = R[((tj * 8 + w) * 8 + sw) * 64 + lane];
    const f32x4 sum = ((v[0] + v[1]) + (v[2] + v[3])) + ((v[4] + v[5]) + (v[6] + v[7]));
    float r[4];
#pragma unroll
    for (int e = 0; e < 4; ++e) r[e] = __builtin_fmaf(gbv[e], sum[e], bf2f(xres[tj][e]));
    const int pc = pcol + 32 * tj;
    const int srow = (pc & ~7) | ((pc & 3) << 1) | ((pc >> 2) & 1);
    *(u32x2*)(out + (size_t)srow * D + dbase) = u32x2{pack_bf16(r[0], r[1]), pack_bf16(r[2], r[3])};
  };
  valid(I0{});
  asm volatile(""
               : "+v"(wd[0]), "+v"(wd[1]), "+v"(wd[2]), "+v"(wd[3]), "+v"(wd[4]), "+v"(wd[5]), "+v"(wd[6]), "+v"(wd[7]),
                 "+v"(wd[8]), "+v"(wd[9]), "+v"(wd[10]), "+v"(wd[11]), "+v"(wd[12]), "+v"(wd[13])::"memory");
  asm volatile(""
               : "+v"(wd[14]), "+v"(wd[15]), "+v"(wd[16]), "+v"(wd[17]), "+v"(wd[18]), "+v"(wd[19]), "+v"(wd[20]),
                 "+v"(wd[21]), "+v"(wd[22]), "+v"(wd[23]), "+v"(wd[24]), "+v"(wd[25]), "+v"(wd[26]), "+v"(wd[27])::"memory");
  YSTAMP(5);
  XSTAMP(0);
  static_for<0, 3>(step);
  {
    const u16* X1T = (const u16*)(ws + OFF_X1T) + (size_t)dbase * S + pcol;
#pragma unroll
    for (int r = 0; r < 8; ++r) xres[r >> 2][r & 3] = X1T[(size_t)(r & 3) * S + (r >> 2) * 32];
    gbv = *(const f32x4*)((const float*)(ws + OFF_MOD) + 5 * D + dbase);
    __builtin_amdgcn_sched_barrier(0);
  }
  static_for<3, 7>(step);
  XSTAMP(1);
  valid(I1{});
  static_for<7, 14>(step);
  XSTAMP(2);
  valid(I2{});
  XSTAMP(3);
  static_for<14, 21>(step);
  wait_lgkm0();
  bar();
  XSTAMP(4);
  valid(I3{});
  static_for<21, 24>(step);
  fin(I0{});
  __builtin_amdgcn_sched_barrier(0);
  static_for<24, 28>(step);
  XSTAMP(5);
  static_for<0, 8>([&](auto cI) { dump1(I1{}, cI); });
  wait_lgkm0();
  bar();
  STAMP(ws, 2, 6);
  fin(I1{});
}

extern "C" __global__ NOPK __launch_bounds__(512, 2) void k2_ffn(char* __restrict__ ws, u16* __restrict__ out) {
  extern __shared__ __attribute__((aligned(16))) char smem[];
  const int w = __builtin_amdgcn_readfirstlane(threadIdx.x >> 6);
  if (blockIdx.x < 32) k2_stats(ws, smem);
  else k2_gemm(ws, out, smem, w);
}

extern "C" __device__ __attribute__((used, noinline)) void k_code_end() {}

extern "C" void kernel_launch(void* const* d_in, const int* in_sizes, int n_in, void* d_out, int out_size, void* d_ws,
                              size_t ws_size, hipStream_t stream) {
  const u16* q = (const u16*)d_in[0];
  const u16* k = (const u16*)d_in[1];
  const u16* v = (const u16*)d_in[2];
  const u16* shift_k = (const u16*)d_in[3];
  const u16* ssm_k = (const u16*)d_in[4];
  const u16* ssm_D = (const u16*)d_in[5];
  const u16* w_gate = (const u16*)d_in[7];
  const u16* w_up = (const u16*)d_in[8];
  const u16* w_down = (const u16*)d_in[9];
  const u16* c = (const u16*)d_in[10];
  const u16* w_mod = (const u16*)d_in[11];
  char* ws = (char*)d_ws;
  (void)hipFuncSetAttribute((const void*)k1_conv, hipFuncAttributeMaxDynamicSharedMemorySize, K1_LDS);
  (void)hipFuncSetAttribute((const void*)k2_ffn, hipFuncAttributeMaxDynamicSharedMemorySize, K2_LDS);
  k0_prep<<<K0_GRID, 256, 0, stream>>>(q, k, v, shift_k, c, w_mod, ssm_k, w_gate, w_up, w_down, (const u16*)d_out, ws);
  k1_conv<<<K1_GRID, 256, K1_LDS, stream>>>(ssm_k, ssm_D, w_gate, w_up, w_down, ws);
  k2_ffn<<<256, 512, K2_LDS, stream>>>(ws, (u16*)d_out);
}
```

```cpp
#include <hip/hip_runtime.h>
#include <hip/hip_bf16.h>
#include <stdint.h>
#include <type_traits>

#define NTAUX 0
#define NTLOAD(p) (*(p))
typedef unsigned short u16;
typedef unsigned int u32;
typedef short bf16x8 __attribute__((ext_vector_type(8)));
typedef short s16x4 __attribute__((ext_vector_type(4)));
typedef float f32x2 __attribute__((ext_vector_type(2)));
typedef float f32x4 __attribute__((ext_vector_type(4)));
typedef float f32x16 __attribute__((ext_vector_type(16)));
typedef u32 u32x2 __attribute__((ext_vector_type(2)));
typedef u32 u32x4 __attribute__((ext_vector_type(4)));
typedef __bf16 bf16x2_t __attribute__((ext_vector_type(2)));
#define LAS __attribute__((address_space(3)))
#define DI __device__ __forceinline__

constexpr int S = 2048, D = 448, F = 1792;

constexpr size_t SZ_DS = (size_t)D * S * 2;
constexpr size_t OFF_RE = 0;
constexpr size_t OFF_RO = OFF_RE + SZ_DS;
constexpr size_t OFF_QT = OFF_RO + SZ_DS;
constexpr size_t OFF_X1T = OFF_QT + SZ_DS;
constexpr size_t OFF_MEAN = OFF_X1T + SZ_DS;
constexpr size_t OFF_RSTD = OFF_MEAN + S * 4;
constexpr size_t OFF_MOD = OFF_RSTD + S * 4;
constexpr size_t OFF_SG = OFF_MOD + 16384;
constexpr size_t OFF_CB = OFF_MOD + 12288;

constexpr size_t OFF_VG = OFF_SG + 3 * S * 8;
constexpr size_t OFF_XS = OFF_VG + 4 * S * 8;
constexpr size_t OFF_VP = OFF_XS + SZ_DS;
constexpr size_t OFF_WGU = OFF_VP + 8 * 2 * 3584 * 4;
constexpr size_t OFF_WD = OFF_WGU + (size_t)2 * D * F * 2;
constexpr size_t OFF_ACT = OFF_WD + (size_t)D * F * 2;
constexpr size_t WS_END = OFF_ACT + (size_t)S * F * 2;

#define STAMP_DECL
#define STAMP(wsp, kid, i)
#define STAMP_FLUSH(wsp, kid)
#define HWID(wsp, kid)
#define XSTAMP(i)
#define YSTAMP(i) STAMP(ws, 2, i)
#if defined(__HIP_DEVICE_COMPILE__)
#define NOPK __attribute__((target("no-packed-fp32-ops")))
#else
#define NOPK
#endif

template <int I, int N, class Fn>
DI void static_for(Fn&& f) {
  if constexpr (I < N) {
    f(std::integral_constant<int, I>{});
    static_for<I + 1, N>(f);
  }
}

DI float lo_f(u32 x) { return __builtin_bit_cast(float, x << 16); }
DI float hi_f(u32 x) { return __builtin_bit_cast(float, x & 0xffff0000u); }
DI u32 pack_bf16(float a, float b) {
  f32x2 v = {a, b};
  return __builtin_bit_cast(u32, __builtin_convertvector(v, bf16x2_t));
}
DI float round_bf16(float a) { return lo_f(pack_bf16(a, 0.f)); }
DI float bf2f(u16 x) { return __builtin_bit_cast(float, (u32)x << 16); }

template <int N>
DI void wait_vm() { asm volatile("s_waitcnt vmcnt(%0)" ::"n"(N) : "memory"); }
DI void wait_lgkm0() { asm volatile("s_waitcnt lgkmcnt(0)" ::: "memory"); }
DI void bar() { __builtin_amdgcn_s_barrier(); }

#define SNOP "s_nop 0\n\t"
#define SNOP2 ""
DI const void* uni(const void* p) {
  const unsigned long long v = (unsigned long long)p;
  const u32 lo = __builtin_amdgcn_readfirstlane((u32)v), hi = __builtin_amdgcn_readfirstlane((u32)(v >> 32));
  return (const void*)(((unsigned long long)hi << 32) | lo);
}
DI void glds16_asm(const void* sbase, u32 voff, u32 lds_addr) {
  u32 keep;
  sbase = uni(sbase);
  lds_addr = __builtin_amdgcn_readfirstlane(lds_addr);
  asm volatile("s_mov_b32 %0, m0\n\ts_mov_b32 m0, %3\n\t" SNOP "global_load_lds_dwordx4 %1, %2\n\ts_mov_b32 m0, %0"
               : "=&s"(keep)
               : "v"(voff), "s"(sbase), "s"(lds_addr)
               : "memory");
}
DI void glds16(const void* g, char* lds) {
  __builtin_amdgcn_global_load_lds((const __attribute__((address_space(1))) void*)g, (LAS void*)lds, 16, 0, NTAUX);
}

template <int CTRL>
DI float dpp_add(float v) {
  const int x = __builtin_bit_cast(int, v);
  return v + __builtin_bit_cast(float, __builtin_amdgcn_update_dpp(x, x, CTRL, 0xf, 0xf, false));
}
DI float sum8(float v) {
  v = dpp_add<0xB1>(v);
  v = dpp_add<0x4E>(v);
  v = dpp_add<0x141>(v);
  return v;
}
DI float sum16(float v) { return dpp_add<0x140>(sum8(v)); }
DI float dot2(u32 a, u32 b, float c) {
  return __builtin_amdgcn_fdot2_f32_bf16(__builtin_bit_cast(bf16x2_t, a), __builtin_bit_cast(bf16x2_t, b), c, false);
}
typedef __amdgpu_buffer_rsrc_t rsrc_t;
DI rsrc_t mk_rsrc(const void* p, u32 bytes) { return __builtin_amdgcn_make_buffer_rsrc((void*)p, 0, bytes, 0x00020000); }
DI void st16_sc1(void* p, u32x4 v) { asm volatile("global_store_dwordx4 %0, %1, off sc1\n\ts_nop 1" ::"v"(p), "v"(v) : "memory"); }
DI void st8_sc1(void* p, u32x2 v) { asm volatile("global_store_dwordx2 %0, %1, off sc1" ::"v"(p), "v"(v) : "memory"); }
DI void st4_sc1(void* p, float v) { asm volatile("global_store_dword %0, %1, off sc1" ::"v"(p), "v"(v) : "memory"); }

#define EXP_E12 2
#define EXP_E13 6
#define EXP_LOCK 4
#define EXP_NW2 14
DI bf16x8 tr_frag(const char* smem, int o0, int o1) {
  s16x4 x0 = __builtin_amdgcn_ds_read_tr16_b64_v4i16((LAS s16x4*)(smem + o0));
  s16x4 x1 = __builtin_amdgcn_ds_read_tr16_b64_v4i16((LAS s16x4*)(smem + o1));
  return __builtin_shufflevector(x0, x1, 0, 1, 2, 3, 4, 5, 6, 7);
}
DI f32x16 mfma32(u32x4 a, bf16x8 b, f32x16 c) {
  return __builtin_amdgcn_mfma_f32_32x32x16_bf16(__builtin_bit_cast(bf16x8, a), b, c, 0, 0, 0);
}

DI void retile(int task, const u16* __restrict__ w_gate, const u16* __restrict__ w_up, const u16* __restrict__ w_down,
               char* __restrict__ ws, char* smem, int w, int lane) {
  const int xr = lane >> 4, slot = lane & 15;
  const int chn = slot ^ ((xr << 2) | (w & 3));
  const int h = lane >> 5, blk = (lane >> 4) & 1, qq = (lane & 15) >> 2, p = lane & 3;
  const bool gu = task < 112;
  int kq = 0, fg = 0, dg = 0, fk = 0;
  if (gu) {
    fg = task % 28, kq = task / 28;
    const u16* src = (chn < 8 ? w_gate : w_up) + (size_t)(kq * 112 + 4 * w + xr) * F + fg * 64 + (chn & 7) * 8;
#pragma unroll
    for (int i = 0; i < 7; ++i) glds16(src + (size_t)i * 16 * F, smem + (w + 4 * i) * 1024);
  } else {
    dg = (task - 112) % 7, fk = (task - 112) / 7;
    const u16* src = w_down + (size_t)(fk * 224 + (chn >> 3) * 112 + 4 * w + xr) * D + dg * 64 + (chn & 7) * 8;
#pragma unroll
    for (int i = 0; i < 7; ++i) glds16(src + (size_t)i * 16 * D, smem + (w + 4 * i) * 1024);
  }
  const int col = gu ? blk * 128 + w * 32 + (p & 1) * 16 + (p >> 1) * 8 : (w >> 1) * 128 + (w & 1) * 64 + blk * 32 + p * 8;
  int o[2];
#pragma unroll
  for (int t = 0; t < 2; ++t)
    o[t] = 256 * (8 * h + 4 * t + qq) + 16 * ((col >> 4) ^ ((qq << 2) | ((2 * h + t) & 3))) + (col & 8);
  u32x4* dst = gu ? (u32x4*)(ws + OFF_WGU) + ((size_t)(fg * 4 + w) * 28 + kq * 7) * 64 + lane
                  : (u32x4*)(ws + OFF_WD) + ((size_t)(dg * 2 + (w & 1)) * 112 + fk * 14 + (w >> 1) * 7) * 64 + lane;
  __syncthreads();
#pragma unroll
  for (int i = 0; i < 7; ++i)
    st16_sc1(dst + i * 64, __builtin_bit_cast(u32x4, tr_frag(smem, o[0] + i * 4096, o[1] + i * 4096)));
}

#define EXP_T1 5
#define EXP_A0 4
#define ACTAUX 16
constexpr int K0_GRID = 504;

extern "C" __global__ __launch_bounds__(256) void k0_prep(const u16* __restrict__ q, const u16* __restrict__ k,
                                               const u16* __restrict__ v, const u16* __restrict__ shift_k,
                                               const u16* __restrict__ c, const u16* __restrict__ w_mod,
                                               const u16* __restrict__ ssm_k, const u16* __restrict__ w_gate,
                                               const u16* __restrict__ w_up, const u16* __restrict__ w_down,
                                               char* __restrict__ ws) {
  __shared__ __attribute__((aligned(16))) char smem[28672];
  float(*red)[64] = (float(*)[64])smem;
  const int b = blockIdx.x, tid = threadIdx.x, lane = tid & 63, w = __builtin_amdgcn_readfirstlane(tid >> 6);
  STAMP_DECL;
  STAMP(ws, 0, 0);
  const int xcd = b & 7, idx = b >> 3;
  u32 cwarm = 0;
  if (idx == 40 || idx == 41) {
    u32 lo, hi, len;
    asm volatile(
        "s_getpc_b64 s[40:41]\n\ts_add_u32 s40, s40, k1_main@rel32@lo+4\n\ts_addc_u32 s41, s41, k1_main@rel32@hi+12\n\t"
        "s_mov_b32 %0, s40\n\ts_mov_b32 %1, s41\n\ts_mov_b32 %2, k_code_end-k1_main"
        : "=s"(lo), "=s"(hi), "=s"(len)::"s40", "s41", "scc");
    const char* code = (const char*)(((unsigned long long)hi << 32) | lo);
    if ((u32)((idx - 40) * 256 + tid) * 128 < len) cwarm = *(const u32*)(code + ((idx - 40) * 256 + tid) * 128);
  }
  if (idx >= 42) {
    retile(b - 336, w_gate, w_up, w_down, ws, smem, w, lane);
    STAMP(ws, 7, 1);
    STAMP_FLUSH(ws, 7);
    return;
  }
  int misc = -1;
  if (xcd == 7) misc = idx < 14 ? 64 + idx : (idx < 36 ? idx - 14 : 1000);
  else if (idx < 4) misc = 64 + 14 + xcd * 4 + idx;
  else if (idx >= 36) misc = 22 + xcd * 6 + (idx - 36);
  if (misc < 0) {
    const int rb = idx - 4;
    const int sq = lane >> 5, cp = lane & 31;
    const int d0 = xcd * 64 + cp * 2;
    const int a = rb * 8 + w * 2 + sq;
    const int s0 = a * 8;
    const rsrc_t rk = mk_rsrc(k, S * D * 2), rv = mk_rsrc(v, S * D * 2), rq = mk_rsrc(q, S * D * 2);
    const int oB = (s0 * D + d0) * 2;
    const int oA = oB - 4 * D * 2, oC = oB + 4 * D * 2;
    u32 kk[12], vv[9], qq[8];
#pragma unroll
    for (int i = 0; i < 4; ++i) kk[i] = __builtin_amdgcn_raw_buffer_load_b32(rk, oA + i * D * 2, 0, NTAUX);
#pragma unroll
    for (int i = 0; i < 4; ++i) kk[4 + i] = __builtin_amdgcn_raw_buffer_load_b32(rk, oB + i * D * 2, 0, NTAUX);
#pragma unroll
    for (int i = 0; i < 4; ++i) kk[8 + i] = __builtin_amdgcn_raw_buffer_load_b32(rk, oC + i * D * 2, 0, NTAUX);
    vv[0] = __builtin_amdgcn_raw_buffer_load_b32(rv, oA + 3 * D * 2, 0, NTAUX);
#pragma unroll
    for (int i = 0; i < 4; ++i) vv[1 + i] = __builtin_amdgcn_raw_buffer_load_b32(rv, oB + i * D * 2, 0, NTAUX);
#pragma unroll
    for (int i = 0; i < 4; ++i) vv[5 + i] = __builtin_amdgcn_raw_buffer_load_b32(rv, oC + i * D * 2, 0, NTAUX);
#pragma unroll
    for (int i = 0; i < 4; ++i) qq[i] = __builtin_amdgcn_raw_buffer_load_b32(rq, oB + i * D * 2, 0, NTAUX);
#pragma unroll
    for (int i = 0; i < 4; ++i) qq[4 + i] = __builtin_amdgcn_raw_buffer_load_b32(rq, oC + i * D * 2, 0, NTAUX);
    const u32x4 sk = *(const u32x4*)(shift_k + d0 * 4);
    u32 warm = 0;
    if (w == 0) warm = NTLOAD((const u32*)((const char*)ssm_k + ((size_t)(xcd * 32 + rb) * 64 + lane) * 128));
    f32x2 wt[4] = {{lo_f(sk[0]), lo_f(sk[2])}, {hi_f(sk[0]), hi_f(sk[2])}, {lo_f(sk[1]), lo_f(sk[3])}, {hi_f(sk[1]), hi_f(sk[3])}};
    f32x2 kf[12], u[9];
#pragma unroll
    for (int i = 0; i < 12; ++i) kf[i] = f32x2{lo_f(kk[i]), hi_f(kk[i])};
#pragma unroll
    for (int i = 0; i < 9; ++i) {
      f32x2 t = kf[i + 3] * wt[0];
      t = __builtin_elementwise_fma(kf[i + 2], wt[1], t);
      t = __builtin_elementwise_fma(kf[i + 1], wt[2], t);
      t = __builtin_elementwise_fma(kf[i], wt[3], t);
      u[i] = t * f32x2{lo_f(vv[i]), hi_f(vv[i])};
    }
    {
      char* dst = ws + (size_t)d0 * (S * 2) + (2040 - s0) * 2;
#pragma unroll
      for (int cI = 0; cI < 2; ++cI) {
        *(u32x4*)(dst + OFF_RO + cI * S * 2) = u32x4{pack_bf16(u[8][cI], u[7][cI]), pack_bf16(u[6][cI], u[5][cI]),
                                                     pack_bf16(u[4][cI], u[3][cI]), pack_bf16(u[2][cI], u[1][cI])};
        *(u32x4*)(dst + OFF_RE + cI * S * 2) = u32x4{pack_bf16(u[7][cI], u[6][cI]), pack_bf16(u[5][cI], u[4][cI]),
                                                     pack_bf16(u[3][cI], u[2][cI]), pack_bf16(u[1][cI], u[0][cI])};
      }
      char* dq = ws + OFF_QT + (size_t)d0 * (S * 2) + s0 * 2;
#pragma unroll
      for (int cI = 0; cI < 2; ++cI) {
        const u32 sel = cI ? 0x07060302u : 0x05040100u;
        *(u32x4*)(dq + cI * S * 2) = u32x4{__builtin_amdgcn_perm(qq[2], qq[0], sel), __builtin_amdgcn_perm(qq[6], qq[4], sel),
                                           __builtin_amdgcn_perm(qq[3], qq[1], sel), __builtin_amdgcn_perm(qq[7], qq[5], sel)};
      }
    }
    {
      u32x4* ap = (u32x4*)(ws + (w < 2 ? OFF_X1T : OFF_XS)) + (size_t)(xcd * 32 + rb) * 512 + (w & 1) * 256 + lane;
      const u32x4 sent = {~0u, ~0u, ~0u, ~0u};
#pragma unroll
      for (int i = 0; i < 4; ++i) st16_sc1(ap + i * 64, sent);
    }
    asm volatile("" ::"v"(warm));
  } else if (misc < 64) {
    const int bb = misc;
    const int row = bb * 32 + w * 8 + (lane >> 3);
    const int sub = lane & 7;
    u32x4 x[7];
#pragma unroll
    for (int i = 0; i < 7; ++i) x[i] = NTLOAD((const u32x4*)(q + (size_t)row * D + (i * 8 + sub) * 8));
    if (sub < 3) ((unsigned long long*)(ws + OFF_SG))[sub * S + row] = 0ull;
    float s = 0.f, s2 = 0.f;
#pragma unroll
    for (int i = 0; i < 7; ++i)
#pragma unroll
      for (int j = 0; j < 4; ++j) {
        const u32 t = x[i][j];
        s = dot2(t, 0x3f803f80u, s);
        s2 = dot2(t, t, s2);
      }
    s = sum8(s);
    s2 = sum8(s2);
    const float mean = s * (1.f / D);
    float var = s2 * (1.f / D) - mean * mean;
    var = var < 0.f ? 0.f : var;
    const float rstd = __builtin_amdgcn_rsqf(var + 1e-5f);
    if (sub < 2) {
      const int p = (row & ~7) | ((row & 1) << 2) | ((row >> 1) & 3);
      ((float*)(ws + OFF_MEAN))[sub * S + p] = sub ? rstd : mean;
    }
  } else if (misc < 106) {
    const int bb = misc - 64;
    const int co = lane & 7, rs = lane >> 3;
    const int col0 = bb * 64 + co * 8;
    u32x4 x[14];
#pragma unroll
    for (int i = 0; i < 14; ++i)
      x[i] = NTLOAD((const u32x4*)(w_mod + (size_t)(w * 112 + rs * 14 + i) * (6 * D) + col0));
    u32 cv[7];
#pragma unroll
    for (int i = 0; i < 7; ++i) cv[i] = *(const u32*)(c + w * 112 + rs * 14 + 2 * i);
    __builtin_amdgcn_sched_barrier(0);
    f32x2 acc[4] = {{0.f, 0.f}, {0.f, 0.f}, {0.f, 0.f}, {0.f, 0.f}};
#pragma unroll
    for (int i = 0; i < 14; ++i) {
      const float cf = (i & 1) ? hi_f(cv[i >> 1]) : lo_f(cv[i >> 1]);
      const float sl = round_bf16(cf * __builtin_amdgcn_rcpf(1.f + __builtin_amdgcn_exp2f(cf * -1.44269504f)));
#pragma unroll
      for (int j = 0; j < 4; ++j)
        acc[j] = __builtin_elementwise_fma(f32x2{lo_f(x[i][j]), hi_f(x[i][j])}, f32x2{sl, sl}, acc[j]);
    }
    *(f32x4*)&red[w * 8 + rs][co * 8] = f32x4{acc[0][0], acc[0][1], acc[1][0], acc[1][1]};
    *(f32x4*)&red[w * 8 + rs][co * 8 + 4] = f32x4{acc[2][0], acc[2][1], acc[3][0], acc[3][1]};
    __syncthreads();
    {
      float t = 0.f;
#pragma unroll
      for (int i = 0; i < 8; ++i) t += red[w * 8 + i][lane];
      red[32 + w][lane] = t;
    }
    __syncthreads();
    if (tid < 64) {
      const float t = (red[32][tid] + red[33][tid]) + (red[34][tid] + red[35][tid]);
      const float m = round_bf16(t);
      const int col = bb * 64 + tid;
      ((float*)(ws + OFF_MOD))[col] = m;
      if (col >= 3 * D && col < 5 * D) {
        const bool isc = col >= 4 * D;
        ((u16*)(ws + OFF_CB))[isc ? col - 4 * D : col - 2 * D] = (u16)pack_bf16(isc ? 1.f + m : m, 0.f);
      }
    }
  }
  asm volatile("" ::"v"(cwarm));
  STAMP(ws, 0, 1);
  STAMP_FLUSH(ws, misc < 0 ? 0 : (misc < 64 ? 4 : 5));
}

constexpr int K1_KSTR = 260 * 16;
constexpr int K1_USTR = 286 * 16;
constexpr int K1_UOFF = 4 * K1_KSTR;
constexpr int K1_XOFF = K1_UOFF + 2 * K1_USTR;
constexpr int K1_CH = K1_XOFF + 32768;

DI void glds16x4_asm(const void* sbase, u32 voff, u32 voff_last, u32 lds_addr) {
  u32 keep;
  sbase = uni(sbase);
  lds_addr = __builtin_amdgcn_readfirstlane(lds_addr);
  asm volatile(
      "s_mov_b32 %0, m0\n\ts_mov_b32 m0, %4\n\t" SNOP
      "global_load_lds_dwordx4 %1, %3\n\t"
      "global_load_lds_dwordx4 %1, %3 offset:1024\n\t"
      "global_load_lds_dwordx4 %1, %3 offset:2048\n\t"
      "global_load_lds_dwordx4 %2, %3 offset:3072\n\t"
      "s_mov_b32 m0, %0"
      : "=&s"(keep)
      : "v"(voff), "v"(voff_last), "s"(sbase), "s"(lds_addr)
      : "memory");
}
#define CAS __attribute__((address_space(4)))
constexpr int K2_XB = D * 256;
constexpr int K2_XH = D * 128;
constexpr int K2_ST = K2_XB;
constexpr int K2_CB = 131072;
constexpr int K2_LDS = K2_CB + 2048;

DI void gload16_asm(u32x4& dst, const void* sbase, u32 voff) {
  sbase = uni(sbase);
  asm volatile(SNOP2 "global_load_dwordx4 %0, %1, %2" : "=v"(dst) : "v"(voff), "s"(sbase) : "memory");
}
DI u32 umin32(u32 v) { return v < 0xFFFFFFFEu ? v : 0xFFFFFFFEu; }
DI u32 umax3(u32 a, u32 b, u32 c) { const u32 t = a > b ? a : b; return t > c ? t : c; }
template <int OFF>
DI void gload16_off(u32x4& dst, const void* sbase, u32 voff) {
  sbase = uni(sbase);
  asm volatile(SNOP2 "global_load_dwordx4 %0, %1, %2 offset:%3" : "=v"(dst) : "v"(voff), "s"(sbase), "n"(OFF) : "memory");
}
DI void glds16_nom0(const void* sbase, u32 voff, u32 lds_addr) {
  sbase = uni(sbase);
  lds_addr = __builtin_amdgcn_readfirstlane(lds_addr);
  asm volatile("s_mov_b32 m0, %2\n\t" SNOP "global_load_lds_dwordx4 %0, %1" ::"v"(voff), "s"(sbase), "s"(lds_addr) : "memory");
}
DI void glds16_sc1(const void* sbase, u32 voff, u32 lds_addr) {
  sbase = uni(sbase);
  lds_addr = __builtin_amdgcn_readfirstlane(lds_addr);
  asm volatile("s_mov_b32 m0, %2\n\t" SNOP "global_load_lds_dwordx4 %0, %1 sc1" ::"v"(voff), "s"(sbase), "s"(lds_addr) : "memory");
}
template <int N>
DI void wait_vm_pin(u32x4& a, u32x4& b) {
  asm volatile("s_waitcnt vmcnt(%2)" : "+v"(a), "+v"(b) : "n"(N) : "memory");
}
DI float xor32(float v) {
  const u32 u = __builtin_bit_cast(u32, v);
  auto r = __builtin_amdgcn_permlane32_swap(u, u, false, false);
  const u32 a = r[0], b = r[1];
  return __builtin_bit_cast(float, (threadIdx.x & 32) ? a : b);
}

typedef unsigned long long u64;
typedef __attribute__((address_space(1))) u64 gu64;
typedef __attribute__((address_space(1))) u32 gu32;
#define RLX_AGENT __ATOMIC_RELAXED, __HIP_MEMORY_SCOPE_AGENT
#define EXP_WE 8
#define EXP_NW0 10
#define EXP_NW1 10
constexpr int NWC = EXP_NW0 + EXP_NW1;
template <bool WF>
DI void conv_body(const u16* __restrict__ ssm_k, const u16* __restrict__ ssm_D, char* __restrict__ ws, char* smem,
                  const int w, u32x4 (&wf)[28], const char* wbase) {
  STAMP_DECL;
  STAMP(ws, 1, 0);
  const int lane = threadIdx.x & 63;
  const int ch = w >> 2, q = w & 3;
  const int cpair = (blockIdx.x & 7) * 32 + (blockIdx.x >> 3);
  const int d = 2 * cpair + ch;
  char* L = smem + ch * K1_CH;
  const u32 L0 = (u32)(size_t)(LAS char*)L;
  const u16* kf = ssm_k + (size_t)d * S;
  const u32 vo = lane * 16;
  {
    const u32 vl = (lane > 62 ? 62 : lane) * 16;
    glds16x4_asm(kf + (2 + 2 * q), vo, vl, L0 + q * K1_KSTR + 64);
    const char* ub = ws + ((q >> 1) ? OFF_RO : OFF_RE) + (size_t)d * S * 2 + (q & 1) * 2048;
    const u32 ul = L0 + K1_UOFF + (q >> 1) * K1_USTR + (q & 1) * 2048;
    glds16_nom0(ub, vo, ul);
    glds16_nom0(ub + 1024, vo, ul + 1024);
  }
  const int n = lane & 15, q4 = lane >> 4;
  const int pbase = 32 * (n >> 1) + 8 * q4 + 4 * (n & 1) + 512 * q;
  const int di = n + q - 15;
  u32 head;
  asm volatile(SNOP2 "global_load_dword %0, %1, %2" : "=v"(head) : "v"((u32)(di < 0 ? 0 : di) * 4), "s"(uni(kf)) : "memory");
  auto ldwf = [&](auto kI) {
    constexpr int k = decltype(kI)::value;
    gload16_off<(k % 4) * 1024>(wf[k], wbase + (k / 4) * 4096, vo);
  };
  bar();
  if constexpr (WF) static_for<0, EXP_NW0>(ldwf);
  const CAS float* MODc = (const CAS float*)(size_t)(ws + OFF_MOD);
  const float sa = MODc[d], ca = MODc[D + d], ga = MODc[2 * D + d], cbv = MODc[4 * D + d];
  const u32 Draw = ((const CAS u32*)(size_t)ssm_D)[cpair];
  if (lane < 14) *(LAS u32x4*)(L + K1_UOFF + (q >> 1) * K1_USTR + (256 + 14 * (q & 1) + lane) * 16) = u32x4{0u, 0u, 0u, 0u};
  u32 Ab = L0 + (n & 3) * K1_KSTR + ((n >> 2) + q4 + 8 * q) * 16;
  u32 Bb = L0 + K1_UOFF + (n & 1) * K1_USTR + (28 - 4 * (n >> 1) + q4 + 8 * q) * 16;
  u32 Hd = L0 + q * K1_KSTR + n * 4;
  asm volatile("" : "+v"(Ab), "+v"(Bb), "+v"(Hd) : "s"(sa), "s"(ca), "s"(ga), "s"(Draw), "s"(cbv));
  asm volatile("s_waitcnt vmcnt(%1)" : "+v"(head) : "n"(WF ? EXP_NW0 : 0) : "memory");
  *(LAS u32*)(size_t)Hd = di < 0 ? 0u : head;
  wait_lgkm0();
  STAMP(ws, 1, 1);
  bar();
  bf16x8 fa[8], fb[8];
  auto rd = [&](int dl) {
#pragma unroll
    for (int g = 0; g < 8; ++g) {
      fa[g] = *(const LAS bf16x8*)(size_t)(Ab + 64 * dl + 512 * g);
      fb[g] = *(const LAS bf16x8*)(size_t)(Bb + 64 * dl + 512 * (7 - g));
    }
  };
  rd(0);
  __builtin_amdgcn_sched_barrier(0);
  u32x2 qv[2];
  f32x4 mu[2], rs[2];
  asm volatile(SNOP2
               "global_load_dwordx2 %0, %6, %8\n\t"
               "global_load_dwordx2 %1, %6, %8 offset:512\n\t"
               "global_load_dwordx4 %2, %7, %9\n\t"
               "global_load_dwordx4 %3, %7, %9 offset:1024\n\t"
               "global_load_dwordx4 %4, %7, %10\n\t"
               "global_load_dwordx4 %5, %7, %10 offset:1024"
               : "=&v"(qv[0]), "=&v"(qv[1]), "=&v"(mu[0]), "=&v"(mu[1]), "=&v"(rs[0]), "=&v"(rs[1])
               : "v"((u32)pbase * 2), "v"((u32)pbase * 4), "s"(uni(ws + OFF_QT + (size_t)d * S * 2)),
                 "s"(uni(ws + OFF_MEAN)), "s"(uni(ws + OFF_RSTD))
               : "memory");
  const u32x4 sent = {~0u, ~0u, ~0u, ~0u};
  const void* fillb = uni(ws + OFF_ACT + (size_t)(cpair * 8 + w) * 4096);
  f32x4 acc[8];
#pragma unroll
  for (int j = 0; j < 8; ++j) acc[j] = f32x4{0.f, 0.f, 0.f, 0.f};
  static_for<0, 72>([&](auto mI) {
    constexpr int m = decltype(mI)::value, t = m % 36;
    constexpr int sd = t < 1 ? 0 : t < 3 ? 1 : t < 6 ? 2 : t < 10 ? 3 : t < 15 ? 4 : t < 21 ? 5 : t < 28 ? 6 : 7;
    constexpr int g = t - sd * (sd + 1) / 2;
    if constexpr (m == 36) {
      rd(1);
      __builtin_amdgcn_sched_barrier(0);
    }
    f32x4 a = __builtin_amdgcn_mfma_f32_16x16x32_bf16(fa[g], fb[sd - g], acc[sd], 0, 0, 0);
    asm volatile("" : "+v"(a));
    acc[sd] = a;
    if constexpr (m % 4 == 3) {
      constexpr int v = m / 4;
      if constexpr (v < 4) {
        u32x4 sv = sent;
        asm volatile("global_store_dwordx4 %0, %1, %2 offset:%3 sc1\n\ts_nop 1" ::"v"(vo), "v"(sv), "s"(fillb), "n"(v * 1024) : "memory");
      } else if constexpr (WF && v - 4 < EXP_NW1) {
        ldwf(std::integral_constant<int, EXP_NW0 + v - 4>{});
      }
      __builtin_amdgcn_sched_barrier(0);
    }
  });
  STAMP(ws, 1, 3);
  LAS f32x4* X = (LAS f32x4*)(L + K1_XOFF);
#pragma unroll
  for (int j = 0; j < 8; ++j) X[(j * 4 + q) * 64 + lane] = acc[j];
  u32x4 uc[2];
#pragma unroll
  for (int j = 0; j < 2; ++j)
    uc[j] = *(const LAS u32x4*)(L + K1_UOFF + K1_USTR + (255 - (32 * (2 * q + j) + 4 * (n >> 1) + q4)) * 16);
  asm volatile("s_waitcnt vmcnt(%6)"
               : "+v"(qv[0]), "+v"(qv[1]), "+v"(mu[0]), "+v"(mu[1]), "+v"(rs[0]), "+v"(rs[1])
               : "n"(WF ? EXP_NW1 : 0)
               : "memory");
  f32x4 qf[2], hg[2], kd[2];
  {
    const int sh = (n & 1) ? 16 : 0;
    const float gc = ga * round_bf16(1.f + ca), gs = ga * sa, Dd = ch ? hi_f(Draw) : lo_f(Draw);
#pragma unroll
    for (int j = 0; j < 2; ++j) {
#pragma unroll
      for (int r = 0; r < 4; ++r) {
        kd[j][r] = __builtin_bit_cast(float, (uc[j][3 - r] << sh) & 0xffff0000u) * Dd;
        qf[j][r] = (r & 1) ? hi_f(qv[j][r >> 1]) : lo_f(qv[j][r >> 1]);
        hg[j][r] = (qf[j][r] - mu[j][r]) * rs[j][r] * gc + gs;
      }
      asm volatile("" : "+v"(kd[j]), "+v"(qf[j]), "+v"(hg[j]));
    }
  }
  wait_lgkm0();
  bar();
  STAMP(ws, 1, 4);
  u16* X1T = (u16*)(ws + OFF_X1T) + (size_t)d * S + pbase;
  u16* XS = (u16*)(ws + OFF_XS) + (size_t)d * S + pbase;
  const float c1 = round_bf16(1.f + cbv);
#pragma unroll
  for (int j = 0; j < 2; ++j) {
    const LAS f32x4* Xj = X + ((2 * q + j) * 4) * 64 + lane;
    const f32x4 y = (Xj[0] + Xj[64]) + (Xj[128] + Xj[192]);
    const f32x4 o = qf[j] + hg[j] * (y + kd[j]);
    const u32 p0 = umin32(pack_bf16(o[0], o[1])), p1 = umin32(pack_bf16(o[2], o[3]));
    st8_sc1(XS + 256 * j,
            u32x2{umin32(pack_bf16(lo_f(p0) * c1, hi_f(p0) * c1)), umin32(pack_bf16(lo_f(p1) * c1, hi_f(p1) * c1))});
    st8_sc1(X1T + 256 * j, u32x2{p0, p1});
  }
  STAMP(ws, 1, 5);
  STAMP_FLUSH(ws, 1);
}

DI void k2_stats(char* __restrict__ ws, char* smem, const int sblk) {
  const int tid = threadIdx.x, lane = tid & 63;
  const int w = __builtin_amdgcn_readfirstlane(tid >> 6);
  const int n = lane & 31, h = lane >> 5;
  STAMP_DECL;
  STAMP(ws, 2, 0);
  {
    const int p0 = sblk * 64;
    const rsrc_t rx = mk_rsrc(ws + OFF_X1T + ((size_t)(w * 56) * S + p0) * 2, 56 * S * 2);
    u32 x[28];
    for (int spin = 0; spin < (1 << 14); ++spin) {
      asm volatile("" ::: "memory");
      u32 m = 0;
#pragma unroll
      for (int i = 0; i < 28; ++i) x[i] = __builtin_amdgcn_raw_buffer_load_b32(rx, (h * S) * 2 + n * 4 + i * (4 * S), 0, 16);
#pragma unroll
      for (int i = 0; i < 28; i += 2) m = umax3(m, x[i], x[i + 1]);
      if (__all(m != ~0u)) break;
      __builtin_amdgcn_s_sleep(4);
    }
    __syncthreads();
    float s0 = 0.f, s1 = 0.f, q0 = 0.f, q1 = 0.f;
#pragma unroll
    for (int i = 0; i < 28; ++i) {
      const float a = lo_f(x[i]), b = hi_f(x[i]);
      s0 += a;
      s1 += b;
      q0 = __builtin_fmaf(a, a, q0);
      q1 = __builtin_fmaf(b, b, q1);
    }
    *(LAS f32x4*)(smem + ((w * 2 + h) * 32 + n) * 16) = f32x4{s0, q0, s1, q1};
    __syncthreads();
    if (tid < 64) {
      float sm = 0.f, sq = 0.f;
#pragma unroll
      for (int i = 0; i < 16; ++i) {
        const f32x2 t = *(const LAS f32x2*)(smem + (i * 64 + tid) * 8);
        sm += t[0];
        sq += t[1];
      }
      const float mean = sm * (1.f / D);
      float var = sq * (1.f / D) - mean * mean;
      var = (var < 0.f ? 0.f : var) + 1e-5f;
      const float rstd = 1.f / sqrtf(var);
      const float sig = var * rstd;
      const float mh = round_bf16(mean), sh = round_bf16(sig);
      const u32 b0 = pack_bf16(-mh, mh - mean), b1 = pack_bf16(-mh, sh), b2 = pack_bf16(sig - sh, sh);
      const float c1 = -2.302208198f * rstd;
      const float c2 = -0.1029432397f * (rstd * rstd * rstd);
      gu64* G = (gu64*)(size_t)(ws + OFF_SG) + p0 + tid;
      __hip_atomic_store(G, ((u64)b1 << 32) | b0, RLX_AGENT);
      __hip_atomic_store(G + S, ((u64)__builtin_bit_cast(u32, var) << 32) | b2, RLX_AGENT);
      __hip_atomic_store(G + 2 * S, ((u64)__builtin_bit_cast(u32, c1) << 32) | __builtin_bit_cast(u32, c2), RLX_AGENT);
    }
    STAMP(ws, 6, 1);
    STAMP(ws, 6, 2);
    STAMP_FLUSH(ws, 6);
  }
}

DI void k2_gemm(const u16* __restrict__ ssm_k, const u16* __restrict__ ssm_D, char* __restrict__ ws, u16* __restrict__ out,
                char* smem, const int w) {
  const int tid = threadIdx.x, lane = tid & 63;
  const int n = lane & 31, h = lane >> 5;
  const int xcd = blockIdx.x & 7, idx = blockIdx.x >> 3;
  const int mt = (xcd >> 1) * 4 + (idx & 3);
  const int dt = idx >> 2, sh2 = xcd & 1;
  const int ft = sh2 * 7 + dt;
  const int tile = ft * 8 + w;
  const u32 lds0 = (u32)(size_t)(LAS char*)smem;
  u32x4 wf[28], wd[28];
  const char* wbase = ws + OFF_WGU + (size_t)(tile * 28) * 1024;
  const u32 wa = lane * 16;
  auto ldwf = [&](auto kI) {
    constexpr int k = decltype(kI)::value;
    gload16_off<(k % 4) * 1024>(wf[k], wbase + (k / 4) * 4096, wa);
  };
  if (w < 2) glds16_nom0(ws + OFF_CB + w * 1024, wa, lds0 + K2_CB + w * 1024);
  if (xcd < 7) {
    conv_body<true>(ssm_k, ssm_D, ws, smem, w, wf, wbase);
  } else {
    static_for<0, NWC>(ldwf);
    wait_vm<0>();
    __builtin_amdgcn_s_sleep(60);
  }
  STAMP_DECL;
  STAMP(ws, 2, 0);
  const char* xbase = ws + OFF_XS + ((size_t)(8 * w) * S + mt * 128) * 2;
  const u32 xoff = (u32)((lane >> 3) * S + ((lane & 7) ^ (((lane >> 4) & 1) << 2)) * 8) * 2;
  const char* wdbase = ws + OFF_WD + ((size_t)(2 * dt) * 112 + 14 * w) * 1024 + 4096;
  const int gsl = w < 6 ? w : w - 6;
  const char* gbase = ws + OFF_SG + (size_t)((gsl >> 1) * S + mt * 128 + (gsl & 1) * 64) * 8;
  auto ldwd = [&](auto kI) {
    constexpr int q = decltype(kI)::value, k = (q & 1) * 14 + (q >> 1), kk = k % 14;
    gload16_off<(kk % 8) * 1024 - 4096>(wd[k], wdbase + (k / 14) * 112 * 1024 + (kk / 8) * 8192, wa);
  };
  auto ldx = [&](auto cI) {
    constexpr int c = decltype(cI)::value;
    glds16_sc1(xbase + (size_t)(c % 7) * (64 * S * 2) + (c / 7) * 128, xoff, lds0 + (64 * c + 8 * w) * 128);
  };
  auto xok = [&](auto c0I, auto c1I) {
    u32 m = 0;
#pragma unroll
    for (int c = decltype(c0I)::value; c < decltype(c1I)::value; ++c) {
      const u32x4 t = *(const LAS u32x4*)(smem + (64 * c + 8 * w) * 128 + lane * 16);
      m = umax3(umax3(m, t[0], t[1]), t[2], t[3]);
    }
    return (bool)__all(m != ~0u);
  };
  auto xvalid = [&](auto c0I, auto c1I) {
    if (__builtin_expect(!xok(c0I, c1I), 0)) {
#pragma nounroll
      for (int spin = 0; spin < (1 << 14); ++spin) {
        __builtin_amdgcn_s_sleep(4);
        static_for<decltype(c0I)::value, decltype(c1I)::value>(ldx);
        wait_vm<0>();
        if (xok(c0I, c1I)) break;
      }
    }
    __builtin_amdgcn_sched_barrier(0);
  };
  wait_lgkm0();
  bar();
  auto pin = [](f32x16& a) { asm volatile("" : "+v"(a)); };
  auto pinw = [&](auto kI) {
    u32x4 t = wf[decltype(kI)::value];
    asm volatile("" : "+v"(t)::"memory");
    wf[decltype(kI)::value] = t;
  };
  u32x4 aext;
  {
    wait_vm<4>();
    static_for<0, NWC>(pinw);
    f32x16 va;
#pragma unroll
    for (int r = 0; r < 16; ++r) va[r] = 0.f;
    const char* cbp = smem + K2_CB + (n & 1) * (D * 2) + h * 16;
    constexpr int NL = 28 - NWC + 7;
    static_for<0, 28>([&](auto kI) {
      constexpr int k = decltype(kI)::value;
      if constexpr (k == NWC) {
        wait_vm<7>();
        static_for<NWC, 28>(pinw);
      }
      va = mfma32(*(const LAS u32x4*)(cbp + k * 32), __builtin_bit_cast(bf16x8, wf[k]), va);
      pin(va);
      if constexpr (k < NWC) {
        static_for<(k * NL) / NWC, ((k + 1) * NL) / NWC>([&](auto lI) {
          constexpr int l = decltype(lI)::value;
          if constexpr (l < 28 - NWC) ldwf(std::integral_constant<int, NWC + l>{});
          else ldx(std::integral_constant<int, l - (28 - NWC)>{});
        });
      }
      __builtin_amdgcn_sched_barrier(0);
    });
    const float v1 = va[0], v2 = va[1];
    const float v1h = round_bf16(v1), v2h = round_bf16(v2);
    u32x4 a = {pack_bf16(v1h, v1h), pack_bf16(v1 - v1h, v2h), pack_bf16(v2h, v2 - v2h), 0u};
    if (h) a = u32x4{0u, 0u, 0u, 0u};
    asm volatile("" : "+v"(a));
    aext = a;
  }
  int xo[2][2];
  {
    const int blk = (lane >> 4) & 1, qq = (lane & 15) >> 2, p = lane & 3;
#pragma unroll
    for (int i = 0; i < 2; ++i)
#pragma unroll
      for (int t = 0; t < 2; ++t)
        xo[i][t] = 128 * (8 * h + 4 * t + qq) + 16 * ((4 * i + 2 * blk + (p >> 1)) ^ ((qq >> 1) << 2)) + 8 * (p & 1);
  }
  f32x16 acc[4];
#pragma unroll
  for (int i = 0; i < 4; ++i)
#pragma unroll
    for (int r = 0; r < 16; ++r) acc[i][r] = 0.f;
  asm volatile("" : "+v"(xo[0][0]), "+v"(xo[0][1]), "+v"(xo[1][0]), "+v"(xo[1][1]));
  constexpr int PF = 3;
  bf16x8 xr[PF][2];
  auto rdx = [&](auto kI, auto iI) {
    constexpr int k = decltype(kI)::value, i = decltype(iI)::value;
    if constexpr (k == 28) {
      int a0 = xo[i][0] + K2_XH, a1 = xo[i][1] + K2_XH;
      asm volatile("" : "+v"(a0), "+v"(a1));
      xo[i][0] = a0;
      xo[i][1] = a1;
    }
    xr[k % PF][i] = tr_frag(smem, xo[i][0] + (k % 28) * 2048, xo[i][1] + (k % 28) * 2048);
  };
  using I0 = std::integral_constant<int, 0>;
  using I1 = std::integral_constant<int, 1>;
  using I2 = std::integral_constant<int, 2>;
  using I3 = std::integral_constant<int, 3>;
  using I7 = std::integral_constant<int, 7>;
  using I14 = std::integral_constant<int, 14>;
  LAS u64* CS = (LAS u64*)(smem + K2_ST);
  wait_vm<0>();
  xvalid(I0{}, I7{});
  bar();
  YSTAMP(1);
  static_for<0, PF - 1>([&](auto jI) {
    rdx(jI, I0{});
    rdx(jI, I1{});
  });
  __builtin_amdgcn_sched_barrier(0);
  u64 gv;
  static_for<0, 28>([&](auto kI) {
    constexpr int k = decltype(kI)::value;
    if constexpr (k == 12) {
      wait_vm<0>();
      xvalid(I7{}, I14{});
      u64 t;
      asm volatile(SNOP2 "global_load_dwordx2 %0, %1, %2 sc1" : "=v"(t) : "v"((u32)lane * 8), "s"(uni(gbase)) : "memory");
      gv = t;
    }
    if constexpr (k == 24) {
      u64 t = gv;
      asm volatile("s_waitcnt vmcnt(%1)" : "+v"(t) : "n"(EXP_WE) : "memory");
      if (!__all((t >> 32) != 0)) {
        for (int spin = 0; spin < (1 << 16); ++spin) {
          __builtin_amdgcn_s_sleep(2);
          t = __hip_atomic_load((gu64*)(size_t)gbase + lane, RLX_AGENT);
          if (__all((t >> 32) != 0)) break;
        }
        __builtin_amdgcn_s_waitcnt(0x0F70);
      }
      CS[gsl * 64 + lane] = t;
      wait_lgkm0();
      __builtin_amdgcn_sched_barrier(0);
      bar();
    }
    __builtin_amdgcn_sched_barrier(0);
    acc[0] = mfma32(wf[k], xr[k % PF][0], acc[0]);
    pin(acc[0]);
    __builtin_amdgcn_sched_barrier(0);
    rdx(std::integral_constant<int, k + PF - 1>{}, I0{});
    __builtin_amdgcn_sched_barrier(0);
    acc[1] = mfma32(wf[k], xr[k % PF][1], acc[1]);
    pin(acc[1]);
    __builtin_amdgcn_sched_barrier(0);
    rdx(std::integral_constant<int, k + PF - 1>{}, I1{});
    if constexpr (k < 7) ldx(std::integral_constant<int, 7 + k>{});
    if constexpr (k >= 13 && k - 13 < EXP_WE) ldwd(std::integral_constant<int, k - 13>{});
    __builtin_amdgcn_sched_barrier(0);
  });
  YSTAMP(2);
  const rsrc_t rw = mk_rsrc(ws + OFF_ACT + ((size_t)(mt * 4) * 112 + tile) * 1024, 337 * 1024);
  const rsrc_t ra = mk_rsrc(ws + OFF_ACT + ((size_t)(mt * 4 + sh2) * 112 + 14 * w) * 1024, 238 * 1024);
  u32x4 af[28];
  auto ldact = [&](auto kI) {
    constexpr int k = decltype(kI)::value;
    af[k] = __builtin_amdgcn_raw_buffer_load_b128(ra, lane * 16 + (k % 14) * 1024, (k / 14) * 224 * 1024, 16);
  };
  float c1[2], c2[2], var[2], pe[2], pm[2], pr[2];
  u32x4 ov[2];
  auto corr = [&](auto iI) {
    constexpr int i = decltype(iI)::value;
    u64 g[3];
#pragma unroll
    for (int k = 0; k < 3; ++k) g[k] = CS[k * 128 + i * 32 + n];
    const u32x4 bext = {(u32)g[0], (u32)(g[0] >> 32), (u32)g[1], 0u};
    acc[i] = mfma32(aext, __builtin_bit_cast(bf16x8, bext), acc[i]);
    c1[i & 1] = __builtin_bit_cast(float, (u32)(g[2] >> 32));
    c2[i & 1] = __builtin_bit_cast(float, (u32)g[2]);
    var[i & 1] = __builtin_bit_cast(float, (u32)(g[1] >> 32));
  };
  auto slice = [&](auto iI, auto uI) {
    constexpr int i = decltype(iI)::value, u = decltype(uI)::value, z = i & 1;
    if constexpr (u >= 1) {
      float o = pm[z] * __builtin_amdgcn_rcpf(__builtin_fmaf(pe[z], var[z], var[z]));
      if constexpr ((u - 1) & 1) {
        u32 t = umin32(pack_bf16(pr[z], o));
        asm volatile("" : "+v"(t));
        ov[z][(u - 1) / 2] = t;
      } else {
        asm volatile("" : "+v"(o));
        pr[z] = o;
      }
    }
    if constexpr (u < 8) {
      const float a = acc[i][u];
      float e = __builtin_amdgcn_exp2f(__builtin_fmaf(a * a, c2[z], c1[z]) * a), m = a * acc[i][u + 8];
      asm volatile("" : "+v"(e), "+v"(m));
      pe[z] = e;
      pm[z] = m;
    } else {
      __builtin_amdgcn_raw_buffer_store_b128(ov[z], rw, lane * 16 + i * 112 * 1024, 0, 16);
      asm volatile("s_nop 1");
    }
  };
  corr(I0{});
  corr(I1{});
  __builtin_amdgcn_sched_barrier(0);
  static_for<0, 28>([&](auto kI) {
    constexpr int k = decltype(kI)::value;
    acc[2] = mfma32(wf[k], xr[(28 + k) % PF][0], acc[2]);
    pin(acc[2]);
    __builtin_amdgcn_sched_barrier(0);
    if constexpr (k + PF - 1 < 28) rdx(std::integral_constant<int, 28 + k + PF - 1>{}, I0{});
    if constexpr (k >= 1 && k <= 9) slice(I0{}, std::integral_constant<int, k - 1>{});
    __builtin_amdgcn_sched_barrier(0);
    acc[3] = mfma32(wf[k], xr[(28 + k) % PF][1], acc[3]);
    pin(acc[3]);
    __builtin_amdgcn_sched_barrier(0);
    if constexpr (k % EXP_LOCK == EXP_LOCK - 1) bar();
    if constexpr (k + PF - 1 < 28) rdx(std::integral_constant<int, 28 + k + PF - 1>{}, I1{});
    if constexpr (k >= 1 && k <= 9) slice(I1{}, std::integral_constant<int, k - 1>{});
    if constexpr (EXP_WE + k < 28) ldwd(std::integral_constant<int, EXP_WE + k>{});
    else if constexpr (EXP_WE + k - 28 < 7) ldact(std::integral_constant<int, EXP_WE + k - 28>{});
    __builtin_amdgcn_sched_barrier(0);
  });
  YSTAMP(3);
  constexpr int A0 = EXP_WE < 7 ? EXP_WE : 7;
  corr(I2{});
  corr(I3{});
  __builtin_amdgcn_sched_barrier(0);
  static_for<0, 9>([&](auto uI) {
    constexpr int u = decltype(uI)::value;
    slice(I2{}, uI);
    slice(I3{}, uI);
    if constexpr (A0 + u < 14 && u < 8) ldact(std::integral_constant<int, A0 + u>{});
    __builtin_amdgcn_sched_barrier(0);
  });
  static_for<(A0 + 8 < 14 ? A0 + 8 : 14), 14>(ldact);
  bar();
  YSTAMP(4);
  auto chk = [&](auto gI) {
    constexpr int g = decltype(gI)::value;
    u32 m = 0;
#pragma unroll
    for (int k = 7 * g; k < 7 * g + 7; ++k) m = umax3(umax3(m, af[k][0], af[k][1]), af[k][2], af[k][3]);
    return (bool)__all(m != ~0u);
  };
  auto valid = [&](auto gI) {
    constexpr int g = decltype(gI)::value;
    if (__builtin_expect(!chk(gI), 0)) {
#pragma nounroll
      for (int spin = 0; spin < (1 << 14); ++spin) {
        __builtin_amdgcn_s_sleep(8);
        asm volatile("" ::: "memory");
        static_for<7 * g, 7 * g + 7>(ldact);
        if (chk(gI)) break;
      }
      __builtin_amdgcn_s_waitcnt(0x0F70);
    }
    __builtin_amdgcn_sched_barrier(0);
  };
  const int pcol = mt * 128 + sh2 * 32 + n;
  const int dbase = dt * 64 + w * 8 + 4 * h;
  __builtin_amdgcn_sched_barrier(0);
  LAS f32x4* R = (LAS f32x4*)smem;
  f32x16 o[4];
#pragma unroll
  for (int i = 0; i < 4; ++i)
#pragma unroll
    for (int r = 0; r < 16; ++r) o[i][r] = 0.f;
  auto dump1 = [&](auto tjI, auto cI) {
    constexpr int tj = decltype(tjI)::value, c = decltype(cI)::value, i = tj * 2 + (c >> 2), q = c & 3;
    R[((tj * 8 + c) * 8 + w) * 64 + lane] = f32x4{o[i][4 * q], o[i][4 * q + 1], o[i][4 * q + 2], o[i][4 * q + 3]};
  };
  auto step = [&](auto kI) {
    constexpr int k = decltype(kI)::value, tj = k / 14;
    const bf16x8 b = __builtin_bit_cast(bf16x8, af[k]);
    o[2 * tj] = mfma32(wd[k % 14], b, o[2 * tj]);
    pin(o[2 * tj]);
    __builtin_amdgcn_sched_barrier(0);
    if constexpr (k >= 15 && k < 19) dump1(I0{}, std::integral_constant<int, 2 * (k - 15)>{});
    __builtin_amdgcn_sched_barrier(0);
    o[2 * tj + 1] = mfma32(wd[14 + k % 14], b, o[2 * tj + 1]);
    pin(o[2 * tj + 1]);
    __builtin_amdgcn_sched_barrier(0);
    if constexpr (k >= 15 && k < 19) dump1(I0{}, std::integral_constant<int, 2 * (k - 15) + 1>{});
    if constexpr (k >= EXP_T1 && k < 14) {
      constexpr int n0 = ((k - EXP_T1) * 14) / (14 - EXP_T1), n1 = ((k + 1 - EXP_T1) * 14) / (14 - EXP_T1);
      static_for<14 + n0, 14 + n1>(ldact);
    }
    __builtin_amdgcn_sched_barrier(0);
  };
  u16 xres[2][4];
  f32x4 gbv;
  auto fin = [&](auto tjI) {
    constexpr int tj = decltype(tjI)::value;
    f32x4 v[8];
#pragma unroll
    for (int sw = 0; sw < 8; ++sw) v[sw] = R[((tj * 8 + w) * 8 + sw) * 64 + lane];
    const f32x4 sum = ((v[0] + v[1]) + (v[2] + v[3])) + ((v[4] + v[5]) + (v[6] + v[7]));
    float r[4];
#pragma unroll
    for (int e = 0; e < 4; ++e) r[e] = __builtin_fmaf(gbv[e], sum[e], bf2f(xres[tj][e]));
    const int pc = pcol + 64 * tj;
    const int srow = (pc & ~7) | ((pc & 3) << 1) | ((pc >> 2) & 1);
    *(u32x2*)(out + (size_t)srow * D + dbase) = u32x2{pack_bf16(r[0], r[1]), pack_bf16(r[2], r[3])};
  };
  valid(I0{});
  asm volatile(""
               : "+v"(wd[0]), "+v"(wd[1]), "+v"(wd[2]), "+v"(wd[3]), "+v"(wd[4]), "+v"(wd[5]), "+v"(wd[6]), "+v"(wd[7]),
                 "+v"(wd[8]), "+v"(wd[9]), "+v"(wd[10]), "+v"(wd[11]), "+v"(wd[12]), "+v"(wd[13])::"memory");
  asm volatile(""
               : "+v"(wd[14]), "+v"(wd[15]), "+v"(wd[16]), "+v"(wd[17]), "+v"(wd[18]), "+v"(wd[19]), "+v"(wd[20]),
                 "+v"(wd[21]), "+v"(wd[22]), "+v"(wd[23]), "+v"(wd[24]), "+v"(wd[25]), "+v"(wd[26]), "+v"(wd[27])::"memory");
  YSTAMP(5);
  static_for<0, 3>(step);
  {
    const rsrc_t rx = mk_rsrc(ws + OFF_X1T, D * S * 2);
#pragma unroll
    for (int r = 0; r < 8; ++r)
      xres[r >> 2][r & 3] = __builtin_amdgcn_raw_buffer_load_b16(rx, ((dbase + (r & 3)) * S + pcol + (r >> 2) * 64) * 2, 0, 16);
    gbv = *(const f32x4*)((const float*)(ws + OFF_MOD) + 5 * D + dbase);
    __builtin_amdgcn_sched_barrier(0);
  }
  static_for<3, 7>(step);
  valid(I1{});
  static_for<7, 14>(step);
  valid(I2{});
  static_for<14, 21>(step);
  wait_lgkm0();
  bar();
  valid(I3{});
  static_for<21, 24>(step);
  fin(I0{});
  __builtin_amdgcn_sched_barrier(0);
  static_for<24, 28>(step);
  static_for<0, 8>([&](auto cI) { dump1(I1{}, cI); });
  wait_lgkm0();
  bar();
  STAMP(ws, 2, 6);
  fin(I1{});
}

extern "C" __global__ NOPK __launch_bounds__(512, 2) void k1_main(const u16* __restrict__ ssm_k,
                                                                  const u16* __restrict__ ssm_D, char* __restrict__ ws,
                                                                  u16* __restrict__ out) {
  extern __shared__ __attribute__((aligned(16))) char smem[];
  const int w = __builtin_amdgcn_readfirstlane(threadIdx.x >> 6);
  if (blockIdx.x >= 224) {
    if ((blockIdx.x & 7) < 7) {
      u32x4 dummy[28];
      conv_body<false>(ssm_k, ssm_D, ws, smem, w, dummy, nullptr);
    } else {
      __builtin_amdgcn_s_sleep(100);
    }
    k2_stats(ws, smem, blockIdx.x - 224);
  } else {
    k2_gemm(ssm_k, ssm_D, ws, out, smem, w);
  }
}

extern "C" __device__ __attribute__((used, noinline)) void k_code_end() {}

extern "C" void kernel_launch(void* const* d_in, const int* in_sizes, int n_in, void* d_out, int out_size, void* d_ws,
                              size_t ws_size, hipStream_t stream) {
  const u16* q = (const u16*)d_in[0];
  const u16* k = (const u16*)d_in[1];
  const u16* v = (const u16*)d_in[2];
  const u16* shift_k = (const u16*)d_in[3];
  const u16* ssm_k = (const u16*)d_in[4];
  const u16* ssm_D = (const u16*)d_in[5];
  const u16* w_gate = (const u16*)d_in[7];
  const u16* w_up = (const u16*)d_in[8];
  const u16* w_down = (const u16*)d_in[9];
  const u16* c = (const u16*)d_in[10];
  const u16* w_mod = (const u16*)d_in[11];
  char* ws = (char*)d_ws;
  (void)hipFuncSetAttribute((const void*)k1_main, hipFuncAttributeMaxDynamicSharedMemorySize, K2_LDS);
  k0_prep<<<K0_GRID, 256, 0, stream>>>(q, k, v, shift_k, c, w_mod, ssm_k, w_gate, w_up, w_down, ws);
  k1_main<<<256, 512, K2_LDS, stream>>>(ssm_k, ssm_D, ws, (u16*)d_out);
}
```

```cpp
#include <hip/hip_runtime.h>
#include <hip/hip_bf16.h>
#include <stdint.h>
#include <type_traits>

#define NTAUX 0
#define NTLOAD(p) (*(p))
typedef unsigned short u16;
typedef unsigned int u32;
typedef short bf16x8 __attribute__((ext_vector_type(8)));
typedef short s16x4 __attribute__((ext_vector_type(4)));
typedef float f32x2 __attribute__((ext_vector_type(2)));
typedef float f32x4 __attribute__((ext_vector_type(4)));
typedef float f32x16 __attribute__((ext_vector_type(16)));
typedef u32 u32x2 __attribute__((ext_vector_type(2)));
typedef u32 u32x4 __attribute__((ext_vector_type(4)));
typedef __bf16 bf16x2_t __attribute__((ext_vector_type(2)));
#define LAS __attribute__((address_space(3)))
#define DI __device__ __forceinline__

constexpr int S = 2048, D = 448, F = 1792;

constexpr size_t SZ_DS = (size_t)D * S * 2;
constexpr size_t OFF_RE = 0;
constexpr size_t OFF_RO = OFF_RE + SZ_DS;
constexpr size_t OFF_QT = OFF_RO + SZ_DS;
constexpr size_t OFF_X1T = OFF_QT + SZ_DS;
constexpr size_t OFF_MEAN = OFF_X1T + SZ_DS;
constexpr size_t OFF_RSTD = OFF_MEAN + S * 4;
constexpr size_t OFF_MOD = OFF_RSTD + S * 4;
constexpr size_t OFF_SG = OFF_MOD + 16384;
constexpr size_t OFF_CB = OFF_MOD + 12288;

constexpr size_t OFF_VG = OFF_SG + 3 * S * 8;
constexpr size_t OFF_XS = OFF_VG + 4 * S * 8;
constexpr size_t OFF_VP = OFF_XS + SZ_DS;
constexpr size_t OFF_WGU = OFF_VP + 8 * 2 * 3584 * 4;
constexpr size_t OFF_WD = OFF_WGU + (size_t)2 * D * F * 2;
constexpr size_t OFF_ACT = OFF_WD + (size_t)D * F * 2;
constexpr size_t WS_END = OFF_ACT + (size_t)S * F * 2;

struct Stamps2 {};
#define STAMP_DECL
#define STAMP(wsp, kid, i)
#define STAMP_FLUSH(wsp, kid)
#define HWID(wsp, kid)
#define XSTAMP(i)
#define YSTAMP(i) STAMP(ws, 2, i)
#if defined(__HIP_DEVICE_COMPILE__)
#define NOPK __attribute__((target("no-packed-fp32-ops")))
#else
#define NOPK
#endif

template <int I, int N, class Fn>
DI void static_for(Fn&& f) {
  if constexpr (I < N) {
    f(std::integral_constant<int, I>{});
    static_for<I + 1, N>(f);
  }
}

DI float lo_f(u32 x) { return __builtin_bit_cast(float, x << 16); }
DI float hi_f(u32 x) { return __builtin_bit_cast(float, x & 0xffff0000u); }
DI u32 pack_bf16(float a, float b) {
  f32x2 v = {a, b};
  return __builtin_bit_cast(u32, __builtin_convertvector(v, bf16x2_t));
}
DI float round_bf16(float a) { return lo_f(pack_bf16(a, 0.f)); }
DI float bf2f(u16 x) { return __builtin_bit_cast(float, (u32)x << 16); }

template <int N>
DI void wait_vm() { asm volatile("s_waitcnt vmcnt(%0)" ::"n"(N) : "memory"); }
DI void wait_lgkm0() { asm volatile("s_waitcnt lgkmcnt(0)" ::: "memory"); }
DI void bar() { __builtin_amdgcn_s_barrier(); }

#define SNOP "s_nop 0\n\t"
#define SNOP2 ""
DI const void* uni(const void* p) {
  const unsigned long long v = (unsigned long long)p;
  const u32 lo = __builtin_amdgcn_readfirstlane((u32)v), hi = __builtin_amdgcn_readfirstlane((u32)(v >> 32));
  return (const void*)(((unsigned long long)hi << 32) | lo);
}
DI void glds16_asm(const void* sbase, u32 voff, u32 lds_addr) {
  u32 keep;
  sbase = uni(sbase);
  lds_addr = __builtin_amdgcn_readfirstlane(lds_addr);
  asm volatile("s_mov_b32 %0, m0\n\ts_mov_b32 m0, %3\n\t" SNOP "global_load_lds_dwordx4 %1, %2\n\ts_mov_b32 m0, %0"
               : "=&s"(keep)
               : "v"(voff), "s"(sbase), "s"(lds_addr)
               : "memory");
}
DI void glds16(const void* g, char* lds) {
  __builtin_amdgcn_global_load_lds((const __attribute__((address_space(1))) void*)g, (LAS void*)lds, 16, 0, NTAUX);
}

template <int CTRL>
DI float dpp_add(float v) {
  const int x = __builtin_bit_cast(int, v);
  return v + __builtin_bit_cast(float, __builtin_amdgcn_update_dpp(x, x, CTRL, 0xf, 0xf, false));
}
DI float sum8(float v) {
  v = dpp_add<0xB1>(v);
  v = dpp_add<0x4E>(v);
  v = dpp_add<0x141>(v);
  return v;
}
DI float sum16(float v) { return dpp_add<0x140>(sum8(v)); }
DI float dot2(u32 a, u32 b, float c) {
  return __builtin_amdgcn_fdot2_f32_bf16(__builtin_bit_cast(bf16x2_t, a), __builtin_bit_cast(bf16x2_t, b), c, false);
}
typedef __amdgpu_buffer_rsrc_t rsrc_t;
DI rsrc_t mk_rsrc(const void* p, u32 bytes) { return __builtin_amdgcn_make_buffer_rsrc((void*)p, 0, bytes, 0x00020000); }
DI void st16_sc1(void* p, u32x4 v) { asm volatile("global_store_dwordx4 %0, %1, off sc1\n\ts_nop 1" ::"v"(p), "v"(v) : "memory"); }
DI void st8_sc1(void* p, u32x2 v) { asm volatile("global_store_dwordx2 %0, %1, off sc1" ::"v"(p), "v"(v) : "memory"); }
DI void st4_sc1(void* p, float v) { asm volatile("global_store_dword %0, %1, off sc1" ::"v"(p), "v"(v) : "memory"); }

#define EXP_E12 2
#define EXP_E13 6
#define EXP_LOCK 4
#define EXP_NW2 14
DI bf16x8 tr_frag(const char* smem, int o0, int o1) {
  s16x4 x0 = __builtin_amdgcn_ds_read_tr16_b64_v4i16((LAS s16x4*)(smem + o0));
  s16x4 x1 = __builtin_amdgcn_ds_read_tr16_b64_v4i16((LAS s16x4*)(smem + o1));
  return __builtin_shufflevector(x0, x1, 0, 1, 2, 3, 4, 5, 6, 7);
}
DI f32x16 mfma32(u32x4 a, bf16x8 b, f32x16 c) {
  return __builtin_amdgcn_mfma_f32_32x32x16_bf16(__builtin_bit_cast(bf16x8, a), b, c, 0, 0, 0);
}

DI void retile(int task, const u16* __restrict__ w_gate, const u16* __restrict__ w_up, const u16* __restrict__ w_down,
               char* __restrict__ ws, char* smem, int w, int lane) {
  const int xr = lane >> 4, slot = lane & 15;
  const int chn = slot ^ ((xr << 2) | (w & 3));
  const int h = lane >> 5, blk = (lane >> 4) & 1, qq = (lane & 15) >> 2, p = lane & 3;
  const bool gu = task < 112;
  int kq = 0, fg = 0, dg = 0, fk = 0;
  if (gu) {
    fg = task % 28, kq = task / 28;
    const u16* src = (chn < 8 ? w_gate : w_up) + (size_t)(kq * 112 + 4 * w + xr) * F + fg * 64 + (chn & 7) * 8;
#pragma unroll
    for (int i = 0; i < 7; ++i) glds16(src + (size_t)i * 16 * F, smem + (w + 4 * i) * 1024);
  } else {
    dg = (task - 112) % 7, fk = (task - 112) / 7;
    const u16* src = w_down + (size_t)(fk * 224 + (chn >> 3) * 112 + 4 * w + xr) * D + dg * 64 + (chn & 7) * 8;
#pragma unroll
    for (int i = 0; i < 7; ++i) glds16(src + (size_t)i * 16 * D, smem + (w + 4 * i) * 1024);
  }
  const int col = gu ? blk * 128 + w * 32 + (p & 1) * 16 + (p >> 1) * 8 : (w >> 1) * 128 + (w & 1) * 64 + blk * 32 + p * 8;
  int o[2];
#pragma unroll
  for (int t = 0; t < 2; ++t)
    o[t] = 256 * (8 * h + 4 * t + qq) + 16 * ((col >> 4) ^ ((qq << 2) | ((2 * h + t) & 3))) + (col & 8);
  u32x4* dst = gu ? (u32x4*)(ws + OFF_WGU) + ((size_t)(fg * 4 + w) * 28 + kq * 7) * 64 + lane
                  : (u32x4*)(ws + OFF_WD) + ((size_t)(dg * 2 + (w & 1)) * 112 + fk * 14 + (w >> 1) * 7) * 64 + lane;
  __syncthreads();
#pragma unroll
  for (int i = 0; i < 7; ++i)
    st16_sc1(dst + i * 64, __builtin_bit_cast(u32x4, tr_frag(smem, o[0] + i * 4096, o[1] + i * 4096)));
}

#define EXP_DELAY 0
#define EXP_T1 5
#define EXP_A0 4
#define ACTAUX 16
constexpr int K0_GRID = 504;

extern "C" __global__ __launch_bounds__(256) void k0_prep(const u16* __restrict__ q, const u16* __restrict__ k,
                                               const u16* __restrict__ v, const u16* __restrict__ shift_k,
                                               const u16* __restrict__ c, const u16* __restrict__ w_mod,
                                               const u16* __restrict__ ssm_k, const u16* __restrict__ w_gate,
                                               const u16* __restrict__ w_up, const u16* __restrict__ w_down,
                                               char* __restrict__ ws) {
  __shared__ __attribute__((aligned(16))) char smem[28672];
  float(*red)[64] = (float(*)[64])smem;
  const int b = blockIdx.x, tid = threadIdx.x, lane = tid & 63, w = __builtin_amdgcn_readfirstlane(tid >> 6);
  STAMP_DECL;
  STAMP(ws, 0, 0);
  const int xcd = b & 7, idx = b >> 3;
  u32 cwarm = 0;
  if (idx == 40 || idx == 41) {
    u32 lo, hi, len;
    asm volatile(
        "s_getpc_b64 s[40:41]\n\ts_add_u32 s40, s40, k1_main@rel32@lo+4\n\ts_addc_u32 s41, s41, k1_main@rel32@hi+12\n\t"
        "s_mov_b32 %0, s40\n\ts_mov_b32 %1, s41\n\ts_mov_b32 %2, k_code_end-k1_main"
        : "=s"(lo), "=s"(hi), "=s"(len)::"s40", "s41", "scc");
    const char* code = (const char*)(((unsigned long long)hi << 32) | lo);
    if ((u32)((idx - 40) * 256 + tid) * 128 < len) cwarm = *(const u32*)(code + ((idx - 40) * 256 + tid) * 128);
  }
  if (idx >= 42) {
    if (b - 336 < 128) {
      const int t = b - 336;
      u32x4* ap = (u32x4*)(ws + (t < 64 ? OFF_X1T : OFF_XS)) + (size_t)(t & 63) * 1792 + tid;
      const u32x4 sent = {~0u, ~0u, ~0u, ~0u};
#pragma unroll
      for (int i = 0; i < 7; ++i) st16_sc1(ap + i * 256, sent);
    }
    retile(b - 336, w_gate, w_up, w_down, ws, smem, w, lane);
    STAMP(ws, 7, 1);
    STAMP_FLUSH(ws, 7);
    return;
  }
  int misc = -1;
  if (xcd == 7) misc = idx < 14 ? 64 + idx : (idx < 36 ? idx - 14 : 1000);
  else if (idx < 4) misc = 64 + 14 + xcd * 4 + idx;
  else if (idx >= 36) misc = 22 + xcd * 6 + (idx - 36);
  if (misc < 0) {
    const int rb = idx - 4;
    const int sq = lane >> 5, cp = lane & 31;
    const int d0 = xcd * 64 + cp * 2;
    const int a = rb * 8 + w * 2 + sq;
    const int s0 = a * 8;
    const rsrc_t rk = mk_rsrc(k, S * D * 2), rv = mk_rsrc(v, S * D * 2), rq = mk_rsrc(q, S * D * 2);
    const int oB = (s0 * D + d0) * 2;
    const int oA = oB - 4 * D * 2, oC = oB + 4 * D * 2;
    u32 kk[12], vv[9], qq[8];
#pragma unroll
    for (int i = 0; i < 4; ++i) kk[i] = __builtin_amdgcn_raw_buffer_load_b32(rk, oA + i * D * 2, 0, NTAUX);
#pragma unroll
    for (int i = 0; i < 4; ++i) kk[4 + i] = __builtin_amdgcn_raw_buffer_load_b32(rk, oB + i * D * 2, 0, NTAUX);
#pragma unroll
    for (int i = 0; i < 4; ++i) kk[8 + i] = __builtin_amdgcn_raw_buffer_load_b32(rk, oC + i * D * 2, 0, NTAUX);
    vv[0] = __builtin_amdgcn_raw_buffer_load_b32(rv, oA + 3 * D * 2, 0, NTAUX);
#pragma unroll
    for (int i = 0; i < 4; ++i) vv[1 + i] = __builtin_amdgcn_raw_buffer_load_b32(rv, oB + i * D * 2, 0, NTAUX);
#pragma unroll
    for (int i = 0; i < 4; ++i) vv[5 + i] = __builtin_amdgcn_raw_buffer_load_b32(rv, oC + i * D * 2, 0, NTAUX);
#pragma unroll
    for (int i = 0; i < 4; ++i) qq[i] = __builtin_amdgcn_raw_buffer_load_b32(rq, oB + i * D * 2, 0, NTAUX);
#pragma unroll
    for (int i = 0; i < 4; ++i) qq[4 + i] = __builtin_amdgcn_raw_buffer_load_b32(rq, oC + i * D * 2, 0, NTAUX);
    const u32x4 sk = *(const u32x4*)(shift_k + d0 * 4);
    u32 warm = 0;
    if (w == 0) warm = NTLOAD((const u32*)((const char*)ssm_k + ((size_t)(xcd * 32 + rb) * 64 + lane) * 128));
    f32x2 wt[4] = {{lo_f(sk[0]), lo_f(sk[2])}, {hi_f(sk[0]), hi_f(sk[2])}, {lo_f(sk[1]), lo_f(sk[3])}, {hi_f(sk[1]), hi_f(sk[3])}};
    f32x2 kf[12], u[9];
#pragma unroll
    for (int i = 0; i < 12; ++i) kf[i] = f32x2{lo_f(kk[i]), hi_f(kk[i])};
#pragma unroll
    for (int i = 0; i < 9; ++i) {
      f32x2 t = kf[i + 3] * wt[0];
      t = __builtin_elementwise_fma(kf[i + 2], wt[1], t);
      t = __builtin_elementwise_fma(kf[i + 1], wt[2], t);
      t = __builtin_elementwise_fma(kf[i], wt[3], t);
      u[i] = t * f32x2{lo_f(vv[i]), hi_f(vv[i])};
    }
    {
      char* dst = ws + (size_t)d0 * (S * 2) + (2040 - s0) * 2;
#pragma unroll
      for (int cI = 0; cI < 2; ++cI) {
        *(u32x4*)(dst + OFF_RO + cI * S * 2) = u32x4{pack_bf16(u[8][cI], u[7][cI]), pack_bf16(u[6][cI], u[5][cI]),
                                                     pack_bf16(u[4][cI], u[3][cI]), pack_bf16(u[2][cI], u[1][cI])};
        *(u32x4*)(dst + OFF_RE + cI * S * 2) = u32x4{pack_bf16(u[7][cI], u[6][cI]), pack_bf16(u[5][cI], u[4][cI]),
                                                     pack_bf16(u[3][cI], u[2][cI]), pack_bf16(u[1][cI], u[0][cI])};
      }
      char* dq = ws + OFF_QT + (size_t)d0 * (S * 2) + s0 * 2;
#pragma unroll
      for (int cI = 0; cI < 2; ++cI) {
        const u32 sel = cI ? 0x07060302u : 0x05040100u;
        *(u32x4*)(dq + cI * S * 2) = u32x4{__builtin_amdgcn_perm(qq[2], qq[0], sel), __builtin_amdgcn_perm(qq[6], qq[4], sel),
                                           __builtin_amdgcn_perm(qq[3], qq[1], sel), __builtin_amdgcn_perm(qq[7], qq[5], sel)};
      }
    }
    asm volatile("" ::"v"(warm));
  } else if (misc < 64) {
    const int bb = misc;
    const int row = bb * 32 + w * 8 + (lane >> 3);
    const int sub = lane & 7;
    u32x4 x[7];
#pragma unroll
    for (int i = 0; i < 7; ++i) x[i] = NTLOAD((const u32x4*)(q + (size_t)row * D + (i * 8 + sub) * 8));
    if (sub < 7) ((unsigned long long*)(ws + OFF_SG))[sub * S + row] = sub < 3 ? 0ull : ~0ull;
    float s = 0.f, s2 = 0.f;
#pragma unroll
    for (int i = 0; i < 7; ++i)
#pragma unroll
      for (int j = 0; j < 4; ++j) {
        const u32 t = x[i][j];
        s = dot2(t, 0x3f803f80u, s);
        s2 = dot2(t, t, s2);
      }
    s = sum8(s);
    s2 = sum8(s2);
    const float mean = s * (1.f / D);
    float var = s2 * (1.f / D) - mean * mean;
    var = var < 0.f ? 0.f : var;
    const float rstd = __builtin_amdgcn_rsqf(var + 1e-5f);
    if (sub < 2) {
      const int p = (row & ~7) | ((row & 1) << 2) | ((row >> 1) & 3);
      ((float*)(ws + OFF_MEAN))[sub * S + p] = sub ? rstd : mean;
    }
  } else if (misc < 106) {
    const int bb = misc - 64;
    const int co = lane & 7, rs = lane >> 3;
    const int col0 = bb * 64 + co * 8;
    u32x4 x[14];
#pragma unroll
    for (int i = 0; i < 14; ++i)
      x[i] = NTLOAD((const u32x4*)(w_mod + (size_t)(w * 112 + rs * 14 + i) * (6 * D) + col0));
    u32 cv[7];
#pragma unroll
    for (int i = 0; i < 7; ++i) cv[i] = *(const u32*)(c + w * 112 + rs * 14 + 2 * i);
    __builtin_amdgcn_sched_barrier(0);
    f32x2 acc[4] = {{0.f, 0.f}, {0.f, 0.f}, {0.f, 0.f}, {0.f, 0.f}};
#pragma unroll
    for (int i = 0; i < 14; ++i) {
      const float cf = (i & 1) ? hi_f(cv[i >> 1]) : lo_f(cv[i >> 1]);
      const float sl = round_bf16(cf * __builtin_amdgcn_rcpf(1.f + __builtin_amdgcn_exp2f(cf * -1.44269504f)));
#pragma unroll
      for (int j = 0; j < 4; ++j)
        acc[j] = __builtin_elementwise_fma(f32x2{lo_f(x[i][j]), hi_f(x[i][j])}, f32x2{sl, sl}, acc[j]);
    }
    *(f32x4*)&red[w * 8 + rs][co * 8] = f32x4{acc[0][0], acc[0][1], acc[1][0], acc[1][1]};
    *(f32x4*)&red[w * 8 + rs][co * 8 + 4] = f32x4{acc[2][0], acc[2][1], acc[3][0], acc[3][1]};
    __syncthreads();
    {
      float t = 0.f;
#pragma unroll
      for (int i = 0; i < 8; ++i) t += red[w * 8 + i][lane];
      red[32 + w][lane] = t;
    }
    __syncthreads();
    if (tid < 64) {
      const float t = (red[32][tid] + red[33][tid]) + (red[34][tid] + red[35][tid]);
      const float m = round_bf16(t);
      const int col = bb * 64 + tid;
      ((float*)(ws + OFF_MOD))[col] = m;
      if (col >= 3 * D && col < 5 * D) {
        const bool isc = col >= 4 * D;
        ((u16*)(ws + OFF_CB))[isc ? col - 4 * D : col - 2 * D] = (u16)pack_bf16(isc ? 1.f + m : m, 0.f);
      }
    }
  }
  asm volatile("" ::"v"(cwarm));
  STAMP(ws, 0, 1);
  STAMP_FLUSH(ws, misc < 0 ? 0 : (misc < 64 ? 4 : 5));
}

constexpr int K1_KSTR = 260 * 16;
constexpr int K1_USTR = 286 * 16;
constexpr int K1_UOFF = 4 * K1_KSTR;
constexpr int K1_XOFF = K1_UOFF + 2 * K1_USTR;
constexpr int K1_CH = K1_XOFF + 32768;

DI void glds16x4_asm(const void* sbase, u32 voff, u32 voff_last, u32 lds_addr) {
  u32 keep;
  sbase = uni(sbase);
  lds_addr = __builtin_amdgcn_readfirstlane(lds_addr);
  asm volatile(
      "s_mov_b32 %0, m0\n\ts_mov_b32 m0, %4\n\t" SNOP
      "global_load_lds_dwordx4 %1, %3\n\t"
      "global_load_lds_dwordx4 %1, %3 offset:1024\n\t"
      "global_load_lds_dwordx4 %1, %3 offset:2048\n\t"
      "global_load_lds_dwordx4 %2, %3 offset:3072\n\t"
      "s_mov_b32 m0, %0"
      : "=&s"(keep)
      : "v"(voff), "v"(voff_last), "s"(sbase), "s"(lds_addr)
      : "memory");
}
#define CAS __attribute__((address_space(4)))
constexpr int K2_XB = D * 256;
constexpr int K2_XH = D * 128;
constexpr int K2_ST = K2_XB;
constexpr int K2_CB = 131072;
constexpr int K2_LDS = K2_CB + 2048;

DI void gload16_asm(u32x4& dst, const void* sbase, u32 voff) {
  sbase = uni(sbase);
  asm volatile(SNOP2 "global_load_dwordx4 %0, %1, %2" : "=v"(dst) : "v"(voff), "s"(sbase) : "memory");
}
DI u32 umin32(u32 v) { return v < 0xFFFFFFFEu ? v : 0xFFFFFFFEu; }
DI u32 umax3(u32 a, u32 b, u32 c) { const u32 t = a > b ? a : b; return t > c ? t : c; }
template <int OFF>
DI void gload16_off(u32x4& dst, const void* sbase, u32 voff) {
  sbase = uni(sbase);
  asm volatile(SNOP2 "global_load_dwordx4 %0, %1, %2 offset:%3" : "=v"(dst) : "v"(voff), "s"(sbase), "n"(OFF) : "memory");
}
DI void glds16_nom0(const void* sbase, u32 voff, u32 lds_addr) {
  sbase = uni(sbase);
  lds_addr = __builtin_amdgcn_readfirstlane(lds_addr);
  asm volatile("s_mov_b32 m0, %2\n\t" SNOP "global_load_lds_dwordx4 %0, %1" ::"v"(voff), "s"(sbase), "s"(lds_addr) : "memory");
}
DI void glds16_sc1(const void* sbase, u32 voff, u32 lds_addr) {
  sbase = uni(sbase);
  lds_addr = __builtin_amdgcn_readfirstlane(lds_addr);
  asm volatile("s_mov_b32 m0, %2\n\t" SNOP "global_load_lds_dwordx4 %0, %1 sc1" ::"v"(voff), "s"(sbase), "s"(lds_addr) : "memory");
}
template <int N>
DI void wait_vm_pin(u32x4& a, u32x4& b) {
  asm volatile("s_waitcnt vmcnt(%2)" : "+v"(a), "+v"(b) : "n"(N) : "memory");
}
DI float xor32(float v) {
  const u32 u = __builtin_bit_cast(u32, v);
  auto r = __builtin_amdgcn_permlane32_swap(u, u, false, false);
  const u32 a = r[0], b = r[1];
  return __builtin_bit_cast(float, (threadIdx.x & 32) ? a : b);
}

typedef unsigned long long u64;
typedef __attribute__((address_space(1))) u64 gu64;
typedef __attribute__((address_space(1))) u32 gu32;
#define RLX_AGENT __ATOMIC_RELAXED, __HIP_MEMORY_SCOPE_AGENT
#define EXP_WE 8
#define EXP_NW0 5
#define EXP_NWH 15
#define EXP_NW2X (EXP_NWH - EXP_NW0 - EXP_NW1)
#define EXP_NW1 5
constexpr int NWC = EXP_NW0 + EXP_NW1;
template <bool WF>
DI void conv_body(const u16* __restrict__ ssm_k, const u16* __restrict__ ssm_D, char* __restrict__ ws, char* smem,
                  const int w, u32x4 (&wf)[28], const char* wbase, Stamps2& s2) {
  STAMP(ws, 1, 0);
  const int lane = threadIdx.x & 63;
  const int ch = w >> 2, q = w & 3;
  const int cpair = (blockIdx.x & 7) * 32 + (blockIdx.x >> 3);
  const int d = 2 * cpair + ch;
  char* L = smem + ch * K1_CH;
  const u32 L0 = (u32)(size_t)(LAS char*)L;
  const u16* kf = ssm_k + (size_t)d * S;
  const u32 vo = lane * 16;
  {
    const u32 vl = (lane > 62 ? 62 : lane) * 16;
    glds16x4_asm(kf + (2 + 2 * q), vo, vl, L0 + q * K1_KSTR + 64);
    const char* ub = ws + ((q >> 1) ? OFF_RO : OFF_RE) + (size_t)d * S * 2 + (q & 1) * 2048;
    const u32 ul = L0 + K1_UOFF + (q >> 1) * K1_USTR + (q & 1) * 2048;
    glds16_nom0(ub, vo, ul);
    glds16_nom0(ub + 1024, vo, ul + 1024);
  }
  const int n = lane & 15, q4 = lane >> 4;
  const int pbase = 32 * (n >> 1) + 8 * q4 + 4 * (n & 1) + 512 * q;
  const int di = n + q - 15;
  u32 head;
  asm volatile(SNOP2 "global_load_dword %0, %1, %2" : "=v"(head) : "v"((u32)(di < 0 ? 0 : di) * 4), "s"(uni(kf)) : "memory");
  auto ldwf = [&](auto kI) {
    constexpr int k = decltype(kI)::value;
    gload16_off<(k % 4) * 1024>(wf[k], wbase + (k / 4) * 4096, vo);
  };
  bar();
  if constexpr (WF) static_for<0, EXP_NW0>(ldwf);
  const CAS float* MODc = (const CAS float*)(size_t)(ws + OFF_MOD);
  const float sa = MODc[d], ca = MODc[D + d], ga = MODc[2 * D + d], cbv = MODc[4 * D + d];
  const u32 Draw = ((const CAS u32*)(size_t)ssm_D)[cpair];
  if (lane < 14) *(LAS u32x4*)(L + K1_UOFF + (q >> 1) * K1_USTR + (256 + 14 * (q & 1) + lane) * 16) = u32x4{0u, 0u, 0u, 0u};
  u32 Ab = L0 + (n & 3) * K1_KSTR + ((n >> 2) + q4 + 8 * q) * 16;
  u32 Bb = L0 + K1_UOFF + (n & 1) * K1_USTR + (28 - 4 * (n >> 1) + q4 + 8 * q) * 16;
  u32 Hd = L0 + q * K1_KSTR + n * 4;
  asm volatile("" : "+v"(Ab), "+v"(Bb), "+v"(Hd) : "s"(sa), "s"(ca), "s"(ga), "s"(Draw), "s"(cbv));
  asm volatile("s_waitcnt vmcnt(%1)" : "+v"(head) : "n"(WF ? EXP_NW0 : 0) : "memory");
  *(LAS u32*)(size_t)Hd = di < 0 ? 0u : head;
  wait_lgkm0();
  STAMP(ws, 1, 1);
  bar();
  bf16x8 fa[8], fb[8];
  auto rd = [&](int dl) {
#pragma unroll
    for (int g = 0; g < 8; ++g) {
      fa[g] = *(const LAS bf16x8*)(size_t)(Ab + 64 * dl + 512 * g);
      fb[g] = *(const LAS bf16x8*)(size_t)(Bb + 64 * dl + 512 * (7 - g));
    }
  };
  rd(0);
  __builtin_amdgcn_sched_barrier(0);
  u32x2 qv[2];
  f32x4 mu[2], rs[2];
  asm volatile(SNOP2
               "global_load_dwordx2 %0, %6, %8\n\t"
               "global_load_dwordx2 %1, %6, %8 offset:512\n\t"
               "global_load_dwordx4 %2, %7, %9\n\t"
               "global_load_dwordx4 %3, %7, %9 offset:1024\n\t"
               "global_load_dwordx4 %4, %7, %10\n\t"
               "global_load_dwordx4 %5, %7, %10 offset:1024"
               : "=&v"(qv[0]), "=&v"(qv[1]), "=&v"(mu[0]), "=&v"(mu[1]), "=&v"(rs[0]), "=&v"(rs[1])
               : "v"((u32)pbase * 2), "v"((u32)pbase * 4), "s"(uni(ws + OFF_QT + (size_t)d * S * 2)),
                 "s"(uni(ws + OFF_MEAN)), "s"(uni(ws + OFF_RSTD))
               : "memory");
  const u32x4 sent = {~0u, ~0u, ~0u, ~0u};
  const void* fillb = uni(ws + OFF_ACT + (size_t)(cpair * 8 + w) * 4096);
  f32x4 acc[8];
#pragma unroll
  for (int j = 0; j < 8; ++j) acc[j] = f32x4{0.f, 0.f, 0.f, 0.f};
  static_for<0, 72>([&](auto mI) {
    constexpr int m = decltype(mI)::value, t = m % 36;
    constexpr int sd = t < 1 ? 0 : t < 3 ? 1 : t < 6 ? 2 : t < 10 ? 3 : t < 15 ? 4 : t < 21 ? 5 : t < 28 ? 6 : 7;
    constexpr int g = t - sd * (sd + 1) / 2;
    if constexpr (m == 36) {
      rd(1);
      __builtin_amdgcn_sched_barrier(0);
    }
    f32x4 a = __builtin_amdgcn_mfma_f32_16x16x32_bf16(fa[g], fb[sd - g], acc[sd], 0, 0, 0);
    asm volatile("" : "+v"(a));
    acc[sd] = a;
    if constexpr (m % 4 == 3) {
      constexpr int v = m / 4;
      if constexpr (v < 4) {
        u32x4 sv = sent;
        asm volatile("global_store_dwordx4 %0, %1, %2 offset:%3 sc1\n\ts_nop 1" ::"v"(vo), "v"(sv), "s"(fillb), "n"(v * 1024) : "memory");
      } else if constexpr (WF && v - 4 < EXP_NW1) {
        ldwf(std::integral_constant<int, EXP_NW0 + v - 4>{});
      }
      __builtin_amdgcn_sched_barrier(0);
    }
  });
  STAMP(ws, 1, 3);
  LAS f32x4* X = (LAS f32x4*)(L + K1_XOFF);
#pragma unroll
  for (int j = 0; j < 8; ++j) X[(j * 4 + q) * 64 + lane] = acc[j];
  u32x4 uc[2];
#pragma unroll
  for (int j = 0; j < 2; ++j)
    uc[j] = *(const LAS u32x4*)(L + K1_UOFF + K1_USTR + (255 - (32 * (2 * q + j) + 4 * (n >> 1) + q4)) * 16);
  asm volatile("s_waitcnt vmcnt(%6)"
               : "+v"(qv[0]), "+v"(qv[1]), "+v"(mu[0]), "+v"(mu[1]), "+v"(rs[0]), "+v"(rs[1])
               : "n"(WF ? EXP_NW1 : 0)
               : "memory");
  f32x4 qf[2], hg[2], kd[2];
  {
    const int sh = (n & 1) ? 16 : 0;
    const float gc = ga * round_bf16(1.f + ca), gs = ga * sa, Dd = ch ? hi_f(Draw) : lo_f(Draw);
#pragma unroll
    for (int j = 0; j < 2; ++j) {
#pragma unroll
      for (int r = 0; r < 4; ++r) {
        kd[j][r] = __builtin_bit_cast(float, (uc[j][3 - r] << sh) & 0xffff0000u) * Dd;
        qf[j][r] = (r & 1) ? hi_f(qv[j][r >> 1]) : lo_f(qv[j][r >> 1]);
        hg[j][r] = (qf[j][r] - mu[j][r]) * rs[j][r] * gc + gs;
      }
      asm volatile("" : "+v"(kd[j]), "+v"(qf[j]), "+v"(hg[j]));
    }
  }
  wait_lgkm0();
  bar();
  STAMP(ws, 1, 4);
  u16* X1T = (u16*)(ws + OFF_X1T) + (size_t)d * S + pbase;
  u16* XS = (u16*)(ws + OFF_XS) + (size_t)d * S + pbase;
  const float c1 = round_bf16(1.f + cbv);
#pragma unroll
  for (int j = 0; j < 2; ++j) {
    const LAS f32x4* Xj = X + ((2 * q + j) * 4) * 64 + lane;
    const f32x4 y = (Xj[0] + Xj[64]) + (Xj[128] + Xj[192]);
    const f32x4 o = qf[j] + hg[j] * (y + kd[j]);
    const u32 p0 = umin32(pack_bf16(o[0], o[1])), p1 = umin32(pack_bf16(o[2], o[3]));
    st8_sc1(XS + 256 * j,
            u32x2{umin32(pack_bf16(lo_f(p0) * c1, hi_f(p0) * c1)), umin32(pack_bf16(lo_f(p1) * c1, hi_f(p1) * c1))});
    st8_sc1(X1T + 256 * j, u32x2{p0, p1});
  }
  STAMP(ws, 1, 5);
}

DI void k2_stats(char* __restrict__ ws, char* smem, const int sblk) {
  const int tid = threadIdx.x, lane = tid & 63;
  const int w = __builtin_amdgcn_readfirstlane(tid >> 6);
  const int n = lane & 31, h = lane >> 5;
  STAMP_DECL;
  STAMP(ws, 2, 0);
  {
    const int p0 = sblk * 64;
    const rsrc_t rx = mk_rsrc(ws + OFF_X1T + ((size_t)(w * 56) * S + p0) * 2, 56 * S * 2);
    u32 x[28];
    for (int spin = 0; spin < (1 << 14); ++spin) {
      asm volatile("" ::: "memory");
      u32 m = 0;
#pragma unroll
      for (int i = 0; i < 28; ++i) x[i] = __builtin_amdgcn_raw_buffer_load_b32(rx, (h * S) * 2 + n * 4 + i * (4 * S), 0, 16);
#pragma unroll
      for (int i = 0; i < 28; i += 2) m = umax3(m, x[i], x[i + 1]);
      if (__all(m != ~0u)) break;
      __builtin_amdgcn_s_sleep(4);
    }
    __syncthreads();
    float s0 = 0.f, s1 = 0.f, q0 = 0.f, q1 = 0.f;
#pragma unroll
    for (int i = 0; i < 28; ++i) {
      const float a = lo_f(x[i]), b = hi_f(x[i]);
      s0 += a;
      s1 += b;
      q0 = __builtin_fmaf(a, a, q0);
      q1 = __builtin_fmaf(b, b, q1);
    }
    *(LAS f32x4*)(smem + ((w * 2 + h) * 32 + n) * 16) = f32x4{s0, q0, s1, q1};
    __syncthreads();
    if (tid < 64) {
      float sm = 0.f, sq = 0.f;
#pragma unroll
      for (int i = 0; i < 16; ++i) {
        const f32x2 t = *(const LAS f32x2*)(smem + (i * 64 + tid) * 8);
        sm += t[0];
        sq += t[1];
      }
      const float mean = sm * (1.f / D);
      float var = sq * (1.f / D) - mean * mean;
      var = (var < 0.f ? 0.f : var) + 1e-5f;
      const float rstd = 1.f / sqrtf(var);
      const float sig = var * rstd;
      const float mh = round_bf16(mean), sh = round_bf16(sig);
      const u32 b0 = pack_bf16(-mh, mh - mean), b1 = pack_bf16(-mh, sh), b2 = pack_bf16(sig - sh, sh);
      const float c1 = -2.302208198f * rstd;
      const float c2 = -0.1029432397f * (rstd * rstd * rstd);
      gu64* G = (gu64*)(size_t)(ws + OFF_SG) + p0 + tid;
      __hip_atomic_store(G, ((u64)b1 << 32) | b0, RLX_AGENT);
      __hip_atomic_store(G + S, ((u64)__builtin_bit_cast(u32, var) << 32) | b2, RLX_AGENT);
      __hip_atomic_store(G + 2 * S, ((u64)__builtin_bit_cast(u32, c1) << 32) | __builtin_bit_cast(u32, c2), RLX_AGENT);
    }
    STAMP(ws, 6, 1);
    STAMP(ws, 6, 2);
    STAMP_FLUSH(ws, 6);
  }
}

DI void k2_gemm(const u16* __restrict__ ssm_k, const u16* __restrict__ ssm_D, char* __restrict__ ws, u16* __restrict__ out,
                char* smem, const int w) {
  const int tid = threadIdx.x, lane = tid & 63;
  const int n = lane & 31, h = lane >> 5;
  const int xcd = blockIdx.x & 7, idx = blockIdx.x >> 3;
  const int mt = (xcd >> 1) * 4 + (idx & 3);
  const int dt = idx >> 2, sh2 = xcd & 1;
  const int ft = sh2 * 7 + dt;
  const int tile = ft * 8 + w;
  const u32 lds0 = (u32)(size_t)(LAS char*)smem;
  u32x4 wf[28], wd[28];
  const char* wbase = ws + OFF_WGU + (size_t)(tile * 28) * 1024;
  const u32 wa = lane * 16;
  auto ldwf = [&](auto kI) {
    constexpr int k = decltype(kI)::value;
    gload16_off<(k % 4) * 1024>(wf[k], wbase + (k / 4) * 4096, wa);
  };
  Stamps2 s2 = {};
  if (xcd < 7) {
    conv_body<true>(ssm_k, ssm_D, ws, smem, w, wf, wbase, s2);
  } else {
    const int task = idx * 8 + w, t = task >> 1, kh = task & 1;
    const u32x4* wsrc = (const u32x4*)(ws + OFF_WGU) + ((size_t)t * 28 + kh * 14) * 64 + lane;
    const u16* cbp = (const u16*)(ws + OFF_CB) + (n & 1) * D + kh * 224 + 8 * h;
    u32x4 fa[14], fb[14];
#pragma unroll
    for (int i = 0; i < 14; ++i) {
      fa[i] = wsrc[i * 64];
      fb[i] = *(const u32x4*)(cbp + 16 * i);
    }
    __builtin_amdgcn_sched_barrier(0);
    f32x16 va;
#pragma unroll
    for (int r = 0; r < 16; ++r) va[r] = 0.f;
#pragma unroll
    for (int i = 0; i < 14; ++i) va = mfma32(fb[i], __builtin_bit_cast(bf16x8, fa[i]), va);
    if (h == 0) {
      const float f0 = va[0], f1 = va[1];
      const u32 a0 = umin32(__builtin_bit_cast(u32, f0)), a1 = __builtin_bit_cast(u32, f1);
      __hip_atomic_store((gu64*)(size_t)(ws + OFF_VG) + kh * 3584 + t * 32 + n, ((u64)a1 << 32) | a0, RLX_AGENT);
    }
    __builtin_amdgcn_sched_barrier(0);
    static_for<0, NWC>(ldwf);
    wait_vm<0>();
  }
  STAMP_DECL;
  STAMP(ws, 2, 0);
  const char* xbase = ws + OFF_XS + ((size_t)(8 * w) * S + mt * 128) * 2;
  const u32 xoff = (u32)((lane >> 3) * S + ((lane & 7) ^ (((lane >> 4) & 1) << 2)) * 8) * 2;
  const char* wdbase = ws + OFF_WD + ((size_t)(2 * dt) * 112 + 14 * w) * 1024 + 4096;
  const int gsl = w < 6 ? w : w - 6;
  const char* gbase = ws + OFF_SG + (size_t)((gsl >> 1) * S + mt * 128 + (gsl & 1) * 64) * 8;
  auto ldwd = [&](auto kI) {
    constexpr int q = decltype(kI)::value, k = (q & 1) * 14 + (q >> 1), kk = k % 14;
    gload16_off<(kk % 8) * 1024 - 4096>(wd[k], wdbase + (k / 14) * 112 * 1024 + (kk / 8) * 8192, wa);
  };
  auto ldx = [&](auto cI) {
    constexpr int c = decltype(cI)::value;
    glds16_sc1(xbase + (size_t)(c % 7) * (64 * S * 2) + (c / 7) * 128, xoff, lds0 + (64 * c + 8 * w) * 128);
  };
  auto xok = [&](auto c0I, auto c1I) {
    u32 m = 0;
#pragma unroll
    for (int c = decltype(c0I)::value; c < decltype(c1I)::value; ++c) {
      const u32x4 t = *(const LAS u32x4*)(smem + (64 * c + 8 * w) * 128 + lane * 16);
      m = umax3(umax3(m, t[0], t[1]), t[2], t[3]);
    }
    return (bool)__all(m != ~0u);
  };
  auto xvalid = [&](auto c0I, auto c1I) {
    if (__builtin_expect(!xok(c0I, c1I), 0)) {
#pragma nounroll
      for (int spin = 0; spin < (1 << 14); ++spin) {
        __builtin_amdgcn_s_sleep(4);
        static_for<decltype(c0I)::value, decltype(c1I)::value>(ldx);
        wait_vm<0>();
        if (xok(c0I, c1I)) break;
      }
    }
    __builtin_amdgcn_sched_barrier(0);
  };
  wait_lgkm0();
  bar();
  XSTAMP(1);
  auto pin = [](f32x16& a) { asm volatile("" : "+v"(a)); };
  auto pinw = [&](auto kI) {
    u32x4 t = wf[decltype(kI)::value];
    asm volatile("" : "+v"(t)::"memory");
    wf[decltype(kI)::value] = t;
  };
  u32x4 aext;
  constexpr int NWH = NWC + EXP_NW2X;
  static_for<NWC, NWH>(ldwf);
  static_for<0, 7>(ldx);
  int xo[2][2];
  {
    const int blk = (lane >> 4) & 1, qq = (lane & 15) >> 2, p = lane & 3;
#pragma unroll
    for (int i = 0; i < 2; ++i)
#pragma unroll
      for (int t = 0; t < 2; ++t)
        xo[i][t] = 128 * (8 * h + 4 * t + qq) + 16 * ((4 * i + 2 * blk + (p >> 1)) ^ ((qq >> 1) << 2)) + 8 * (p & 1);
  }
  f32x16 acc[4];
#pragma unroll
  for (int i = 0; i < 4; ++i)
#pragma unroll
    for (int r = 0; r < 16; ++r) acc[i][r] = 0.f;
  asm volatile("" : "+v"(xo[0][0]), "+v"(xo[0][1]), "+v"(xo[1][0]), "+v"(xo[1][1]));
  constexpr int PF = 3;
  bf16x8 xr[PF][2];
  auto rdx = [&](auto kI, auto iI) {
    constexpr int k = decltype(kI)::value, i = decltype(iI)::value;
    if constexpr (k == 28) {
      int a0 = xo[i][0] + K2_XH, a1 = xo[i][1] + K2_XH;
      asm volatile("" : "+v"(a0), "+v"(a1));
      xo[i][0] = a0;
      xo[i][1] = a1;
    }
    xr[k % PF][i] = tr_frag(smem, xo[i][0] + (k % 28) * 2048, xo[i][1] + (k % 28) * 2048);
  };
  using I0 = std::integral_constant<int, 0>;
  using I1 = std::integral_constant<int, 1>;
  using I2 = std::integral_constant<int, 2>;
  using I3 = std::integral_constant<int, 3>;
  using I7 = std::integral_constant<int, 7>;
  using I14 = std::integral_constant<int, 14>;
  LAS u64* CS = (LAS u64*)(smem + K2_ST);
  XSTAMP(4);
  wait_vm<0>();
  static_for<0, NWH>(pinw);
  xvalid(I0{}, I7{});
  XSTAMP(5);
  bar();
  YSTAMP(1);
  static_for<0, PF - 1>([&](auto jI) {
    rdx(jI, I0{});
    rdx(jI, I1{});
  });
  __builtin_amdgcn_sched_barrier(0);
  u64 gv, gp0, gp1;
  const char* vbase = ws + OFF_VG + (size_t)tile * 256;
  constexpr int RW = 28 - NWH, KX = RW + 7, KV = 25;
  static_assert(KX + 4 < KV && 27 - KX <= 9, "stream does not fit");
  static_for<0, 28>([&](auto kI) {
    constexpr int k = decltype(kI)::value;
    if constexpr (k >= NWH) {
      constexpr int issued = k + (k > KX ? 2 : 0);
      u32x4 t = wf[k];
      asm volatile("s_waitcnt vmcnt(%1)" : "+v"(t) : "n"(issued - 1 - (k - NWH)) : "memory");
      wf[k] = t;
    }
    if constexpr (k == KV) {
      u64 t = gv, p0 = gp0, p1 = gp1;
      asm volatile("s_waitcnt vmcnt(%3)" : "+v"(t), "+v"(p0), "+v"(p1) : "n"(KV - KX - 1) : "memory");
      xvalid(I7{}, I14{});
      if (!__all((t >> 32) != 0 && (u32)p0 != ~0u && (u32)p1 != ~0u)) {
        for (int spin = 0; spin < (1 << 16); ++spin) {
          __builtin_amdgcn_s_sleep(2);
          t = __hip_atomic_load((gu64*)(size_t)gbase + lane, RLX_AGENT);
          p0 = __hip_atomic_load((gu64*)(size_t)vbase + n, RLX_AGENT);
          p1 = __hip_atomic_load((gu64*)(size_t)vbase + 3584 + n, RLX_AGENT);
          if (__all((t >> 32) != 0 && (u32)p0 != ~0u && (u32)p1 != ~0u)) break;
        }
        __builtin_amdgcn_s_waitcnt(0x0F70);
      }
      CS[gsl * 64 + lane] = t;
      const float v1 = __builtin_bit_cast(float, (u32)p0) + __builtin_bit_cast(float, (u32)p1);
      const float v2 = __builtin_bit_cast(float, (u32)(p0 >> 32)) + __builtin_bit_cast(float, (u32)(p1 >> 32));
      const float v1h = round_bf16(v1), v2h = round_bf16(v2);
      u32x4 a = {pack_bf16(v1h, v1h), pack_bf16(v1 - v1h, v2h), pack_bf16(v2h, v2 - v2h), 0u};
      if (h) a = u32x4{0u, 0u, 0u, 0u};
      asm volatile("" : "+v"(a));
      aext = a;
      wait_lgkm0();
      __builtin_amdgcn_sched_barrier(0);
      bar();
    }
    __builtin_amdgcn_sched_barrier(0);
    acc[0] = mfma32(wf[k], xr[k % PF][0], acc[0]);
    pin(acc[0]);
    __builtin_amdgcn_sched_barrier(0);
    rdx(std::integral_constant<int, k + PF - 1>{}, I0{});
    __builtin_amdgcn_sched_barrier(0);
    acc[1] = mfma32(wf[k], xr[k % PF][1], acc[1]);
    pin(acc[1]);
    __builtin_amdgcn_sched_barrier(0);
    rdx(std::integral_constant<int, k + PF - 1>{}, I1{});
    if constexpr (k < RW) ldwf(std::integral_constant<int, NWH + k>{});
    else if constexpr (k < KX) ldx(std::integral_constant<int, 7 + k - RW>{});
    else if constexpr (k == KX) {
      u64 t, t0, t1;
      asm volatile(SNOP2 "global_load_dwordx2 %0, %1, %2 sc1" : "=v"(t) : "v"((u32)lane * 8), "s"(uni(gbase)) : "memory");
      asm volatile(SNOP2 "global_load_dwordx2 %0, %1, %2 sc1" : "=v"(t0) : "v"((u32)n * 8), "s"(uni(vbase)) : "memory");
      asm volatile(SNOP2 "global_load_dwordx2 %0, %1, %2 sc1" : "=v"(t1) : "v"((u32)n * 8), "s"(uni(vbase + 3584 * 8)) : "memory");
      gv = t;
      gp0 = t0;
      gp1 = t1;
    } else {
      ldwd(std::integral_constant<int, k - KX - 1>{});
    }
    __builtin_amdgcn_sched_barrier(0);
  });
  constexpr int WE = 27 - KX;
  YSTAMP(2);
  const rsrc_t rw = mk_rsrc(ws + OFF_ACT + ((size_t)(mt * 4) * 112 + tile) * 1024, 337 * 1024);
  const rsrc_t ra = mk_rsrc(ws + OFF_ACT + ((size_t)(mt * 4 + sh2) * 112 + 14 * w) * 1024, 238 * 1024);
  u32x4 af[28];
  auto ldact = [&](auto kI) {
    constexpr int k = decltype(kI)::value;
    af[k] = __builtin_amdgcn_raw_buffer_load_b128(ra, lane * 16 + (k % 14) * 1024, (k / 14) * 224 * 1024, 16);
  };
  float c1[2], c2[2], var[2], pe[2], pm[2], pr[2];
  u32x4 ov[2];
  auto corr = [&](auto iI) {
    constexpr int i = decltype(iI)::value;
    u64 g[3];
#pragma unroll
    for (int k = 0; k < 3; ++k) g[k] = CS[k * 128 + i * 32 + n];
    const u32x4 bext = {(u32)g[0], (u32)(g[0] >> 32), (u32)g[1], 0u};
    acc[i] = mfma32(aext, __builtin_bit_cast(bf16x8, bext), acc[i]);
    c1[i & 1] = __builtin_bit_cast(float, (u32)(g[2] >> 32));
    c2[i & 1] = __builtin_bit_cast(float, (u32)g[2]);
    var[i & 1] = __builtin_bit_cast(float, (u32)(g[1] >> 32));
  };
  auto slice = [&](auto iI, auto uI) {
    constexpr int i = decltype(iI)::value, u = decltype(uI)::value, z = i & 1;
    if constexpr (u >= 1) {
      float o = pm[z] * __builtin_amdgcn_rcpf(__builtin_fmaf(pe[z], var[z], var[z]));
      if constexpr ((u - 1) & 1) {
        u32 t = umin32(pack_bf16(pr[z], o));
        asm volatile("" : "+v"(t));
        ov[z][(u - 1) / 2] = t;
      } else {
        asm volatile("" : "+v"(o));
        pr[z] = o;
      }
    }
    if constexpr (u < 8) {
      const float a = acc[i][u];
      float e = __builtin_amdgcn_exp2f(__builtin_fmaf(a * a, c2[z], c1[z]) * a), m = a * acc[i][u + 8];
      asm volatile("" : "+v"(e), "+v"(m));
      pe[z] = e;
      pm[z] = m;
    } else {
      __builtin_amdgcn_raw_buffer_store_b128(ov[z], rw, lane * 16 + i * 112 * 1024, 0, 16);
      asm volatile("s_nop 1");
    }
  };
  corr(I0{});
  corr(I1{});
  __builtin_amdgcn_sched_barrier(0);
  static_for<0, 28>([&](auto kI) {
    constexpr int k = decltype(kI)::value;
    acc[2] = mfma32(wf[k], xr[(28 + k) % PF][0], acc[2]);
    pin(acc[2]);
    __builtin_amdgcn_sched_barrier(0);
    if constexpr (k + PF - 1 < 28) rdx(std::integral_constant<int, 28 + k + PF - 1>{}, I0{});
    if constexpr (k >= 1 && k <= 9) slice(I0{}, std::integral_constant<int, k - 1>{});
    __builtin_amdgcn_sched_barrier(0);
    acc[3] = mfma32(wf[k], xr[(28 + k) % PF][1], acc[3]);
    pin(acc[3]);
    __builtin_amdgcn_sched_barrier(0);
    if constexpr (k % EXP_LOCK == EXP_LOCK - 1) bar();
    if constexpr (k + PF - 1 < 28) rdx(std::integral_constant<int, 28 + k + PF - 1>{}, I1{});
    if constexpr (k >= 1 && k <= 9) slice(I1{}, std::integral_constant<int, k - 1>{});
    if constexpr (WE + k < 28) ldwd(std::integral_constant<int, WE + k>{});
    else if constexpr (WE + k - 28 < 7) ldact(std::integral_constant<int, WE + k - 28>{});
    __builtin_amdgcn_sched_barrier(0);
  });
  YSTAMP(3);
  constexpr int A0 = WE < 7 ? WE : 7;
  corr(I2{});
  corr(I3{});
  __builtin_amdgcn_sched_barrier(0);
  static_for<0, 9>([&](auto uI) {
    constexpr int u = decltype(uI)::value;
    slice(I2{}, uI);
    slice(I3{}, uI);
    if constexpr (A0 + u < 14 && u < 8) ldact(std::integral_constant<int, A0 + u>{});
    __builtin_amdgcn_sched_barrier(0);
  });
  static_for<(A0 + 8 < 14 ? A0 + 8 : 14), 14>(ldact);
  bar();
  YSTAMP(4);
  auto chk = [&](auto gI) {
    constexpr int g = decltype(gI)::value;
    u32 m = 0;
#pragma unroll
    for (int k = 7 * g; k < 7 * g + 7; ++k) m = umax3(umax3(m, af[k][0], af[k][1]), af[k][2], af[k][3]);
    return (bool)__all(m != ~0u);
  };
  auto valid = [&](auto gI) {
    constexpr int g = decltype(gI)::value;
    if (__builtin_expect(!chk(gI), 0)) {
#pragma nounroll
      for (int spin = 0; spin < (1 << 14); ++spin) {
        __builtin_amdgcn_s_sleep(8);
        asm volatile("" ::: "memory");
        static_for<7 * g, 7 * g + 7>(ldact);
        if (chk(gI)) break;
      }
      __builtin_amdgcn_s_waitcnt(0x0F70);
    }
    __builtin_amdgcn_sched_barrier(0);
  };
  const int pcol = mt * 128 + sh2 * 32 + n;
  const int dbase = dt * 64 + w * 8 + 4 * h;
  __builtin_amdgcn_sched_barrier(0);
  LAS f32x4* R = (LAS f32x4*)smem;
  f32x16 o[4];
#pragma unroll
  for (int i = 0; i < 4; ++i)
#pragma unroll
    for (int r = 0; r < 16; ++r) o[i][r] = 0.f;
  auto dump1 = [&](auto tjI, auto cI) {
    constexpr int tj = decltype(tjI)::value, c = decltype(cI)::value, i = tj * 2 + (c >> 2), q = c & 3;
    R[((tj * 8 + c) * 8 + w) * 64 + lane] = f32x4{o[i][4 * q], o[i][4 * q + 1], o[i][4 * q + 2], o[i][4 * q + 3]};
  };
  auto step = [&](auto kI) {
    constexpr int k = decltype(kI)::value, tj = k / 14;
    const bf16x8 b = __builtin_bit_cast(bf16x8, af[k]);
    o[2 * tj] = mfma32(wd[k % 14], b, o[2 * tj]);
    pin(o[2 * tj]);
    __builtin_amdgcn_sched_barrier(0);
    if constexpr (k >= 15 && k < 19) dump1(I0{}, std::integral_constant<int, 2 * (k - 15)>{});
    __builtin_amdgcn_sched_barrier(0);
    o[2 * tj + 1] = mfma32(wd[14 + k % 14], b, o[2 * tj + 1]);
    pin(o[2 * tj + 1]);
    __builtin_amdgcn_sched_barrier(0);
    if constexpr (k >= 15 && k < 19) dump1(I0{}, std::integral_constant<int, 2 * (k - 15) + 1>{});
    if constexpr (k >= EXP_T1 && k < 14) {
      constexpr int n0 = ((k - EXP_T1) * 14) / (14 - EXP_T1), n1 = ((k + 1 - EXP_T1) * 14) / (14 - EXP_T1);
      static_for<14 + n0, 14 + n1>(ldact);
    }
    __builtin_amdgcn_sched_barrier(0);
  };
  u16 xres[2][4];
  f32x4 gbv;
  auto fin = [&](auto tjI) {
    constexpr int tj = decltype(tjI)::value;
    f32x4 v[8];
#pragma unroll
    for (int sw = 0; sw < 8; ++sw) v[sw] = R[((tj * 8 + w) * 8 + sw) * 64 + lane];
    const f32x4 sum = ((v[0] + v[1]) + (v[2] + v[3])) + ((v[4] + v[5]) + (v[6] + v[7]));
    float r[4];
#pragma unroll
    for (int e = 0; e < 4; ++e) r[e] = __builtin_fmaf(gbv[e], sum[e], bf2f(xres[tj][e]));
    const int pc = pcol + 64 * tj;
    const int srow = (pc & ~7) | ((pc & 3) << 1) | ((pc >> 2) & 1);
    *(u32x2*)(out + (size_t)srow * D + dbase) = u32x2{pack_bf16(r[0], r[1]), pack_bf16(r[2], r[3])};
  };
  valid(I0{});
  asm volatile(""
               : "+v"(wd[0]), "+v"(wd[1]), "+v"(wd[2]), "+v"(wd[3]), "+v"(wd[4]), "+v"(wd[5]), "+v"(wd[6]), "+v"(wd[7]),
                 "+v"(wd[8]), "+v"(wd[9]), "+v"(wd[10]), "+v"(wd[11]), "+v"(wd[12]), "+v"(wd[13])::"memory");
  asm volatile(""
               : "+v"(wd[14]), "+v"(wd[15]), "+v"(wd[16]), "+v"(wd[17]), "+v"(wd[18]), "+v"(wd[19]), "+v"(wd[20]),
                 "+v"(wd[21]), "+v"(wd[22]), "+v"(wd[23]), "+v"(wd[24]), "+v"(wd[25]), "+v"(wd[26]), "+v"(wd[27])::"memory");
  YSTAMP(5);
  static_for<0, 3>(step);
  {
    const rsrc_t rx = mk_rsrc(ws + OFF_X1T, D * S * 2);
#pragma unroll
    for (int r = 0; r < 8; ++r)
      xres[r >> 2][r & 3] = __builtin_amdgcn_raw_buffer_load_b16(rx, ((dbase + (r & 3)) * S + pcol + (r >> 2) * 64) * 2, 0, 16);
    gbv = *(const f32x4*)((const float*)(ws + OFF_MOD) + 5 * D + dbase);
    __builtin_amdgcn_sched_barrier(0);
  }
  static_for<3, 7>(step);
  valid(I1{});
  static_for<7, 14>(step);
  valid(I2{});
  static_for<14, 21>(step);
  wait_lgkm0();
  bar();
  valid(I3{});
  static_for<21, 24>(step);
  fin(I0{});
  __builtin_amdgcn_sched_barrier(0);
  static_for<24, 28>(step);
  static_for<0, 8>([&](auto cI) { dump1(I1{}, cI); });
  wait_lgkm0();
  bar();
  STAMP(ws, 2, 6);
  fin(I1{});
}

extern "C" __global__ NOPK __launch_bounds__(512, 2) void k1_main(const u16* __restrict__ ssm_k,
                                                                  const u16* __restrict__ ssm_D, char* __restrict__ ws,
                                                                  u16* __restrict__ out) {
  extern __shared__ __attribute__((aligned(16))) char smem[];
  const int w = __builtin_amdgcn_readfirstlane(threadIdx.x >> 6);
  if (blockIdx.x >= 224) {
    if ((blockIdx.x & 7) < 7) {
      u32x4 dummy[28];
      Stamps2 s2 = {};
      conv_body<false>(ssm_k, ssm_D, ws, smem, w, dummy, nullptr, s2);
    } else {
      __builtin_amdgcn_s_sleep(100);
    }
    k2_stats(ws, smem, blockIdx.x - 224);
  } else {
    k2_gemm(ssm_k, ssm_D, ws, out, smem, w);
  }
}

extern "C" __device__ __attribute__((used, noinline)) void k_code_end() {}

extern "C" void kernel_launch(void* const* d_in, const int* in_sizes, int n_in, void* d_out, int out_size, void* d_ws,
                              size_t ws_size, hipStream_t stream) {
  const u16* q = (const u16*)d_in[0];
  const u16* k = (const u16*)d_in[1];
  const u16* v = (const u16*)d_in[2];
  const u16* shift_k = (const u16*)d_in[3];
  const u16* ssm_k = (const u16*)d_in[4];
  const u16* ssm_D = (const u16*)d_in[5];
  const u16* w_gate = (const u16*)d_in[7];
  const u16* w_up = (const u16*)d_in[8];
  const u16* w_down = (const u16*)d_in[9];
  const u16* c = (const u16*)d_in[10];
  const u16* w_mod = (const u16*)d_in[11];
  char* ws = (char*)d_ws;
  (void)hipFuncSetAttribute((const void*)k1_main, hipFuncAttributeMaxDynamicSharedMemorySize, K2_LDS);
  k0_prep<<<K0_GRID, 256, 0, stream>>>(q, k, v, shift_k, c, w_mod, ssm_k, w_gate, w_up, w_down, ws);
  k1_main<<<256, 512, K2_LDS, stream>>>(ssm_k, ssm_D, ws, (u16*)d_out);
}
```

```cpp
#include <hip/hip_runtime.h>
#include <hip/hip_bf16.h>
#include <stdint.h>
#include <type_traits>

#define NTAUX 2
#define NTLOAD(p) __builtin_nontemporal_load(p)
typedef unsigned short u16;
typedef unsigned int u32;
typedef short bf16x8 __attribute__((ext_vector_type(8)));
typedef short s16x4 __attribute__((ext_vector_type(4)));
typedef float f32x2 __attribute__((ext_vector_type(2)));
typedef float f32x4 __attribute__((ext_vector_type(4)));
typedef float f32x16 __attribute__((ext_vector_type(16)));
typedef u32 u32x2 __attribute__((ext_vector_type(2)));
typedef u32 u32x4 __attribute__((ext_vector_type(4)));
typedef __bf16 bf16x2_t __attribute__((ext_vector_type(2)));
#define LAS __attribute__((address_space(3)))
#define DI __device__ __forceinline__

constexpr int S = 2048, D = 448, F = 1792;

constexpr size_t SZ_DS = (size_t)D * S * 2;
constexpr size_t OFF_RE = 0;
constexpr size_t OFF_RO = OFF_RE + SZ_DS;
constexpr size_t OFF_QT = OFF_RO + SZ_DS;
constexpr size_t OFF_X1T = OFF_QT + SZ_DS;
constexpr size_t OFF_MEAN = OFF_X1T + SZ_DS;
constexpr size_t OFF_RSTD = OFF_MEAN + S * 4;
constexpr size_t OFF_MOD = OFF_RSTD + S * 4;
constexpr size_t OFF_SG = OFF_MOD + 16384;
constexpr size_t OFF_CB = OFF_MOD + 12288;

constexpr size_t OFF_VG = OFF_SG + 3 * S * 8;
constexpr size_t OFF_XS = OFF_VG + 4 * S * 8;
constexpr size_t OFF_VP = OFF_XS + SZ_DS;
constexpr size_t OFF_WGU = OFF_VP + 8 * 2 * 3584 * 4;
constexpr size_t OFF_WD = OFF_WGU + (size_t)2 * D * F * 2;
constexpr size_t OFF_ACT = OFF_WD + (size_t)D * F * 2;
constexpr size_t WS_END = OFF_ACT + (size_t)S * F * 2;

struct Stamps2 {};
#define STAMP_DECL
#define STAMP(wsp, kid, i)
#define STAMP_FLUSH(wsp, kid)
#define HWID(wsp, kid)
#define XSTAMP(i)
#define YSTAMP(i) STAMP(ws, 2, i)
#if defined(__HIP_DEVICE_COMPILE__)
#define NOPK __attribute__((target("no-packed-fp32-ops")))
#else
#define NOPK
#endif

template <int I, int N, class Fn>
DI void static_for(Fn&& f) {
  if constexpr (I < N) {
    f(std::integral_constant<int, I>{});
    static_for<I + 1, N>(f);
  }
}

DI float lo_f(u32 x) { return __builtin_bit_cast(float, x << 16); }
DI float hi_f(u32 x) { return __builtin_bit_cast(float, x & 0xffff0000u); }
DI u32 pack_bf16(float a, float b) {
  f32x2 v = {a, b};
  return __builtin_bit_cast(u32, __builtin_convertvector(v, bf16x2_t));
}
DI float round_bf16(float a) { return lo_f(pack_bf16(a, 0.f)); }
DI float bf2f(u16 x) { return __builtin_bit_cast(float, (u32)x << 16); }

template <int N>
DI void wait_vm() { asm volatile("s_waitcnt vmcnt(%0)" ::"n"(N) : "memory"); }
DI void wait_lgkm0() { asm volatile("s_waitcnt lgkmcnt(0)" ::: "memory"); }
DI void bar() { __builtin_amdgcn_s_barrier(); }

#define SNOP "s_nop 0\n\t"
#define SNOP2 ""
DI const void* uni(const void* p) {
  const unsigned long long v = (unsigned long long)p;
  const u32 lo = __builtin_amdgcn_readfirstlane((u32)v), hi = __builtin_amdgcn_readfirstlane((u32)(v >> 32));
  return (const void*)(((unsigned long long)hi << 32) | lo);
}
DI void glds16_asm(const void* sbase, u32 voff, u32 lds_addr) {
  u32 keep;
  sbase = uni(sbase);
  lds_addr = __builtin_amdgcn_readfirstlane(lds_addr);
  asm volatile("s_mov_b32 %0, m0\n\ts_mov_b32 m0, %3\n\t" SNOP "global_load_lds_dwordx4 %1, %2\n\ts_mov_b32 m0, %0"
               : "=&s"(keep)
               : "v"(voff), "s"(sbase), "s"(lds_addr)
               : "memory");
}
DI void glds16(const void* g, char* lds) {
  __builtin_amdgcn_global_load_lds((const __attribute__((address_space(1))) void*)g, (LAS void*)lds, 16, 0, NTAUX);
}

template <int CTRL>
DI float dpp_add(float v) {
  const int x = __builtin_bit_cast(int, v);
  return v + __builtin_bit_cast(float, __builtin_amdgcn_update_dpp(x, x, CTRL, 0xf, 0xf, false));
}
DI float sum8(float v) {
  v = dpp_add<0xB1>(v);
  v = dpp_add<0x4E>(v);
  v = dpp_add<0x141>(v);
  return v;
}
DI float sum16(float v) { return dpp_add<0x140>(sum8(v)); }
DI float dot2(u32 a, u32 b, float c) {
  return __builtin_amdgcn_fdot2_f32_bf16(__builtin_bit_cast(bf16x2_t, a), __builtin_bit_cast(bf16x2_t, b), c, false);
}
typedef __amdgpu_buffer_rsrc_t rsrc_t;
DI rsrc_t mk_rsrc(const void* p, u32 bytes) { return __builtin_amdgcn_make_buffer_rsrc((void*)p, 0, bytes, 0x00020000); }
DI void st16_sc1(void* p, u32x4 v) { asm volatile("global_store_dwordx4 %0, %1, off sc1\n\ts_nop 1" ::"v"(p), "v"(v) : "memory"); }
DI void st8_sc1(void* p, u32x2 v) { asm volatile("global_store_dwordx2 %0, %1, off sc1" ::"v"(p), "v"(v) : "memory"); }
DI void st4_sc1(void* p, float v) { asm volatile("global_store_dword %0, %1, off sc1" ::"v"(p), "v"(v) : "memory"); }

#define EXP_E12 2
#define EXP_E13 6
#define EXP_LOCK 4
#define EXP_NW2 14
DI bf16x8 tr_frag(const char* smem, int o0, int o1) {
  s16x4 x0 = __builtin_amdgcn_ds_read_tr16_b64_v4i16((LAS s16x4*)(smem + o0));
  s16x4 x1 = __builtin_amdgcn_ds_read_tr16_b64_v4i16((LAS s16x4*)(smem + o1));
  return __builtin_shufflevector(x0, x1, 0, 1, 2, 3, 4, 5, 6, 7);
}
DI f32x16 mfma32(u32x4 a, bf16x8 b, f32x16 c) {
  return __builtin_amdgcn_mfma_f32_32x32x16_bf16(__builtin_bit_cast(bf16x8, a), b, c, 0, 0, 0);
}

DI void retile(int task, const u16* __restrict__ w_gate, const u16* __restrict__ w_up, const u16* __restrict__ w_down,
               char* __restrict__ ws, char* smem, int w, int lane) {
  const int xr = lane >> 4, slot = lane & 15;
  const int chn = slot ^ ((xr << 2) | (w & 3));
  const int h = lane >> 5, blk = (lane >> 4) & 1, qq = (lane & 15) >> 2, p = lane & 3;
  const bool gu = task < 112;
  int kq = 0, fg = 0, dg = 0, fk = 0;
  if (gu) {
    fg = task % 28, kq = task / 28;
    const u16* src = (chn < 8 ? w_gate : w_up) + (size_t)(kq * 112 + 4 * w + xr) * F + fg * 64 + (chn & 7) * 8;
#pragma unroll
    for (int i = 0; i < 7; ++i) glds16(src + (size_t)i * 16 * F, smem + (w + 4 * i) * 1024);
  } else {
    dg = (task - 112) % 7, fk = (task - 112) / 7;
    const u16* src = w_down + (size_t)(fk * 224 + (chn >> 3) * 112 + 4 * w + xr) * D + dg * 64 + (chn & 7) * 8;
#pragma unroll
    for (int i = 0; i < 7; ++i) glds16(src + (size_t)i * 16 * D, smem + (w + 4 * i) * 1024);
  }
  const int col = gu ? blk * 128 + w * 32 + (p & 1) * 16 + (p >> 1) * 8 : (w >> 1) * 128 + (w & 1) * 64 + blk * 32 + p * 8;
  int o[2];
#pragma unroll
  for (int t = 0; t < 2; ++t)
    o[t] = 256 * (8 * h + 4 * t + qq) + 16 * ((col >> 4) ^ ((qq << 2) | ((2 * h + t) & 3))) + (col & 8);
  u32x4* dst = gu ? (u32x4*)(ws + OFF_WGU) + ((size_t)(fg * 4 + w) * 28 + kq * 7) * 64 + lane
                  : (u32x4*)(ws + OFF_WD) + ((size_t)(dg * 2 + (w & 1)) * 112 + fk * 14 + (w >> 1) * 7) * 64 + lane;
  __syncthreads();
#pragma unroll
  for (int i = 0; i < 7; ++i)
    st16_sc1(dst + i * 64, __builtin_bit_cast(u32x4, tr_frag(smem, o[0] + i * 4096, o[1] + i * 4096)));
}

#define EXP_DELAY 0
#define EXP_T1 5
#define EXP_A0 4
#define ACTAUX 16
constexpr int K0_GRID = 504;

extern "C" __global__ __launch_bounds__(256) void k0_prep(const u16* __restrict__ q, const u16* __restrict__ k,
                                               const u16* __restrict__ v, const u16* __restrict__ shift_k,
                                               const u16* __restrict__ c, const u16* __restrict__ w_mod,
                                               const u16* __restrict__ ssm_k, const u16* __restrict__ w_gate,
                                               const u16* __restrict__ w_up, const u16* __restrict__ w_down,
                                               char* __restrict__ ws) {
  __shared__ __attribute__((aligned(16))) char smem[28672];
  float(*red)[64] = (float(*)[64])smem;
  const int b = blockIdx.x, tid = threadIdx.x, lane = tid & 63, w = __builtin_amdgcn_readfirstlane(tid >> 6);
  STAMP_DECL;
  STAMP(ws, 0, 0);
  const int xcd = b & 7, idx = b >> 3;
  u32 cwarm = 0;
  if (idx < 2) {
    u32 lo, hi, len;
    asm volatile(
        "s_getpc_b64 s[40:41]\n\ts_add_u32 s40, s40, k0_prep@rel32@lo+4\n\ts_addc_u32 s41, s41, k0_prep@rel32@hi+12\n\t"
        "s_mov_b32 %0, s40\n\ts_mov_b32 %1, s41\n\ts_mov_b32 %2, k_code_end-k0_prep"
        : "=s"(lo), "=s"(hi), "=s"(len)::"s40", "s41", "scc");
    const char* code = (const char*)(((unsigned long long)hi << 32) | lo);
    if ((u32)(idx * 256 + tid) * 128 < len) cwarm = *(const __attribute__((address_space(1))) u32*)(size_t)(code + (idx * 256 + tid) * 128);
  }
  asm volatile("" ::"s"(q), "s"(k), "s"(v), "s"(shift_k), "s"(c), "s"(w_mod), "s"(ssm_k), "s"(w_gate), "s"(w_up), "s"(w_down),
               "s"(ws));
  if (__builtin_expect(idx >= 42, 0)) {
    if (b - 336 < 128) {
      const int t = b - 336;
      u32x4* ap = (u32x4*)(ws + (t < 64 ? OFF_X1T : OFF_XS)) + (size_t)(t & 63) * 1792 + tid;
      const u32x4 sent = {~0u, ~0u, ~0u, ~0u};
#pragma unroll
      for (int i = 0; i < 7; ++i) st16_sc1(ap + i * 256, sent);
    }
    retile(b - 336, w_gate, w_up, w_down, ws, smem, w, lane);
    STAMP(ws, 7, 1);
    STAMP_FLUSH(ws, 7);
    return;
  }
  int misc = -1;
  if (xcd == 7) misc = idx < 14 ? 64 + idx : (idx < 36 ? idx - 14 : 1000);
  else if (idx < 4) misc = 64 + 14 + xcd * 4 + idx;
  else if (idx >= 36) misc = 22 + xcd * 6 + (idx - 36);
  if (__builtin_expect(misc < 0, 1)) {
    const int rb = idx - 4;
    const int sq = lane >> 5, cp = lane & 31;
    const int d0 = xcd * 64 + cp * 2;
    const int a = rb * 8 + w * 2 + sq;
    const int s0 = a * 8;
    const rsrc_t rk = mk_rsrc(k, S * D * 2), rv = mk_rsrc(v, S * D * 2), rq = mk_rsrc(q, S * D * 2);
    const int oB = (s0 * D + d0) * 2;
    const int oA = oB - 4 * D * 2, oC = oB + 4 * D * 2;
    u32 kk[12], vv[9], qq[8];
#pragma unroll
    for (int i = 0; i < 4; ++i) kk[i] = __builtin_amdgcn_raw_buffer_load_b32(rk, oA + i * D * 2, 0, NTAUX);
#pragma unroll
    for (int i = 0; i < 4; ++i) kk[4 + i] = __builtin_amdgcn_raw_buffer_load_b32(rk, oB + i * D * 2, 0, NTAUX);
#pragma unroll
    for (int i = 0; i < 4; ++i) kk[8 + i] = __builtin_amdgcn_raw_buffer_load_b32(rk, oC + i * D * 2, 0, NTAUX);
    vv[0] = __builtin_amdgcn_raw_buffer_load_b32(rv, oA + 3 * D * 2, 0, NTAUX);
#pragma unroll
    for (int i = 0; i < 4; ++i) vv[1 + i] = __builtin_amdgcn_raw_buffer_load_b32(rv, oB + i * D * 2, 0, NTAUX);
#pragma unroll
    for (int i = 0; i < 4; ++i) vv[5 + i] = __builtin_amdgcn_raw_buffer_load_b32(rv, oC + i * D * 2, 0, NTAUX);
#pragma unroll
    for (int i = 0; i < 4; ++i) qq[i] = __builtin_amdgcn_raw_buffer_load_b32(rq, oB + i * D * 2, 0, NTAUX);
#pragma unroll
    for (int i = 0; i < 4; ++i) qq[4 + i] = __builtin_amdgcn_raw_buffer_load_b32(rq, oC + i * D * 2, 0, NTAUX);
    const u32x4 sk = *(const u32x4*)(shift_k + d0 * 4);
    u32 warm = 0;
    if (w == 0) warm = NTLOAD((const u32*)((const char*)ssm_k + ((size_t)(xcd * 32 + rb) * 64 + lane) * 128));
    f32x2 wt[4] = {{lo_f(sk[0]), lo_f(sk[2])}, {hi_f(sk[0]), hi_f(sk[2])}, {lo_f(sk[1]), lo_f(sk[3])}, {hi_f(sk[1]), hi_f(sk[3])}};
    f32x2 kf[12], u[9];
#pragma unroll
    for (int i = 0; i < 12; ++i) kf[i] = f32x2{lo_f(kk[i]), hi_f(kk[i])};
#pragma unroll
    for (int i = 0; i < 9; ++i) {
      f32x2 t = kf[i + 3] * wt[0];
      t = __builtin_elementwise_fma(kf[i + 2], wt[1], t);
      t = __builtin_elementwise_fma(kf[i + 1], wt[2], t);
      t = __builtin_elementwise_fma(kf[i], wt[3], t);
      u[i] = t * f32x2{lo_f(vv[i]), hi_f(vv[i])};
    }
    {
      char* dst = ws + (size_t)d0 * (S * 2) + (2040 - s0) * 2;
#pragma unroll
      for (int cI = 0; cI < 2; ++cI) {
        *(u32x4*)(dst + OFF_RO + cI * S * 2) = u32x4{pack_bf16(u[8][cI], u[7][cI]), pack_bf16(u[6][cI], u[5][cI]),
                                                     pack_bf16(u[4][cI], u[3][cI]), pack_bf16(u[2][cI], u[1][cI])};
        *(u32x4*)(dst + OFF_RE + cI * S * 2) = u32x4{pack_bf16(u[7][cI], u[6][cI]), pack_bf16(u[5][cI], u[4][cI]),
                                                     pack_bf16(u[3][cI], u[2][cI]), pack_bf16(u[1][cI], u[0][cI])};
      }
      char* dq = ws + OFF_QT + (size_t)d0 * (S * 2) + s0 * 2;
#pragma unroll
      for (int cI = 0; cI < 2; ++cI) {
        const u32 sel = cI ? 0x07060302u : 0x05040100u;
        *(u32x4*)(dq + cI * S * 2) = u32x4{__builtin_amdgcn_perm(qq[2], qq[0], sel), __builtin_amdgcn_perm(qq[6], qq[4], sel),
                                           __builtin_amdgcn_perm(qq[3], qq[1], sel), __builtin_amdgcn_perm(qq[7], qq[5], sel)};
      }
    }
    asm volatile("" ::"v"(warm));
  } else if (misc < 64) {
    const int bb = misc;
    const int row = bb * 32 + w * 8 + (lane >> 3);
    const int sub = lane & 7;
    u32x4 x[7];
#pragma unroll
    for (int i = 0; i < 7; ++i) x[i] = NTLOAD((const u32x4*)(q + (size_t)row * D + (i * 8 + sub) * 8));
    if (sub < 7) ((unsigned long long*)(ws + OFF_SG))[sub * S + row] = sub < 3 ? 0ull : ~0ull;
    float s = 0.f, s2 = 0.f;
#pragma unroll
    for (int i = 0; i < 7; ++i)
#pragma unroll
      for (int j = 0; j < 4; ++j) {
        const u32 t = x[i][j];
        s = dot2(t, 0x3f803f80u, s);
        s2 = dot2(t, t, s2);
      }
    s = sum8(s);
    s2 = sum8(s2);
    const float mean = s * (1.f / D);
    float var = s2 * (1.f / D) - mean * mean;
    var = var < 0.f ? 0.f : var;
    const float rstd = __builtin_amdgcn_rsqf(var + 1e-5f);
    if (sub < 2) {
      const int p = (row & ~7) | ((row & 1) << 2) | ((row >> 1) & 3);
      ((float*)(ws + OFF_MEAN))[sub * S + p] = sub ? rstd : mean;
    }
  } else if (misc < 106) {
    const int bb = misc - 64;
    const int co = lane & 7, rs = lane >> 3;
    const int col0 = bb * 64 + co * 8;
    u32x4 x[14];
#pragma unroll
    for (int i = 0; i < 14; ++i)
      x[i] = NTLOAD((const u32x4*)(w_mod + (size_t)(w * 112 + rs * 14 + i) * (6 * D) + col0));
    u32 cv[7];
#pragma unroll
    for (int i = 0; i < 7; ++i) cv[i] = *(const u32*)(c + w * 112 + rs * 14 + 2 * i);
    __builtin_amdgcn_sched_barrier(0);
    f32x2 acc[4] = {{0.f, 0.f}, {0.f, 0.f}, {0.f, 0.f}, {0.f, 0.f}};
#pragma unroll
    for (int i = 0; i < 14; ++i) {
      const float cf = (i & 1) ? hi_f(cv[i >> 1]) : lo_f(cv[i >> 1]);
      const float sl = round_bf16(cf * __builtin_amdgcn_rcpf(1.f + __builtin_amdgcn_exp2f(cf * -1.44269504f)));
#pragma unroll
      for (int j = 0; j < 4; ++j)
        acc[j] = __builtin_elementwise_fma(f32x2{lo_f(x[i][j]), hi_f(x[i][j])}, f32x2{sl, sl}, acc[j]);
    }
    *(f32x4*)&red[w * 8 + rs][co * 8] = f32x4{acc[0][0], acc[0][1], acc[1][0], acc[1][1]};
    *(f32x4*)&red[w * 8 + rs][co * 8 + 4] = f32x4{acc[2][0], acc[2][1], acc[3][0], acc[3][1]};
    __syncthreads();
    {
      float t = 0.f;
#pragma unroll
      for (int i = 0; i < 8; ++i) t += red[w * 8 + i][lane];
      red[32 + w][lane] = t;
    }
    __syncthreads();
    if (tid < 64) {
      const float t = (red[32][tid] + red[33][tid]) + (red[34][tid] + red[35][tid]);
      const float m = round_bf16(t);
      const int col = bb * 64 + tid;
      ((float*)(ws + OFF_MOD))[col] = m;
      if (col >= 3 * D && col < 5 * D) {
        const bool isc = col >= 4 * D;
        ((u16*)(ws + OFF_CB))[isc ? col - 4 * D : col - 2 * D] = (u16)pack_bf16(isc ? 1.f + m : m, 0.f);
      }
    }
  }
  asm volatile("" ::"v"(cwarm));
  STAMP(ws, 0, 1);
  STAMP_FLUSH(ws, misc < 0 ? 0 : (misc < 64 ? 4 : 5));
}

constexpr int K1_KSTR = 260 * 16;
constexpr int K1_USTR = 286 * 16;
constexpr int K1_UOFF = 4 * K1_KSTR;
constexpr int K1_XOFF = K1_UOFF + 2 * K1_USTR;
constexpr int K1_CH = K1_XOFF + 32768;

DI void glds16x4_asm(const void* sbase, u32 voff, u32 voff_last, u32 lds_addr) {
  u32 keep;
  sbase = uni(sbase);
  lds_addr = __builtin_amdgcn_readfirstlane(lds_addr);
  asm volatile(
      "s_mov_b32 %0, m0\n\ts_mov_b32 m0, %4\n\t" SNOP
      "global_load_lds_dwordx4 %1, %3\n\t"
      "global_load_lds_dwordx4 %1, %3 offset:1024\n\t"
      "global_load_lds_dwordx4 %1, %3 offset:2048\n\t"
      "global_load_lds_dwordx4 %2, %3 offset:3072\n\t"
      "s_mov_b32 m0, %0"
      : "=&s"(keep)
      : "v"(voff), "v"(voff_last), "s"(sbase), "s"(lds_addr)
      : "memory");
}
#define CAS __attribute__((address_space(4)))
constexpr int K2_XB = D * 256;
constexpr int K2_XH = D * 128;
constexpr int K2_ST = K2_XB;
constexpr int K2_CB = 131072;
constexpr int K2_LDS = K2_CB + 2048;

DI void gload16_asm(u32x4& dst, const void* sbase, u32 voff) {
  sbase = uni(sbase);
  asm volatile(SNOP2 "global_load_dwordx4 %0, %1, %2" : "=v"(dst) : "v"(voff), "s"(sbase) : "memory");
}
DI u32 umin32(u32 v) { return v < 0xFFFFFFFEu ? v : 0xFFFFFFFEu; }
DI u32 umax3(u32 a, u32 b, u32 c) { const u32 t = a > b ? a : b; return t > c ? t : c; }
template <int OFF>
DI void gload16_off(u32x4& dst, const void* sbase, u32 voff) {
  sbase = uni(sbase);
  asm volatile(SNOP2 "global_load_dwordx4 %0, %1, %2 offset:%3" : "=v"(dst) : "v"(voff), "s"(sbase), "n"(OFF) : "memory");
}
DI void glds16_nom0(const void* sbase, u32 voff, u32 lds_addr) {
  sbase = uni(sbase);
  lds_addr = __builtin_amdgcn_readfirstlane(lds_addr);
  asm volatile("s_mov_b32 m0, %2\n\t" SNOP "global_load_lds_dwordx4 %0, %1" ::"v"(voff), "s"(sbase), "s"(lds_addr) : "memory");
}
DI void glds16_sc1(const void* sbase, u32 voff, u32 lds_addr) {
  sbase = uni(sbase);
  lds_addr = __builtin_amdgcn_readfirstlane(lds_addr);
  asm volatile("s_mov_b32 m0, %2\n\t" SNOP "global_load_lds_dwordx4 %0, %1 sc1" ::"v"(voff), "s"(sbase), "s"(lds_addr) : "memory");
}
template <int N>
DI void wait_vm_pin(u32x4& a, u32x4& b) {
  asm volatile("s_waitcnt vmcnt(%2)" : "+v"(a), "+v"(b) : "n"(N) : "memory");
}
DI float xor32(float v) {
  const u32 u = __builtin_bit_cast(u32, v);
  auto r = __builtin_amdgcn_permlane32_swap(u, u, false, false);
  const u32 a = r[0], b = r[1];
  return __builtin_bit_cast(float, (threadIdx.x & 32) ? a : b);
}

typedef unsigned long long u64;
typedef __attribute__((address_space(1))) u64 gu64;
typedef __attribute__((address_space(1))) u32 gu32;
#define RLX_AGENT __ATOMIC_RELAXED, __HIP_MEMORY_SCOPE_AGENT
#define EXP_WE 8
#define EXP_NW0 5
#define EXP_NWH 15
#define EXP_NW2X (EXP_NWH - EXP_NW0 - EXP_NW1)
#define EXP_NW1 5
constexpr int NWC = EXP_NW0 + EXP_NW1;
template <bool WF>
DI void conv_body(const u16* __restrict__ ssm_k, const u16* __restrict__ ssm_D, char* __restrict__ ws, char* smem,
                  const int w, u32x4 (&wf)[28], const char* wbase, Stamps2& s2) {
  STAMP(ws, 1, 0);
  const int lane = threadIdx.x & 63;
  const int ch = w >> 2, q = w & 3;
  const int cpair = (blockIdx.x & 7) * 32 + (blockIdx.x >> 3);
  const int d = 2 * cpair + ch;
  char* L = smem + ch * K1_CH;
  const u32 L0 = (u32)(size_t)(LAS char*)L;
  const u16* kf = ssm_k + (size_t)d * S;
  const u32 vo = lane * 16;
  {
    const u32 vl = (lane > 62 ? 62 : lane) * 16;
    glds16x4_asm(kf + (2 + 2 * q), vo, vl, L0 + q * K1_KSTR + 64);
    const char* ub = ws + ((q >> 1) ? OFF_RO : OFF_RE) + (size_t)d * S * 2 + (q & 1) * 2048;
    const u32 ul = L0 + K1_UOFF + (q >> 1) * K1_USTR + (q & 1) * 2048;
    glds16_nom0(ub, vo, ul);
    glds16_nom0(ub + 1024, vo, ul + 1024);
  }
  const int n = lane & 15, q4 = lane >> 4;
  const int pbase = 32 * (n >> 1) + 8 * q4 + 4 * (n & 1) + 512 * q;
  const int di = n + q - 15;
  u32 head;
  asm volatile(SNOP2 "global_load_dword %0, %1, %2" : "=v"(head) : "v"((u32)(di < 0 ? 0 : di) * 4), "s"(uni(kf)) : "memory");
  auto ldwf = [&](auto kI) {
    constexpr int k = decltype(kI)::value;
    gload16_off<(k % 4) * 1024>(wf[k], wbase + (k / 4) * 4096, vo);
  };
  bar();
  if constexpr (WF) static_for<0, EXP_NW0>(ldwf);
  const CAS float* MODc = (const CAS float*)(size_t)(ws + OFF_MOD);
  const float sa = MODc[d], ca = MODc[D + d], ga = MODc[2 * D + d], cbv = MODc[4 * D + d];
  const u32 Draw = ((const CAS u32*)(size_t)ssm_D)[cpair];
  if (lane < 14) *(LAS u32x4*)(L + K1_UOFF + (q >> 1) * K1_USTR + (256 + 14 * (q & 1) + lane) * 16) = u32x4{0u, 0u, 0u, 0u};
  u32 Ab = L0 + (n & 3) * K1_KSTR + ((n >> 2) + q4 + 8 * q) * 16;
  u32 Bb = L0 + K1_UOFF + (n & 1) * K1_USTR + (28 - 4 * (n >> 1) + q4 + 8 * q) * 16;
  u32 Hd = L0 + q * K1_KSTR + n * 4;
  asm volatile("" : "+v"(Ab), "+v"(Bb), "+v"(Hd) : "s"(sa), "s"(ca), "s"(ga), "s"(Draw), "s"(cbv));
  asm volatile("s_waitcnt vmcnt(%1)" : "+v"(head) : "n"(WF ? EXP_NW0 : 0) : "memory");
  *(LAS u32*)(size_t)Hd = di < 0 ? 0u : head;
  wait_lgkm0();
  STAMP(ws, 1, 1);
  bar();
  bf16x8 fa[8], fb[8];
  auto rd = [&](int dl) {
#pragma unroll
    for (int g = 0; g < 8; ++g) {
      fa[g] = *(const LAS bf16x8*)(size_t)(Ab + 64 * dl + 512 * g);
      fb[g] = *(const LAS bf16x8*)(size_t)(Bb + 64 * dl + 512 * (7 - g));
    }
  };
  rd(0);
  __builtin_amdgcn_sched_barrier(0);
  u32x2 qv[2];
  f32x4 mu[2], rs[2];
  asm volatile(SNOP2
               "global_load_dwordx2 %0, %6, %8\n\t"
               "global_load_dwordx2 %1, %6, %8 offset:512\n\t"
               "global_load_dwordx4 %2, %7, %9\n\t"
               "global_load_dwordx4 %3, %7, %9 offset:1024\n\t"
               "global_load_dwordx4 %4, %7, %10\n\t"
               "global_load_dwordx4 %5, %7, %10 offset:1024"
               : "=&v"(qv[0]), "=&v"(qv[1]), "=&v"(mu[0]), "=&v"(mu[1]), "=&v"(rs[0]), "=&v"(rs[1])
               : "v"((u32)pbase * 2), "v"((u32)pbase * 4), "s"(uni(ws + OFF_QT + (size_t)d * S * 2)),
                 "s"(uni(ws + OFF_MEAN)), "s"(uni(ws + OFF_RSTD))
               : "memory");
  const u32x4 sent = {~0u, ~0u, ~0u, ~0u};
  const void* fillb = uni(ws + OFF_ACT + (size_t)(cpair * 8 + w) * 4096);
  f32x4 acc[8];
#pragma unroll
  for (int j = 0; j < 8; ++j) acc[j] = f32x4{0.f, 0.f, 0.f, 0.f};
  static_for<0, 72>([&](auto mI) {
    constexpr int m = decltype(mI)::value, t = m % 36;
    constexpr int sd = t < 1 ? 0 : t < 3 ? 1 : t < 6 ? 2 : t < 10 ? 3 : t < 15 ? 4 : t < 21 ? 5 : t < 28 ? 6 : 7;
    constexpr int g = t - sd * (sd + 1) / 2;
    if constexpr (m == 36) {
      rd(1);
      __builtin_amdgcn_sched_barrier(0);
    }
    f32x4 a = __builtin_amdgcn_mfma_f32_16x16x32_bf16(fa[g], fb[sd - g], acc[sd], 0, 0, 0);
    asm volatile("" : "+v"(a));
    acc[sd] = a;
    if constexpr (m % 4 == 3) {
      constexpr int v = m / 4;
      if constexpr (v < 4) {
        u32x4 sv = sent;
        asm volatile("global_store_dwordx4 %0, %1, %2 offset:%3 sc1\n\ts_nop 1" ::"v"(vo), "v"(sv), "s"(fillb), "n"(v * 1024) : "memory");
      } else if constexpr (WF && v - 4 < EXP_NW1) {
        ldwf(std::integral_constant<int, EXP_NW0 + v - 4>{});
      }
      __builtin_amdgcn_sched_barrier(0);
    }
  });
  STAMP(ws, 1, 3);
  LAS f32x4* X = (LAS f32x4*)(L + K1_XOFF);
#pragma unroll
  for (int j = 0; j < 8; ++j) X[(j * 4 + q) * 64 + lane] = acc[j];
  u32x4 uc[2];
#pragma unroll
  for (int j = 0; j < 2; ++j)
    uc[j] = *(const LAS u32x4*)(L + K1_UOFF + K1_USTR + (255 - (32 * (2 * q + j) + 4 * (n >> 1) + q4)) * 16);
  asm volatile("s_waitcnt vmcnt(%6)"
               : "+v"(qv[0]), "+v"(qv[1]), "+v"(mu[0]), "+v"(mu[1]), "+v"(rs[0]), "+v"(rs[1])
               : "n"(WF ? EXP_NW1 : 0)
               : "memory");
  f32x4 qf[2], hg[2], kd[2];
  {
    const int sh = (n & 1) ? 16 : 0;
    const float gc = ga * round_bf16(1.f + ca), gs = ga * sa, Dd = ch ? hi_f(Draw) : lo_f(Draw);
#pragma unroll
    for (int j = 0; j < 2; ++j) {
#pragma unroll
      for (int r = 0; r < 4; ++r) {
        kd[j][r] = __builtin_bit_cast(float, (uc[j][3 - r] << sh) & 0xffff0000u) * Dd;
        qf[j][r] = (r & 1) ? hi_f(qv[j][r >> 1]) : lo_f(qv[j][r >> 1]);
        hg[j][r] = (qf[j][r] - mu[j][r]) * rs[j][r] * gc + gs;
      }
      asm volatile("" : "+v"(kd[j]), "+v"(qf[j]), "+v"(hg[j]));
    }
  }
  wait_lgkm0();
  bar();
  STAMP(ws, 1, 4);
  u16* X1T = (u16*)(ws + OFF_X1T) + (size_t)d * S + pbase;
  u16* XS = (u16*)(ws + OFF_XS) + (size_t)d * S + pbase;
  const float c1 = round_bf16(1.f + cbv);
#pragma unroll
  for (int j = 0; j < 2; ++j) {
    const LAS f32x4* Xj = X + ((2 * q + j) * 4) * 64 + lane;
    const f32x4 y = (Xj[0] + Xj[64]) + (Xj[128] + Xj[192]);
    const f32x4 o = qf[j] + hg[j] * (y + kd[j]);
    const u32 p0 = umin32(pack_bf16(o[0], o[1])), p1 = umin32(pack_bf16(o[2], o[3]));
    st8_sc1(XS + 256 * j,
            u32x2{umin32(pack_bf16(lo_f(p0) * c1, hi_f(p0) * c1)), umin32(pack_bf16(lo_f(p1) * c1, hi_f(p1) * c1))});
    st8_sc1(X1T + 256 * j, u32x2{p0, p1});
  }
  STAMP(ws, 1, 5);
}

DI void k2_stats(char* __restrict__ ws, char* smem, const int sblk) {
  const int tid = threadIdx.x, lane = tid & 63;
  const int w = __builtin_amdgcn_readfirstlane(tid >> 6);
  const int n = lane & 31, h = lane >> 5;
  STAMP_DECL;
  STAMP(ws, 2, 0);
  {
    const int p0 = sblk * 64;
    const rsrc_t rx = mk_rsrc(ws + OFF_X1T + ((size_t)(w * 56) * S + p0) * 2, 56 * S * 2);
    u32 x[28];
    for (int spin = 0; spin < (1 << 14); ++spin) {
      asm volatile("" ::: "memory");
      u32 m = 0;
#pragma unroll
      for (int i = 0; i < 28; ++i) x[i] = __builtin_amdgcn_raw_buffer_load_b32(rx, (h * S) * 2 + n * 4 + i * (4 * S), 0, 16);
#pragma unroll
      for (int i = 0; i < 28; i += 2) m = umax3(m, x[i], x[i + 1]);
      if (__all(m != ~0u)) break;
      __builtin_amdgcn_s_sleep(4);
    }
    __syncthreads();
    float s0 = 0.f, s1 = 0.f, q0 = 0.f, q1 = 0.f;
#pragma unroll
    for (int i = 0; i < 28; ++i) {
      const float a = lo_f(x[i]), b = hi_f(x[i]);
      s0 += a;
      s1 += b;
      q0 = __builtin_fmaf(a, a, q0);
      q1 = __builtin_fmaf(b, b, q1);
    }
    *(LAS f32x4*)(smem + ((w * 2 + h) * 32 + n) * 16) = f32x4{s0, q0, s1, q1};
    __syncthreads();
    if (tid < 64) {
      float sm = 0.f, sq = 0.f;
#pragma unroll
      for (int i = 0; i < 16; ++i) {
        const f32x2 t = *(const LAS f32x2*)(smem + (i * 64 + tid) * 8);
        sm += t[0];
        sq += t[1];
      }
      const float mean = sm * (1.f / D);
      float var = sq * (1.f / D) - mean * mean;
      var = (var < 0.f ? 0.f : var) + 1e-5f;
      const float rstd = 1.f / sqrtf(var);
      const float sig = var * rstd;
      const float mh = round_bf16(mean), sh = round_bf16(sig);
      const u32 b0 = pack_bf16(-mh, mh - mean), b1 = pack_bf16(-mh, sh), b2 = pack_bf16(sig - sh, sh);
      const float c1 = -2.302208198f * rstd;
      const float c2 = -0.1029432397f * (rstd * rstd * rstd);
      gu64* G = (gu64*)(size_t)(ws + OFF_SG) + p0 + tid;
      __hip_atomic_store(G, ((u64)b1 << 32) | b0, RLX_AGENT);
      __hip_atomic_store(G + S, ((u64)__builtin_bit_cast(u32, var) << 32) | b2, RLX_AGENT);
      __hip_atomic_store(G + 2 * S, ((u64)__builtin_bit_cast(u32, c1) << 32) | __builtin_bit_cast(u32, c2), RLX_AGENT);
    }
    STAMP(ws, 6, 1);
    STAMP(ws, 6, 2);
    STAMP_FLUSH(ws, 6);
  }
}

DI void k2_gemm(const u16* __restrict__ ssm_k, const u16* __restrict__ ssm_D, char* __restrict__ ws, u16* __restrict__ out,
                char* smem, const int w) {
  const int tid = threadIdx.x, lane = tid & 63;
  const int n = lane & 31, h = lane >> 5;
  const int xcd = blockIdx.x & 7, idx = blockIdx.x >> 3;
  const int mt = (xcd >> 1) * 4 + (idx & 3);
  const int dt = idx >> 2, sh2 = xcd & 1;
  const int ft = sh2 * 7 + dt;
  const int tile = ft * 8 + w;
  const u32 lds0 = (u32)(size_t)(LAS char*)smem;
  u32x4 wf[28], wd[28];
  const char* wbase = ws + OFF_WGU + (size_t)(tile * 28) * 1024;
  const u32 wa = lane * 16;
  auto ldwf = [&](auto kI) {
    constexpr int k = decltype(kI)::value;
    gload16_off<(k % 4) * 1024>(wf[k], wbase + (k / 4) * 4096, wa);
  };
  Stamps2 s2 = {};
  if (xcd < 7) {
    conv_body<true>(ssm_k, ssm_D, ws, smem, w, wf, wbase, s2);
  } else {
    const int task = idx * 8 + w, t = task >> 1, kh = task & 1;
    const u32x4* wsrc = (const u32x4*)(ws + OFF_WGU) + ((size_t)t * 28 + kh * 14) * 64 + lane;
    const u16* cbp = (const u16*)(ws + OFF_CB) + (n & 1) * D + kh * 224 + 8 * h;
    u32x4 fa[14], fb[14];
#pragma unroll
    for (int i = 0; i < 14; ++i) {
      fa[i] = wsrc[i * 64];
      fb[i] = *(const u32x4*)(cbp + 16 * i);
    }
    __builtin_amdgcn_sched_barrier(0);
    f32x16 va;
#pragma unroll
    for (int r = 0; r < 16; ++r) va[r] = 0.f;
#pragma unroll
    for (int i = 0; i < 14; ++i) va = mfma32(fb[i], __builtin_bit_cast(bf16x8, fa[i]), va);
    if (h == 0) {
      const float f0 = va[0], f1 = va[1];
      const u32 a0 = umin32(__builtin_bit_cast(u32, f0)), a1 = __builtin_bit_cast(u32, f1);
      __hip_atomic_store((gu64*)(size_t)(ws + OFF_VG) + kh * 3584 + t * 32 + n, ((u64)a1 << 32) | a0, RLX_AGENT);
    }
    __builtin_amdgcn_sched_barrier(0);
    static_for<0, NWC>(ldwf);
    wait_vm<0>();
  }
  STAMP_DECL;
  STAMP(ws, 2, 0);
  const char* xbase = ws + OFF_XS + ((size_t)(8 * w) * S + mt * 128) * 2;
  const u32 xoff = (u32)((lane >> 3) * S + ((lane & 7) ^ (((lane >> 4) & 1) << 2)) * 8) * 2;
  const char* wdbase = ws + OFF_WD + ((size_t)(2 * dt) * 112 + 14 * w) * 1024 + 4096;
  const int gsl = w < 6 ? w : w - 6;
  const char* gbase = ws + OFF_SG + (size_t)((gsl >> 1) * S + mt * 128 + (gsl & 1) * 64) * 8;
  auto ldwd = [&](auto kI) {
    constexpr int q = decltype(kI)::value, k = (q & 1) * 14 + (q >> 1), kk = k % 14;
    gload16_off<(kk % 8) * 1024 - 4096>(wd[k], wdbase + (k / 14) * 112 * 1024 + (kk / 8) * 8192, wa);
  };
  auto ldx = [&](auto cI) {
    constexpr int c = decltype(cI)::value;
    glds16_sc1(xbase + (size_t)(c % 7) * (64 * S * 2) + (c / 7) * 128, xoff, lds0 + (64 * c + 8 * w) * 128);
  };
  auto xok = [&](auto c0I, auto c1I) {
    u32 m = 0;
#pragma unroll
    for (int c = decltype(c0I)::value; c < decltype(c1I)::value; ++c) {
      const u32x4 t = *(const LAS u32x4*)(smem + (64 * c + 8 * w) * 128 + lane * 16);
      m = umax3(umax3(m, t[0], t[1]), t[2], t[3]);
    }
    return (bool)__all(m != ~0u);
  };
  auto xvalid = [&](auto c0I, auto c1I) {
    if (__builtin_expect(!xok(c0I, c1I), 0)) {
#pragma nounroll
      for (int spin = 0; spin < (1 << 14); ++spin) {
        __builtin_amdgcn_s_sleep(4);
        static_for<decltype(c0I)::value, decltype(c1I)::value>(ldx);
        wait_vm<0>();
        if (xok(c0I, c1I)) break;
      }
    }
    __builtin_amdgcn_sched_barrier(0);
  };
  wait_lgkm0();
  bar();
  XSTAMP(1);
  auto pin = [](f32x16& a) { asm volatile("" : "+v"(a)); };
  auto pinw = [&](auto kI) {
    u32x4 t = wf[decltype(kI)::value];
    asm volatile("" : "+v"(t)::"memory");
    wf[decltype(kI)::value] = t;
  };
  u32x4 aext;
  constexpr int NWH = NWC + EXP_NW2X;
  static_for<NWC, NWH>(ldwf);
  static_for<0, 7>(ldx);
  int xo[2][2];
  {
    const int blk = (lane >> 4) & 1, qq = (lane & 15) >> 2, p = lane & 3;
#pragma unroll
    for (int i = 0; i < 2; ++i)
#pragma unroll
      for (int t = 0; t < 2; ++t)
        xo[i][t] = 128 * (8 * h + 4 * t + qq) + 16 * ((4 * i + 2 * blk + (p >> 1)) ^ ((qq >> 1) << 2)) + 8 * (p & 1);
  }
  f32x16 acc[4];
#pragma unroll
  for (int i = 0; i < 4; ++i)
#pragma unroll
    for (int r = 0; r < 16; ++r) acc[i][r] = 0.f;
  asm volatile("" : "+v"(xo[0][0]), "+v"(xo[0][1]), "+v"(xo[1][0]), "+v"(xo[1][1]));
  constexpr int PF = 3;
  bf16x8 xr[PF][2];
  auto rdx = [&](auto kI, auto iI) {
    constexpr int k = decltype(kI)::value, i = decltype(iI)::value;
    if constexpr (k == 28) {
      int a0 = xo[i][0] + K2_XH, a1 = xo[i][1] + K2_XH;
      asm volatile("" : "+v"(a0), "+v"(a1));
      xo[i][0] = a0;
      xo[i][1] = a1;
    }
    xr[k % PF][i] = tr_frag(smem, xo[i][0] + (k % 28) * 2048, xo[i][1] + (k % 28) * 2048);
  };
  using I0 = std::integral_constant<int, 0>;
  using I1 = std::integral_constant<int, 1>;
  using I2 = std::integral_constant<int, 2>;
  using I3 = std::integral_constant<int, 3>;
  using I7 = std::integral_constant<int, 7>;
  using I14 = std::integral_constant<int, 14>;
  LAS u64* CS = (LAS u64*)(smem + K2_ST);
  XSTAMP(4);
  wait_vm<0>();
  static_for<0, NWH>(pinw);
  xvalid(I0{}, I7{});
  XSTAMP(5);
  bar();
  YSTAMP(1);
  static_for<0, PF - 1>([&](auto jI) {
    rdx(jI, I0{});
    rdx(jI, I1{});
  });
  __builtin_amdgcn_sched_barrier(0);
  u64 gv, gp0, gp1;
  const char* vbase = ws + OFF_VG + (size_t)tile * 256;
  constexpr int RW = 28 - NWH, KX = RW + 7, KV = 25;
  static_assert(KX + 4 < KV && 27 - KX <= 9, "stream does not fit");
  static_for<0, 28>([&](auto kI) {
    constexpr int k = decltype(kI)::value;
    if constexpr (k >= NWH) {
      constexpr int issued = k + (k > KX ? 2 : 0);
      u32x4 t = wf[k];
      asm volatile("s_waitcnt vmcnt(%1)" : "+v"(t) : "n"(issued - 1 - (k - NWH)) : "memory");
      wf[k] = t;
    }
    if constexpr (k == KV) {
      u64 t = gv, p0 = gp0, p1 = gp1;
      asm volatile("s_waitcnt vmcnt(%3)" : "+v"(t), "+v"(p0), "+v"(p1) : "n"(KV - KX - 1) : "memory");
      xvalid(I7{}, I14{});
      if (!__all((t >> 32) != 0 && (u32)p0 != ~0u && (u32)p1 != ~0u)) {
        for (int spin = 0; spin < (1 << 16); ++spin) {
          __builtin_amdgcn_s_sleep(2);
          t = __hip_atomic_load((gu64*)(size_t)gbase + lane, RLX_AGENT);
          p0 = __hip_atomic_load((gu64*)(size_t)vbase + n, RLX_AGENT);
          p1 = __hip_atomic_load((gu64*)(size_t)vbase + 3584 + n, RLX_AGENT);
          if (__all((t >> 32) != 0 && (u32)p0 != ~0u && (u32)p1 != ~0u)) break;
        }
        __builtin_amdgcn_s_waitcnt(0x0F70);
      }
      CS[gsl * 64 + lane] = t;
      const float v1 = __builtin_bit_cast(float, (u32)p0) + __builtin_bit_cast(float, (u32)p1);
      const float v2 = __builtin_bit_cast(float, (u32)(p0 >> 32)) + __builtin_bit_cast(float, (u32)(p1 >> 32));
      const float v1h = round_bf16(v1), v2h = round_bf16(v2);
      u32x4 a = {pack_bf16(v1h, v1h), pack_bf16(v1 - v1h, v2h), pack_bf16(v2h, v2 - v2h), 0u};
      if (h) a = u32x4{0u, 0u, 0u, 0u};
      asm volatile("" : "+v"(a));
      aext = a;
      wait_lgkm0();
      __builtin_amdgcn_sched_barrier(0);
      bar();
    }
    __builtin_amdgcn_sched_barrier(0);
    acc[0] = mfma32(wf[k], xr[k % PF][0], acc[0]);
    pin(acc[0]);
    __builtin_amdgcn_sched_barrier(0);
    rdx(std::integral_constant<int, k + PF - 1>{}, I0{});
    __builtin_amdgcn_sched_barrier(0);
    acc[1] = mfma32(wf[k], xr[k % PF][1], acc[1]);
    pin(acc[1]);
    __builtin_amdgcn_sched_barrier(0);
    rdx(std::integral_constant<int, k + PF - 1>{}, I1{});
    if constexpr (k < RW) ldwf(std::integral_constant<int, NWH + k>{});
    else if constexpr (k < KX) ldx(std::integral_constant<int, 7 + k - RW>{});
    else if constexpr (k == KX) {
      u64 t, t0, t1;
      asm volatile(SNOP2 "global_load_dwordx2 %0, %1, %2 sc1" : "=v"(t) : "v"((u32)lane * 8), "s"(uni(gbase)) : "memory");
      asm volatile(SNOP2 "global_load_dwordx2 %0, %1, %2 sc1" : "=v"(t0) : "v"((u32)n * 8), "s"(uni(vbase)) : "memory");
      asm volatile(SNOP2 "global_load_dwordx2 %0, %1, %2 sc1" : "=v"(t1) : "v"((u32)n * 8), "s"(uni(vbase + 3584 * 8)) : "memory");
      gv = t;
      gp0 = t0;
      gp1 = t1;
    } else {
      ldwd(std::integral_constant<int, k - KX - 1>{});
    }
    __builtin_amdgcn_sched_barrier(0);
  });
  constexpr int WE = 27 - KX;
  YSTAMP(2);
  const rsrc_t rw = mk_rsrc(ws + OFF_ACT + ((size_t)(mt * 4) * 112 + tile) * 1024, 337 * 1024);
  const rsrc_t ra = mk_rsrc(ws + OFF_ACT + ((size_t)(mt * 4 + sh2) * 112 + 14 * w) * 1024, 238 * 1024);
  u32x4 af[28];
  auto ldact = [&](auto kI) {
    constexpr int k = decltype(kI)::value;
    af[k] = __builtin_amdgcn_raw_buffer_load_b128(ra, lane * 16 + (k % 14) * 1024, (k / 14) * 224 * 1024, 16);
  };
  float c1[2], c2[2], var[2], pe[2], pm[2], pr[2];
  u32x4 ov[2];
  auto corr = [&](auto iI) {
    constexpr int i = decltype(iI)::value;
    u64 g[3];
#pragma unroll
    for (int k = 0; k < 3; ++k) g[k] = CS[k * 128 + i * 32 + n];
    const u32x4 bext = {(u32)g[0], (u32)(g[0] >> 32), (u32)g[1], 0u};
    acc[i] = mfma32(aext, __builtin_bit_cast(bf16x8, bext), acc[i]);
    c1[i & 1] = __builtin_bit_cast(float, (u32)(g[2] >> 32));
    c2[i & 1] = __builtin_bit_cast(float, (u32)g[2]);
    var[i & 1] = __builtin_bit_cast(float, (u32)(g[1] >> 32));
  };
  auto slice = [&](auto iI, auto uI) {
    constexpr int i = decltype(iI)::value, u = decltype(uI)::value, z = i & 1;
    if constexpr (u >= 1) {
      float o = pm[z] * __builtin_amdgcn_rcpf(__builtin_fmaf(pe[z], var[z], var[z]));
      if constexpr ((u - 1) & 1) {
        u32 t = umin32(pack_bf16(pr[z], o));
        asm volatile("" : "+v"(t));
        ov[z][(u - 1) / 2] = t;
      } else {
        asm volatile("" : "+v"(o));
        pr[z] = o;
      }
    }
    if constexpr (u < 8) {
      const float a = acc[i][u];
      float e = __builtin_amdgcn_exp2f(__builtin_fmaf(a * a, c2[z], c1[z]) * a), m = a * acc[i][u + 8];
      asm volatile("" : "+v"(e), "+v"(m));
      pe[z] = e;
      pm[z] = m;
    } else {
      __builtin_amdgcn_raw_buffer_store_b128(ov[z], rw, lane * 16 + i * 112 * 1024, 0, 16);
      asm volatile("s_nop 1");
    }
  };
  corr(I0{});
  corr(I1{});
  __builtin_amdgcn_sched_barrier(0);
  static_for<0, 28>([&](auto kI) {
    constexpr int k = decltype(kI)::value;
    acc[2] = mfma32(wf[k], xr[(28 + k) % PF][0], acc[2]);
    pin(acc[2]);
    __builtin_amdgcn_sched_barrier(0);
    if constexpr (k + PF - 1 < 28) rdx(std::integral_constant<int, 28 + k + PF - 1>{}, I0{});
    if constexpr (k >= 1 && k <= 9) slice(I0{}, std::integral_constant<int, k - 1>{});
    __builtin_amdgcn_sched_barrier(0);
    acc[3] = mfma32(wf[k], xr[(28 + k) % PF][1], acc[3]);
    pin(acc[3]);
    __builtin_amdgcn_sched_barrier(0);
    if constexpr (k % EXP_LOCK == EXP_LOCK - 1) bar();
    if constexpr (k + PF - 1 < 28) rdx(std::integral_constant<int, 28 + k + PF - 1>{}, I1{});
    if constexpr (k >= 1 && k <= 9) slice(I1{}, std::integral_constant<int, k - 1>{});
    if constexpr (WE + k < 28) ldwd(std::integral_constant<int, WE + k>{});
    else if constexpr (WE + k - 28 < 7) ldact(std::integral_constant<int, WE + k - 28>{});
    __builtin_amdgcn_sched_barrier(0);
  });
  YSTAMP(3);
  constexpr int A0 = WE < 7 ? WE : 7;
  corr(I2{});
  corr(I3{});
  __builtin_amdgcn_sched_barrier(0);
  static_for<0, 9>([&](auto uI) {
    constexpr int u = decltype(uI)::value;
    slice(I2{}, uI);
    slice(I3{}, uI);
    if constexpr (A0 + u < 14 && u < 8) ldact(std::integral_constant<int, A0 + u>{});
    __builtin_amdgcn_sched_barrier(0);
  });
  static_for<(A0 + 8 < 14 ? A0 + 8 : 14), 14>(ldact);
  bar();
  YSTAMP(4);
  auto chk = [&](auto gI) {
    constexpr int g = decltype(gI)::value;
    u32 m = 0;
#pragma unroll
    for (int k = 7 * g; k < 7 * g + 7; ++k) m = umax3(umax3(m, af[k][0], af[k][1]), af[k][2], af[k][3]);
    return (bool)__all(m != ~0u);
  };
  auto valid = [&](auto gI) {
    constexpr int g = decltype(gI)::value;
    if (__builtin_expect(!chk(gI), 0)) {
#pragma nounroll
      for (int spin = 0; spin < (1 << 14); ++spin) {
        __builtin_amdgcn_s_sleep(8);
        asm volatile("" ::: "memory");
        static_for<7 * g, 7 * g + 7>(ldact);
        if (chk(gI)) break;
      }
      __builtin_amdgcn_s_waitcnt(0x0F70);
    }
    __builtin_amdgcn_sched_barrier(0);
  };
  const int pcol = mt * 128 + sh2 * 32 + n;
  const int dbase = dt * 64 + w * 8 + 4 * h;
  __builtin_amdgcn_sched_barrier(0);
  LAS f32x4* R = (LAS f32x4*)smem;
  f32x16 o[4];
#pragma unroll
  for (int i = 0; i < 4; ++i)
#pragma unroll
    for (int r = 0; r < 16; ++r) o[i][r] = 0.f;
  auto dump1 = [&](auto tjI, auto cI) {
    constexpr int tj = decltype(tjI)::value, c = decltype(cI)::value, i = tj * 2 + (c >> 2), q = c & 3;
    R[((tj * 8 + c) * 8 + w) * 64 + lane] = f32x4{o[i][4 * q], o[i][4 * q + 1], o[i][4 * q + 2], o[i][4 * q + 3]};
  };
  auto step = [&](auto kI) {
    constexpr int k = decltype(kI)::value, tj = k / 14;
    const bf16x8 b = __builtin_bit_cast(bf16x8, af[k]);
    o[2 * tj] = mfma32(wd[k % 14], b, o[2 * tj]);
    pin(o[2 * tj]);
    __builtin_amdgcn_sched_barrier(0);
    if constexpr (k >= 15 && k < 19) dump1(I0{}, std::integral_constant<int, 2 * (k - 15)>{});
    __builtin_amdgcn_sched_barrier(0);
    o[2 * tj + 1] = mfma32(wd[14 + k % 14], b, o[2 * tj + 1]);
    pin(o[2 * tj + 1]);
    __builtin_amdgcn_sched_barrier(0);
    if constexpr (k >= 15 && k < 19) dump1(I0{}, std::integral_constant<int, 2 * (k - 15) + 1>{});
    if constexpr (k >= EXP_T1 && k < 14) {
      constexpr int n0 = ((k - EXP_T1) * 14) / (14 - EXP_T1), n1 = ((k + 1 - EXP_T1) * 14) / (14 - EXP_T1);
      static_for<14 + n0, 14 + n1>(ldact);
    }
    __builtin_amdgcn_sched_barrier(0);
  };
  u16 xres[2][4];
  f32x4 gbv;
  auto fin = [&](auto tjI) {
    constexpr int tj = decltype(tjI)::value;
    f32x4 v[8];
#pragma unroll
    for (int sw = 0; sw < 8; ++sw) v[sw] = R[((tj * 8 + w) * 8 + sw) * 64 + lane];
    const f32x4 sum = ((v[0] + v[1]) + (v[2] + v[3])) + ((v[4] + v[5]) + (v[6] + v[7]));
    float r[4];
#pragma unroll
    for (int e = 0; e < 4; ++e) r[e] = __builtin_fmaf(gbv[e], sum[e], bf2f(xres[tj][e]));
    const int pc = pcol + 64 * tj;
    const int srow = (pc & ~7) | ((pc & 3) << 1) | ((pc >> 2) & 1);
    *(u32x2*)(out + (size_t)srow * D + dbase) = u32x2{pack_bf16(r[0], r[1]), pack_bf16(r[2], r[3])};
  };
  valid(I0{});
  asm volatile(""
               : "+v"(wd[0]), "+v"(wd[1]), "+v"(wd[2]), "+v"(wd[3]), "+v"(wd[4]), "+v"(wd[5]), "+v"(wd[6]), "+v"(wd[7]),
                 "+v"(wd[8]), "+v"(wd[9]), "+v"(wd[10]), "+v"(wd[11]), "+v"(wd[12]), "+v"(wd[13])::"memory");
  asm volatile(""
               : "+v"(wd[14]), "+v"(wd[15]), "+v"(wd[16]), "+v"(wd[17]), "+v"(wd[18]), "+v"(wd[19]), "+v"(wd[20]),
                 "+v"(wd[21]), "+v"(wd[22]), "+v"(wd[23]), "+v"(wd[24]), "+v"(wd[25]), "+v"(wd[26]), "+v"(wd[27])::"memory");
  YSTAMP(5);
  static_for<0, 3>(step);
  {
    const rsrc_t rx = mk_rsrc(ws + OFF_X1T, D * S * 2);
#pragma unroll
    for (int r = 0; r < 8; ++r)
      xres[r >> 2][r & 3] = __builtin_amdgcn_raw_buffer_load_b16(rx, ((dbase + (r & 3)) * S + pcol + (r >> 2) * 64) * 2, 0, 16);
    gbv = *(const f32x4*)((const float*)(ws + OFF_MOD) + 5 * D + dbase);
    __builtin_amdgcn_sched_barrier(0);
  }
  static_for<3, 7>(step);
  valid(I1{});
  static_for<7, 14>(step);
  valid(I2{});
  static_for<14, 21>(step);
  wait_lgkm0();
  bar();
  valid(I3{});
  static_for<21, 24>(step);
  fin(I0{});
  __builtin_amdgcn_sched_barrier(0);
  static_for<24, 28>(step);
  static_for<0, 8>([&](auto cI) { dump1(I1{}, cI); });
  wait_lgkm0();
  bar();
  STAMP(ws, 2, 6);
  fin(I1{});
}

extern "C" __global__ NOPK __launch_bounds__(512, 2) void k1_main(const u16* __restrict__ ssm_k,
                                                                  const u16* __restrict__ ssm_D, char* __restrict__ ws,
                                                                  u16* __restrict__ out) {
  extern __shared__ __attribute__((aligned(16))) char smem[];
  const int w = __builtin_amdgcn_readfirstlane(threadIdx.x >> 6);
  if (blockIdx.x >= 224) {
    if ((blockIdx.x & 7) < 7) {
      u32x4 dummy[28];
      Stamps2 s2 = {};
      conv_body<false>(ssm_k, ssm_D, ws, smem, w, dummy, nullptr, s2);
    } else {
      __builtin_amdgcn_s_sleep(100);
    }
    k2_stats(ws, smem, blockIdx.x - 224);
  } else {
    k2_gemm(ssm_k, ssm_D, ws, out, smem, w);
  }
}

extern "C" __device__ __attribute__((used, noinline)) void k_code_end() {}

extern "C" void kernel_launch(void* const* d_in, const int* in_sizes, int n_in, void* d_out, int out_size, void* d_ws,
                              size_t ws_size, hipStream_t stream) {
  const u16* q = (const u16*)d_in[0];
  const u16* k = (const u16*)d_in[1];
  const u16* v = (const u16*)d_in[2];
  const u16* shift_k = (const u16*)d_in[3];
  const u16* ssm_k = (const u16*)d_in[4];
  const u16* ssm_D = (const u16*)d_in[5];
  const u16* w_gate = (const u16*)d_in[7];
  const u16* w_up = (const u16*)d_in[8];
  const u16* w_down = (const u16*)d_in[9];
  const u16* c = (const u16*)d_in[10];
  const u16* w_mod = (const u16*)d_in[11];
  char* ws = (char*)d_ws;
  (void)hipFuncSetAttribute((const void*)k1_main, hipFuncAttributeMaxDynamicSharedMemorySize, K2_LDS);
  k0_prep<<<K0_GRID, 256, 0, stream>>>(q, k, v, shift_k, c, w_mod, ssm_k, w_gate, w_up, w_down, ws);
  k1_main<<<256, 512, K2_LDS, stream>>>(ssm_k, ssm_D, ws, (u16*)d_out);
}
```

```cpp
#include <hip/hip_runtime.h>
#include <hip/hip_bf16.h>
#include <stdint.h>
#include <type_traits>

#define NTAUX 0
#define NTLOAD(p) (*(p))
typedef unsigned short u16;
typedef unsigned int u32;
typedef short bf16x8 __attribute__((ext_vector_type(8)));
typedef short s16x4 __attribute__((ext_vector_type(4)));
typedef float f32x2 __attribute__((ext_vector_type(2)));
typedef float f32x4 __attribute__((ext_vector_type(4)));
typedef float f32x16 __attribute__((ext_vector_type(16)));
typedef u32 u32x2 __attribute__((ext_vector_type(2)));
typedef u32 u32x4 __attribute__((ext_vector_type(4)));
typedef __bf16 bf16x2_t __attribute__((ext_vector_type(2)));
#define LAS __attribute__((address_space(3)))
#define DI __device__ __forceinline__

constexpr int S = 2048, D = 448, F = 1792;

constexpr size_t SZ_DS = (size_t)D * S * 2;
constexpr size_t OFF_RE = 0;
constexpr size_t OFF_RO = OFF_RE + SZ_DS;
constexpr size_t OFF_QT = OFF_RO + SZ_DS;
constexpr size_t OFF_X1T = OFF_QT + SZ_DS;
constexpr size_t OFF_MEAN = OFF_X1T + SZ_DS;
constexpr size_t OFF_RSTD = OFF_MEAN + S * 4;
constexpr size_t OFF_MOD = OFF_RSTD + S * 4;
constexpr size_t OFF_SG = OFF_MOD + 16384;
constexpr size_t OFF_CB = OFF_MOD + 12288;

constexpr size_t OFF_VG = OFF_SG + 3 * S * 8;
constexpr size_t OFF_XS = OFF_VG + 4 * S * 8;
constexpr size_t OFF_VP = OFF_XS + SZ_DS;
constexpr size_t OFF_WGU = OFF_VP + 8 * 2 * 3584 * 4;
constexpr size_t OFF_WD = OFF_WGU + (size_t)2 * D * F * 2;
constexpr size_t OFF_ACT = OFF_WD + (size_t)D * F * 2;
constexpr size_t WS_END = OFF_ACT + (size_t)S * F * 2;

struct Stamps2 {};
#define STAMP_DECL
#define STAMP(wsp, kid, i)
#define STAMP_FLUSH(wsp, kid)
#define HWID(wsp, kid)
#define XSTAMP(i)
#define YSTAMP(i) STAMP(ws, 2, i)
#if defined(__HIP_DEVICE_COMPILE__)
#define NOPK __attribute__((target("no-packed-fp32-ops")))
#else
#define NOPK
#endif

template <int I, int N, class Fn>
DI void static_for(Fn&& f) {
  if constexpr (I < N) {
    f(std::integral_constant<int, I>{});
    static_for<I + 1, N>(f);
  }
}

DI float lo_f(u32 x) { return __builtin_bit_cast(float, x << 16); }
DI float hi_f(u32 x) { return __builtin_bit_cast(float, x & 0xffff0000u); }
DI u32 pack_bf16(float a, float b) {
  f32x2 v = {a, b};
  return __builtin_bit_cast(u32, __builtin_convertvector(v, bf16x2_t));
}
DI float round_bf16(float a) { return lo_f(pack_bf16(a, 0.f)); }
DI float bf2f(u16 x) { return __builtin_bit_cast(float, (u32)x << 16); }

template <int N>
DI void wait_vm() { asm volatile("s_waitcnt vmcnt(%0)" ::"n"(N) : "memory"); }
DI void wait_lgkm0() { asm volatile("s_waitcnt lgkmcnt(0)" ::: "memory"); }
DI void bar() { __builtin_amdgcn_s_barrier(); }

#define SNOP "s_nop 0\n\t"
#define SNOP2 ""
DI const void* uni(const void* p) {
  const unsigned long long v = (unsigned long long)p;
  const u32 lo = __builtin_amdgcn_readfirstlane((u32)v), hi = __builtin_amdgcn_readfirstlane((u32)(v >> 32));
  return (const void*)(((unsigned long long)hi << 32) | lo);
}
DI void glds16_asm(const void* sbase, u32 voff, u32 lds_addr) {
  u32 keep;
  sbase = uni(sbase);
  lds_addr = __builtin_amdgcn_readfirstlane(lds_addr);
  asm volatile("s_mov_b32 %0, m0\n\ts_mov_b32 m0, %3\n\t" SNOP "global_load_lds_dwordx4 %1, %2\n\ts_mov_b32 m0, %0"
               : "=&s"(keep)
               : "v"(voff), "s"(sbase), "s"(lds_addr)
               : "memory");
}
DI void glds16(const void* g, char* lds) {
  __builtin_amdgcn_global_load_lds((const __attribute__((address_space(1))) void*)g, (LAS void*)lds, 16, 0, NTAUX);
}

template <int CTRL>
DI float dpp_add(float v) {
  const int x = __builtin_bit_cast(int, v);
  return v + __builtin_bit_cast(float, __builtin_amdgcn_update_dpp(x, x, CTRL, 0xf, 0xf, false));
}
DI float sum8(float v) {
  v = dpp_add<0xB1>(v);
  v = dpp_add<0x4E>(v);
  v = dpp_add<0x141>(v);
  return v;
}
DI float sum16(float v) { return dpp_add<0x140>(sum8(v)); }
DI float dot2(u32 a, u32 b, float c) {
  return __builtin_amdgcn_fdot2_f32_bf16(__builtin_bit_cast(bf16x2_t, a), __builtin_bit_cast(bf16x2_t, b), c, false);
}
typedef __amdgpu_buffer_rsrc_t rsrc_t;
DI rsrc_t mk_rsrc(const void* p, u32 bytes) { return __builtin_amdgcn_make_buffer_rsrc((void*)p, 0, bytes, 0x00020000); }
DI void st16_sc1(void* p, u32x4 v) { asm volatile("global_store_dwordx4 %0, %1, off sc1\n\ts_nop 1" ::"v"(p), "v"(v) : "memory"); }
DI void st8_sc1(void* p, u32x2 v) { asm volatile("global_store_dwordx2 %0, %1, off sc1" ::"v"(p), "v"(v) : "memory"); }
DI void st4_sc1(void* p, float v) { asm volatile("global_store_dword %0, %1, off sc1" ::"v"(p), "v"(v) : "memory"); }

#define EXP_E12 2
#define EXP_E13 6
#define EXP_LOCK 4
#define EXP_NW2 14
DI bf16x8 tr_frag(const char* smem, int o0, int o1) {
  s16x4 x0 = __builtin_amdgcn_ds_read_tr16_b64_v4i16((LAS s16x4*)(smem + o0));
  s16x4 x1 = __builtin_amdgcn_ds_read_tr16_b64_v4i16((LAS s16x4*)(smem + o1));
  return __builtin_shufflevector(x0, x1, 0, 1, 2, 3, 4, 5, 6, 7);
}
DI f32x16 mfma32(u32x4 a, bf16x8 b, f32x16 c) {
  return __builtin_amdgcn_mfma_f32_32x32x16_bf16(__builtin_bit_cast(bf16x8, a), b, c, 0, 0, 0);
}

DI void retile(int task, const u16* __restrict__ w_gate, const u16* __restrict__ w_up, const u16* __restrict__ w_down,
               char* __restrict__ ws, char* smem, int w, int lane) {
  const int xr = lane >> 4, slot = lane & 15;
  const int chn = slot ^ ((xr << 2) | (w & 3));
  const int h = lane >> 5, blk = (lane >> 4) & 1, qq = (lane & 15) >> 2, p = lane & 3;
  const bool gu = task < 112;
  int kq = 0, fg = 0, dg = 0, fk = 0;
  if (gu) {
    fg = task % 28, kq = task / 28;
    const u16* src = (chn < 8 ? w_gate : w_up) + (size_t)(kq * 112 + 4 * w + xr) * F + fg * 64 + (chn & 7) * 8;
#pragma unroll
    for (int i = 0; i < 7; ++i) glds16(src + (size_t)i * 16 * F, smem + (w + 4 * i) * 1024);
  } else {
    dg = (task - 112) % 7, fk = (task - 112) / 7;
    const u16* src = w_down + (size_t)(fk * 224 + (chn >> 3) * 112 + 4 * w + xr) * D + dg * 64 + (chn & 7) * 8;
#pragma unroll
    for (int i = 0; i < 7; ++i) glds16(src + (size_t)i * 16 * D, smem + (w + 4 * i) * 1024);
  }
  const int col = gu ? blk * 128 + w * 32 + (p & 1) * 16 + (p >> 1) * 8 : (w >> 1) * 128 + (w & 1) * 64 + blk * 32 + p * 8;
  int o[2];
#pragma unroll
  for (int t = 0; t < 2; ++t)
    o[t] = 256 * (8 * h + 4 * t + qq) + 16 * ((col >> 4) ^ ((qq << 2) | ((2 * h + t) & 3))) + (col & 8);
  u32x4* dst = gu ? (u32x4*)(ws + OFF_WGU) + ((size_t)(fg * 4 + w) * 28 + kq * 7) * 64 + lane
                  : (u32x4*)(ws + OFF_WD) + ((size_t)(dg * 2 + (w & 1)) * 112 + fk * 14 + (w >> 1) * 7) * 64 + lane;
  __syncthreads();
#pragma unroll
  for (int i = 0; i < 7; ++i)
    st16_sc1(dst + i * 64, __builtin_bit_cast(u32x4, tr_frag(smem, o[0] + i * 4096, o[1] + i * 4096)));
}

#define EXP_DELAY 0
#define EXP_T1 5
#define EXP_A0 4
#define ACTAUX 16
constexpr int K0_GRID = 504;

extern "C" __global__ __launch_bounds__(256) void k0_prep(const u16* __restrict__ q, const u16* __restrict__ k,
                                               const u16* __restrict__ v, const u16* __restrict__ shift_k,
                                               const u16* __restrict__ c, const u16* __restrict__ w_mod,
                                               const u16* __restrict__ ssm_k, const u16* __restrict__ w_gate,
                                               const u16* __restrict__ w_up, const u16* __restrict__ w_down,
                                               char* __restrict__ ws) {
  __shared__ __attribute__((aligned(16))) char smem[28672];
  float(*red)[64] = (float(*)[64])smem;
  const int b = blockIdx.x, tid = threadIdx.x, lane = tid & 63, w = __builtin_amdgcn_readfirstlane(tid >> 6);
  STAMP_DECL;
  STAMP(ws, 0, 0);
  const int xcd = b & 7, idx = b >> 3;
  const unsigned long long ts0 = __builtin_amdgcn_s_memrealtime();
  u32 cwarm = 0;
  if (idx < 2) {
    u32 lo, hi, len;
    asm volatile(
        "s_getpc_b64 s[40:41]\n\ts_add_u32 s40, s40, k0_prep@rel32@lo+4\n\ts_addc_u32 s41, s41, k0_prep@rel32@hi+12\n\t"
        "s_mov_b32 %0, s40\n\ts_mov_b32 %1, s41\n\ts_mov_b32 %2, k_code_end-k0_prep"
        : "=s"(lo), "=s"(hi), "=s"(len)::"s40", "s41", "scc");
    const char* code = (const char*)(((unsigned long long)hi << 32) | lo);
    if ((u32)(idx * 256 + tid) * 128 < len) cwarm = *(const __attribute__((address_space(1))) u32*)(size_t)(code + (idx * 256 + tid) * 128);
  }
  asm volatile("" ::"s"(q), "s"(k), "s"(v), "s"(shift_k), "s"(c), "s"(w_mod), "s"(ssm_k), "s"(w_gate), "s"(w_up), "s"(w_down),
               "s"(ws));
  if (__builtin_expect(idx >= 42, 0)) {
    if (b - 336 < 128) {
      const int t = b - 336;
      u32x4* ap = (u32x4*)(ws + (t < 64 ? OFF_X1T : OFF_XS)) + (size_t)(t & 63) * 1792 + tid;
      const u32x4 sent = {~0u, ~0u, ~0u, ~0u};
#pragma unroll
      for (int i = 0; i < 7; ++i) st16_sc1(ap + i * 256, sent);
    }
    retile(b - 336, w_gate, w_up, w_down, ws, smem, w, lane);
    if (tid == 0) { ((unsigned long long*)(ws + WS_END))[b] = ts0; ((unsigned long long*)(ws + WS_END))[512 + b] = __builtin_amdgcn_s_memrealtime(); }
    STAMP(ws, 7, 1);
    STAMP_FLUSH(ws, 7);
    return;
  }
  int misc = -1;
  if (xcd == 7) misc = idx < 14 ? 64 + idx : (idx < 36 ? idx - 14 : 1000);
  else if (idx < 4) misc = 64 + 14 + xcd * 4 + idx;
  else if (idx >= 36) misc = 22 + xcd * 6 + (idx - 36);
  if (__builtin_expect(misc < 0, 1)) {
    const int rb = idx - 4;
    const int sq = lane >> 5, cp = lane & 31;
    const int d0 = xcd * 64 + cp * 2;
    const int a = rb * 8 + w * 2 + sq;
    const int s0 = a * 8;
    const rsrc_t rk = mk_rsrc(k, S * D * 2), rv = mk_rsrc(v, S * D * 2), rq = mk_rsrc(q, S * D * 2);
    const int oB = (s0 * D + d0) * 2;
    const int oA = oB - 4 * D * 2, oC = oB + 4 * D * 2;
    u32 kk[12], vv[9], qq[8];
#pragma unroll
    for (int i = 0; i < 4; ++i) kk[i] = __builtin_amdgcn_raw_buffer_load_b32(rk, oA + i * D * 2, 0, NTAUX);
#pragma unroll
    for (int i = 0; i < 4; ++i) kk[4 + i] = __builtin_amdgcn_raw_buffer_load_b32(rk, oB + i * D * 2, 0, NTAUX);
#pragma unroll
    for (int i = 0; i < 4; ++i) kk[8 + i] = __builtin_amdgcn_raw_buffer_load_b32(rk, oC + i * D * 2, 0, NTAUX);
    vv[0] = __builtin_amdgcn_raw_buffer_load_b32(rv, oA + 3 * D * 2, 0, NTAUX);
#pragma unroll
    for (int i = 0; i < 4; ++i) vv[1 + i] = __builtin_amdgcn_raw_buffer_load_b32(rv, oB + i * D * 2, 0, NTAUX);
#pragma unroll
    for (int i = 0; i < 4; ++i) vv[5 + i] = __builtin_amdgcn_raw_buffer_load_b32(rv, oC + i * D * 2, 0, NTAUX);
#pragma unroll
    for (int i = 0; i < 4; ++i) qq[i] = __builtin_amdgcn_raw_buffer_load_b32(rq, oB + i * D * 2, 0, NTAUX);
#pragma unroll
    for (int i = 0; i < 4; ++i) qq[4 + i] = __builtin_amdgcn_raw_buffer_load_b32(rq, oC + i * D * 2, 0, NTAUX);
    const u32x4 sk = *(const u32x4*)(shift_k + d0 * 4);
    u32 warm = 0;
    if (w == 0) warm = NTLOAD((const u32*)((const char*)ssm_k + ((size_t)(xcd * 32 + rb) * 64 + lane) * 128));
    f32x2 wt[4] = {{lo_f(sk[0]), lo_f(sk[2])}, {hi_f(sk[0]), hi_f(sk[2])}, {lo_f(sk[1]), lo_f(sk[3])}, {hi_f(sk[1]), hi_f(sk[3])}};
    f32x2 kf[12], u[9];
#pragma unroll
    for (int i = 0; i < 12; ++i) kf[i] = f32x2{lo_f(kk[i]), hi_f(kk[i])};
#pragma unroll
    for (int i = 0; i < 9; ++i) {
      f32x2 t = kf[i + 3] * wt[0];
      t = __builtin_elementwise_fma(kf[i + 2], wt[1], t);
      t = __builtin_elementwise_fma(kf[i + 1], wt[2], t);
      t = __builtin_elementwise_fma(kf[i], wt[3], t);
      u[i] = t * f32x2{lo_f(vv[i]), hi_f(vv[i])};
    }
    {
      char* dst = ws + (size_t)d0 * (S * 2) + (2040 - s0) * 2;
#pragma unroll
      for (int cI = 0; cI < 2; ++cI) {
        *(u32x4*)(dst + OFF_RO + cI * S * 2) = u32x4{pack_bf16(u[8][cI], u[7][cI]), pack_bf16(u[6][cI], u[5][cI]),
                                                     pack_bf16(u[4][cI], u[3][cI]), pack_bf16(u[2][cI], u[1][cI])};
        *(u32x4*)(dst + OFF_RE + cI * S * 2) = u32x4{pack_bf16(u[7][cI], u[6][cI]), pack_bf16(u[5][cI], u[4][cI]),
                                                     pack_bf16(u[3][cI], u[2][cI]), pack_bf16(u[1][cI], u[0][cI])};
      }
      char* dq = ws + OFF_QT + (size_t)d0 * (S * 2) + s0 * 2;
#pragma unroll
      for (int cI = 0; cI < 2; ++cI) {
        const u32 sel = cI ? 0x07060302u : 0x05040100u;
        *(u32x4*)(dq + cI * S * 2) = u32x4{__builtin_amdgcn_perm(qq[2], qq[0], sel), __builtin_amdgcn_perm(qq[6], qq[4], sel),
                                           __builtin_amdgcn_perm(qq[3], qq[1], sel), __builtin_amdgcn_perm(qq[7], qq[5], sel)};
      }
    }
    asm volatile("" ::"v"(warm));
  } else if (misc < 64) {
    const int bb = misc;
    const int row = bb * 32 + w * 8 + (lane >> 3);
    const int sub = lane & 7;
    u32x4 x[7];
#pragma unroll
    for (int i = 0; i < 7; ++i) x[i] = NTLOAD((const u32x4*)(q + (size_t)row * D + (i * 8 + sub) * 8));
    if (sub < 7) ((unsigned long long*)(ws + OFF_SG))[sub * S + row] = sub < 3 ? 0ull : ~0ull;
    float s = 0.f, s2 = 0.f;
#pragma unroll
    for (int i = 0; i < 7; ++i)
#pragma unroll
      for (int j = 0; j < 4; ++j) {
        const u32 t = x[i][j];
        s = dot2(t, 0x3f803f80u, s);
        s2 = dot2(t, t, s2);
      }
    s = sum8(s);
    s2 = sum8(s2);
    const float mean = s * (1.f / D);
    float var = s2 * (1.f / D) - mean * mean;
    var = var < 0.f ? 0.f : var;
    const float rstd = __builtin_amdgcn_rsqf(var + 1e-5f);
    if (sub < 2) {
      const int p = (row & ~7) | ((row & 1) << 2) | ((row >> 1) & 3);
      ((float*)(ws + OFF_MEAN))[sub * S + p] = sub ? rstd : mean;
    }
  } else if (misc < 106) {
    const int bb = misc - 64;
    const int co = lane & 7, rs = lane >> 3;
    const int col0 = bb * 64 + co * 8;
    u32x4 x[14];
#pragma unroll
    for (int i = 0; i < 14; ++i)
      x[i] = NTLOAD((const u32x4*)(w_mod + (size_t)(w * 112 + rs * 14 + i) * (6 * D) + col0));
    u32 cv[7];
#pragma unroll
    for (int i = 0; i < 7; ++i) cv[i] = *(const u32*)(c + w * 112 + rs * 14 + 2 * i);
    __builtin_amdgcn_sched_barrier(0);
    f32x2 acc[4] = {{0.f, 0.f}, {0.f, 0.f}, {0.f, 0.f}, {0.f, 0.f}};
#pragma unroll
    for (int i = 0; i < 14; ++i) {
      const float cf = (i & 1) ? hi_f(cv[i >> 1]) : lo_f(cv[i >> 1]);
      const float sl = round_bf16(cf * __builtin_amdgcn_rcpf(1.f + __builtin_amdgcn_exp2f(cf * -1.44269504f)));
#pragma unroll
      for (int j = 0; j < 4; ++j)
        acc[j] = __builtin_elementwise_fma(f32x2{lo_f(x[i][j]), hi_f(x[i][j])}, f32x2{sl, sl}, acc[j]);
    }
    *(f32x4*)&red[w * 8 + rs][co * 8] = f32x4{acc[0][0], acc[0][1], acc[1][0], acc[1][1]};
    *(f32x4*)&red[w * 8 + rs][co * 8 + 4] = f32x4{acc[2][0], acc[2][1], acc[3][0], acc[3][1]};
    __syncthreads();
    {
      float t = 0.f;
#pragma unroll
      for (int i = 0; i < 8; ++i) t += red[w * 8 + i][lane];
      red[32 + w][lane] = t;
    }
    __syncthreads();
    if (tid < 64) {
      const float t = (red[32][tid] + red[33][tid]) + (red[34][tid] + red[35][tid]);
      const float m = round_bf16(t);
      const int col = bb * 64 + tid;
      ((float*)(ws + OFF_MOD))[col] = m;
      if (col >= 3 * D && col < 5 * D) {
        const bool isc = col >= 4 * D;
        ((u16*)(ws + OFF_CB))[isc ? col - 4 * D : col - 2 * D] = (u16)pack_bf16(isc ? 1.f + m : m, 0.f);
      }
    }
  }
  asm volatile("" ::"v"(cwarm));
  if (tid == 0) { ((unsigned long long*)(ws + WS_END))[b] = ts0; ((unsigned long long*)(ws + WS_END))[512 + b] = __builtin_amdgcn_s_memrealtime(); }
  STAMP(ws, 0, 1);
  STAMP_FLUSH(ws, misc < 0 ? 0 : (misc < 64 ? 4 : 5));
}

constexpr int K1_KSTR = 260 * 16;
constexpr int K1_USTR = 286 * 16;
constexpr int K1_UOFF = 4 * K1_KSTR;
constexpr int K1_XOFF = K1_UOFF + 2 * K1_USTR;
constexpr int K1_CH = K1_XOFF + 32768;

DI void glds16x4_asm(const void* sbase, u32 voff, u32 voff_last, u32 lds_addr) {
  u32 keep;
  sbase = uni(sbase);
  lds_addr = __builtin_amdgcn_readfirstlane(lds_addr);
  asm volatile(
      "s_mov_b32 %0, m0\n\ts_mov_b32 m0, %4\n\t" SNOP
      "global_load_lds_dwordx4 %1, %3\n\t"
      "global_load_lds_dwordx4 %1, %3 offset:1024\n\t"
      "global_load_lds_dwordx4 %1, %3 offset:2048\n\t"
      "global_load_lds_dwordx4 %2, %3 offset:3072\n\t"
      "s_mov_b32 m0, %0"
      : "=&s"(keep)
      : "v"(voff), "v"(voff_last), "s"(sbase), "s"(lds_addr)
      : "memory");
}
#define CAS __attribute__((address_space(4)))
constexpr int K2_XB = D * 256;
constexpr int K2_XH = D * 128;
constexpr int K2_ST = K2_XB;
constexpr int K2_CB = 131072;
constexpr int K2_LDS = K2_CB + 2048;

DI void gload16_asm(u32x4& dst, const void* sbase, u32 voff) {
  sbase = uni(sbase);
  asm volatile(SNOP2 "global_load_dwordx4 %0, %1, %2" : "=v"(dst) : "v"(voff), "s"(sbase) : "memory");
}
DI u32 umin32(u32 v) { return v < 0xFFFFFFFEu ? v : 0xFFFFFFFEu; }
DI u32 umax3(u32 a, u32 b, u32 c) { const u32 t = a > b ? a : b; return t > c ? t : c; }
template <int OFF>
DI void gload16_off(u32x4& dst, const void* sbase, u32 voff) {
  sbase = uni(sbase);
  asm volatile(SNOP2 "global_load_dwordx4 %0, %1, %2 offset:%3" : "=v"(dst) : "v"(voff), "s"(sbase), "n"(OFF) : "memory");
}
DI void glds16_nom0(const void* sbase, u32 voff, u32 lds_addr) {
  sbase = uni(sbase);
  lds_addr = __builtin_amdgcn_readfirstlane(lds_addr);
  asm volatile("s_mov_b32 m0, %2\n\t" SNOP "global_load_lds_dwordx4 %0, %1" ::"v"(voff), "s"(sbase), "s"(lds_addr) : "memory");
}
DI void glds16_sc1(const void* sbase, u32 voff, u32 lds_addr) {
  sbase = uni(sbase);
  lds_addr = __builtin_amdgcn_readfirstlane(lds_addr);
  asm volatile("s_mov_b32 m0, %2\n\t" SNOP "global_load_lds_dwordx4 %0, %1 sc1" ::"v"(voff), "s"(sbase), "s"(lds_addr) : "memory");
}
template <int N>
DI void wait_vm_pin(u32x4& a, u32x4& b) {
  asm volatile("s_waitcnt vmcnt(%2)" : "+v"(a), "+v"(b) : "n"(N) : "memory");
}
DI float xor32(float v) {
  const u32 u = __builtin_bit_cast(u32, v);
  auto r = __builtin_amdgcn_permlane32_swap(u, u, false, false);
  const u32 a = r[0], b = r[1];
  return __builtin_bit_cast(float, (threadIdx.x & 32) ? a : b);
}

typedef unsigned long long u64;
typedef __attribute__((address_space(1))) u64 gu64;
typedef __attribute__((address_space(1))) u32 gu32;
#define RLX_AGENT __ATOMIC_RELAXED, __HIP_MEMORY_SCOPE_AGENT
#define EXP_WE 8
#define EXP_NW0 5
#define EXP_NWH 15
#define EXP_NW2X (EXP_NWH - EXP_NW0 - EXP_NW1)
#define EXP_NW1 5
constexpr int NWC = EXP_NW0 + EXP_NW1;
template <bool WF>
DI void conv_body(const u16* __restrict__ ssm_k, const u16* __restrict__ ssm_D, char* __restrict__ ws, char* smem,
                  const int w, u32x4 (&wf)[28], const char* wbase, Stamps2& s2) {
  STAMP(ws, 1, 0);
  const int lane = threadIdx.x & 63;
  const int ch = w >> 2, q = w & 3;
  const int cpair = (blockIdx.x & 7) * 32 + (blockIdx.x >> 3);
  const int d = 2 * cpair + ch;
  char* L = smem + ch * K1_CH;
  const u32 L0 = (u32)(size_t)(LAS char*)L;
  const u16* kf = ssm_k + (size_t)d * S;
  const u32 vo = lane * 16;
  {
    const u32 vl = (lane > 62 ? 62 : lane) * 16;
    glds16x4_asm(kf + (2 + 2 * q), vo, vl, L0 + q * K1_KSTR + 64);
    const char* ub = ws + ((q >> 1) ? OFF_RO : OFF_RE) + (size_t)d * S * 2 + (q & 1) * 2048;
    const u32 ul = L0 + K1_UOFF + (q >> 1) * K1_USTR + (q & 1) * 2048;
    glds16_nom0(ub, vo, ul);
    glds16_nom0(ub + 1024, vo, ul + 1024);
  }
  const int n = lane & 15, q4 = lane >> 4;
  const int pbase = 32 * (n >> 1) + 8 * q4 + 4 * (n & 1) + 512 * q;
  const int di = n + q - 15;
  u32 head;
  asm volatile(SNOP2 "global_load_dword %0, %1, %2" : "=v"(head) : "v"((u32)(di < 0 ? 0 : di) * 4), "s"(uni(kf)) : "memory");
  auto ldwf = [&](auto kI) {
    constexpr int k = decltype(kI)::value;
    gload16_off<(k % 4) * 1024>(wf[k], wbase + (k / 4) * 4096, vo);
  };
  bar();
  if constexpr (WF) static_for<0, EXP_NW0>(ldwf);
  const CAS float* MODc = (const CAS float*)(size_t)(ws + OFF_MOD);
  const float sa = MODc[d], ca = MODc[D + d], ga = MODc[2 * D + d], cbv = MODc[4 * D + d];
  const u32 Draw = ((const CAS u32*)(size_t)ssm_D)[cpair];
  if (lane < 14) *(LAS u32x4*)(L + K1_UOFF + (q >> 1) * K1_USTR + (256 + 14 * (q & 1) + lane) * 16) = u32x4{0u, 0u, 0u, 0u};
  u32 Ab = L0 + (n & 3) * K1_KSTR + ((n >> 2) + q4 + 8 * q) * 16;
  u32 Bb = L0 + K1_UOFF + (n & 1) * K1_USTR + (28 - 4 * (n >> 1) + q4 + 8 * q) * 16;
  u32 Hd = L0 + q * K1_KSTR + n * 4;
  asm volatile("" : "+v"(Ab), "+v"(Bb), "+v"(Hd) : "s"(sa), "s"(ca), "s"(ga), "s"(Draw), "s"(cbv));
  asm volatile("s_waitcnt vmcnt(%1)" : "+v"(head) : "n"(WF ? EXP_NW0 : 0) : "memory");
  *(LAS u32*)(size_t)Hd = di < 0 ? 0u : head;
  wait_lgkm0();
  STAMP(ws, 1, 1);
  bar();
  bf16x8 fa[8], fb[8];
  auto rd = [&](int dl) {
#pragma unroll
    for (int g = 0; g < 8; ++g) {
      fa[g] = *(const LAS bf16x8*)(size_t)(Ab + 64 * dl + 512 * g);
      fb[g] = *(const LAS bf16x8*)(size_t)(Bb + 64 * dl + 512 * (7 - g));
    }
  };
  rd(0);
  __builtin_amdgcn_sched_barrier(0);
  u32x2 qv[2];
  f32x4 mu[2], rs[2];
  asm volatile(SNOP2
               "global_load_dwordx2 %0, %6, %8\n\t"
               "global_load_dwordx2 %1, %6, %8 offset:512\n\t"
               "global_load_dwordx4 %2, %7, %9\n\t"
               "global_load_dwordx4 %3, %7, %9 offset:1024\n\t"
               "global_load_dwordx4 %4, %7, %10\n\t"
               "global_load_dwordx4 %5, %7, %10 offset:1024"
               : "=&v"(qv[0]), "=&v"(qv[1]), "=&v"(mu[0]), "=&v"(mu[1]), "=&v"(rs[0]), "=&v"(rs[1])
               : "v"((u32)pbase * 2), "v"((u32)pbase * 4), "s"(uni(ws + OFF_QT + (size_t)d * S * 2)),
                 "s"(uni(ws + OFF_MEAN)), "s"(uni(ws + OFF_RSTD))
               : "memory");
  const u32x4 sent = {~0u, ~0u, ~0u, ~0u};
  const void* fillb = uni(ws + OFF_ACT + (size_t)(cpair * 8 + w) * 4096);
  f32x4 acc[8];
#pragma unroll
  for (int j = 0; j < 8; ++j) acc[j] = f32x4{0.f, 0.f, 0.f, 0.f};
  static_for<0, 72>([&](auto mI) {
    constexpr int m = decltype(mI)::value, t = m % 36;
    constexpr int sd = t < 1 ? 0 : t < 3 ? 1 : t < 6 ? 2 : t < 10 ? 3 : t < 15 ? 4 : t < 21 ? 5 : t < 28 ? 6 : 7;
    constexpr int g = t - sd * (sd + 1) / 2;
    if constexpr (m == 36) {
      rd(1);
      __builtin_amdgcn_sched_barrier(0);
    }
    f32x4 a = __builtin_amdgcn_mfma_f32_16x16x32_bf16(fa[g], fb[sd - g], acc[sd], 0, 0, 0);
    asm volatile("" : "+v"(a));
    acc[sd] = a;
    if constexpr (m % 4 == 3) {
      constexpr int v = m / 4;
      if constexpr (v < 4) {
        u32x4 sv = sent;
        asm volatile("global_store_dwordx4 %0, %1, %2 offset:%3 sc1\n\ts_nop 1" ::"v"(vo), "v"(sv), "s"(fillb), "n"(v * 1024) : "memory");
      } else if constexpr (WF && v - 4 < EXP_NW1) {
        ldwf(std::integral_constant<int, EXP_NW0 + v - 4>{});
      }
      __builtin_amdgcn_sched_barrier(0);
    }
  });
  STAMP(ws, 1, 3);
  LAS f32x4* X = (LAS f32x4*)(L + K1_XOFF);
#pragma unroll
  for (int j = 0; j < 8; ++j) X[(j * 4 + q) * 64 + lane] = acc[j];
  u32x4 uc[2];
#pragma unroll
  for (int j = 0; j < 2; ++j)
    uc[j] = *(const LAS u32x4*)(L + K1_UOFF + K1_USTR + (255 - (32 * (2 * q + j) + 4 * (n >> 1) + q4)) * 16);
  asm volatile("s_waitcnt vmcnt(%6)"
               : "+v"(qv[0]), "+v"(qv[1]), "+v"(mu[0]), "+v"(mu[1]), "+v"(rs[0]), "+v"(rs[1])
               : "n"(WF ? EXP_NW1 : 0)
               : "memory");
  f32x4 qf[2], hg[2], kd[2];
  {
    const int sh = (n & 1) ? 16 : 0;
    const float gc = ga * round_bf16(1.f + ca), gs = ga * sa, Dd = ch ? hi_f(Draw) : lo_f(Draw);
#pragma unroll
    for (int j = 0; j < 2; ++j) {
#pragma unroll
      for (int r = 0; r < 4; ++r) {
        kd[j][r] = __builtin_bit_cast(float, (uc[j][3 - r] << sh) & 0xffff0000u) * Dd;
        qf[j][r] = (r & 1) ? hi_f(qv[j][r >> 1]) : lo_f(qv[j][r >> 1]);
        hg[j][r] = (qf[j][r] - mu[j][r]) * rs[j][r] * gc + gs;
      }
      asm volatile("" : "+v"(kd[j]), "+v"(qf[j]), "+v"(hg[j]));
    }
  }
  wait_lgkm0();
  bar();
  STAMP(ws, 1, 4);
  u16* X1T = (u16*)(ws + OFF_X1T) + (size_t)d * S + pbase;
  u16* XS = (u16*)(ws + OFF_XS) + (size_t)d * S + pbase;
  const float c1 = round_bf16(1.f + cbv);
#pragma unroll
  for (int j = 0; j < 2; ++j) {
    const LAS f32x4* Xj = X + ((2 * q + j) * 4) * 64 + lane;
    const f32x4 y = (Xj[0] + Xj[64]) + (Xj[128] + Xj[192]);
    const f32x4 o = qf[j] + hg[j] * (y + kd[j]);
    const u32 p0 = umin32(pack_bf16(o[0], o[1])), p1 = umin32(pack_bf16(o[2], o[3]));
    st8_sc1(XS + 256 * j,
            u32x2{umin32(pack_bf16(lo_f(p0) * c1, hi_f(p0) * c1)), umin32(pack_bf16(lo_f(p1) * c1, hi_f(p1) * c1))});
    st8_sc1(X1T + 256 * j, u32x2{p0, p1});
  }
  STAMP(ws, 1, 5);
}

DI void k2_stats(char* __restrict__ ws, char* smem, const int sblk) {
  const int tid = threadIdx.x, lane = tid & 63;
  const int w = __builtin_amdgcn_readfirstlane(tid >> 6);
  const int n = lane & 31, h = lane >> 5;
  STAMP_DECL;
  STAMP(ws, 2, 0);
  {
    const int p0 = sblk * 64;
    const rsrc_t rx = mk_rsrc(ws + OFF_X1T + ((size_t)(w * 56) * S + p0) * 2, 56 * S * 2);
    u32 x[28];
    for (int spin = 0; spin < (1 << 14); ++spin) {
      asm volatile("" ::: "memory");
      u32 m = 0;
#pragma unroll
      for (int i = 0; i < 28; ++i) x[i] = __builtin_amdgcn_raw_buffer_load_b32(rx, (h * S) * 2 + n * 4 + i * (4 * S), 0, 16);
#pragma unroll
      for (int i = 0; i < 28; i += 2) m = umax3(m, x[i], x[i + 1]);
      if (__all(m != ~0u)) break;
      __builtin_amdgcn_s_sleep(4);
    }
    __syncthreads();
    float s0 = 0.f, s1 = 0.f, q0 = 0.f, q1 = 0.f;
#pragma unroll
    for (int i = 0; i < 28; ++i) {
      const float a = lo_f(x[i]), b = hi_f(x[i]);
      s0 += a;
      s1 += b;
      q0 = __builtin_fmaf(a, a, q0);
      q1 = __builtin_fmaf(b, b, q1);
    }
    *(LAS f32x4*)(smem + ((w * 2 + h) * 32 + n) * 16) = f32x4{s0, q0, s1, q1};
    __syncthreads();
    if (tid < 64) {
      float sm = 0.f, sq = 0.f;
#pragma unroll
      for (int i = 0; i < 16; ++i) {
        const f32x2 t = *(const LAS f32x2*)(smem + (i * 64 + tid) * 8);
        sm += t[0];
        sq += t[1];
      }
      const float mean = sm * (1.f / D);
      float var = sq * (1.f / D) - mean * mean;
      var = (var < 0.f ? 0.f : var) + 1e-5f;
      const float rstd = 1.f / sqrtf(var);
      const float sig = var * rstd;
      const float mh = round_bf16(mean), sh = round_bf16(sig);
      const u32 b0 = pack_bf16(-mh, mh - mean), b1 = pack_bf16(-mh, sh), b2 = pack_bf16(sig - sh, sh);
      const float c1 = -2.302208198f * rstd;
      const float c2 = -0.1029432397f * (rstd * rstd * rstd);
      gu64* G = (gu64*)(size_t)(ws + OFF_SG) + p0 + tid;
      __hip_atomic_store(G, ((u64)b1 << 32) | b0, RLX_AGENT);
      __hip_atomic_store(G + S, ((u64)__builtin_bit_cast(u32, var) << 32) | b2, RLX_AGENT);
      __hip_atomic_store(G + 2 * S, ((u64)__builtin_bit_cast(u32, c1) << 32) | __builtin_bit_cast(u32, c2), RLX_AGENT);
    }
    STAMP(ws, 6, 1);
    STAMP(ws, 6, 2);
    STAMP_FLUSH(ws, 6);
  }
}

DI void k2_gemm(const u16* __restrict__ ssm_k, const u16* __restrict__ ssm_D, char* __restrict__ ws, u16* __restrict__ out,
                char* smem, const int w) {
  const int tid = threadIdx.x, lane = tid & 63;
  const int n = lane & 31, h = lane >> 5;
  const int xcd = blockIdx.x & 7, idx = blockIdx.x >> 3;
  const int mt = (xcd >> 1) * 4 + (idx & 3);
  const int dt = idx >> 2, sh2 = xcd & 1;
  const int ft = sh2 * 7 + dt;
  const int tile = ft * 8 + w;
  const u32 lds0 = (u32)(size_t)(LAS char*)smem;
  u32x4 wf[28], wd[28];
  const char* wbase = ws + OFF_WGU + (size_t)(tile * 28) * 1024;
  const u32 wa = lane * 16;
  auto ldwf = [&](auto kI) {
    constexpr int k = decltype(kI)::value;
    gload16_off<(k % 4) * 1024>(wf[k], wbase + (k / 4) * 4096, wa);
  };
  Stamps2 s2 = {};
  if (xcd < 7) {
    conv_body<true>(ssm_k, ssm_D, ws, smem, w, wf, wbase, s2);
  } else {
    const int task = idx * 8 + w, t = task >> 1, kh = task & 1;
    const u32x4* wsrc = (const u32x4*)(ws + OFF_WGU) + ((size_t)t * 28 + kh * 14) * 64 + lane;
    const u16* cbp = (const u16*)(ws + OFF_CB) + (n & 1) * D + kh * 224 + 8 * h;
    u32x4 fa[14], fb[14];
#pragma unroll
    for (int i = 0; i < 14; ++i) {
      fa[i] = wsrc[i * 64];
      fb[i] = *(const u32x4*)(cbp + 16 * i);
    }
    __builtin_amdgcn_sched_barrier(0);
    f32x16 va;
#pragma unroll
    for (int r = 0; r < 16; ++r) va[r] = 0.f;
#pragma unroll
    for (int i = 0; i < 14; ++i) va = mfma32(fb[i], __builtin_bit_cast(bf16x8, fa[i]), va);
    if (h == 0) {
      const float f0 = va[0], f1 = va[1];
      const u32 a0 = umin32(__builtin_bit_cast(u32, f0)), a1 = __builtin_bit_cast(u32, f1);
      __hip_atomic_store((gu64*)(size_t)(ws + OFF_VG) + kh * 3584 + t * 32 + n, ((u64)a1 << 32) | a0, RLX_AGENT);
    }
    __builtin_amdgcn_sched_barrier(0);
    static_for<0, NWC>(ldwf);
    wait_vm<0>();
  }
  STAMP_DECL;
  STAMP(ws, 2, 0);
  const char* xbase = ws + OFF_XS + ((size_t)(8 * w) * S + mt * 128) * 2;
  const u32 xoff = (u32)((lane >> 3) * S + ((lane & 7) ^ (((lane >> 4) & 1) << 2)) * 8) * 2;
  const char* wdbase = ws + OFF_WD + ((size_t)(2 * dt) * 112 + 14 * w) * 1024 + 4096;
  const int gsl = w < 6 ? w : w - 6;
  const char* gbase = ws + OFF_SG + (size_t)((gsl >> 1) * S + mt * 128 + (gsl & 1) * 64) * 8;
  auto ldwd = [&](auto kI) {
    constexpr int q = decltype(kI)::value, k = (q & 1) * 14 + (q >> 1), kk = k % 14;
    gload16_off<(kk % 8) * 1024 - 4096>(wd[k], wdbase + (k / 14) * 112 * 1024 + (kk / 8) * 8192, wa);
  };
  auto ldx = [&](auto cI) {
    constexpr int c = decltype(cI)::value;
    glds16_sc1(xbase + (size_t)(c % 7) * (64 * S * 2) + (c / 7) * 128, xoff, lds0 + (64 * c + 8 * w) * 128);
  };
  auto xok = [&](auto c0I, auto c1I) {
    u32 m = 0;
#pragma unroll
    for (int c = decltype(c0I)::value; c < decltype(c1I)::value; ++c) {
      const u32x4 t = *(const LAS u32x4*)(smem + (64 * c + 8 * w) * 128 + lane * 16);
      m = umax3(umax3(m, t[0], t[1]), t[2], t[3]);
    }
    return (bool)__all(m != ~0u);
  };
  auto xvalid = [&](auto c0I, auto c1I) {
    if (__builtin_expect(!xok(c0I, c1I), 0)) {
#pragma nounroll
      for (int spin = 0; spin < (1 << 14); ++spin) {
        __builtin_amdgcn_s_sleep(4);
        static_for<decltype(c0I)::value, decltype(c1I)::value>(ldx);
        wait_vm<0>();
        if (xok(c0I, c1I)) break;
      }
    }
    __builtin_amdgcn_sched_barrier(0);
  };
  wait_lgkm0();
  bar();
  XSTAMP(1);
  auto pin = [](f32x16& a) { asm volatile("" : "+v"(a)); };
  auto pinw = [&](auto kI) {
    u32x4 t = wf[decltype(kI)::value];
    asm volatile("" : "+v"(t)::"memory");
    wf[decltype(kI)::value] = t;
  };
  u32x4 aext;
  constexpr int NWH = NWC + EXP_NW2X;
  static_for<NWC, NWH>(ldwf);
  static_for<0, 7>(ldx);
  int xo[2][2];
  {
    const int blk = (lane >> 4) & 1, qq = (lane & 15) >> 2, p = lane & 3;
#pragma unroll
    for (int i = 0; i < 2; ++i)
#pragma unroll
      for (int t = 0; t < 2; ++t)
        xo[i][t] = 128 * (8 * h + 4 * t + qq) + 16 * ((4 * i + 2 * blk + (p >> 1)) ^ ((qq >> 1) << 2)) + 8 * (p & 1);
  }
  f32x16 acc[4];
#pragma unroll
  for (int i = 0; i < 4; ++i)
#pragma unroll
    for (int r = 0; r < 16; ++r) acc[i][r] = 0.f;
  asm volatile("" : "+v"(xo[0][0]), "+v"(xo[0][1]), "+v"(xo[1][0]), "+v"(xo[1][1]));
  constexpr int PF = 3;
  bf16x8 xr[PF][2];
  auto rdx = [&](auto kI, auto iI) {
    constexpr int k = decltype(kI)::value, i = decltype(iI)::value;
    if constexpr (k == 28) {
      int a0 = xo[i][0] + K2_XH, a1 = xo[i][1] + K2_XH;
      asm volatile("" : "+v"(a0), "+v"(a1));
      xo[i][0] = a0;
      xo[i][1] = a1;
    }
    xr[k % PF][i] = tr_frag(smem, xo[i][0] + (k % 28) * 2048, xo[i][1] + (k % 28) * 2048);
  };
  using I0 = std::integral_constant<int, 0>;
  using I1 = std::integral_constant<int, 1>;
  using I2 = std::integral_constant<int, 2>;
  using I3 = std::integral_constant<int, 3>;
  using I7 = std::integral_constant<int, 7>;
  using I14 = std::integral_constant<int, 14>;
  LAS u64* CS = (LAS u64*)(smem + K2_ST);
  XSTAMP(4);
  wait_vm<0>();
  static_for<0, NWH>(pinw);
  xvalid(I0{}, I7{});
  XSTAMP(5);
  bar();
  YSTAMP(1);
  static_for<0, PF - 1>([&](auto jI) {
    rdx(jI, I0{});
    rdx(jI, I1{});
  });
  __builtin_amdgcn_sched_barrier(0);
  u64 gv, gp0, gp1;
  const char* vbase = ws + OFF_VG + (size_t)tile * 256;
  constexpr int RW = 28 - NWH, KX = RW + 7, KV = 25;
  static_assert(KX + 4 < KV && 27 - KX <= 9, "stream does not fit");
  static_for<0, 28>([&](auto kI) {
    constexpr int k = decltype(kI)::value;
    if constexpr (k >= NWH) {
      constexpr int issued = k + (k > KX ? 2 : 0);
      u32x4 t = wf[k];
      asm volatile("s_waitcnt vmcnt(%1)" : "+v"(t) : "n"(issued - 1 - (k - NWH)) : "memory");
      wf[k] = t;
    }
    if constexpr (k == KV) {
      u64 t = gv, p0 = gp0, p1 = gp1;
      asm volatile("s_waitcnt vmcnt(%3)" : "+v"(t), "+v"(p0), "+v"(p1) : "n"(KV - KX - 1) : "memory");
      xvalid(I7{}, I14{});
      if (!__all((t >> 32) != 0 && (u32)p0 != ~0u && (u32)p1 != ~0u)) {
        for (int spin = 0; spin < (1 << 16); ++spin) {
          __builtin_amdgcn_s_sleep(2);
          t = __hip_atomic_load((gu64*)(size_t)gbase + lane, RLX_AGENT);
          p0 = __hip_atomic_load((gu64*)(size_t)vbase + n, RLX_AGENT);
          p1 = __hip_atomic_load((gu64*)(size_t)vbase + 3584 + n, RLX_AGENT);
          if (__all((t >> 32) != 0 && (u32)p0 != ~0u && (u32)p1 != ~0u)) break;
        }
        __builtin_amdgcn_s_waitcnt(0x0F70);
      }
      CS[gsl * 64 + lane] = t;
      const float v1 = __builtin_bit_cast(float, (u32)p0) + __builtin_bit_cast(float, (u32)p1);
      const float v2 = __builtin_bit_cast(float, (u32)(p0 >> 32)) + __builtin_bit_cast(float, (u32)(p1 >> 32));
      const float v1h = round_bf16(v1), v2h = round_bf16(v2);
      u32x4 a = {pack_bf16(v1h, v1h), pack_bf16(v1 - v1h, v2h), pack_bf16(v2h, v2 - v2h), 0u};
      if (h) a = u32x4{0u, 0u, 0u, 0u};
      asm volatile("" : "+v"(a));
      aext = a;
      wait_lgkm0();
      __builtin_amdgcn_sched_barrier(0);
      bar();
    }
    __builtin_amdgcn_sched_barrier(0);
    acc[0] = mfma32(wf[k], xr[k % PF][0], acc[0]);
    pin(acc[0]);
    __builtin_amdgcn_sched_barrier(0);
    rdx(std::integral_constant<int, k + PF - 1>{}, I0{});
    __builtin_amdgcn_sched_barrier(0);
    acc[1] = mfma32(wf[k], xr[k % PF][1], acc[1]);
    pin(acc[1]);
    __builtin_amdgcn_sched_barrier(0);
    rdx(std::integral_constant<int, k + PF - 1>{}, I1{});
    if constexpr (k < RW) ldwf(std::integral_constant<int, NWH + k>{});
    else if constexpr (k < KX) ldx(std::integral_constant<int, 7 + k - RW>{});
    else if constexpr (k == KX) {
      u64 t, t0, t1;
      asm volatile(SNOP2 "global_load_dwordx2 %0, %1, %2 sc1" : "=v"(t) : "v"((u32)lane * 8), "s"(uni(gbase)) : "memory");
      asm volatile(SNOP2 "global_load_dwordx2 %0, %1, %2 sc1" : "=v"(t0) : "v"((u32)n * 8), "s"(uni(vbase)) : "memory");
      asm volatile(SNOP2 "global_load_dwordx2 %0, %1, %2 sc1" : "=v"(t1) : "v"((u32)n * 8), "s"(uni(vbase + 3584 * 8)) : "memory");
      gv = t;
      gp0 = t0;
      gp1 = t1;
    } else {
      ldwd(std::integral_constant<int, k - KX - 1>{});
    }
    __builtin_amdgcn_sched_barrier(0);
  });
  constexpr int WE = 27 - KX;
  YSTAMP(2);
  const rsrc_t rw = mk_rsrc(ws + OFF_ACT + ((size_t)(mt * 4) * 112 + tile) * 1024, 337 * 1024);
  const rsrc_t ra = mk_rsrc(ws + OFF_ACT + ((size_t)(mt * 4 + sh2) * 112 + 14 * w) * 1024, 238 * 1024);
  u32x4 af[28];
  auto ldact = [&](auto kI) {
    constexpr int k = decltype(kI)::value;
    af[k] = __builtin_amdgcn_raw_buffer_load_b128(ra, lane * 16 + (k % 14) * 1024, (k / 14) * 224 * 1024, 16);
  };
  float c1[2], c2[2], var[2], pe[2], pm[2], pr[2];
  u32x4 ov[2];
  auto corr = [&](auto iI) {
    constexpr int i = decltype(iI)::value;
    u64 g[3];
#pragma unroll
    for (int k = 0; k < 3; ++k) g[k] = CS[k * 128 + i * 32 + n];
    const u32x4 bext = {(u32)g[0], (u32)(g[0] >> 32), (u32)g[1], 0u};
    acc[i] = mfma32(aext, __builtin_bit_cast(bf16x8, bext), acc[i]);
    c1[i & 1] = __builtin_bit_cast(float, (u32)(g[2] >> 32));
    c2[i & 1] = __builtin_bit_cast(float, (u32)g[2]);
    var[i & 1] = __builtin_bit_cast(float, (u32)(g[1] >> 32));
  };
  auto slice = [&](auto iI, auto uI) {
    constexpr int i = decltype(iI)::value, u = decltype(uI)::value, z = i & 1;
    if constexpr (u >= 1) {
      float o = pm[z] * __builtin_amdgcn_rcpf(__builtin_fmaf(pe[z], var[z], var[z]));
      if constexpr ((u - 1) & 1) {
        u32 t = umin32(pack_bf16(pr[z], o));
        asm volatile("" : "+v"(t));
        ov[z][(u - 1) / 2] = t;
      } else {
        asm volatile("" : "+v"(o));
        pr[z] = o;
      }
    }
    if constexpr (u < 8) {
      const float a = acc[i][u];
      float e = __builtin_amdgcn_exp2f(__builtin_fmaf(a * a, c2[z], c1[z]) * a), m = a * acc[i][u + 8];
      asm volatile("" : "+v"(e), "+v"(m));
      pe[z] = e;
      pm[z] = m;
    } else {
      __builtin_amdgcn_raw_buffer_store_b128(ov[z], rw, lane * 16 + i * 112 * 1024, 0, 16);
      asm volatile("s_nop 1");
    }
  };
  corr(I0{});
  corr(I1{});
  __builtin_amdgcn_sched_barrier(0);
  static_for<0, 28>([&](auto kI) {
    constexpr int k = decltype(kI)::value;
    acc[2] = mfma32(wf[k], xr[(28 + k) % PF][0], acc[2]);
    pin(acc[2]);
    __builtin_amdgcn_sched_barrier(0);
    if constexpr (k + PF - 1 < 28) rdx(std::integral_constant<int, 28 + k + PF - 1>{}, I0{});
    if constexpr (k >= 1 && k <= 9) slice(I0{}, std::integral_constant<int, k - 1>{});
    __builtin_amdgcn_sched_barrier(0);
    acc[3] = mfma32(wf[k], xr[(28 + k) % PF][1], acc[3]);
    pin(acc[3]);
    __builtin_amdgcn_sched_barrier(0);
    if constexpr (k % EXP_LOCK == EXP_LOCK - 1) bar();
    if constexpr (k + PF - 1 < 28) rdx(std::integral_constant<int, 28 + k + PF - 1>{}, I1{});
    if constexpr (k >= 1 && k <= 9) slice(I1{}, std::integral_constant<int, k - 1>{});
    if constexpr (WE + k < 28) ldwd(std::integral_constant<int, WE + k>{});
    else if constexpr (WE + k - 28 < 7) ldact(std::integral_constant<int, WE + k - 28>{});
    __builtin_amdgcn_sched_barrier(0);
  });
  YSTAMP(3);
  constexpr int A0 = WE < 7 ? WE : 7;
  corr(I2{});
  corr(I3{});
  __builtin_amdgcn_sched_barrier(0);
  static_for<0, 9>([&](auto uI) {
    constexpr int u = decltype(uI)::value;
    slice(I2{}, uI);
    slice(I3{}, uI);
    if constexpr (A0 + u < 14 && u < 8) ldact(std::integral_constant<int, A0 + u>{});
    __builtin_amdgcn_sched_barrier(0);
  });
  static_for<(A0 + 8 < 14 ? A0 + 8 : 14), 14>(ldact);
  bar();
  YSTAMP(4);
  auto chk = [&](auto gI) {
    constexpr int g = decltype(gI)::value;
    u32 m = 0;
#pragma unroll
    for (int k = 7 * g; k < 7 * g + 7; ++k) m = umax3(umax3(m, af[k][0], af[k][1]), af[k][2], af[k][3]);
    return (bool)__all(m != ~0u);
  };
  auto valid = [&](auto gI) {
    constexpr int g = decltype(gI)::value;
    if (__builtin_expect(!chk(gI), 0)) {
#pragma nounroll
      for (int spin = 0; spin < (1 << 14); ++spin) {
        __builtin_amdgcn_s_sleep(8);
        asm volatile("" ::: "memory");
        static_for<7 * g, 7 * g + 7>(ldact);
        if (chk(gI)) break;
      }
      __builtin_amdgcn_s_waitcnt(0x0F70);
    }
    __builtin_amdgcn_sched_barrier(0);
  };
  const int pcol = mt * 128 + sh2 * 32 + n;
  const int dbase = dt * 64 + w * 8 + 4 * h;
  __builtin_amdgcn_sched_barrier(0);
  LAS f32x4* R = (LAS f32x4*)smem;
  f32x16 o[4];
#pragma unroll
  for (int i = 0; i < 4; ++i)
#pragma unroll
    for (int r = 0; r < 16; ++r) o[i][r] = 0.f;
  auto dump1 = [&](auto tjI, auto cI) {
    constexpr int tj = decltype(tjI)::value, c = decltype(cI)::value, i = tj * 2 + (c >> 2), q = c & 3;
    R[((tj * 8 + c) * 8 + w) * 64 + lane] = f32x4{o[i][4 * q], o[i][4 * q + 1], o[i][4 * q + 2], o[i][4 * q + 3]};
  };
  auto step = [&](auto kI) {
    constexpr int k = decltype(kI)::value, tj = k / 14;
    const bf16x8 b = __builtin_bit_cast(bf16x8, af[k]);
    o[2 * tj] = mfma32(wd[k % 14], b, o[2 * tj]);
    pin(o[2 * tj]);
    __builtin_amdgcn_sched_barrier(0);
    if constexpr (k >= 15 && k < 19) dump1(I0{}, std::integral_constant<int, 2 * (k - 15)>{});
    __builtin_amdgcn_sched_barrier(0);
    o[2 * tj + 1] = mfma32(wd[14 + k % 14], b, o[2 * tj + 1]);
    pin(o[2 * tj + 1]);
    __builtin_amdgcn_sched_barrier(0);
    if constexpr (k >= 15 && k < 19) dump1(I0{}, std::integral_constant<int, 2 * (k - 15) + 1>{});
    if constexpr (k >= EXP_T1 && k < 14) {
      constexpr int n0 = ((k - EXP_T1) * 14) / (14 - EXP_T1), n1 = ((k + 1 - EXP_T1) * 14) / (14 - EXP_T1);
      static_for<14 + n0, 14 + n1>(ldact);
    }
    __builtin_amdgcn_sched_barrier(0);
  };
  u16 xres[2][4];
  f32x4 gbv;
  auto fin = [&](auto tjI) {
    constexpr int tj = decltype(tjI)::value;
    f32x4 v[8];
#pragma unroll
    for (int sw = 0; sw < 8; ++sw) v[sw] = R[((tj * 8 + w) * 8 + sw) * 64 + lane];
    const f32x4 sum = ((v[0] + v[1]) + (v[2] + v[3])) + ((v[4] + v[5]) + (v[6] + v[7]));
    float r[4];
#pragma unroll
    for (int e = 0; e < 4; ++e) r[e] = __builtin_fmaf(gbv[e], sum[e], bf2f(xres[tj][e]));
    const int pc = pcol + 64 * tj;
    const int srow = (pc & ~7) | ((pc & 3) << 1) | ((pc >> 2) & 1);
    *(u32x2*)(out + (size_t)srow * D + dbase) = u32x2{pack_bf16(r[0], r[1]), pack_bf16(r[2], r[3])};
  };
  valid(I0{});
  asm volatile(""
               : "+v"(wd[0]), "+v"(wd[1]), "+v"(wd[2]), "+v"(wd[3]), "+v"(wd[4]), "+v"(wd[5]), "+v"(wd[6]), "+v"(wd[7]),
                 "+v"(wd[8]), "+v"(wd[9]), "+v"(wd[10]), "+v"(wd[11]), "+v"(wd[12]), "+v"(wd[13])::"memory");
  asm volatile(""
               : "+v"(wd[14]), "+v"(wd[15]), "+v"(wd[16]), "+v"(wd[17]), "+v"(wd[18]), "+v"(wd[19]), "+v"(wd[20]),
                 "+v"(wd[21]), "+v"(wd[22]), "+v"(wd[23]), "+v"(wd[24]), "+v"(wd[25]), "+v"(wd[26]), "+v"(wd[27])::"memory");
  YSTAMP(5);
  static_for<0, 3>(step);
  {
    const rsrc_t rx = mk_rsrc(ws + OFF_X1T, D * S * 2);
#pragma unroll
    for (int r = 0; r < 8; ++r)
      xres[r >> 2][r & 3] = __builtin_amdgcn_raw_buffer_load_b16(rx, ((dbase + (r & 3)) * S + pcol + (r >> 2) * 64) * 2, 0, 16);
    gbv = *(const f32x4*)((const float*)(ws + OFF_MOD) + 5 * D + dbase);
    __builtin_amdgcn_sched_barrier(0);
  }
  static_for<3, 7>(step);
  valid(I1{});
  static_for<7, 14>(step);
  valid(I2{});
  static_for<14, 21>(step);
  wait_lgkm0();
  bar();
  valid(I3{});
  static_for<21, 24>(step);
  fin(I0{});
  __builtin_amdgcn_sched_barrier(0);
  static_for<24, 28>(step);
  static_for<0, 8>([&](auto cI) { dump1(I1{}, cI); });
  wait_lgkm0();
  bar();
  STAMP(ws, 2, 6);
  fin(I1{});
}

extern "C" __global__ NOPK __launch_bounds__(512, 2) void k1_main(const u16* __restrict__ ssm_k,
                                                                  const u16* __restrict__ ssm_D, char* __restrict__ ws,
                                                                  u16* __restrict__ out) {
  extern __shared__ __attribute__((aligned(16))) char smem[];
  const int w = __builtin_amdgcn_readfirstlane(threadIdx.x >> 6);
  if (blockIdx.x >= 224) {
    if ((blockIdx.x & 7) < 7) {
      u32x4 dummy[28];
      Stamps2 s2 = {};
      conv_body<false>(ssm_k, ssm_D, ws, smem, w, dummy, nullptr, s2);
    } else {
      __builtin_amdgcn_s_sleep(100);
    }
    k2_stats(ws, smem, blockIdx.x - 224);
  } else {
    k2_gemm(ssm_k, ssm_D, ws, out, smem, w);
    if (blockIdx.x == 0 && threadIdx.x < 64) {
      const unsigned long long* E = (const unsigned long long*)(ws + WS_END);
      unsigned long long smin = ~0ull, smax = 0, emax = 0;
      for (int i = threadIdx.x; i < 504; i += 64) { smin = E[i] < smin ? E[i] : smin; smax = E[i] > smax ? E[i] : smax; emax = E[512 + i] > emax ? E[512 + i] : emax; }
      for (int o = 1; o < 64; o <<= 1) {
        unsigned long long a = __shfl_xor(smin, o), b2 = __shfl_xor(smax, o), c2 = __shfl_xor(emax, o);
        smin = a < smin ? a : smin; smax = b2 > smax ? b2 : smax; emax = c2 > emax ? c2 : emax;
      }
      const unsigned long long now = __builtin_amdgcn_s_memrealtime();
      unsigned long long d = 0 * (smax - smin);
      if (d > 20000) d = 20000;
      while (__builtin_amdgcn_s_memrealtime() - now < d) __builtin_amdgcn_s_sleep(1);
    }
  }
}

extern "C" __device__ __attribute__((used, noinline)) void k_code_end() {}

extern "C" void kernel_launch(void* const* d_in, const int* in_sizes, int n_in, void* d_out, int out_size, void* d_ws,
                              size_t ws_size, hipStream_t stream) {
  const u16* q = (const u16*)d_in[0];
  const u16* k = (const u16*)d_in[1];
  const u16* v = (const u16*)d_in[2];
  const u16* shift_k = (const u16*)d_in[3];
  const u16* ssm_k = (const u16*)d_in[4];
  const u16* ssm_D = (const u16*)d_in[5];
  const u16* w_gate = (const u16*)d_in[7];
  const u16* w_up = (const u16*)d_in[8];
  const u16* w_down = (const u16*)d_in[9];
  const u16* c = (const u16*)d_in[10];
  const u16* w_mod = (const u16*)d_in[11];
  char* ws = (char*)d_ws;
  (void)hipFuncSetAttribute((const void*)k1_main, hipFuncAttributeMaxDynamicSharedMemorySize, K2_LDS);
  k0_prep<<<K0_GRID, 256, 0, stream>>>(q, k, v, shift_k, c, w_mod, ssm_k, w_gate, w_up, w_down, ws);
  k1_main<<<256, 512, K2_LDS, stream>>>(ssm_k, ssm_D, ws, (u16*)d_out);
}
```

```cpp
#include <hip/hip_runtime.h>
#include <hip/hip_bf16.h>
#include <stdint.h>
#include <type_traits>

#define NTAUX 0
#define NTLOAD(p) (*(p))
typedef unsigned short u16;
typedef unsigned int u32;
typedef short bf16x8 __attribute__((ext_vector_type(8)));
typedef short s16x4 __attribute__((ext_vector_type(4)));
typedef float f32x2 __attribute__((ext_vector_type(2)));
typedef float f32x4 __attribute__((ext_vector_type(4)));
typedef float f32x16 __attribute__((ext_vector_type(16)));
typedef u32 u32x2 __attribute__((ext_vector_type(2)));
typedef u32 u32x4 __attribute__((ext_vector_type(4)));
typedef __bf16 bf16x2_t __attribute__((ext_vector_type(2)));
#define LAS __attribute__((address_space(3)))
#define DI __device__ __forceinline__

constexpr int S = 2048, D = 448, F = 1792;

constexpr size_t SZ_DS = (size_t)D * S * 2;
constexpr size_t OFF_RE = 0;
constexpr size_t OFF_RO = OFF_RE + SZ_DS;
constexpr size_t OFF_QT = OFF_RO + SZ_DS;
constexpr size_t OFF_X1T = OFF_QT + SZ_DS;
constexpr size_t OFF_MEAN = OFF_X1T + SZ_DS;
constexpr size_t OFF_RSTD = OFF_MEAN + S * 4;
constexpr size_t OFF_MOD = OFF_RSTD + S * 4;
constexpr size_t OFF_SG = OFF_MOD + 16384;
constexpr size_t OFF_CB = OFF_MOD + 12288;

constexpr size_t OFF_VG = OFF_SG + 3 * S * 8;
constexpr size_t OFF_XS = OFF_VG + 4 * S * 8;
constexpr size_t OFF_VP = OFF_XS + SZ_DS;
constexpr size_t OFF_WGU = OFF_VP + 8 * 2 * 3584 * 4;
constexpr size_t OFF_WD = OFF_WGU + (size_t)2 * D * F * 2;
constexpr size_t OFF_ACT = OFF_WD + (size_t)D * F * 2;
constexpr size_t WS_END = OFF_ACT + (size_t)S * F * 2;

struct Stamps2 {};
#define STAMP_DECL
#define STAMP(wsp, kid, i)
#define STAMP_FLUSH(wsp, kid)
#define HWID(wsp, kid)
#define XSTAMP(i)
#define YSTAMP(i) STAMP(ws, 2, i)
#if defined(__HIP_DEVICE_COMPILE__)
#define NOPK __attribute__((target("no-packed-fp32-ops")))
#else
#define NOPK
#endif

template <int I, int N, class Fn>
DI void static_for(Fn&& f) {
  if constexpr (I < N) {
    f(std::integral_constant<int, I>{});
    static_for<I + 1, N>(f);
  }
}

DI float lo_f(u32 x) { return __builtin_bit_cast(float, x << 16); }
DI float hi_f(u32 x) { return __builtin_bit_cast(float, x & 0xffff0000u); }
DI u32 pack_bf16(float a, float b) {
  f32x2 v = {a, b};
  return __builtin_bit_cast(u32, __builtin_convertvector(v, bf16x2_t));
}
DI float round_bf16(float a) { return lo_f(pack_bf16(a, 0.f)); }
DI float bf2f(u16 x) { return __builtin_bit_cast(float, (u32)x << 16); }

template <int N>
DI void wait_vm() { asm volatile("s_waitcnt vmcnt(%0)" ::"n"(N) : "memory"); }
DI void wait_lgkm0() { asm volatile("s_waitcnt lgkmcnt(0)" ::: "memory"); }
DI void bar() { __builtin_amdgcn_s_barrier(); }

#define SNOP "s_nop 0\n\t"
#define SNOP2 ""
DI const void* uni(const void* p) {
  const unsigned long long v = (unsigned long long)p;
  const u32 lo = __builtin_amdgcn_readfirstlane((u32)v), hi = __builtin_amdgcn_readfirstlane((u32)(v >> 32));
  return (const void*)(((unsigned long long)hi << 32) | lo);
}
DI void glds16_asm(const void* sbase, u32 voff, u32 lds_addr) {
  u32 keep;
  sbase = uni(sbase);
  lds_addr = __builtin_amdgcn_readfirstlane(lds_addr);
  asm volatile("s_mov_b32 %0, m0\n\ts_mov_b32 m0, %3\n\t" SNOP "global_load_lds_dwordx4 %1, %2\n\ts_mov_b32 m0, %0"
               : "=&s"(keep)
               : "v"(voff), "s"(sbase), "s"(lds_addr)
               : "memory");
}
DI void glds16(const void* g, char* lds) {
  __builtin_amdgcn_global_load_lds((const __attribute__((address_space(1))) void*)g, (LAS void*)lds, 16, 0, NTAUX);
}

template <int CTRL>
DI float dpp_add(float v) {
  const int x = __builtin_bit_cast(int, v);
  return v + __builtin_bit_cast(float, __builtin_amdgcn_update_dpp(x, x, CTRL, 0xf, 0xf, false));
}
DI float sum8(float v) {
  v = dpp_add<0xB1>(v);
  v = dpp_add<0x4E>(v);
  v = dpp_add<0x141>(v);
  return v;
}
DI float sum16(float v) { return dpp_add<0x140>(sum8(v)); }
DI float dot2(u32 a, u32 b, float c) {
  return __builtin_amdgcn_fdot2_f32_bf16(__builtin_bit_cast(bf16x2_t, a), __builtin_bit_cast(bf16x2_t, b), c, false);
}
typedef __amdgpu_buffer_rsrc_t rsrc_t;
DI rsrc_t mk_rsrc(const void* p, u32 bytes) { return __builtin_amdgcn_make_buffer_rsrc((void*)p, 0, bytes, 0x00020000); }
DI void st16_sc1(void* p, u32x4 v) { asm volatile("global_store_dwordx4 %0, %1, off sc1\n\ts_nop 1" ::"v"(p), "v"(v) : "memory"); }
DI void st8_sc1(void* p, u32x2 v) { asm volatile("global_store_dwordx2 %0, %1, off sc1" ::"v"(p), "v"(v) : "memory"); }
DI void st4_sc1(void* p, float v) { asm volatile("global_store_dword %0, %1, off sc1" ::"v"(p), "v"(v) : "memory"); }

#define EXP_E12 2
#define EXP_E13 6
#define EXP_LOCK 4
#define EXP_NW2 14
DI bf16x8 tr_frag(const char* smem, int o0, int o1) {
  s16x4 x0 = __builtin_amdgcn_ds_read_tr16_b64_v4i16((LAS s16x4*)(smem + o0));
  s16x4 x1 = __builtin_amdgcn_ds_read_tr16_b64_v4i16((LAS s16x4*)(smem + o1));
  return __builtin_shufflevector(x0, x1, 0, 1, 2, 3, 4, 5, 6, 7);
}
DI f32x16 mfma32(u32x4 a, bf16x8 b, f32x16 c) {
  return __builtin_amdgcn_mfma_f32_32x32x16_bf16(__builtin_bit_cast(bf16x8, a), b, c, 0, 0, 0);
}

DI void retile(int task, const u16* __restrict__ w_gate, const u16* __restrict__ w_up, const u16* __restrict__ w_down,
               char* __restrict__ ws, char* smem, int w, int lane) {
  const int xr = lane >> 4, slot = lane & 15;
  const int chn = slot ^ ((xr << 2) | (w & 3));
  const int h = lane >> 5, blk = (lane >> 4) & 1, qq = (lane & 15) >> 2, p = lane & 3;
  const bool gu = task < 112;
  int kq = 0, fg = 0, dg = 0, fk = 0;
  if (gu) {
    fg = task % 28, kq = task / 28;
    const u16* src = (chn < 8 ? w_gate : w_up) + (size_t)(kq * 112 + 4 * w + xr) * F + fg * 64 + (chn & 7) * 8;
#pragma unroll
    for (int i = 0; i < 7; ++i) glds16(src + (size_t)i * 16 * F, smem + (w + 4 * i) * 1024);
  } else {
    dg = (task - 112) % 7, fk = (task - 112) / 7;
    const u16* src = w_down + (size_t)(fk * 224 + (chn >> 3) * 112 + 4 * w + xr) * D + dg * 64 + (chn & 7) * 8;
#pragma unroll
    for (int i = 0; i < 7; ++i) glds16(src + (size_t)i * 16 * D, smem + (w + 4 * i) * 1024);
  }
  const int col = gu ? blk * 128 + w * 32 + (p & 1) * 16 + (p >> 1) * 8 : (w >> 1) * 128 + (w & 1) * 64 + blk * 32 + p * 8;
  int o[2];
#pragma unroll
  for (int t = 0; t < 2; ++t)
    o[t] = 256 * (8 * h + 4 * t + qq) + 16 * ((col >> 4) ^ ((qq << 2) | ((2 * h + t) & 3))) + (col & 8);
  u32x4* dst = gu ? (u32x4*)(ws + OFF_WGU) + ((size_t)(fg * 4 + w) * 28 + kq * 7) * 64 + lane
                  : (u32x4*)(ws + OFF_WD) + ((size_t)(dg * 2 + (w & 1)) * 112 + fk * 14 + (w >> 1) * 7) * 64 + lane;
  __syncthreads();
#pragma unroll
  for (int i = 0; i < 7; ++i)
    st16_sc1(dst + i * 64, __builtin_bit_cast(u32x4, tr_frag(smem, o[0] + i * 4096, o[1] + i * 4096)));
}

#define EXP_DELAY 0
#define EXP_T1 5
#define EXP_A0 4
#define ACTAUX 16
constexpr int K0_GRID = 504;

extern "C" __global__ __launch_bounds__(256) void k0_prep(const u16* __restrict__ q, const u16* __restrict__ k,
                                               const u16* __restrict__ v, const u16* __restrict__ shift_k,
                                               const u16* __restrict__ c, const u16* __restrict__ w_mod,
                                               const u16* __restrict__ ssm_k, const u16* __restrict__ w_gate,
                                               const u16* __restrict__ w_up, const u16* __restrict__ w_down,
                                               char* __restrict__ ws) {
  __shared__ __attribute__((aligned(16))) char smem[28672];
  float(*red)[64] = (float(*)[64])smem;
  const int b = blockIdx.x, tid = threadIdx.x, lane = tid & 63, w = __builtin_amdgcn_readfirstlane(tid >> 6);
  STAMP_DECL;
  STAMP(ws, 0, 0);
  const int xcd = b & 7, idx = b >> 3;
  const unsigned long long ts0 = __builtin_amdgcn_s_memrealtime();
  u32 cwarm = 0;
  if (idx < 2) {
    u32 lo, hi, len;
    asm volatile(
        "s_getpc_b64 s[40:41]\n\ts_add_u32 s40, s40, k0_prep@rel32@lo+4\n\ts_addc_u32 s41, s41, k0_prep@rel32@hi+12\n\t"
        "s_mov_b32 %0, s40\n\ts_mov_b32 %1, s41\n\ts_mov_b32 %2, k_code_end-k0_prep"
        : "=s"(lo), "=s"(hi), "=s"(len)::"s40", "s41", "scc");
    const char* code = (const char*)(((unsigned long long)hi << 32) | lo);
    if ((u32)(idx * 256 + tid) * 128 < len) cwarm = *(const __attribute__((address_space(1))) u32*)(size_t)(code + (idx * 256 + tid) * 128);
  }
  asm volatile("" ::"s"(q), "s"(k), "s"(v), "s"(shift_k), "s"(c), "s"(w_mod), "s"(ssm_k), "s"(w_gate), "s"(w_up), "s"(w_down),
               "s"(ws));
  if (__builtin_expect(idx >= 42, 0)) {
    if (b - 336 < 128) {
      const int t = b - 336;
      u32x4* ap = (u32x4*)(ws + (t < 64 ? OFF_X1T : OFF_XS)) + (size_t)(t & 63) * 1792 + tid;
      const u32x4 sent = {~0u, ~0u, ~0u, ~0u};
#pragma unroll
      for (int i = 0; i < 7; ++i) st16_sc1(ap + i * 256, sent);
    }
    retile(b - 336, w_gate, w_up, w_down, ws, smem, w, lane);
    if (tid == 0) { ((unsigned long long*)(ws + WS_END))[b] = ts0; ((unsigned long long*)(ws + WS_END))[512 + b] = __builtin_amdgcn_s_memrealtime(); }
    STAMP(ws, 7, 1);
    STAMP_FLUSH(ws, 7);
    return;
  }
  int misc = -1;
  if (xcd == 7) misc = idx < 14 ? 64 + idx : (idx < 36 ? idx - 14 : 1000);
  else if (idx < 4) misc = 64 + 14 + xcd * 4 + idx;
  else if (idx >= 36) misc = 22 + xcd * 6 + (idx - 36);
  if (__builtin_expect(misc < 0, 1)) {
    const int rb = idx - 4;
    const int sq = lane >> 5, cp = lane & 31;
    const int d0 = xcd * 64 + cp * 2;
    const int a = rb * 8 + w * 2 + sq;
#define TST(p, v) (*(u32x4*)(p) = (v))
    const int s0 = a * 8;
    const rsrc_t rk = mk_rsrc(k, S * D * 2), rv = mk_rsrc(v, S * D * 2), rq = mk_rsrc(q, S * D * 2);
    const int oB = (s0 * D + d0) * 2;
    const int oA = oB - 4 * D * 2, oC = oB + 4 * D * 2;
    u32 kk[12], vv[9], qq[8];
#pragma unroll
    for (int i = 0; i < 4; ++i) kk[i] = __builtin_amdgcn_raw_buffer_load_b32(rk, oA + i * D * 2, 0, NTAUX);
#pragma unroll
    for (int i = 0; i < 4; ++i) kk[4 + i] = __builtin_amdgcn_raw_buffer_load_b32(rk, oB + i * D * 2, 0, NTAUX);
#pragma unroll
    for (int i = 0; i < 4; ++i) kk[8 + i] = __builtin_amdgcn_raw_buffer_load_b32(rk, oC + i * D * 2, 0, NTAUX);
    vv[0] = __builtin_amdgcn_raw_buffer_load_b32(rv, oA + 3 * D * 2, 0, NTAUX);
#pragma unroll
    for (int i = 0; i < 4; ++i) vv[1 + i] = __builtin_amdgcn_raw_buffer_load_b32(rv, oB + i * D * 2, 0, NTAUX);
#pragma unroll
    for (int i = 0; i < 4; ++i) vv[5 + i] = __builtin_amdgcn_raw_buffer_load_b32(rv, oC + i * D * 2, 0, NTAUX);
#pragma unroll
    for (int i = 0; i < 4; ++i) qq[i] = __builtin_amdgcn_raw_buffer_load_b32(rq, oB + i * D * 2, 0, NTAUX);
#pragma unroll
    for (int i = 0; i < 4; ++i) qq[4 + i] = __builtin_amdgcn_raw_buffer_load_b32(rq, oC + i * D * 2, 0, NTAUX);
    const u32x4 sk = *(const u32x4*)(shift_k + d0 * 4);
    u32 warm = 0;
    f32x2 wt[4] = {{lo_f(sk[0]), lo_f(sk[2])}, {hi_f(sk[0]), hi_f(sk[2])}, {lo_f(sk[1]), lo_f(sk[3])}, {hi_f(sk[1]), hi_f(sk[3])}};
    f32x2 kf[12], u[9];
#pragma unroll
    for (int i = 0; i < 12; ++i) kf[i] = f32x2{lo_f(kk[i]), hi_f(kk[i])};
#pragma unroll
    for (int i = 0; i < 9; ++i) {
      f32x2 t = kf[i + 3] * wt[0];
      t = __builtin_elementwise_fma(kf[i + 2], wt[1], t);
      t = __builtin_elementwise_fma(kf[i + 1], wt[2], t);
      t = __builtin_elementwise_fma(kf[i], wt[3], t);
      u[i] = t * f32x2{lo_f(vv[i]), hi_f(vv[i])};
    }
    {
      char* dst = ws + (size_t)d0 * (S * 2) + (2040 - s0) * 2;
#pragma unroll
      for (int cI = 0; cI < 2; ++cI) {
        TST(dst + OFF_RO + cI * S * 2, (u32x4{pack_bf16(u[8][cI], u[7][cI]), pack_bf16(u[6][cI], u[5][cI]),
                                              pack_bf16(u[4][cI], u[3][cI]), pack_bf16(u[2][cI], u[1][cI])}));
        TST(dst + OFF_RE + cI * S * 2, (u32x4{pack_bf16(u[7][cI], u[6][cI]), pack_bf16(u[5][cI], u[4][cI]),
                                              pack_bf16(u[3][cI], u[2][cI]), pack_bf16(u[1][cI], u[0][cI])}));
      }
      char* dq = ws + OFF_QT + (size_t)d0 * (S * 2) + s0 * 2;
#pragma unroll
      for (int cI = 0; cI < 2; ++cI) {
        const u32 sel = cI ? 0x07060302u : 0x05040100u;
        TST(dq + cI * S * 2, (u32x4{__builtin_amdgcn_perm(qq[2], qq[0], sel), __builtin_amdgcn_perm(qq[6], qq[4], sel),
                                    __builtin_amdgcn_perm(qq[3], qq[1], sel), __builtin_amdgcn_perm(qq[7], qq[5], sel)}));
      }
    }
    asm volatile("" ::"v"(warm));
  } else if (misc < 64) {
    const int bb = misc;
    const int row = bb * 32 + w * 8 + (lane >> 3);
    const int sub = lane & 7;
    u32x4 x[7];
#pragma unroll
    for (int i = 0; i < 7; ++i) x[i] = NTLOAD((const u32x4*)(q + (size_t)row * D + (i * 8 + sub) * 8));
    if (sub < 7) ((unsigned long long*)(ws + OFF_SG))[sub * S + row] = sub < 3 ? 0ull : ~0ull;
    float s = 0.f, s2 = 0.f;
#pragma unroll
    for (int i = 0; i < 7; ++i)
#pragma unroll
      for (int j = 0; j < 4; ++j) {
        const u32 t = x[i][j];
        s = dot2(t, 0x3f803f80u, s);
        s2 = dot2(t, t, s2);
      }
    s = sum8(s);
    s2 = sum8(s2);
    const float mean = s * (1.f / D);
    float var = s2 * (1.f / D) - mean * mean;
    var = var < 0.f ? 0.f : var;
    const float rstd = __builtin_amdgcn_rsqf(var + 1e-5f);
    if (sub < 2) {
      const int p = (row & ~7) | ((row & 1) << 2) | ((row >> 1) & 3);
      ((float*)(ws + OFF_MEAN))[sub * S + p] = sub ? rstd : mean;
    }
  } else if (misc < 106) {
    const int bb = misc - 64;
    const int co = lane & 7, rs = lane >> 3;
    const int col0 = bb * 64 + co * 8;
    u32x4 x[14];
#pragma unroll
    for (int i = 0; i < 14; ++i)
      x[i] = NTLOAD((const u32x4*)(w_mod + (size_t)(w * 112 + rs * 14 + i) * (6 * D) + col0));
    u32 cv[7];
#pragma unroll
    for (int i = 0; i < 7; ++i) cv[i] = *(const u32*)(c + w * 112 + rs * 14 + 2 * i);
    __builtin_amdgcn_sched_barrier(0);
    f32x2 acc[4] = {{0.f, 0.f}, {0.f, 0.f}, {0.f, 0.f}, {0.f, 0.f}};
#pragma unroll
    for (int i = 0; i < 14; ++i) {
      const float cf = (i & 1) ? hi_f(cv[i >> 1]) : lo_f(cv[i >> 1]);
      const float sl = round_bf16(cf * __builtin_amdgcn_rcpf(1.f + __builtin_amdgcn_exp2f(cf * -1.44269504f)));
#pragma unroll
      for (int j = 0; j < 4; ++j)
        acc[j] = __builtin_elementwise_fma(f32x2{lo_f(x[i][j]), hi_f(x[i][j])}, f32x2{sl, sl}, acc[j]);
    }
    *(f32x4*)&red[w * 8 + rs][co * 8] = f32x4{acc[0][0], acc[0][1], acc[1][0], acc[1][1]};
    *(f32x4*)&red[w * 8 + rs][co * 8 + 4] = f32x4{acc[2][0], acc[2][1], acc[3][0], acc[3][1]};
    __syncthreads();
    {
      float t = 0.f;
#pragma unroll
      for (int i = 0; i < 8; ++i) t += red[w * 8 + i][lane];
      red[32 + w][lane] = t;
    }
    __syncthreads();
    if (tid < 64) {
      const float t = (red[32][tid] + red[33][tid]) + (red[34][tid] + red[35][tid]);
      const float m = round_bf16(t);
      const int col = bb * 64 + tid;
      ((float*)(ws + OFF_MOD))[col] = m;
      if (col >= 3 * D && col < 5 * D) {
        const bool isc = col >= 4 * D;
        ((u16*)(ws + OFF_CB))[isc ? col - 4 * D : col - 2 * D] = (u16)pack_bf16(isc ? 1.f + m : m, 0.f);
      }
    }
  }
  asm volatile("" ::"v"(cwarm));
  if (tid == 0) { ((unsigned long long*)(ws + WS_END))[b] = ts0; ((unsigned long long*)(ws + WS_END))[512 + b] = __builtin_amdgcn_s_memrealtime(); }
  STAMP(ws, 0, 1);
  STAMP_FLUSH(ws, misc < 0 ? 0 : (misc < 64 ? 4 : 5));
}

constexpr int K1_KSTR = 260 * 16;
constexpr int K1_USTR = 286 * 16;
constexpr int K1_UOFF = 4 * K1_KSTR;
constexpr int K1_XOFF = K1_UOFF + 2 * K1_USTR;
constexpr int K1_CH = K1_XOFF + 32768;

DI void glds16x4_asm(const void* sbase, u32 voff, u32 voff_last, u32 lds_addr) {
  u32 keep;
  sbase = uni(sbase);
  lds_addr = __builtin_amdgcn_readfirstlane(lds_addr);
  asm volatile(
      "s_mov_b32 %0, m0\n\ts_mov_b32 m0, %4\n\t" SNOP
      "global_load_lds_dwordx4 %1, %3\n\t"
      "global_load_lds_dwordx4 %1, %3 offset:1024\n\t"
      "global_load_lds_dwordx4 %1, %3 offset:2048\n\t"
      "global_load_lds_dwordx4 %2, %3 offset:3072\n\t"
      "s_mov_b32 m0, %0"
      : "=&s"(keep)
      : "v"(voff), "v"(voff_last), "s"(sbase), "s"(lds_addr)
      : "memory");
}
#define CAS __attribute__((address_space(4)))
constexpr int K2_XB = D * 256;
constexpr int K2_XH = D * 128;
constexpr int K2_ST = K2_XB;
constexpr int K2_CB = 131072;
constexpr int K2_LDS = K2_CB + 2048;

DI void gload16_asm(u32x4& dst, const void* sbase, u32 voff) {
  sbase = uni(sbase);
  asm volatile(SNOP2 "global_load_dwordx4 %0, %1, %2" : "=v"(dst) : "v"(voff), "s"(sbase) : "memory");
}
DI u32 umin32(u32 v) { return v < 0xFFFFFFFEu ? v : 0xFFFFFFFEu; }
DI u32 umax3(u32 a, u32 b, u32 c) { const u32 t = a > b ? a : b; return t > c ? t : c; }
template <int OFF>
DI void gload16_off(u32x4& dst, const void* sbase, u32 voff) {
  sbase = uni(sbase);
  asm volatile(SNOP2 "global_load_dwordx4 %0, %1, %2 offset:%3" : "=v"(dst) : "v"(voff), "s"(sbase), "n"(OFF) : "memory");
}
DI void glds16_nom0(const void* sbase, u32 voff, u32 lds_addr) {
  sbase = uni(sbase);
  lds_addr = __builtin_amdgcn_readfirstlane(lds_addr);
  asm volatile("s_mov_b32 m0, %2\n\t" SNOP "global_load_lds_dwordx4 %0, %1" ::"v"(voff), "s"(sbase), "s"(lds_addr) : "memory");
}
DI void glds16_sc1(const void* sbase, u32 voff, u32 lds_addr) {
  sbase = uni(sbase);
  lds_addr = __builtin_amdgcn_readfirstlane(lds_addr);
  asm volatile("s_mov_b32 m0, %2\n\t" SNOP "global_load_lds_dwordx4 %0, %1 sc1" ::"v"(voff), "s"(sbase), "s"(lds_addr) : "memory");
}
template <int N>
DI void wait_vm_pin(u32x4& a, u32x4& b) {
  asm volatile("s_waitcnt vmcnt(%2)" : "+v"(a), "+v"(b) : "n"(N) : "memory");
}
DI float xor32(float v) {
  const u32 u = __builtin_bit_cast(u32, v);
  auto r = __builtin_amdgcn_permlane32_swap(u, u, false, false);
  const u32 a = r[0], b = r[1];
  return __builtin_bit_cast(float, (threadIdx.x & 32) ? a : b);
}

typedef unsigned long long u64;
typedef __attribute__((address_space(1))) u64 gu64;
typedef __attribute__((address_space(1))) u32 gu32;
#define RLX_AGENT __ATOMIC_RELAXED, __HIP_MEMORY_SCOPE_AGENT
#define EXP_WE 8
#define EXP_NW0 5
#define EXP_NWP 3
#define EXP_KG 4
#define EXP_NWH 15
#define EXP_NW2X (EXP_NWH - EXP_NW0 - EXP_NW1)
#define EXP_NW1 5
constexpr int NWC = EXP_NW0 + EXP_NW1;
template <bool WF>
DI void conv_body(const u16* __restrict__ ssm_k, const u16* __restrict__ ssm_D, char* __restrict__ ws, char* smem,
                  const int w, u32x4 (&wf)[28], const char* wbase, Stamps2& s2) {
  STAMP(ws, 1, 0);
  const int lane = threadIdx.x & 63;
  const int ch = w >> 2, q = w & 3;
  const int cpair = (blockIdx.x & 7) * 32 + (blockIdx.x >> 3);
  const int d = 2 * cpair + ch;
  char* L = smem + ch * K1_CH;
  const u32 L0 = (u32)(size_t)(LAS char*)L;
  const u16* kf = ssm_k + (size_t)d * S;
  const u32 vo = lane * 16;
  {
    const u32 vl = (lane > 62 ? 62 : lane) * 16;
    glds16x4_asm(kf + (2 + 2 * q), vo, vl, L0 + q * K1_KSTR + 64);
    const char* ub = ws + ((q >> 1) ? OFF_RO : OFF_RE) + (size_t)d * S * 2 + (q & 1) * 2048;
    const u32 ul = L0 + K1_UOFF + (q >> 1) * K1_USTR + (q & 1) * 2048;
    glds16_nom0(ub, vo, ul);
    glds16_nom0(ub + 1024, vo, ul + 1024);
  }
  const int n = lane & 15, q4 = lane >> 4;
  const int pbase = 32 * (n >> 1) + 8 * q4 + 4 * (n & 1) + 512 * q;
  const int di = n + q - 15;
  u32 head;
  asm volatile(SNOP2 "global_load_dword %0, %1, %2" : "=v"(head) : "v"((u32)(di < 0 ? 0 : di) * 4), "s"(uni(kf)) : "memory");
  auto ldwf = [&](auto kI) {
    constexpr int k = decltype(kI)::value;
    gload16_off<(k % 4) * 1024>(wf[k], wbase + (k / 4) * 4096, vo);
  };
  bar();
  if constexpr (WF) static_for<0, EXP_NW0>(ldwf);
  const CAS float* MODc = (const CAS float*)(size_t)(ws + OFF_MOD);
  const float sa = MODc[d], ca = MODc[D + d], ga = MODc[2 * D + d], cbv = MODc[4 * D + d];
  const u32 Draw = ((const CAS u32*)(size_t)ssm_D)[cpair];
  if (lane < 14) *(LAS u32x4*)(L + K1_UOFF + (q >> 1) * K1_USTR + (256 + 14 * (q & 1) + lane) * 16) = u32x4{0u, 0u, 0u, 0u};
  u32 Ab = L0 + (n & 3) * K1_KSTR + ((n >> 2) + q4 + 8 * q) * 16;
  u32 Bb = L0 + K1_UOFF + (n & 1) * K1_USTR + (28 - 4 * (n >> 1) + q4 + 8 * q) * 16;
  u32 Hd = L0 + q * K1_KSTR + n * 4;
  asm volatile("" : "+v"(Ab), "+v"(Bb), "+v"(Hd) : "s"(sa), "s"(ca), "s"(ga), "s"(Draw), "s"(cbv));
  asm volatile("s_waitcnt vmcnt(%1)" : "+v"(head) : "n"(WF ? EXP_NW0 : 0) : "memory");
  *(LAS u32*)(size_t)Hd = di < 0 ? 0u : head;
  wait_lgkm0();
  STAMP(ws, 1, 1);
  bar();
  bf16x8 fa[8], fb[8];
  auto rd = [&](int dl) {
#pragma unroll
    for (int g = 0; g < 8; ++g) {
      fa[g] = *(const LAS bf16x8*)(size_t)(Ab + 64 * dl + 512 * g);
      fb[g] = *(const LAS bf16x8*)(size_t)(Bb + 64 * dl + 512 * (7 - g));
    }
  };
  rd(0);
  __builtin_amdgcn_sched_barrier(0);
  u32x2 qv[2];
  f32x4 mu[2], rs[2];
  asm volatile(SNOP2
               "global_load_dwordx2 %0, %6, %8\n\t"
               "global_load_dwordx2 %1, %6, %8 offset:512\n\t"
               "global_load_dwordx4 %2, %7, %9\n\t"
               "global_load_dwordx4 %3, %7, %9 offset:1024\n\t"
               "global_load_dwordx4 %4, %7, %10\n\t"
               "global_load_dwordx4 %5, %7, %10 offset:1024"
               : "=&v"(qv[0]), "=&v"(qv[1]), "=&v"(mu[0]), "=&v"(mu[1]), "=&v"(rs[0]), "=&v"(rs[1])
               : "v"((u32)pbase * 2), "v"((u32)pbase * 4), "s"(uni(ws + OFF_QT + (size_t)d * S * 2)),
                 "s"(uni(ws + OFF_MEAN)), "s"(uni(ws + OFF_RSTD))
               : "memory");
  const u32x4 sent = {~0u, ~0u, ~0u, ~0u};
  const void* fillb = uni(ws + OFF_ACT + (size_t)(cpair * 8 + w) * 4096);
  f32x4 acc[8];
#pragma unroll
  for (int j = 0; j < 8; ++j) acc[j] = f32x4{0.f, 0.f, 0.f, 0.f};
  static_for<0, 72>([&](auto mI) {
    constexpr int m = decltype(mI)::value, t = m % 36;
    constexpr int sd = t < 1 ? 0 : t < 3 ? 1 : t < 6 ? 2 : t < 10 ? 3 : t < 15 ? 4 : t < 21 ? 5 : t < 28 ? 6 : 7;
    constexpr int g = t - sd * (sd + 1) / 2;
    if constexpr (m == 36) {
      rd(1);
      __builtin_amdgcn_sched_barrier(0);
    }
    f32x4 a = __builtin_amdgcn_mfma_f32_16x16x32_bf16(fa[g], fb[sd - g], acc[sd], 0, 0, 0);
    asm volatile("" : "+v"(a));
    acc[sd] = a;
    if constexpr (m % 4 == 3) {
      constexpr int v = m / 4;
      if constexpr (v < 4) {
        u32x4 sv = sent;
        asm volatile("global_store_dwordx4 %0, %1, %2 offset:%3 sc1\n\ts_nop 1" ::"v"(vo), "v"(sv), "s"(fillb), "n"(v * 1024) : "memory");
      } else if constexpr (WF && v - 4 < EXP_NW1) {
        ldwf(std::integral_constant<int, EXP_NW0 + v - 4>{});
      }
      __builtin_amdgcn_sched_barrier(0);
    }
  });
  STAMP(ws, 1, 3);
  LAS f32x4* X = (LAS f32x4*)(L + K1_XOFF);
#pragma unroll
  for (int j = 0; j < 8; ++j) X[(j * 4 + q) * 64 + lane] = acc[j];
  u32x4 uc[2];
#pragma unroll
  for (int j = 0; j < 2; ++j)
    uc[j] = *(const LAS u32x4*)(L + K1_UOFF + K1_USTR + (255 - (32 * (2 * q + j) + 4 * (n >> 1) + q4)) * 16);
  asm volatile("s_waitcnt vmcnt(%6)"
               : "+v"(qv[0]), "+v"(qv[1]), "+v"(mu[0]), "+v"(mu[1]), "+v"(rs[0]), "+v"(rs[1])
               : "n"(WF ? EXP_NW1 : 0)
               : "memory");
  f32x4 qf[2], hg[2], kd[2];
  {
    const int sh = (n & 1) ? 16 : 0;
    const float gc = ga * round_bf16(1.f + ca), gs = ga * sa, Dd = ch ? hi_f(Draw) : lo_f(Draw);
#pragma unroll
    for (int j = 0; j < 2; ++j) {
#pragma unroll
      for (int r = 0; r < 4; ++r) {
        kd[j][r] = __builtin_bit_cast(float, (uc[j][3 - r] << sh) & 0xffff0000u) * Dd;
        qf[j][r] = (r & 1) ? hi_f(qv[j][r >> 1]) : lo_f(qv[j][r >> 1]);
        hg[j][r] = (qf[j][r] - mu[j][r]) * rs[j][r] * gc + gs;
      }
      asm volatile("" : "+v"(kd[j]), "+v"(qf[j]), "+v"(hg[j]));
    }
  }
  wait_lgkm0();
  bar();
  STAMP(ws, 1, 4);
  u16* X1T = (u16*)(ws + OFF_X1T) + (size_t)d * S + pbase;
  u16* XS = (u16*)(ws + OFF_XS) + (size_t)d * S + pbase;
  const float c1 = round_bf16(1.f + cbv);
#pragma unroll
  for (int j = 0; j < 2; ++j) {
    const LAS f32x4* Xj = X + ((2 * q + j) * 4) * 64 + lane;
    const f32x4 y = (Xj[0] + Xj[64]) + (Xj[128] + Xj[192]);
    const f32x4 o = qf[j] + hg[j] * (y + kd[j]);
    const u32 p0 = umin32(pack_bf16(o[0], o[1])), p1 = umin32(pack_bf16(o[2], o[3]));
    st8_sc1(XS + 256 * j,
            u32x2{umin32(pack_bf16(lo_f(p0) * c1, hi_f(p0) * c1)), umin32(pack_bf16(lo_f(p1) * c1, hi_f(p1) * c1))});
    st8_sc1(X1T + 256 * j, u32x2{p0, p1});
  }
  STAMP(ws, 1, 5);
}

DI void k2_stats(char* __restrict__ ws, char* smem, const int sblk) {
  const int tid = threadIdx.x, lane = tid & 63;
  const int w = __builtin_amdgcn_readfirstlane(tid >> 6);
  const int n = lane & 31, h = lane >> 5;
  STAMP_DECL;
  STAMP(ws, 2, 0);
  {
    const int p0 = sblk * 64;
    __syncthreads();
    const rsrc_t rx = mk_rsrc(ws + OFF_X1T + ((size_t)(w * 56) * S + p0) * 2, 56 * S * 2);
    const int c8 = lane & 7, r8 = lane >> 3;
    u32x4 x[7];
    for (int spin = 0; spin < (1 << 15); ++spin) {
      asm volatile("" ::: "memory");
      u32 m = 0;
#pragma unroll
      for (int i = 0; i < 7; ++i) x[i] = __builtin_amdgcn_raw_buffer_load_b128(rx, (r8 + 8 * i) * (S * 2) + c8 * 16, 0, 16);
#pragma unroll
      for (int i = 0; i < 7; ++i) m = umax3(umax3(m, x[i][0], x[i][1]), x[i][2], x[i][3]);
      if (__all(m != ~0u)) break;
      __builtin_amdgcn_s_sleep(1);
    }
    float sa[8], qa[8];
#pragma unroll
    for (int e = 0; e < 8; ++e) sa[e] = qa[e] = 0.f;
#pragma unroll
    for (int i = 0; i < 7; ++i)
#pragma unroll
      for (int e = 0; e < 8; ++e) {
        const float a = (e & 1) ? hi_f(x[i][e >> 1]) : lo_f(x[i][e >> 1]);
        sa[e] += a;
        qa[e] = __builtin_fmaf(a, a, qa[e]);
      }
    LAS float* P = (LAS float*)smem;
    {
      LAS float* pp = P + (w * 8 + r8) * 128 + c8 * 8;
      *(LAS f32x4*)pp = f32x4{sa[0], sa[1], sa[2], sa[3]};
      *(LAS f32x4*)(pp + 4) = f32x4{sa[4], sa[5], sa[6], sa[7]};
      *(LAS f32x4*)(pp + 64) = f32x4{qa[0], qa[1], qa[2], qa[3]};
      *(LAS f32x4*)(pp + 68) = f32x4{qa[4], qa[5], qa[6], qa[7]};
    }
    wait_lgkm0();
    {
      float s1 = 0.f, q1 = 0.f;
#pragma unroll
      for (int i = 0; i < 8; ++i) {
        s1 += P[(w * 8 + i) * 128 + lane];
        q1 += P[(w * 8 + i) * 128 + 64 + lane];
      }
      *(LAS f32x2*)(P + 8192 + (w * 64 + lane) * 2) = f32x2{s1, q1};
    }
    __syncthreads();
    if (tid < 64) {
      float sm = 0.f, sq = 0.f;
#pragma unroll
      for (int i = 0; i < 8; ++i) {
        const f32x2 t = *(const LAS f32x2*)(P + 8192 + (i * 64 + tid) * 2);
        sm += t[0];
        sq += t[1];
      }
      const float mean = sm * (1.f / D);
      float var = sq * (1.f / D) - mean * mean;
      var = (var < 0.f ? 0.f : var) + 1e-5f;
      const float rstd = 1.f / sqrtf(var);
      const float sig = var * rstd;
      const float mh = round_bf16(mean), sh = round_bf16(sig);
      const u32 b0 = pack_bf16(-mh, mh - mean), b1 = pack_bf16(-mh, sh), b2 = pack_bf16(sig - sh, sh);
      const float c1 = -2.302208198f * rstd;
      const float c2 = -0.1029432397f * (rstd * rstd * rstd);
      gu64* G = (gu64*)(size_t)(ws + OFF_SG) + p0 + tid;
      __hip_atomic_store(G, ((u64)b1 << 32) | b0, RLX_AGENT);
      __hip_atomic_store(G + S, ((u64)__builtin_bit_cast(u32, var) << 32) | b2, RLX_AGENT);
      __hip_atomic_store(G + 2 * S, ((u64)__builtin_bit_cast(u32, c1) << 32) | __builtin_bit_cast(u32, c2), RLX_AGENT);
    }
    STAMP(ws, 6, 1);
    STAMP(ws, 6, 2);
    STAMP_FLUSH(ws, 6);
  }
}

DI void k2_gemm(const u16* __restrict__ ssm_k, const u16* __restrict__ ssm_D, char* __restrict__ ws, u16* __restrict__ out,
                char* smem, const int w) {
  const int tid = threadIdx.x, lane = tid & 63;
  const int n = lane & 31, h = lane >> 5;
  const int xcd = blockIdx.x & 7, idx = blockIdx.x >> 3;
  const int mt = (xcd >> 1) * 4 + (idx & 3);
  const int dt = idx >> 2, sh2 = xcd & 1;
  const int ft = sh2 * 7 + dt;
  const int tile = ft * 8 + w;
  const u32 lds0 = (u32)(size_t)(LAS char*)smem;
  u32x4 wf[28], wd[28];
  const char* wbase = ws + OFF_WGU + (size_t)(tile * 28) * 1024;
  const u32 wa = lane * 16;
  auto ldwf = [&](auto kI) {
    constexpr int k = decltype(kI)::value;
    gload16_off<(k % 4) * 1024>(wf[k], wbase + (k / 4) * 4096, wa);
  };
  Stamps2 s2 = {};
  if (xcd < 7) {
    conv_body<true>(ssm_k, ssm_D, ws, smem, w, wf, wbase, s2);
  } else {
    const int task = idx * 8 + w, t = task >> 1, kh = task & 1;
    const u32x4* wsrc = (const u32x4*)(ws + OFF_WGU) + ((size_t)t * 28 + kh * 14) * 64 + lane;
    const u16* cbp = (const u16*)(ws + OFF_CB) + (n & 1) * D + kh * 224 + 8 * h;
    u32x4 fa[14], fb[14];
#pragma unroll
    for (int i = 0; i < 14; ++i) {
      fa[i] = wsrc[i * 64];
      fb[i] = *(const u32x4*)(cbp + 16 * i);
    }
    __builtin_amdgcn_sched_barrier(0);
    f32x16 va;
#pragma unroll
    for (int r = 0; r < 16; ++r) va[r] = 0.f;
#pragma unroll
    for (int i = 0; i < 14; ++i) va = mfma32(fb[i], __builtin_bit_cast(bf16x8, fa[i]), va);
    if (h == 0) {
      const float f0 = va[0], f1 = va[1];
      const u32 a0 = umin32(__builtin_bit_cast(u32, f0)), a1 = __builtin_bit_cast(u32, f1);
      __hip_atomic_store((gu64*)(size_t)(ws + OFF_VG) + kh * 3584 + t * 32 + n, ((u64)a1 << 32) | a0, RLX_AGENT);
    }
    __builtin_amdgcn_sched_barrier(0);
    static_for<0, NWC>(ldwf);
    wait_vm<0>();
  }
  STAMP_DECL;
  STAMP(ws, 2, 0);
  const char* xbase = ws + OFF_XS + ((size_t)(8 * w) * S + mt * 128) * 2;
  const u32 xoff = (u32)((lane >> 3) * S + ((lane & 7) ^ (((lane >> 4) & 1) << 2)) * 8) * 2;
  const char* wdbase = ws + OFF_WD + ((size_t)(2 * dt) * 112 + 14 * w) * 1024 + 4096;
  const int gsl = w < 6 ? w : w - 6;
  const char* gbase = ws + OFF_SG + (size_t)((gsl >> 1) * S + mt * 128 + (gsl & 1) * 64) * 8;
  auto ldwd = [&](auto kI) {
    constexpr int q = decltype(kI)::value, k = (q & 1) * 14 + (q >> 1), kk = k % 14;
    gload16_off<(kk % 8) * 1024 - 4096>(wd[k], wdbase + (k / 14) * 112 * 1024 + (kk / 8) * 8192, wa);
  };
  auto ldx = [&](auto cI) {
    constexpr int c = decltype(cI)::value;
    glds16_sc1(xbase + (size_t)(c % 7) * (64 * S * 2) + (c / 7) * 128, xoff, lds0 + (64 * c + 8 * w) * 128);
  };
  auto xok = [&](auto c0I, auto c1I) {
    u32 m = 0;
#pragma unroll
    for (int c = decltype(c0I)::value; c < decltype(c1I)::value; ++c) {
      const u32x4 t = *(const LAS u32x4*)(smem + (64 * c + 8 * w) * 128 + lane * 16);
      m = umax3(umax3(m, t[0], t[1]), t[2], t[3]);
    }
    return (bool)__all(m != ~0u);
  };
  auto xvalid = [&](auto c0I, auto c1I) {
    if (__builtin_expect(!xok(c0I, c1I), 0)) {
#pragma nounroll
      for (int spin = 0; spin < (1 << 14); ++spin) {
        __builtin_amdgcn_s_sleep(4);
        static_for<decltype(c0I)::value, decltype(c1I)::value>(ldx);
        wait_vm<0>();
        if (xok(c0I, c1I)) break;
      }
    }
    __builtin_amdgcn_sched_barrier(0);
  };
  wait_lgkm0();
  bar();
  XSTAMP(1);
  auto pin = [](f32x16& a) { asm volatile("" : "+v"(a)); };
  auto pinw = [&](auto kI) {
    u32x4 t = wf[decltype(kI)::value];
    asm volatile("" : "+v"(t)::"memory");
    wf[decltype(kI)::value] = t;
  };
  u32x4 aext;
  constexpr int NWH = NWC + EXP_NW2X;
  constexpr int NWP = NWC + EXP_NWP;
  static_for<NWC, NWP>(ldwf);
  static_for<0, 7>(ldx);
  static_for<NWP, NWH>(ldwf);
  int xo[2][2];
  {
    const int blk = (lane >> 4) & 1, qq = (lane & 15) >> 2, p = lane & 3;
#pragma unroll
    for (int i = 0; i < 2; ++i)
#pragma unroll
      for (int t = 0; t < 2; ++t)
        xo[i][t] = 128 * (8 * h + 4 * t + qq) + 16 * ((4 * i + 2 * blk + (p >> 1)) ^ ((qq >> 1) << 2)) + 8 * (p & 1);
  }
  f32x16 acc[4];
#pragma unroll
  for (int i = 0; i < 4; ++i)
#pragma unroll
    for (int r = 0; r < 16; ++r) acc[i][r] = 0.f;
  asm volatile("" : "+v"(xo[0][0]), "+v"(xo[0][1]), "+v"(xo[1][0]), "+v"(xo[1][1]));
  constexpr int PF = 3;
  bf16x8 xr[PF][2];
  auto rdx = [&](auto kI, auto iI) {
    constexpr int k = decltype(kI)::value, i = decltype(iI)::value;
    if constexpr (k == 28) {
      int a0 = xo[i][0] + K2_XH, a1 = xo[i][1] + K2_XH;
      asm volatile("" : "+v"(a0), "+v"(a1));
      xo[i][0] = a0;
      xo[i][1] = a1;
    }
    xr[k % PF][i] = tr_frag(smem, xo[i][0] + (k % 28) * 2048, xo[i][1] + (k % 28) * 2048);
  };
  using I0 = std::integral_constant<int, 0>;
  using I1 = std::integral_constant<int, 1>;
  using I2 = std::integral_constant<int, 2>;
  using I3 = std::integral_constant<int, 3>;
  using I7 = std::integral_constant<int, 7>;
  using I14 = std::integral_constant<int, 14>;
  LAS u64* CS = (LAS u64*)(smem + K2_ST);
  XSTAMP(4);
  wait_vm<4 + NWH - NWP>();
  static_for<0, NWP>(pinw);
  xvalid(I0{}, I3{});
  XSTAMP(5);
  bar();
  YSTAMP(1);
  static_for<0, PF - 1>([&](auto jI) {
    rdx(jI, I0{});
    rdx(jI, I1{});
  });
  __builtin_amdgcn_sched_barrier(0);
  u64 gv, gp0, gp1;
  const char* vbase = ws + OFF_VG + (size_t)tile * 256;
  constexpr int RW = 28 - NWH, KX = RW + 7, KV = 25;
  constexpr int WE = 27 - KX;
  static_assert(KX + 4 <= KV && WE <= 9, "stream does not fit");
  static_for<0, 28>([&](auto kI) {
    constexpr int k = decltype(kI)::value;
    if constexpr (k == 6) {
      wait_vm<6>();
      static_for<NWP, NWH>(pinw);
      xvalid(I3{}, I7{});
      bar();
    }
    if constexpr (k >= NWH) {
      u32x4 t = wf[k];
      asm volatile("s_waitcnt vmcnt(%1)" : "+v"(t) : "n"(NWH - 1) : "memory");
      wf[k] = t;
    }
    if constexpr (k == KV) {
      wait_vm<KV - KX>();
      xvalid(I7{}, I14{});
      bar();
    }
    __builtin_amdgcn_sched_barrier(0);
    acc[0] = mfma32(wf[k], xr[k % PF][0], acc[0]);
    pin(acc[0]);
    __builtin_amdgcn_sched_barrier(0);
    rdx(std::integral_constant<int, k + PF - 1>{}, I0{});
    __builtin_amdgcn_sched_barrier(0);
    acc[1] = mfma32(wf[k], xr[k % PF][1], acc[1]);
    pin(acc[1]);
    __builtin_amdgcn_sched_barrier(0);
    rdx(std::integral_constant<int, k + PF - 1>{}, I1{});
    if constexpr (k < RW) ldwf(std::integral_constant<int, NWH + k>{});
    else if constexpr (k < KX) ldx(std::integral_constant<int, 7 + k - RW>{});
    else if constexpr (k < 27) ldwd(std::integral_constant<int, k - KX>{});
    else {
      u64 t, t0, t1;
      asm volatile(SNOP2 "global_load_dwordx2 %0, %1, %2 sc1" : "=v"(t) : "v"((u32)lane * 8), "s"(uni(gbase)) : "memory");
      asm volatile(SNOP2 "global_load_dwordx2 %0, %1, %2 sc1" : "=v"(t0) : "v"((u32)n * 8), "s"(uni(vbase)) : "memory");
      asm volatile(SNOP2 "global_load_dwordx2 %0, %1, %2 sc1" : "=v"(t1) : "v"((u32)n * 8), "s"(uni(vbase + 3584 * 8)) : "memory");
      gv = t;
      gp0 = t0;
      gp1 = t1;
    }
    __builtin_amdgcn_sched_barrier(0);
  });
  YSTAMP(2);
  const rsrc_t rw = mk_rsrc(ws + OFF_ACT + ((size_t)(mt * 4) * 112 + tile) * 1024, 337 * 1024);
  const rsrc_t ra = mk_rsrc(ws + OFF_ACT + ((size_t)(mt * 4 + sh2) * 112 + 14 * w) * 1024, 238 * 1024);
  u32x4 af[28];
  auto ldact = [&](auto kI) {
    constexpr int k = decltype(kI)::value;
    af[k] = __builtin_amdgcn_raw_buffer_load_b128(ra, lane * 16 + (k % 14) * 1024, (k / 14) * 224 * 1024, 16);
  };
  float c1[2], c2[2], var[2], pe[2], pm[2], pr[2];
  u32x4 ov[2];
  auto corr = [&](auto iI) {
    constexpr int i = decltype(iI)::value;
    u64 g[3];
#pragma unroll
    for (int k = 0; k < 3; ++k) g[k] = CS[k * 128 + i * 32 + n];
    const u32x4 bext = {(u32)g[0], (u32)(g[0] >> 32), (u32)g[1], 0u};
    acc[i] = mfma32(aext, __builtin_bit_cast(bf16x8, bext), acc[i]);
    c1[i & 1] = __builtin_bit_cast(float, (u32)(g[2] >> 32));
    c2[i & 1] = __builtin_bit_cast(float, (u32)g[2]);
    var[i & 1] = __builtin_bit_cast(float, (u32)(g[1] >> 32));
  };
  auto slice = [&](auto iI, auto uI) {
    constexpr int i = decltype(iI)::value, u = decltype(uI)::value, z = i & 1;
    if constexpr (u >= 1) {
      float o = pm[z] * __builtin_amdgcn_rcpf(__builtin_fmaf(pe[z], var[z], var[z]));
      if constexpr ((u - 1) & 1) {
        u32 t = umin32(pack_bf16(pr[z], o));
        asm volatile("" : "+v"(t));
        ov[z][(u - 1) / 2] = t;
      } else {
        asm volatile("" : "+v"(o));
        pr[z] = o;
      }
    }
    if constexpr (u < 8) {
      const float a = acc[i][u];
      float e = __builtin_amdgcn_exp2f(__builtin_fmaf(a * a, c2[z], c1[z]) * a), m = a * acc[i][u + 8];
      asm volatile("" : "+v"(e), "+v"(m));
      pe[z] = e;
      pm[z] = m;
    } else {
      __builtin_amdgcn_raw_buffer_store_b128(ov[z], rw, lane * 16 + i * 112 * 1024, 0, 16);
      asm volatile("s_nop 1");
    }
  };
  constexpr int KG = EXP_KG;
  static_for<0, 28>([&](auto kI) {
    constexpr int k = decltype(kI)::value;
    if constexpr (k == KG) {
      u64 t = gv, p0 = gp0, p1 = gp1;
      asm volatile("s_waitcnt vmcnt(%3)" : "+v"(t), "+v"(p0), "+v"(p1) : "n"(KG) : "memory");
      if (!__all((t >> 32) != 0 && (u32)p0 != ~0u && (u32)p1 != ~0u)) {
        for (int spin = 0; spin < (1 << 16); ++spin) {
          __builtin_amdgcn_s_sleep(2);
          t = __hip_atomic_load((gu64*)(size_t)gbase + lane, RLX_AGENT);
          p0 = __hip_atomic_load((gu64*)(size_t)vbase + n, RLX_AGENT);
          p1 = __hip_atomic_load((gu64*)(size_t)vbase + 3584 + n, RLX_AGENT);
          if (__all((t >> 32) != 0 && (u32)p0 != ~0u && (u32)p1 != ~0u)) break;
        }
        __builtin_amdgcn_s_waitcnt(0x0F70);
      }
      CS[gsl * 64 + lane] = t;
      const float v1 = __builtin_bit_cast(float, (u32)p0) + __builtin_bit_cast(float, (u32)p1);
      const float v2 = __builtin_bit_cast(float, (u32)(p0 >> 32)) + __builtin_bit_cast(float, (u32)(p1 >> 32));
      const float v1h = round_bf16(v1), v2h = round_bf16(v2);
      u32x4 a = {pack_bf16(v1h, v1h), pack_bf16(v1 - v1h, v2h), pack_bf16(v2h, v2 - v2h), 0u};
      if (h) a = u32x4{0u, 0u, 0u, 0u};
      asm volatile("" : "+v"(a));
      aext = a;
      wait_lgkm0();
      __builtin_amdgcn_sched_barrier(0);
      bar();
      corr(I0{});
      corr(I1{});
      __builtin_amdgcn_sched_barrier(0);
    }
    acc[2] = mfma32(wf[k], xr[(28 + k) % PF][0], acc[2]);
    pin(acc[2]);
    __builtin_amdgcn_sched_barrier(0);
    if constexpr (k + PF - 1 < 28) rdx(std::integral_constant<int, 28 + k + PF - 1>{}, I0{});
    if constexpr (k > KG && k <= KG + 9) slice(I0{}, std::integral_constant<int, k - KG - 1>{});
    __builtin_amdgcn_sched_barrier(0);
    acc[3] = mfma32(wf[k], xr[(28 + k) % PF][1], acc[3]);
    pin(acc[3]);
    __builtin_amdgcn_sched_barrier(0);
    if constexpr (k % EXP_LOCK == EXP_LOCK - 1) bar();
    if constexpr (k + PF - 1 < 28) rdx(std::integral_constant<int, 28 + k + PF - 1>{}, I1{});
    if constexpr (k > KG && k <= KG + 9) slice(I1{}, std::integral_constant<int, k - KG - 1>{});
    if constexpr (WE + k < 28) ldwd(std::integral_constant<int, WE + k>{});
    else if constexpr (WE + k - 28 < 7) ldact(std::integral_constant<int, WE + k - 28>{});
    __builtin_amdgcn_sched_barrier(0);
  });
  YSTAMP(3);
  constexpr int A0 = WE < 7 ? WE : 7;
  corr(I2{});
  corr(I3{});
  __builtin_amdgcn_sched_barrier(0);
  static_for<0, 9>([&](auto uI) {
    constexpr int u = decltype(uI)::value;
    slice(I2{}, uI);
    slice(I3{}, uI);
    if constexpr (A0 + u < 14 && u < 8) ldact(std::integral_constant<int, A0 + u>{});
    __builtin_amdgcn_sched_barrier(0);
  });
  static_for<(A0 + 8 < 14 ? A0 + 8 : 14), 14>(ldact);
  bar();
  YSTAMP(4);
  auto chk = [&](auto gI) {
    constexpr int g = decltype(gI)::value;
    u32 m = 0;
#pragma unroll
    for (int k = 7 * g; k < 7 * g + 7; ++k) m = umax3(umax3(m, af[k][0], af[k][1]), af[k][2], af[k][3]);
    return (bool)__all(m != ~0u);
  };
  auto valid = [&](auto gI) {
    constexpr int g = decltype(gI)::value;
    if (__builtin_expect(!chk(gI), 0)) {
#pragma nounroll
      for (int spin = 0; spin < (1 << 14); ++spin) {
        __builtin_amdgcn_s_sleep(8);
        asm volatile("" ::: "memory");
        static_for<7 * g, 7 * g + 7>(ldact);
        if (chk(gI)) break;
      }
      __builtin_amdgcn_s_waitcnt(0x0F70);
    }
    __builtin_amdgcn_sched_barrier(0);
  };
  const int pcol = mt * 128 + sh2 * 32 + n;
  const int dbase = dt * 64 + w * 8 + 4 * h;
  __builtin_amdgcn_sched_barrier(0);
  LAS f32x4* R = (LAS f32x4*)smem;
  f32x16 o[4];
#pragma unroll
  for (int i = 0; i < 4; ++i)
#pragma unroll
    for (int r = 0; r < 16; ++r) o[i][r] = 0.f;
  auto dump1 = [&](auto tjI, auto cI) {
    constexpr int tj = decltype(tjI)::value, c = decltype(cI)::value, i = tj * 2 + (c >> 2), q = c & 3;
    R[((tj * 8 + c) * 8 + w) * 64 + lane] = f32x4{o[i][4 * q], o[i][4 * q + 1], o[i][4 * q + 2], o[i][4 * q + 3]};
  };
  auto step = [&](auto kI) {
    constexpr int k = decltype(kI)::value, tj = k / 14;
    const bf16x8 b = __builtin_bit_cast(bf16x8, af[k]);
    o[2 * tj] = mfma32(wd[k % 14], b, o[2 * tj]);
    pin(o[2 * tj]);
    __builtin_amdgcn_sched_barrier(0);
    if constexpr (k >= 15 && k < 19) dump1(I0{}, std::integral_constant<int, 2 * (k - 15)>{});
    __builtin_amdgcn_sched_barrier(0);
    o[2 * tj + 1] = mfma32(wd[14 + k % 14], b, o[2 * tj + 1]);
    pin(o[2 * tj + 1]);
    __builtin_amdgcn_sched_barrier(0);
    if constexpr (k >= 15 && k < 19) dump1(I0{}, std::integral_constant<int, 2 * (k - 15) + 1>{});
    if constexpr (k >= EXP_T1 && k < 14) {
      constexpr int n0 = ((k - EXP_T1) * 14) / (14 - EXP_T1), n1 = ((k + 1 - EXP_T1) * 14) / (14 - EXP_T1);
      static_for<14 + n0, 14 + n1>(ldact);
    }
    __builtin_amdgcn_sched_barrier(0);
  };
  u16 xres[2][4];
  f32x4 gbv;
  auto fin = [&](auto tjI) {
    constexpr int tj = decltype(tjI)::value;
    f32x4 v[8];
#pragma unroll
    for (int sw = 0; sw < 8; ++sw) v[sw] = R[((tj * 8 + w) * 8 + sw) * 64 + lane];
    const f32x4 sum = ((v[0] + v[1]) + (v[2] + v[3])) + ((v[4] + v[5]) + (v[6] + v[7]));
    float r[4];
#pragma unroll
    for (int e = 0; e < 4; ++e) r[e] = __builtin_fmaf(gbv[e], sum[e], bf2f(xres[tj][e]));
    const int pc = pcol + 64 * tj;
    const int srow = (pc & ~7) | ((pc & 3) << 1) | ((pc >> 2) & 1);
    *(u32x2*)(out + (size_t)srow * D + dbase) = u32x2{pack_bf16(r[0], r[1]), pack_bf16(r[2], r[3])};
  };
  valid(I0{});
  asm volatile(""
               : "+v"(wd[0]), "+v"(wd[1]), "+v"(wd[2]), "+v"(wd[3]), "+v"(wd[4]), "+v"(wd[5]), "+v"(wd[6]), "+v"(wd[7]),
                 "+v"(wd[8]), "+v"(wd[9]), "+v"(wd[10]), "+v"(wd[11]), "+v"(wd[12]), "+v"(wd[13])::"memory");
  asm volatile(""
               : "+v"(wd[14]), "+v"(wd[15]), "+v"(wd[16]), "+v"(wd[17]), "+v"(wd[18]), "+v"(wd[19]), "+v"(wd[20]),
                 "+v"(wd[21]), "+v"(wd[22]), "+v"(wd[23]), "+v"(wd[24]), "+v"(wd[25]), "+v"(wd[26]), "+v"(wd[27])::"memory");
  YSTAMP(5);
  static_for<0, 3>(step);
  {
    const rsrc_t rx = mk_rsrc(ws + OFF_X1T, D * S * 2);
#pragma unroll
    for (int r = 0; r < 8; ++r)
      xres[r >> 2][r & 3] = __builtin_amdgcn_raw_buffer_load_b16(rx, ((dbase + (r & 3)) * S + pcol + (r >> 2) * 64) * 2, 0, 16);
    gbv = *(const f32x4*)((const float*)(ws + OFF_MOD) + 5 * D + dbase);
    __builtin_amdgcn_sched_barrier(0);
  }
  static_for<3, 7>(step);
  valid(I1{});
  static_for<7, 14>(step);
  valid(I2{});
  static_for<14, 21>(step);
  wait_lgkm0();
  bar();
  valid(I3{});
  static_for<21, 24>(step);
  fin(I0{});
  __builtin_amdgcn_sched_barrier(0);
  static_for<24, 28>(step);
  static_for<0, 8>([&](auto cI) { dump1(I1{}, cI); });
  wait_lgkm0();
  bar();
  STAMP(ws, 2, 6);
  fin(I1{});
}

extern "C" __global__ NOPK __launch_bounds__(512, 2) void k1_main(const u16* __restrict__ ssm_k,
                                                                  const u16* __restrict__ ssm_D, char* __restrict__ ws,
                                                                  u16* __restrict__ out) {
  extern __shared__ __attribute__((aligned(16))) char smem[];
  const int w = __builtin_amdgcn_readfirstlane(threadIdx.x >> 6);
  if (blockIdx.x >= 224) {
    if ((blockIdx.x & 7) < 7) {
      u32x4 dummy[28];
      Stamps2 s2 = {};
      conv_body<false>(ssm_k, ssm_D, ws, smem, w, dummy, nullptr, s2);
    } else {
      __builtin_amdgcn_s_sleep(100);
    }
    k2_stats(ws, smem, blockIdx.x - 224);
  } else {
    k2_gemm(ssm_k, ssm_D, ws, out, smem, w);
    if (blockIdx.x == 0 && threadIdx.x < 64) {
      const unsigned long long* E = (const unsigned long long*)(ws + WS_END);
      unsigned long long smin = ~0ull, emax = 0;
      for (int i = threadIdx.x; i < 504; i += 64) {
        const int x = i & 7, id = i >> 3;
        smin = E[i] < smin ? E[i] : smin;
        const unsigned long long e = E[512 + i];
        if ((x == 99) && e > emax) emax = e;
      }
      for (int o = 1; o < 64; o <<= 1) {
        unsigned long long a = __shfl_xor(smin, o), c2 = __shfl_xor(emax, o);
        smin = a < smin ? a : smin; emax = c2 > emax ? c2 : emax;
      }
      const unsigned long long now = __builtin_amdgcn_s_memrealtime();
      unsigned long long d = emax ? 4 * (emax - smin) : 0;
      if (d > 20000) d = 20000;
      while (__builtin_amdgcn_s_memrealtime() - now < d) __builtin_amdgcn_s_sleep(1);
    }
  }
}

extern "C" __device__ __attribute__((used, noinline)) void k_code_end() {}

extern "C" void kernel_launch(void* const* d_in, const int* in_sizes, int n_in, void* d_out, int out_size, void* d_ws,
                              size_t ws_size, hipStream_t stream) {
  const u16* q = (const u16*)d_in[0];
  const u16* k = (const u16*)d_in[1];
  const u16* v = (const u16*)d_in[2];
  const u16* shift_k = (const u16*)d_in[3];
  const u16* ssm_k = (const u16*)d_in[4];
  const u16* ssm_D = (const u16*)d_in[5];
  const u16* w_gate = (const u16*)d_in[7];
  const u16* w_up = (const u16*)d_in[8];
  const u16* w_down = (const u16*)d_in[9];
  const u16* c = (const u16*)d_in[10];
  const u16* w_mod = (const u16*)d_in[11];
  char* ws = (char*)d_ws;
  (void)hipFuncSetAttribute((const void*)k1_main, hipFuncAttributeMaxDynamicSharedMemorySize, K2_LDS);
  k0_prep<<<K0_GRID, 256, 0, stream>>>(q, k, v, shift_k, c, w_mod, ssm_k, w_gate, w_up, w_down, ws);
  k1_main<<<256, 512, K2_LDS, stream>>>(ssm_k, ssm_D, ws, (u16*)d_out);
}
```

```cpp
#include <hip/hip_runtime.h>
#include <hip/hip_bf16.h>
#include <stdint.h>
#include <type_traits>

typedef unsigned short u16;
typedef unsigned int u32;
typedef short bf16x8 __attribute__((ext_vector_type(8)));
typedef short s16x4 __attribute__((ext_vector_type(4)));
typedef float f32x2 __attribute__((ext_vector_type(2)));
typedef float f32x4 __attribute__((ext_vector_type(4)));
typedef float f32x16 __attribute__((ext_vector_type(16)));
typedef u32 u32x2 __attribute__((ext_vector_type(2)));
typedef u32 u32x4 __attribute__((ext_vector_type(4)));
typedef __bf16 bf16x2_t __attribute__((ext_vector_type(2)));
#define LAS __attribute__((address_space(3)))
#define DI __device__ __forceinline__

constexpr int S = 2048, D = 448, F = 1792;

constexpr size_t SZ_DS = (size_t)D * S * 2;
constexpr size_t OFF_RE = 0;
constexpr size_t OFF_RO = OFF_RE + SZ_DS;
constexpr size_t OFF_QT = OFF_RO + SZ_DS;
constexpr size_t OFF_X1T = OFF_QT + SZ_DS;
constexpr size_t OFF_MEAN = OFF_X1T + SZ_DS;
constexpr size_t OFF_RSTD = OFF_MEAN + S * 4;
constexpr size_t OFF_MOD = OFF_RSTD + S * 4;
constexpr size_t OFF_SG = OFF_MOD + 16384;
constexpr size_t OFF_CB = OFF_MOD + 12288;

constexpr size_t OFF_VG = OFF_SG + 3 * S * 8;
constexpr size_t OFF_XS = OFF_VG + 4 * S * 8;
constexpr size_t OFF_WGU = OFF_XS + SZ_DS;
constexpr size_t OFF_WD = OFF_WGU + (size_t)2 * D * F * 2;
constexpr size_t OFF_ACT = OFF_WD + (size_t)D * F * 2;

#if defined(__HIP_DEVICE_COMPILE__)
#define NOPK __attribute__((target("no-packed-fp32-ops")))
#else
#define NOPK
#endif

template <int I, int N, class Fn>
DI void static_for(Fn&& f) {
  if constexpr (I < N) {
    f(std::integral_constant<int, I>{});
    static_for<I + 1, N>(f);
  }
}

DI float lo_f(u32 x) { return __builtin_bit_cast(float, x << 16); }
DI float hi_f(u32 x) { return __builtin_bit_cast(float, x & 0xffff0000u); }
DI u32 pack_bf16(float a, float b) {
  f32x2 v = {a, b};
  return __builtin_bit_cast(u32, __builtin_convertvector(v, bf16x2_t));
}
DI float round_bf16(float a) { return lo_f(pack_bf16(a, 0.f)); }
DI float bf2f(u16 x) { return __builtin_bit_cast(float, (u32)x << 16); }

template <int N>
DI void wait_vm() { asm volatile("s_waitcnt vmcnt(%0)" ::"n"(N) : "memory"); }
DI void wait_lgkm0() { asm volatile("s_waitcnt lgkmcnt(0)" ::: "memory"); }
DI void bar() { __builtin_amdgcn_s_barrier(); }

DI const void* uni(const void* p) {
  const unsigned long long v = (unsigned long long)p;
  const u32 lo = __builtin_amdgcn_readfirstlane((u32)v), hi = __builtin_amdgcn_readfirstlane((u32)(v >> 32));
  return (const void*)(((unsigned long long)hi << 32) | lo);
}
DI void glds16(const void* g, char* lds) {
  __builtin_amdgcn_global_load_lds((const __attribute__((address_space(1))) void*)g, (LAS void*)lds, 16, 0, 0);
}

template <int CTRL>
DI float dpp_add(float v) {
  const int x = __builtin_bit_cast(int, v);
  return v + __builtin_bit_cast(float, __builtin_amdgcn_update_dpp(x, x, CTRL, 0xf, 0xf, false));
}
DI float sum8(float v) {
  v = dpp_add<0xB1>(v);
  v = dpp_add<0x4E>(v);
  v = dpp_add<0x141>(v);
  return v;
}
DI float dot2(u32 a, u32 b, float c) {
  return __builtin_amdgcn_fdot2_f32_bf16(__builtin_bit_cast(bf16x2_t, a), __builtin_bit_cast(bf16x2_t, b), c, false);
}
typedef __amdgpu_buffer_rsrc_t rsrc_t;
DI rsrc_t mk_rsrc(const void* p, u32 bytes) { return __builtin_amdgcn_make_buffer_rsrc((void*)p, 0, bytes, 0x00020000); }
DI void st16_sc1(void* p, u32x4 v) { asm volatile("global_store_dwordx4 %0, %1, off sc1\n\ts_nop 1" ::"v"(p), "v"(v) : "memory"); }
DI void st8_sc1(void* p, u32x2 v) { asm volatile("global_store_dwordx2 %0, %1, off sc1" ::"v"(p), "v"(v) : "memory"); }

DI bf16x8 tr_frag(const char* smem, int o0, int o1) {
  s16x4 x0 = __builtin_amdgcn_ds_read_tr16_b64_v4i16((LAS s16x4*)(smem + o0));
  s16x4 x1 = __builtin_amdgcn_ds_read_tr16_b64_v4i16((LAS s16x4*)(smem + o1));
  return __builtin_shufflevector(x0, x1, 0, 1, 2, 3, 4, 5, 6, 7);
}
DI f32x16 mfma32(u32x4 a, bf16x8 b, f32x16 c) {
  return __builtin_amdgcn_mfma_f32_32x32x16_bf16(__builtin_bit_cast(bf16x8, a), b, c, 0, 0, 0);
}

DI void retile(int task, const u16* __restrict__ w_gate, const u16* __restrict__ w_up, const u16* __restrict__ w_down,
               char* __restrict__ ws, char* smem, int w, int lane) {
  const int xr = lane >> 4, slot = lane & 15;
  const int chn = slot ^ ((xr << 2) | (w & 3));
  const int h = lane >> 5, blk = (lane >> 4) & 1, qq = (lane & 15) >> 2, p = lane & 3;
  const bool gu = task < 112;
  int kq = 0, fg = 0, dg = 0, fk = 0;
  if (gu) {
    fg = task % 28, kq = task / 28;
    const u16* src = (chn < 8 ? w_gate : w_up) + (size_t)(kq * 112 + 4 * w + xr) * F + fg * 64 + (chn & 7) * 8;
#pragma unroll
    for (int i = 0; i < 7; ++i) glds16(src + (size_t)i * 16 * F, smem + (w + 4 * i) * 1024);
  } else {
    dg = (task - 112) % 7, fk = (task - 112) / 7;
    const u16* src = w_down + (size_t)(fk * 224 + (chn >> 3) * 112 + 4 * w + xr) * D + dg * 64 + (chn & 7) * 8;
#pragma unroll
    for (int i = 0; i < 7; ++i) glds16(src + (size_t)i * 16 * D, smem + (w + 4 * i) * 1024);
  }
  const int col = gu ? blk * 128 + w * 32 + (p & 1) * 16 + (p >> 1) * 8 : (w >> 1) * 128 + (w & 1) * 64 + blk * 32 + p * 8;
  int o[2];
#pragma unroll
  for (int t = 0; t < 2; ++t)
    o[t] = 256 * (8 * h + 4 * t + qq) + 16 * ((col >> 4) ^ ((qq << 2) | ((2 * h + t) & 3))) + (col & 8);
  u32x4* dst = gu ? (u32x4*)(ws + OFF_WGU) + ((size_t)(fg * 4 + w) * 28 + kq * 7) * 64 + lane
                  : (u32x4*)(ws + OFF_WD) + ((size_t)(dg * 2 + (w & 1)) * 112 + fk * 14 + (w >> 1) * 7) * 64 + lane;
  __syncthreads();
#pragma unroll
  for (int i = 0; i < 7; ++i)
    st16_sc1(dst + i * 64, __builtin_bit_cast(u32x4, tr_frag(smem, o[0] + i * 4096, o[1] + i * 4096)));
}

constexpr int K0_GRID = 504;

extern "C" __global__ __launch_bounds__(256) void k0_prep(const u16* __restrict__ q, const u16* __restrict__ k,
                                               const u16* __restrict__ v, const u16* __restrict__ shift_k,
                                               const u16* __restrict__ c, const u16* __restrict__ w_mod,
                                               const u16* __restrict__ w_gate,
                                               const u16* __restrict__ w_up, const u16* __restrict__ w_down,
                                               char* __restrict__ ws) {
  __shared__ __attribute__((aligned(16))) char smem[28672];
  float(*red)[64] = (float(*)[64])smem;
  const int b = blockIdx.x, tid = threadIdx.x, lane = tid & 63, w = __builtin_amdgcn_readfirstlane(tid >> 6);
  const int xcd = b & 7, idx = b >> 3;
  u32 cwarm = 0;
  if (idx < 2) {
    u32 lo, hi, len;
    asm volatile(
        "s_getpc_b64 s[40:41]\n\ts_add_u32 s40, s40, k0_prep@rel32@lo+4\n\ts_addc_u32 s41, s41, k0_prep@rel32@hi+12\n\t"
        "s_mov_b32 %0, s40\n\ts_mov_b32 %1, s41\n\ts_mov_b32 %2, k_code_end-k0_prep"
        : "=s"(lo), "=s"(hi), "=s"(len)::"s40", "s41", "scc");
    const char* code = (const char*)(((unsigned long long)hi << 32) | lo);
    if ((u32)(idx * 256 + tid) * 128 < len) cwarm = *(const __attribute__((address_space(1))) u32*)(size_t)(code + (idx * 256 + tid) * 128);
  }
  asm volatile("" ::"s"(q), "s"(k), "s"(v), "s"(shift_k), "s"(c), "s"(w_mod), "s"(w_gate), "s"(w_up), "s"(w_down),
               "s"(ws));
  if (__builtin_expect(idx >= 42, 0)) {
    retile(b - 336, w_gate, w_up, w_down, ws, smem, w, lane);
    return;
  }
  int misc = -1;
  if (idx < 6) misc = 64 + b;
  else if (idx >= 34) misc = xcd * 8 + (idx - 34);
  const int rb = xcd * 4 + ((idx - 6) & 3), cg = (idx - 6) >> 2;
  if (__builtin_expect(misc < 0, 1)) {
    const int sq = lane >> 5, cp = lane & 31;
    const int d0 = cg * 64 + cp * 2;
    const int a = rb * 8 + w * 2 + sq;
    const int s0 = a * 8;
    const rsrc_t rk = mk_rsrc(k, S * D * 2), rv = mk_rsrc(v, S * D * 2), rq = mk_rsrc(q, S * D * 2);
    const int oB = (s0 * D + d0) * 2;
    const int oA = oB - 4 * D * 2, oC = oB + 4 * D * 2;
    u32 kk[12], vv[9], qq[8];
#pragma unroll
    for (int i = 0; i < 4; ++i) kk[i] = __builtin_amdgcn_raw_buffer_load_b32(rk, oA + i * D * 2, 0, 0);
#pragma unroll
    for (int i = 0; i < 4; ++i) kk[4 + i] = __builtin_amdgcn_raw_buffer_load_b32(rk, oB + i * D * 2, 0, 0);
#pragma unroll
    for (int i = 0; i < 4; ++i) kk[8 + i] = __builtin_amdgcn_raw_buffer_load_b32(rk, oC + i * D * 2, 0, 0);
    vv[0] = __builtin_amdgcn_raw_buffer_load_b32(rv, oA + 3 * D * 2, 0, 0);
#pragma unroll
    for (int i = 0; i < 4; ++i) vv[1 + i] = __builtin_amdgcn_raw_buffer_load_b32(rv, oB + i * D * 2, 0, 0);
#pragma unroll
    for (int i = 0; i < 4; ++i) vv[5 + i] = __builtin_amdgcn_raw_buffer_load_b32(rv, oC + i * D * 2, 0, 0);
#pragma unroll
    for (int i = 0; i < 4; ++i) qq[i] = __builtin_amdgcn_raw_buffer_load_b32(rq, oB + i * D * 2, 0, 0);
#pragma unroll
    for (int i = 0; i < 4; ++i) qq[4 + i] = __builtin_amdgcn_raw_buffer_load_b32(rq, oC + i * D * 2, 0, 0);
    const u32x4 sk = *(const u32x4*)(shift_k + d0 * 4);
    f32x2 wt[4] = {{lo_f(sk[0]), lo_f(sk[2])}, {hi_f(sk[0]), hi_f(sk[2])}, {lo_f(sk[1]), lo_f(sk[3])}, {hi_f(sk[1]), hi_f(sk[3])}};
    f32x2 kf[12], u[9];
#pragma unroll
    for (int i = 0; i < 12; ++i) kf[i] = f32x2{lo_f(kk[i]), hi_f(kk[i])};
#pragma unroll
    for (int i = 0; i < 9; ++i) {
      f32x2 t = kf[i + 3] * wt[0];
      t = __builtin_elementwise_fma(kf[i + 2], wt[1], t);
      t = __builtin_elementwise_fma(kf[i + 1], wt[2], t);
      t = __builtin_elementwise_fma(kf[i], wt[3], t);
      u[i] = t * f32x2{lo_f(vv[i]), hi_f(vv[i])};
    }
    {
      char* dst = ws + (size_t)d0 * (S * 2) + (2040 - s0) * 2;
#pragma unroll
      for (int cI = 0; cI < 2; ++cI) {
        *(u32x4*)(dst + OFF_RO + cI * S * 2) = u32x4{pack_bf16(u[8][cI], u[7][cI]), pack_bf16(u[6][cI], u[5][cI]),
                                              pack_bf16(u[4][cI], u[3][cI]), pack_bf16(u[2][cI], u[1][cI])};
        *(u32x4*)(dst + OFF_RE + cI * S * 2) = u32x4{pack_bf16(u[7][cI], u[6][cI]), pack_bf16(u[5][cI], u[4][cI]),
                                              pack_bf16(u[3][cI], u[2][cI]), pack_bf16(u[1][cI], u[0][cI])};
      }
      char* dq = ws + OFF_QT + (size_t)d0 * (S * 2) + s0 * 2;
#pragma unroll
      for (int cI = 0; cI < 2; ++cI) {
        const u32 sel = cI ? 0x07060302u : 0x05040100u;
        *(u32x4*)(dq + cI * S * 2) = u32x4{__builtin_amdgcn_perm(qq[2], qq[0], sel), __builtin_amdgcn_perm(qq[6], qq[4], sel),
                                    __builtin_amdgcn_perm(qq[3], qq[1], sel), __builtin_amdgcn_perm(qq[7], qq[5], sel)};
      }
    }
  } else if (misc < 64) {
    const int bb = misc;
    const int row = bb * 32 + w * 8 + (lane >> 3);
    const int sub = lane & 7;
    u32x4 x[7];
#pragma unroll
    for (int i = 0; i < 7; ++i) x[i] = *(const u32x4*)(q + (size_t)row * D + (i * 8 + sub) * 8);
    if (sub < 7) ((unsigned long long*)(ws + OFF_SG))[sub * S + row] = sub < 3 ? 0ull : ~0ull;
    float s = 0.f, s2 = 0.f;
#pragma unroll
    for (int i = 0; i < 7; ++i)
#pragma unroll
      for (int j = 0; j < 4; ++j) {
        const u32 t = x[i][j];
        s = dot2(t, 0x3f803f80u, s);
        s2 = dot2(t, t, s2);
      }
    s = sum8(s);
    s2 = sum8(s2);
    const float mean = s * (1.f / D);
    float var = s2 * (1.f / D) - mean * mean;
    var = var < 0.f ? 0.f : var;
    const float rstd = __builtin_amdgcn_rsqf(var + 1e-5f);
    if (sub < 2) {
      const int p = (row & ~7) | ((row & 1) << 2) | ((row >> 1) & 3);
      ((float*)(ws + OFF_MEAN))[sub * S + p] = sub ? rstd : mean;
    }
    {
      const u32x4 sent = {~0u, ~0u, ~0u, ~0u};
#pragma unroll
      for (int i = 0; i < 14; ++i)
        st16_sc1((u32x4*)(ws + (i < 7 ? OFF_X1T : OFF_XS)) + (size_t)bb * 1792 + (i % 7) * 256 + tid, sent);
    }
  } else if (misc < 106) {
    const int bb = misc - 64;
    const int co = lane & 7, rs = lane >> 3;
    const int col0 = bb * 64 + co * 8;
    u32x4 x[14];
#pragma unroll
    for (int i = 0; i < 14; ++i)
      x[i] = *(const u32x4*)(w_mod + (size_t)(w * 112 + rs * 14 + i) * (6 * D) + col0);
    u32 cv[7];
#pragma unroll
    for (int i = 0; i < 7; ++i) cv[i] = *(const u32*)(c + w * 112 + rs * 14 + 2 * i);
    __builtin_amdgcn_sched_barrier(0);
    f32x2 acc[4] = {{0.f, 0.f}, {0.f, 0.f}, {0.f, 0.f}, {0.f, 0.f}};
#pragma unroll
    for (int i = 0; i < 14; ++i) {
      const float cf = (i & 1) ? hi_f(cv[i >> 1]) : lo_f(cv[i >> 1]);
      const float sl = round_bf16(cf * __builtin_amdgcn_rcpf(1.f + __builtin_amdgcn_exp2f(cf * -1.44269504f)));
#pragma unroll
      for (int j = 0; j < 4; ++j)
        acc[j] = __builtin_elementwise_fma(f32x2{lo_f(x[i][j]), hi_f(x[i][j])}, f32x2{sl, sl}, acc[j]);
    }
    *(f32x4*)&red[w * 8 + rs][co * 8] = f32x4{acc[0][0], acc[0][1], acc[1][0], acc[1][1]};
    *(f32x4*)&red[w * 8 + rs][co * 8 + 4] = f32x4{acc[2][0], acc[2][1], acc[3][0], acc[3][1]};
    __syncthreads();
    {
      float t = 0.f;
#pragma unroll
      for (int i = 0; i < 8; ++i) t += red[w * 8 + i][lane];
      red[32 + w][lane] = t;
    }
    __syncthreads();
    if (tid < 64) {
      const float t = (red[32][tid] + red[33][tid]) + (red[34][tid] + red[35][tid]);
      const float m = round_bf16(t);
      const int col = bb * 64 + tid;
      ((float*)(ws + OFF_MOD))[col] = m;
      if (col >= 3 * D && col < 5 * D) {
        const bool isc = col >= 4 * D;
        ((u16*)(ws + OFF_CB))[isc ? col - 4 * D : col - 2 * D] = (u16)pack_bf16(isc ? 1.f + m : m, 0.f);
      }
    }
  }
  asm volatile("" ::"v"(cwarm));
}

constexpr int K1_KSTR = 260 * 16;
constexpr int K1_USTR = 286 * 16;
constexpr int K1_UOFF = 4 * K1_KSTR;
constexpr int K1_XOFF = K1_UOFF + 2 * K1_USTR;
constexpr int K1_CH = K1_XOFF + 32768;

DI void glds16x4_asm(const void* sbase, u32 voff, u32 voff_last, u32 lds_addr) {
  u32 keep;
  sbase = uni(sbase);
  lds_addr = __builtin_amdgcn_readfirstlane(lds_addr);
  asm volatile(
      "s_mov_b32 %0, m0\n\ts_mov_b32 m0, %4\n\ts_nop 0\n\t"
      "global_load_lds_dwordx4 %1, %3\n\t"
      "global_load_lds_dwordx4 %1, %3 offset:1024\n\t"
      "global_load_lds_dwordx4 %1, %3 offset:2048\n\t"
      "global_load_lds_dwordx4 %2, %3 offset:3072\n\t"
      "s_mov_b32 m0, %0"
      : "=&s"(keep)
      : "v"(voff), "v"(voff_last), "s"(sbase), "s"(lds_addr)
      : "memory");
}
#define CAS __attribute__((address_space(4)))
constexpr int K2_XB = D * 256;
constexpr int K2_XH = D * 128;
constexpr int K2_ST = K2_XB;
constexpr int K2_LDS = 131072;

DI u32 umin32(u32 v) { return v < 0xFFFFFFFEu ? v : 0xFFFFFFFEu; }
DI u32 umax3(u32 a, u32 b, u32 c) { const u32 t = a > b ? a : b; return t > c ? t : c; }
template <int OFF>
DI void gload16_off(u32x4& dst, const void* sbase, u32 voff) {
  sbase = uni(sbase);
  asm volatile("global_load_dwordx4 %0, %1, %2 offset:%3" : "=v"(dst) : "v"(voff), "s"(sbase), "n"(OFF) : "memory");
}
DI void glds16_nom0(const void* sbase, u32 voff, u32 lds_addr) {
  sbase = uni(sbase);
  lds_addr = __builtin_amdgcn_readfirstlane(lds_addr);
  asm volatile("s_mov_b32 m0, %2\n\ts_nop 0\n\tglobal_load_lds_dwordx4 %0, %1" ::"v"(voff), "s"(sbase), "s"(lds_addr) : "memory");
}
DI void glds16_sc1(const void* sbase, u32 voff, u32 lds_addr) {
  sbase = uni(sbase);
  lds_addr = __builtin_amdgcn_readfirstlane(lds_addr);
  asm volatile("s_mov_b32 m0, %2\n\ts_nop 0\n\tglobal_load_lds_dwordx4 %0, %1 sc1" ::"v"(voff), "s"(sbase), "s"(lds_addr) : "memory");
}
typedef unsigned long long u64;
typedef __attribute__((address_space(1))) u64 gu64;
#define RLX_AGENT __ATOMIC_RELAXED, __HIP_MEMORY_SCOPE_AGENT
constexpr int NW0 = 5, NW1 = 5;
constexpr int NWP_ = 3, NWH_ = 15;
constexpr int KF_ = 25, KG_ = 1;
constexpr int T1 = 5;
constexpr int LOCKSTEP = 4;
constexpr int AUX_SLEEP = 50;
constexpr int NWC = NW0 + NW1;
template <bool WF>
DI void conv_body(const u16* __restrict__ ssm_k, const u16* __restrict__ ssm_D, char* __restrict__ ws, char* smem,
                  const int w, u32x4 (&wf)[28], const char* wbase) {
  const int lane = threadIdx.x & 63;
  const int ch = w >> 2, q = w & 3;
  const int cpair = blockIdx.x;
  const int d = 2 * cpair + ch;
  char* L = smem + ch * K1_CH;
  const u32 L0 = (u32)(size_t)(LAS char*)L;
  const u16* kf = ssm_k + (size_t)d * S;
  const u32 vo = lane * 16;
  {
    const u32 vl = (lane > 62 ? 62 : lane) * 16;
    glds16x4_asm(kf + (2 + 2 * q), vo, vl, L0 + q * K1_KSTR + 64);
    const char* ub = ws + ((q >> 1) ? OFF_RO : OFF_RE) + (size_t)d * S * 2 + (q & 1) * 2048;
    const u32 ul = L0 + K1_UOFF + (q >> 1) * K1_USTR + (q & 1) * 2048;
    glds16_nom0(ub, vo, ul);
    glds16_nom0(ub + 1024, vo, ul + 1024);
  }
  const int n = lane & 15, q4 = lane >> 4;
  const int pbase = 32 * (n >> 1) + 8 * q4 + 4 * (n & 1) + 512 * q;
  const int di = n + q - 15;
  u32 head;
  asm volatile("global_load_dword %0, %1, %2" : "=v"(head) : "v"((u32)(di < 0 ? 0 : di) * 4), "s"(uni(kf)) : "memory");
  auto ldwf = [&](auto kI) {
    constexpr int k = decltype(kI)::value;
    gload16_off<(k % 4) * 1024>(wf[k], wbase + (k / 4) * 4096, vo);
  };
  bar();
  if constexpr (WF) static_for<0, NW0>(ldwf);
  const CAS float* MODc = (const CAS float*)(size_t)(ws + OFF_MOD);
  const float sa = MODc[d], ca = MODc[D + d], ga = MODc[2 * D + d], cbv = MODc[4 * D + d];
  const u32 Draw = ((const CAS u32*)(size_t)ssm_D)[cpair];
  if (lane < 14) *(LAS u32x4*)(L + K1_UOFF + (q >> 1) * K1_USTR + (256 + 14 * (q & 1) + lane) * 16) = u32x4{0u, 0u, 0u, 0u};
  u32 Ab = L0 + (n & 3) * K1_KSTR + ((n >> 2) + q4 + 8 * q) * 16;
  u32 Bb = L0 + K1_UOFF + (n & 1) * K1_USTR + (28 - 4 * (n >> 1) + q4 + 8 * q) * 16;
  u32 Hd = L0 + q * K1_KSTR + n * 4;
  asm volatile("" : "+v"(Ab), "+v"(Bb), "+v"(Hd) : "s"(sa), "s"(ca), "s"(ga), "s"(Draw), "s"(cbv));
  asm volatile("s_waitcnt vmcnt(%1)" : "+v"(head) : "n"(WF ? NW0 : 0) : "memory");
  *(LAS u32*)(size_t)Hd = di < 0 ? 0u : head;
  wait_lgkm0();
  bar();
  bf16x8 fa[8], fb[8];
  auto rd = [&](int dl) {
#pragma unroll
    for (int g = 0; g < 8; ++g) {
      fa[g] = *(const LAS bf16x8*)(size_t)(Ab + 64 * dl + 512 * g);
      fb[g] = *(const LAS bf16x8*)(size_t)(Bb + 64 * dl + 512 * (7 - g));
    }
  };
  rd(0);
  __builtin_amdgcn_sched_barrier(0);
  u32x2 qv[2];
  f32x4 mu[2], rs[2];
  asm volatile("global_load_dwordx2 %0, %6, %8\n\t"
               "global_load_dwordx2 %1, %6, %8 offset:512\n\t"
               "global_load_dwordx4 %2, %7, %9\n\t"
               "global_load_dwordx4 %3, %7, %9 offset:1024\n\t"
               "global_load_dwordx4 %4, %7, %10\n\t"
               "global_load_dwordx4 %5, %7, %10 offset:1024"
               : "=&v"(qv[0]), "=&v"(qv[1]), "=&v"(mu[0]), "=&v"(mu[1]), "=&v"(rs[0]), "=&v"(rs[1])
               : "v"((u32)pbase * 2), "v"((u32)pbase * 4), "s"(uni(ws + OFF_QT + (size_t)d * S * 2)),
                 "s"(uni(ws + OFF_MEAN)), "s"(uni(ws + OFF_RSTD))
               : "memory");
  const u32x4 sent = {~0u, ~0u, ~0u, ~0u};
  const void* fillb = uni(ws + OFF_ACT + (size_t)(cpair * 8 + w) * 4096);
  f32x4 acc[8];
#pragma unroll
  for (int j = 0; j < 8; ++j) acc[j] = f32x4{0.f, 0.f, 0.f, 0.f};
  static_for<0, 72>([&](auto mI) {
    constexpr int m = decltype(mI)::value, t = m % 36;
    constexpr int sd = t < 1 ? 0 : t < 3 ? 1 : t < 6 ? 2 : t < 10 ? 3 : t < 15 ? 4 : t < 21 ? 5 : t < 28 ? 6 : 7;
    constexpr int g = t - sd * (sd + 1) / 2;
    if constexpr (m == 36) {
      rd(1);
      __builtin_amdgcn_sched_barrier(0);
    }
    f32x4 a = __builtin_amdgcn_mfma_f32_16x16x32_bf16(fa[g], fb[sd - g], acc[sd], 0, 0, 0);
    asm volatile("" : "+v"(a));
    acc[sd] = a;
    if constexpr (m % 4 == 3) {
      constexpr int v = m / 4;
      if constexpr (v < 4) {
        u32x4 sv = sent;
        asm volatile("global_store_dwordx4 %0, %1, %2 offset:%3 sc1\n\ts_nop 1" ::"v"(vo), "v"(sv), "s"(fillb), "n"(v * 1024) : "memory");
      } else if constexpr (WF && v - 4 < NW1) {
        ldwf(std::integral_constant<int, NW0 + v - 4>{});
      }
      __builtin_amdgcn_sched_barrier(0);
    }
  });
  LAS f32x4* X = (LAS f32x4*)(L + K1_XOFF);
#pragma unroll
  for (int j = 0; j < 8; ++j) X[(j * 4 + q) * 64 + lane] = acc[j];
  u32x4 uc[2];
#pragma unroll
  for (int j = 0; j < 2; ++j)
    uc[j] = *(const LAS u32x4*)(L + K1_UOFF + K1_USTR + (255 - (32 * (2 * q + j) + 4 * (n >> 1) + q4)) * 16);
  asm volatile("s_waitcnt vmcnt(%6)"
               : "+v"(qv[0]), "+v"(qv[1]), "+v"(mu[0]), "+v"(mu[1]), "+v"(rs[0]), "+v"(rs[1])
               : "n"(WF ? NW1 : 0)
               : "memory");
  f32x4 qf[2], hg[2], kd[2];
  {
    const int sh = (n & 1) ? 16 : 0;
    const float gc = ga * round_bf16(1.f + ca), gs = ga * sa, Dd = ch ? hi_f(Draw) : lo_f(Draw);
#pragma unroll
    for (int j = 0; j < 2; ++j) {
#pragma unroll
      for (int r = 0; r < 4; ++r) {
        kd[j][r] = __builtin_bit_cast(float, (uc[j][3 - r] << sh) & 0xffff0000u) * Dd;
        qf[j][r] = (r & 1) ? hi_f(qv[j][r >> 1]) : lo_f(qv[j][r >> 1]);
        hg[j][r] = (qf[j][r] - mu[j][r]) * rs[j][r] * gc + gs;
      }
      asm volatile("" : "+v"(kd[j]), "+v"(qf[j]), "+v"(hg[j]));
    }
  }
  wait_lgkm0();
  bar();
  u16* X1T = (u16*)(ws + OFF_X1T) + (size_t)d * S + pbase;
  u16* XS = (u16*)(ws + OFF_XS) + (size_t)d * S + pbase;
  const float c1 = round_bf16(1.f + cbv);
#pragma unroll
  for (int j = 0; j < 2; ++j) {
    const LAS f32x4* Xj = X + ((2 * q + j) * 4) * 64 + lane;
    const f32x4 y = (Xj[0] + Xj[64]) + (Xj[128] + Xj[192]);
    const f32x4 o = qf[j] + hg[j] * (y + kd[j]);
    const u32 p0 = umin32(pack_bf16(o[0], o[1])), p1 = umin32(pack_bf16(o[2], o[3]));
    st8_sc1(XS + 256 * j,
            u32x2{umin32(pack_bf16(lo_f(p0) * c1, hi_f(p0) * c1)), umin32(pack_bf16(lo_f(p1) * c1, hi_f(p1) * c1))});
    st8_sc1(X1T + 256 * j, u32x2{p0, p1});
  }
}

DI void k2_stats(char* __restrict__ ws, char* smem, const int sblk) {
  const int tid = threadIdx.x, lane = tid & 63;
  const int w = __builtin_amdgcn_readfirstlane(tid >> 6);
  const int n = lane & 31, h = lane >> 5;
  {
    const int p0 = sblk * 64;
    if (w < 7) {
      const int task = sblk * 7 + w, t = task >> 1, kh = task & 1;
      const u32x4* wsrc = (const u32x4*)(ws + OFF_WGU) + ((size_t)t * 28 + kh * 14) * 64 + lane;
      const u16* cbp = (const u16*)(ws + OFF_CB) + (n & 1) * D + kh * 224 + 8 * h;
      u32x4 fa[14], fb[14];
#pragma unroll
      for (int i = 0; i < 14; ++i) {
        fa[i] = wsrc[i * 64];
        fb[i] = *(const u32x4*)(cbp + 16 * i);
      }
      __builtin_amdgcn_sched_barrier(0);
      f32x16 va;
#pragma unroll
      for (int r = 0; r < 16; ++r) va[r] = 0.f;
#pragma unroll
      for (int i = 0; i < 14; ++i) va = mfma32(fb[i], __builtin_bit_cast(bf16x8, fa[i]), va);
      if (h == 0) {
        const float f0 = va[0], f1 = va[1];
        const u32 a0 = umin32(__builtin_bit_cast(u32, f0)), a1 = __builtin_bit_cast(u32, f1);
        __hip_atomic_store((gu64*)(size_t)(ws + OFF_VG) + kh * 3584 + t * 32 + n, ((u64)a1 << 32) | a0, RLX_AGENT);
      }
    }
    __syncthreads();
    __builtin_amdgcn_s_sleep(AUX_SLEEP);
    const rsrc_t rx = mk_rsrc(ws + OFF_X1T + ((size_t)(w * 56) * S + p0) * 2, 56 * S * 2);
    const int c8 = lane & 7, r8 = lane >> 3;
    u32x4 x[7];
    for (int spin = 0; spin < (1 << 15); ++spin) {
      asm volatile("" ::: "memory");
      u32 m = 0;
#pragma unroll
      for (int i = 0; i < 7; ++i) x[i] = __builtin_amdgcn_raw_buffer_load_b128(rx, (r8 + 8 * i) * (S * 2) + c8 * 16, 0, 16);
#pragma unroll
      for (int i = 0; i < 7; ++i) m = umax3(umax3(m, x[i][0], x[i][1]), x[i][2], x[i][3]);
      if (__all(m != ~0u)) break;
      __builtin_amdgcn_s_sleep(1);
    }
    float sa[8], qa[8];
#pragma unroll
    for (int e = 0; e < 8; ++e) sa[e] = qa[e] = 0.f;
#pragma unroll
    for (int i = 0; i < 7; ++i)
#pragma unroll
      for (int e = 0; e < 8; ++e) {
        const float a = (e & 1) ? hi_f(x[i][e >> 1]) : lo_f(x[i][e >> 1]);
        sa[e] += a;
        qa[e] = __builtin_fmaf(a, a, qa[e]);
      }
    LAS float* P = (LAS float*)smem;
    {
      LAS float* pp = P + (w * 8 + r8) * 128 + c8 * 8;
      *(LAS f32x4*)pp = f32x4{sa[0], sa[1], sa[2], sa[3]};
      *(LAS f32x4*)(pp + 4) = f32x4{sa[4], sa[5], sa[6], sa[7]};
      *(LAS f32x4*)(pp + 64) = f32x4{qa[0], qa[1], qa[2], qa[3]};
      *(LAS f32x4*)(pp + 68) = f32x4{qa[4], qa[5], qa[6], qa[7]};
    }
    wait_lgkm0();
    {
      float s1 = 0.f, q1 = 0.f;
#pragma unroll
      for (int i = 0; i < 8; ++i) {
        s1 += P[(w * 8 + i) * 128 + lane];
        q1 += P[(w * 8 + i) * 128 + 64 + lane];
      }
      *(LAS f32x2*)(P + 8192 + (w * 64 + lane) * 2) = f32x2{s1, q1};
    }
    __syncthreads();
    if (tid < 64) {
      float sm = 0.f, sq = 0.f;
#pragma unroll
      for (int i = 0; i < 8; ++i) {
        const f32x2 t = *(const LAS f32x2*)(P + 8192 + (i * 64 + tid) * 2);
        sm += t[0];
        sq += t[1];
      }
      const float mean = sm * (1.f / D);
      float var = sq * (1.f / D) - mean * mean;
      var = (var < 0.f ? 0.f : var) + 1e-5f;
      const float rstd = __builtin_amdgcn_rsqf(var);
      const float sig = var * rstd;
      const float mh = round_bf16(mean), sh = round_bf16(sig);
      const u32 b0 = pack_bf16(-mh, mh - mean), b1 = pack_bf16(-mh, sh), b2 = pack_bf16(sig - sh, sh);
      const float c1 = -2.302208198f * rstd;
      const float c2 = -0.1029432397f * (rstd * rstd * rstd);
      gu64* G = (gu64*)(size_t)(ws + OFF_SG) + p0 + tid;
      __hip_atomic_store(G, ((u64)b1 << 32) | b0, RLX_AGENT);
      __hip_atomic_store(G + S, ((u64)__builtin_bit_cast(u32, var) << 32) | b2, RLX_AGENT);
      __hip_atomic_store(G + 2 * S, ((u64)__builtin_bit_cast(u32, c1) << 32) | __builtin_bit_cast(u32, c2), RLX_AGENT);
    }
  }
}

DI void k2_gemm(const u16* __restrict__ ssm_k, const u16* __restrict__ ssm_D, char* __restrict__ ws, u16* __restrict__ out,
                char* smem, const int w) {
  const int tid = threadIdx.x, lane = tid & 63;
  const int n = lane & 31, h = lane >> 5;
  const int xcd = blockIdx.x & 7, idx = blockIdx.x >> 3;
  const int mt = (xcd >> 1) * 4 + (idx & 3);
  const int dt = idx >> 2, sh2 = xcd & 1;
  const int ft = sh2 * 7 + dt;
  const int tile = ft * 8 + w;
  const u32 lds0 = (u32)(size_t)(LAS char*)smem;
  u32x4 wf[28], wd[28];
  const char* wbase = ws + OFF_WGU + (size_t)(tile * 28) * 1024;
  const u32 wa = lane * 16;
  auto ldwf = [&](auto kI) {
    constexpr int k = decltype(kI)::value;
    gload16_off<(k % 4) * 1024>(wf[k], wbase + (k / 4) * 4096, wa);
  };
  conv_body<true>(ssm_k, ssm_D, ws, smem, w, wf, wbase);
  const char* xbase = ws + OFF_XS + ((size_t)(8 * w) * S + mt * 128) * 2;
  const u32 xoff = (u32)((lane >> 3) * S + ((lane & 7) ^ (((lane >> 4) & 1) << 2)) * 8) * 2;
  const char* wdbase = ws + OFF_WD + ((size_t)(2 * dt) * 112 + 14 * w) * 1024 + 4096;
  const int gsl = w < 6 ? w : w - 6;
  const char* gbase = ws + OFF_SG + (size_t)((gsl >> 1) * S + mt * 128 + (gsl & 1) * 64) * 8;
  auto ldwd = [&](auto kI) {
    constexpr int q = decltype(kI)::value, k = (q & 1) * 14 + (q >> 1), kk = k % 14;
    gload16_off<(kk % 8) * 1024 - 4096>(wd[k], wdbase + (k / 14) * 112 * 1024 + (kk / 8) * 8192, wa);
  };
  auto ldx = [&](auto cI) {
    constexpr int c = decltype(cI)::value;
    glds16_sc1(xbase + (size_t)(c % 7) * (64 * S * 2) + (c / 7) * 128, xoff, lds0 + (64 * c + 8 * w) * 128);
  };
  auto xok = [&](auto c0I, auto c1I) {
    u32 m = 0;
#pragma unroll
    for (int c = decltype(c0I)::value; c < decltype(c1I)::value; ++c) {
      const u32x4 t = *(const LAS u32x4*)(smem + (64 * c + 8 * w) * 128 + lane * 16);
      m = umax3(umax3(m, t[0], t[1]), t[2], t[3]);
    }
    return (bool)__all(m != ~0u);
  };
  auto xvalid = [&](auto c0I, auto c1I) {
    if (__builtin_expect(!xok(c0I, c1I), 0)) {
#pragma nounroll
      for (int spin = 0; spin < (1 << 14); ++spin) {
        __builtin_amdgcn_s_sleep(4);
        static_for<decltype(c0I)::value, decltype(c1I)::value>(ldx);
        wait_vm<0>();
        if (xok(c0I, c1I)) break;
      }
    }
    __builtin_amdgcn_sched_barrier(0);
  };
  wait_lgkm0();
  bar();
  auto pin = [](f32x16& a) { asm volatile("" : "+v"(a)); };
  auto pinw = [&](auto kI) {
    u32x4 t = wf[decltype(kI)::value];
    asm volatile("" : "+v"(t)::"memory");
    wf[decltype(kI)::value] = t;
  };
  u32x4 aext;
  constexpr int NWH = NWC + (NWH_ - NWC);
  constexpr int NWP = NWC + NWP_;
  static_for<NWC, NWP>(ldwf);
  static_for<0, 7>(ldx);
  static_for<NWP, NWH>(ldwf);
  int xo[2][2];
  {
    const int blk = (lane >> 4) & 1, qq = (lane & 15) >> 2, p = lane & 3;
#pragma unroll
    for (int i = 0; i < 2; ++i)
#pragma unroll
      for (int t = 0; t < 2; ++t)
        xo[i][t] = 128 * (8 * h + 4 * t + qq) + 16 * ((4 * i + 2 * blk + (p >> 1)) ^ ((qq >> 1) << 2)) + 8 * (p & 1);
  }
  f32x16 acc[4];
#pragma unroll
  for (int i = 0; i < 4; ++i)
#pragma unroll
    for (int r = 0; r < 16; ++r) acc[i][r] = 0.f;
  asm volatile("" : "+v"(xo[0][0]), "+v"(xo[0][1]), "+v"(xo[1][0]), "+v"(xo[1][1]));
  constexpr int PF = 3;
  bf16x8 xr[PF][2];
  auto rdx = [&](auto kI, auto iI) {
    constexpr int k = decltype(kI)::value, i = decltype(iI)::value;
    if constexpr (k == 28) {
      int a0 = xo[i][0] + K2_XH, a1 = xo[i][1] + K2_XH;
      asm volatile("" : "+v"(a0), "+v"(a1));
      xo[i][0] = a0;
      xo[i][1] = a1;
    }
    xr[k % PF][i] = tr_frag(smem, xo[i][0] + (k % 28) * 2048, xo[i][1] + (k % 28) * 2048);
  };
  using I0 = std::integral_constant<int, 0>;
  using I1 = std::integral_constant<int, 1>;
  using I2 = std::integral_constant<int, 2>;
  using I3 = std::integral_constant<int, 3>;
  using I7 = std::integral_constant<int, 7>;
  using I14 = std::integral_constant<int, 14>;
  LAS u64* CS = (LAS u64*)(smem + K2_ST);
  wait_vm<4 + NWH - NWP>();
  static_for<0, NWP>(pinw);
  xvalid(I0{}, I3{});
  bar();
  static_for<0, PF - 1>([&](auto jI) {
    rdx(jI, I0{});
    rdx(jI, I1{});
  });
  __builtin_amdgcn_sched_barrier(0);
  u64 gv, gp0, gp1;
  const char* vbase = ws + OFF_VG + (size_t)tile * 256;
  constexpr int RW = 28 - NWH, KX = RW + 7, KV = 25;
  constexpr int KF = KF_;
  constexpr int WE = 27 - KX;
  static_assert(KF >= KX && KF <= 27, "");
  static_assert(KX + 4 <= KV && WE <= 9, "stream does not fit");
  static_for<0, 28>([&](auto kI) {
    constexpr int k = decltype(kI)::value;
    if constexpr (k == 6) {
      wait_vm<6>();
      static_for<NWP, NWH>(pinw);
      xvalid(I3{}, I7{});
      bar();
    }
    if constexpr (k >= NWH) {
      u32x4 t = wf[k];
      asm volatile("s_waitcnt vmcnt(%1)" : "+v"(t) : "n"(NWH - 1 + (k > KF ? 2 : 0)) : "memory");
      wf[k] = t;
    }
    if constexpr (k == KV) {
      wait_vm<KV - KX + (KV > KF ? 2 : 0)>();
      xvalid(I7{}, I14{});
      bar();
    }
    __builtin_amdgcn_sched_barrier(0);
    acc[0] = mfma32(wf[k], xr[k % PF][0], acc[0]);
    pin(acc[0]);
    __builtin_amdgcn_sched_barrier(0);
    rdx(std::integral_constant<int, k + PF - 1>{}, I0{});
    __builtin_amdgcn_sched_barrier(0);
    acc[1] = mfma32(wf[k], xr[k % PF][1], acc[1]);
    pin(acc[1]);
    __builtin_amdgcn_sched_barrier(0);
    rdx(std::integral_constant<int, k + PF - 1>{}, I1{});
    if constexpr (k < RW) ldwf(std::integral_constant<int, NWH + k>{});
    else if constexpr (k < KX) ldx(std::integral_constant<int, 7 + k - RW>{});
    else if constexpr (k != KF) ldwd(std::integral_constant<int, k - KX - (k > KF ? 1 : 0)>{});
    else {
      u64 t, t0, t1;
      asm volatile("global_load_dwordx2 %0, %1, %2 sc1" : "=v"(t) : "v"((u32)lane * 8), "s"(uni(gbase)) : "memory");
      asm volatile("global_load_dwordx2 %0, %1, %2 sc1" : "=v"(t0) : "v"((u32)n * 8), "s"(uni(vbase)) : "memory");
      asm volatile("global_load_dwordx2 %0, %1, %2 sc1" : "=v"(t1) : "v"((u32)n * 8), "s"(uni(vbase + 3584 * 8)) : "memory");
      gv = t;
      gp0 = t0;
      gp1 = t1;
    }
    __builtin_amdgcn_sched_barrier(0);
  });
  const rsrc_t rw = mk_rsrc(ws + OFF_ACT + ((size_t)(mt * 4) * 112 + tile) * 1024, 337 * 1024);
  const rsrc_t ra = mk_rsrc(ws + OFF_ACT + ((size_t)(mt * 4 + sh2) * 112 + 14 * w) * 1024, 238 * 1024);
  u32x4 af[28];
  auto ldact = [&](auto kI) {
    constexpr int k = decltype(kI)::value;
    af[k] = __builtin_amdgcn_raw_buffer_load_b128(ra, lane * 16 + (k % 14) * 1024, (k / 14) * 224 * 1024, 16);
  };
  float c1[2], c2[2], var[2], pe[2], pm[2], pr[2];
  u32x4 ov[2];
  auto corr = [&](auto iI) {
    constexpr int i = decltype(iI)::value;
    u64 g[3];
#pragma unroll
    for (int k = 0; k < 3; ++k) g[k] = CS[k * 128 + i * 32 + n];
    const u32x4 bext = {(u32)g[0], (u32)(g[0] >> 32), (u32)g[1], 0u};
    acc[i] = mfma32(aext, __builtin_bit_cast(bf16x8, bext), acc[i]);
    c1[i & 1] = __builtin_bit_cast(float, (u32)(g[2] >> 32));
    c2[i & 1] = __builtin_bit_cast(float, (u32)g[2]);
    var[i & 1] = __builtin_bit_cast(float, (u32)(g[1] >> 32));
  };
  auto slice = [&](auto iI, auto uI) {
    constexpr int i = decltype(iI)::value, u = decltype(uI)::value, z = i & 1;
    if constexpr (u >= 1) {
      float o = pm[z] * __builtin_amdgcn_rcpf(__builtin_fmaf(pe[z], var[z], var[z]));
      if constexpr ((u - 1) & 1) {
        u32 t = umin32(pack_bf16(pr[z], o));
        asm volatile("" : "+v"(t));
        ov[z][(u - 1) / 2] = t;
      } else {
        asm volatile("" : "+v"(o));
        pr[z] = o;
      }
    }
    if constexpr (u < 8) {
      const float a = acc[i][u];
      float e = __builtin_amdgcn_exp2f(__builtin_fmaf(a * a, c2[z], c1[z]) * a), m = a * acc[i][u + 8];
      asm volatile("" : "+v"(e), "+v"(m));
      pe[z] = e;
      pm[z] = m;
    } else {
      __builtin_amdgcn_raw_buffer_store_b128(ov[z], rw, lane * 16 + i * 112 * 1024, 0, 16);
      asm volatile("s_nop 1");
    }
  };
  constexpr int KG = KG_;
  static_for<0, 28>([&](auto kI) {
    constexpr int k = decltype(kI)::value;
    if constexpr (k == KG) {
      u64 t = gv, p0 = gp0, p1 = gp1;
      asm volatile("s_waitcnt vmcnt(%3)" : "+v"(t), "+v"(p0), "+v"(p1) : "n"(KG + 27 - KF) : "memory");
      if (!__all((t >> 32) != 0 && (u32)p0 != ~0u && (u32)p1 != ~0u)) {
        for (int spin = 0; spin < (1 << 16); ++spin) {
          __builtin_amdgcn_s_sleep(2);
          t = __hip_atomic_load((gu64*)(size_t)gbase + lane, RLX_AGENT);
          p0 = __hip_atomic_load((gu64*)(size_t)vbase + n, RLX_AGENT);
          p1 = __hip_atomic_load((gu64*)(size_t)vbase + 3584 + n, RLX_AGENT);
          if (__all((t >> 32) != 0 && (u32)p0 != ~0u && (u32)p1 != ~0u)) break;
        }
        __builtin_amdgcn_s_waitcnt(0x0F70);
      }
      CS[gsl * 64 + lane] = t;
      const float v1 = __builtin_bit_cast(float, (u32)p0) + __builtin_bit_cast(float, (u32)p1);
      const float v2 = __builtin_bit_cast(float, (u32)(p0 >> 32)) + __builtin_bit_cast(float, (u32)(p1 >> 32));
      const float v1h = round_bf16(v1), v2h = round_bf16(v2);
      u32x4 a = {pack_bf16(v1h, v1h), pack_bf16(v1 - v1h, v2h), pack_bf16(v2h, v2 - v2h), 0u};
      if (h) a = u32x4{0u, 0u, 0u, 0u};
      asm volatile("" : "+v"(a));
      aext = a;
      wait_lgkm0();
      __builtin_amdgcn_sched_barrier(0);
      bar();
      corr(I0{});
      corr(I1{});
      __builtin_amdgcn_sched_barrier(0);
    }
    acc[2] = mfma32(wf[k], xr[(28 + k) % PF][0], acc[2]);
    pin(acc[2]);
    __builtin_amdgcn_sched_barrier(0);
    if constexpr (k + PF - 1 < 28) rdx(std::integral_constant<int, 28 + k + PF - 1>{}, I0{});
    if constexpr (k > KG && k <= KG + 9) slice(I0{}, std::integral_constant<int, k - KG - 1>{});
    __builtin_amdgcn_sched_barrier(0);
    acc[3] = mfma32(wf[k], xr[(28 + k) % PF][1], acc[3]);
    pin(acc[3]);
    __builtin_amdgcn_sched_barrier(0);
    if constexpr (k % LOCKSTEP == LOCKSTEP - 1) bar();
    if constexpr (k + PF - 1 < 28) rdx(std::integral_constant<int, 28 + k + PF - 1>{}, I1{});
    if constexpr (k > KG && k <= KG + 9) slice(I1{}, std::integral_constant<int, k - KG - 1>{});
    if constexpr (WE + k < 28) ldwd(std::integral_constant<int, WE + k>{});
    else if constexpr (WE + k - 28 < 7) ldact(std::integral_constant<int, WE + k - 28>{});
    __builtin_amdgcn_sched_barrier(0);
  });
  constexpr int A0 = WE < 7 ? WE : 7;
  corr(I2{});
  corr(I3{});
  __builtin_amdgcn_sched_barrier(0);
  static_for<0, 9>([&](auto uI) {
    constexpr int u = decltype(uI)::value;
    slice(I2{}, uI);
    slice(I3{}, uI);
    if constexpr (A0 + u < 14 && u < 8) ldact(std::integral_constant<int, A0 + u>{});
    __builtin_amdgcn_sched_barrier(0);
  });
  static_for<(A0 + 8 < 14 ? A0 + 8 : 14), 14>(ldact);
  bar();
  auto chk = [&](auto gI) {
    constexpr int g = decltype(gI)::value;
    u32 m = 0;
#pragma unroll
    for (int k = 7 * g; k < 7 * g + 7; ++k) m = umax3(umax3(m, af[k][0], af[k][1]), af[k][2], af[k][3]);
    return (bool)__all(m != ~0u);
  };
  auto valid = [&](auto gI) {
    constexpr int g = decltype(gI)::value;
    if (__builtin_expect(!chk(gI), 0)) {
#pragma nounroll
      for (int spin = 0; spin < (1 << 14); ++spin) {
        __builtin_amdgcn_s_sleep(8);
        asm volatile("" ::: "memory");
        static_for<7 * g, 7 * g + 7>(ldact);
        if (chk(gI)) break;
      }
      __builtin_amdgcn_s_waitcnt(0x0F70);
    }
    __builtin_amdgcn_sched_barrier(0);
  };
  const int pcol = mt * 128 + sh2 * 32 + n;
  const int dbase = dt * 64 + w * 8 + 4 * h;
  __builtin_amdgcn_sched_barrier(0);
  LAS f32x4* R = (LAS f32x4*)smem;
  f32x16 o[4];
#pragma unroll
  for (int i = 0; i < 4; ++i)
#pragma unroll
    for (int r = 0; r < 16; ++r) o[i][r] = 0.f;
  auto dump1 = [&](auto tjI, auto cI) {
    constexpr int tj = decltype(tjI)::value, c = decltype(cI)::value, i = tj * 2 + (c >> 2), q = c & 3;
    R[((tj * 8 + c) * 8 + w) * 64 + lane] = f32x4{o[i][4 * q], o[i][4 * q + 1], o[i][4 * q + 2], o[i][4 * q + 3]};
  };
  auto step = [&](auto kI) {
    constexpr int k = decltype(kI)::value, tj = k / 14;
    const bf16x8 b = __builtin_bit_cast(bf16x8, af[k]);
    o[2 * tj] = mfma32(wd[k % 14], b, o[2 * tj]);
    pin(o[2 * tj]);
    __builtin_amdgcn_sched_barrier(0);
    if constexpr (k >= 15 && k < 19) dump1(I0{}, std::integral_constant<int, 2 * (k - 15)>{});
    __builtin_amdgcn_sched_barrier(0);
    o[2 * tj + 1] = mfma32(wd[14 + k % 14], b, o[2 * tj + 1]);
    pin(o[2 * tj + 1]);
    __builtin_amdgcn_sched_barrier(0);
    if constexpr (k >= 15 && k < 19) dump1(I0{}, std::integral_constant<int, 2 * (k - 15) + 1>{});
    if constexpr (k >= T1 && k < 14) {
      constexpr int n0 = ((k - T1) * 14) / (14 - T1), n1 = ((k + 1 - T1) * 14) / (14 - T1);
      static_for<14 + n0, 14 + n1>(ldact);
    }
    __builtin_amdgcn_sched_barrier(0);
  };
  u16 xres[2][4];
  f32x4 gbv;
  auto fin = [&](auto tjI) {
    constexpr int tj = decltype(tjI)::value;
    f32x4 v[8];
#pragma unroll
    for (int sw = 0; sw < 8; ++sw) v[sw] = R[((tj * 8 + w) * 8 + sw) * 64 + lane];
    const f32x4 sum = ((v[0] + v[1]) + (v[2] + v[3])) + ((v[4] + v[5]) + (v[6] + v[7]));
    float r[4];
#pragma unroll
    for (int e = 0; e < 4; ++e) r[e] = __builtin_fmaf(gbv[e], sum[e], bf2f(xres[tj][e]));
    const int pc = pcol + 64 * tj;
    const int srow = (pc & ~7) | ((pc & 3) << 1) | ((pc >> 2) & 1);
    *(u32x2*)(out + (size_t)srow * D + dbase) = u32x2{pack_bf16(r[0], r[1]), pack_bf16(r[2], r[3])};
  };
  valid(I0{});
  asm volatile(""
               : "+v"(wd[0]), "+v"(wd[1]), "+v"(wd[2]), "+v"(wd[3]), "+v"(wd[4]), "+v"(wd[5]), "+v"(wd[6]), "+v"(wd[7]),
                 "+v"(wd[8]), "+v"(wd[9]), "+v"(wd[10]), "+v"(wd[11]), "+v"(wd[12]), "+v"(wd[13])::"memory");
  asm volatile(""
               : "+v"(wd[14]), "+v"(wd[15]), "+v"(wd[16]), "+v"(wd[17]), "+v"(wd[18]), "+v"(wd[19]), "+v"(wd[20]),
                 "+v"(wd[21]), "+v"(wd[22]), "+v"(wd[23]), "+v"(wd[24]), "+v"(wd[25]), "+v"(wd[26]), "+v"(wd[27])::"memory");
  static_for<0, 3>(step);
  {
    const rsrc_t rx = mk_rsrc(ws + OFF_X1T, D * S * 2);
#pragma unroll
    for (int r = 0; r < 8; ++r)
      xres[r >> 2][r & 3] = __builtin_amdgcn_raw_buffer_load_b16(rx, ((dbase + (r & 3)) * S + pcol + (r >> 2) * 64) * 2, 0, 16);
    gbv = *(const f32x4*)((const float*)(ws + OFF_MOD) + 5 * D + dbase);
    __builtin_amdgcn_sched_barrier(0);
  }
  static_for<3, 7>(step);
  valid(I1{});
  static_for<7, 14>(step);
  valid(I2{});
  static_for<14, 21>(step);
  wait_lgkm0();
  bar();
  valid(I3{});
  static_for<21, 24>(step);
  fin(I0{});
  __builtin_amdgcn_sched_barrier(0);
  static_for<24, 28>(step);
  static_for<0, 8>([&](auto cI) { dump1(I1{}, cI); });
  wait_lgkm0();
  bar();
  fin(I1{});
}

extern "C" __global__ NOPK __launch_bounds__(512, 2) void k1_main(const u16* __restrict__ ssm_k,
                                                                  const u16* __restrict__ ssm_D, char* __restrict__ ws,
                                                                  u16* __restrict__ out) {
  extern __shared__ __attribute__((aligned(16))) char smem[];
  const int w = __builtin_amdgcn_readfirstlane(threadIdx.x >> 6);
  if (blockIdx.x >= 224) {
    k2_stats(ws, smem, blockIdx.x - 224);
  } else {
    k2_gemm(ssm_k, ssm_D, ws, out, smem, w);
  }
}

extern "C" __device__ __attribute__((used, noinline)) void k_code_end() {}

extern "C" void kernel_launch(void* const* d_in, const int* in_sizes, int n_in, void* d_out, int out_size, void* d_ws,
                              size_t ws_size, hipStream_t stream) {
  const u16* q = (const u16*)d_in[0];
  const u16* k = (const u16*)d_in[1];
  const u16* v = (const u16*)d_in[2];
  const u16* shift_k = (const u16*)d_in[3];
  const u16* ssm_k = (const u16*)d_in[4];
  const u16* ssm_D = (const u16*)d_in[5];
  const u16* w_gate = (const u16*)d_in[7];
  const u16* w_up = (const u16*)d_in[8];
  const u16* w_down = (const u16*)d_in[9];
  const u16* c = (const u16*)d_in[10];
  const u16* w_mod = (const u16*)d_in[11];
  char* ws = (char*)d_ws;
  (void)hipFuncSetAttribute((const void*)k1_main, hipFuncAttributeMaxDynamicSharedMemorySize, K2_LDS);
  k0_prep<<<K0_GRID, 256, 0, stream>>>(q, k, v, shift_k, c, w_mod, w_gate, w_up, w_down, ws);
  k1_main<<<256, 512, K2_LDS, stream>>>(ssm_k, ssm_D, ws, (u16*)d_out);
}
```

```cpp
#include <hip/hip_runtime.h>
#include <hip/hip_bf16.h>
#include <stdint.h>
#include <type_traits>

typedef unsigned short u16;
typedef unsigned int u32;
typedef short bf16x8 __attribute__((ext_vector_type(8)));
typedef short s16x4 __attribute__((ext_vector_type(4)));
typedef float f32x2 __attribute__((ext_vector_type(2)));
typedef float f32x4 __attribute__((ext_vector_type(4)));
typedef float f32x16 __attribute__((ext_vector_type(16)));
typedef u32 u32x2 __attribute__((ext_vector_type(2)));
typedef u32 u32x4 __attribute__((ext_vector_type(4)));
typedef __bf16 bf16x2_t __attribute__((ext_vector_type(2)));
#define LAS __attribute__((address_space(3)))
#define DI __device__ __forceinline__

constexpr int S = 2048, D = 448, F = 1792;

constexpr size_t SZ_DS = (size_t)D * S * 2;
constexpr size_t OFF_RO = SZ_DS;
constexpr size_t OFF_QT = OFF_RO + SZ_DS;
constexpr size_t OFF_X1T = OFF_QT + SZ_DS;
constexpr size_t OFF_MEAN = OFF_X1T + SZ_DS;
constexpr size_t OFF_RSTD = OFF_MEAN + S * 4;
constexpr size_t OFF_MOD = OFF_RSTD + S * 4;
constexpr size_t OFF_SG = OFF_MOD + 16384;
constexpr size_t OFF_CB = OFF_MOD + 12288;

constexpr size_t OFF_VG = OFF_SG + 3 * S * 8;
constexpr size_t OFF_XS = OFF_VG + 4 * S * 8;
constexpr size_t OFF_WGU = OFF_XS + SZ_DS;
constexpr size_t OFF_WD = OFF_WGU + (size_t)2 * D * F * 2;
constexpr size_t OFF_ACT = OFF_WD + (size_t)D * F * 2;

#if defined(__HIP_DEVICE_COMPILE__)
#define NOPK __attribute__((target("no-packed-fp32-ops")))
#else
#define NOPK
#endif

constexpr int C_KF = 25;
constexpr int C_KG = 1;
constexpr int C_LOCK = 4;
constexpr int C_NW0 = 5;
constexpr int C_NW1 = 5;
constexpr int C_NWH = 15;
constexpr int C_NWP = 3;
constexpr int C_SSLEEP = 50;
constexpr int C_T1 = 5;

template <int I, int N, class Fn>
DI void static_for(Fn&& f) {
  if constexpr (I < N) {
    f(std::integral_constant<int, I>{});
    static_for<I + 1, N>(f);
  }
}

DI float lo_f(u32 x) { return __builtin_bit_cast(float, x << 16); }
DI float hi_f(u32 x) { return __builtin_bit_cast(float, x & 0xffff0000u); }
DI u32 pack_bf16(float a, float b) {
  f32x2 v = {a, b};
  return __builtin_bit_cast(u32, __builtin_convertvector(v, bf16x2_t));
}
DI float round_bf16(float a) { return lo_f(pack_bf16(a, 0.f)); }
DI float bf2f(u16 x) { return __builtin_bit_cast(float, (u32)x << 16); }

template <int N>
DI void wait_vm() { asm volatile("s_waitcnt vmcnt(%0)" ::"n"(N) : "memory"); }
DI void wait_lgkm0() { asm volatile("s_waitcnt lgkmcnt(0)" ::: "memory"); }
DI void bar() { __builtin_amdgcn_s_barrier(); }

DI const void* uni(const void* p) {
  const unsigned long long v = (unsigned long long)p;
  const u32 lo = __builtin_amdgcn_readfirstlane((u32)v), hi = __builtin_amdgcn_readfirstlane((u32)(v >> 32));
  return (const void*)(((unsigned long long)hi << 32) | lo);
}
DI void glds16(const void* g, char* lds) {
  __builtin_amdgcn_global_load_lds((const __attribute__((address_space(1))) void*)g, (LAS void*)lds, 16, 0, 0);
}

template <int CTRL>
DI float dpp_add(float v) {
  const int x = __builtin_bit_cast(int, v);
  return v + __builtin_bit_cast(float, __builtin_amdgcn_update_dpp(x, x, CTRL, 0xf, 0xf, false));
}
DI float sum8(float v) {
  v = dpp_add<0xB1>(v);
  v = dpp_add<0x4E>(v);
  v = dpp_add<0x141>(v);
  return v;
}
DI float dot2(u32 a, u32 b, float c) {
  return __builtin_amdgcn_fdot2_f32_bf16(__builtin_bit_cast(bf16x2_t, a), __builtin_bit_cast(bf16x2_t, b), c, false);
}
typedef __amdgpu_buffer_rsrc_t rsrc_t;
DI rsrc_t mk_rsrc(const void* p, u32 bytes) { return __builtin_amdgcn_make_buffer_rsrc((void*)p, 0, bytes, 0x00020000); }
DI void st16_sc1(void* p, u32x4 v) { asm volatile("global_store_dwordx4 %0, %1, off sc1\n\ts_nop 1" ::"v"(p), "v"(v) : "memory"); }
DI void st8_sc1(void* p, u32x2 v) { asm volatile("global_store_dwordx2 %0, %1, off sc1" ::"v"(p), "v"(v) : "memory"); }

DI bf16x8 tr_frag(const char* smem, int o0, int o1) {
  s16x4 x0 = __builtin_amdgcn_ds_read_tr16_b64_v4i16((LAS s16x4*)(smem + o0));
  s16x4 x1 = __builtin_amdgcn_ds_read_tr16_b64_v4i16((LAS s16x4*)(smem + o1));
  return __builtin_shufflevector(x0, x1, 0, 1, 2, 3, 4, 5, 6, 7);
}
DI f32x16 mfma32(u32x4 a, bf16x8 b, f32x16 c) {
  return __builtin_amdgcn_mfma_f32_32x32x16_bf16(__builtin_bit_cast(bf16x8, a), b, c, 0, 0, 0);
}

DI void retile(int task, const u16* __restrict__ w_gate, const u16* __restrict__ w_up, const u16* __restrict__ w_down,
               char* __restrict__ ws, char* smem, int w, int lane) {
  const int xr = lane >> 4, slot = lane & 15;
  const int chn = slot ^ ((xr << 2) | (w & 3));
  const int h = lane >> 5, blk = (lane >> 4) & 1, qq = (lane & 15) >> 2, p = lane & 3;
  const bool gu = task < 112;
  int kq = 0, fg = 0, dg = 0, fk = 0;
  if (gu) {
    fg = task % 28, kq = task / 28;
    const u16* src = (chn < 8 ? w_gate : w_up) + (size_t)(kq * 112 + 4 * w + xr) * F + fg * 64 + (chn & 7) * 8;
#pragma unroll
    for (int i = 0; i < 7; ++i) glds16(src + (size_t)i * 16 * F, smem + (w + 4 * i) * 1024);
  } else {
    dg = (task - 112) % 7, fk = (task - 112) / 7;
    const u16* src = w_down + (size_t)(fk * 224 + (chn >> 3) * 112 + 4 * w + xr) * D + dg * 64 + (chn & 7) * 8;
#pragma unroll
    for (int i = 0; i < 7; ++i) glds16(src + (size_t)i * 16 * D, smem + (w + 4 * i) * 1024);
  }
  const int col = gu ? blk * 128 + w * 32 + (p & 1) * 16 + (p >> 1) * 8 : (w >> 1) * 128 + (w & 1) * 64 + blk * 32 + p * 8;
  int o[2];
#pragma unroll
  for (int t = 0; t < 2; ++t)
    o[t] = 256 * (8 * h + 4 * t + qq) + 16 * ((col >> 4) ^ ((qq << 2) | ((2 * h + t) & 3))) + (col & 8);
  u32x4* dst = gu ? (u32x4*)(ws + OFF_WGU) + ((size_t)(fg * 4 + w) * 28 + kq * 7) * 64 + lane
                  : (u32x4*)(ws + OFF_WD) + ((size_t)(dg * 2 + (w & 1)) * 112 + fk * 14 + (w >> 1) * 7) * 64 + lane;
  __syncthreads();
#pragma unroll
  for (int i = 0; i < 7; ++i)
    st16_sc1(dst + i * 64, __builtin_bit_cast(u32x4, tr_frag(smem, o[0] + i * 4096, o[1] + i * 4096)));
}

constexpr int K0_GRID = 504;

extern "C" __global__ __launch_bounds__(256) void k0_prep(const u16* __restrict__ q, const u16* __restrict__ k,
                                               const u16* __restrict__ v, const u16* __restrict__ shift_k,
                                               const u16* __restrict__ c, const u16* __restrict__ w_mod,
                                               const u16* __restrict__ w_gate,
                                               const u16* __restrict__ w_up, const u16* __restrict__ w_down,
                                               char* __restrict__ ws) {
  __shared__ __attribute__((aligned(16))) char smem[28672];
  float(*red)[64] = (float(*)[64])smem;
  const int b = blockIdx.x, tid = threadIdx.x, lane = tid & 63, w = __builtin_amdgcn_readfirstlane(tid >> 6);
  const int xcd = b & 7, idx = b >> 3;
  u32 cwarm = 0;
  if (idx < 2) {
    u32 lo, hi, len;
    asm volatile(
        "s_getpc_b64 s[40:41]\n\ts_add_u32 s40, s40, k0_prep@rel32@lo+4\n\ts_addc_u32 s41, s41, k0_prep@rel32@hi+12\n\t"
        "s_mov_b32 %0, s40\n\ts_mov_b32 %1, s41\n\ts_mov_b32 %2, k_code_end-k0_prep"
        : "=s"(lo), "=s"(hi), "=s"(len)::"s40", "s41", "scc");
    const char* code = (const char*)(((unsigned long long)hi << 32) | lo);
    if ((u32)(idx * 256 + tid) * 128 < len) cwarm = *(const __attribute__((address_space(1))) u32*)(size_t)(code + (idx * 256 + tid) * 128);
  }
  asm volatile("" ::"s"(q), "s"(k), "s"(v), "s"(shift_k), "s"(c), "s"(w_mod), "s"(w_gate), "s"(w_up), "s"(w_down),
               "s"(ws));
  if (__builtin_expect(idx >= 42, 0)) {
    retile(b - 336, w_gate, w_up, w_down, ws, smem, w, lane);
    return;
  }
  int misc = -1;
  if (idx < 6) misc = 64 + b;
  else if (idx >= 34) misc = xcd * 8 + (idx - 34);
  const int rb = xcd * 4 + ((idx - 6) & 3), cg = (idx - 6) >> 2;
  if (__builtin_expect(misc < 0, 1)) {
    const int sq = lane >> 5, cp = lane & 31;
    const int d0 = cg * 64 + cp * 2;
    const int a = rb * 8 + w * 2 + sq;
    const int s0 = a * 8;
    const rsrc_t rk = mk_rsrc(k, S * D * 2), rv = mk_rsrc(v, S * D * 2), rq = mk_rsrc(q, S * D * 2);
    const int oB = (s0 * D + d0) * 2;
    const int oA = oB - 4 * D * 2, oC = oB + 4 * D * 2;
    u32 kk[12], vv[9], qq[8];
#pragma unroll
    for (int i = 1; i < 4; ++i) kk[i] = __builtin_amdgcn_raw_buffer_load_b32(rk, oA + i * D * 2, 0, 0);
#pragma unroll
    for (int i = 0; i < 4; ++i) kk[4 + i] = __builtin_amdgcn_raw_buffer_load_b32(rk, oB + i * D * 2, 0, 0);
#pragma unroll
    for (int i = 0; i < 4; ++i) kk[8 + i] = __builtin_amdgcn_raw_buffer_load_b32(rk, oC + i * D * 2, 0, 0);
#pragma unroll
    for (int i = 0; i < 4; ++i) vv[1 + i] = __builtin_amdgcn_raw_buffer_load_b32(rv, oB + i * D * 2, 0, 0);
#pragma unroll
    for (int i = 0; i < 4; ++i) vv[5 + i] = __builtin_amdgcn_raw_buffer_load_b32(rv, oC + i * D * 2, 0, 0);
#pragma unroll
    for (int i = 0; i < 4; ++i) qq[i] = __builtin_amdgcn_raw_buffer_load_b32(rq, oB + i * D * 2, 0, 0);
#pragma unroll
    for (int i = 0; i < 4; ++i) qq[4 + i] = __builtin_amdgcn_raw_buffer_load_b32(rq, oC + i * D * 2, 0, 0);
    const u32x4 sk = *(const u32x4*)(shift_k + d0 * 4);
    f32x2 wt[4] = {{lo_f(sk[0]), lo_f(sk[2])}, {hi_f(sk[0]), hi_f(sk[2])}, {lo_f(sk[1]), lo_f(sk[3])}, {hi_f(sk[1]), hi_f(sk[3])}};
    f32x2 kf[12], u[9];
#pragma unroll
    for (int i = 1; i < 12; ++i) kf[i] = f32x2{lo_f(kk[i]), hi_f(kk[i])};
#pragma unroll
    for (int i = 1; i < 9; ++i) {
      f32x2 t = kf[i + 3] * wt[0];
      t = __builtin_elementwise_fma(kf[i + 2], wt[1], t);
      t = __builtin_elementwise_fma(kf[i + 1], wt[2], t);
      t = __builtin_elementwise_fma(kf[i], wt[3], t);
      u[i] = t * f32x2{lo_f(vv[i]), hi_f(vv[i])};
    }
    {
      char* dst = ws + (size_t)d0 * (S * 2) + (2040 - s0) * 2;
#pragma unroll
      for (int cI = 0; cI < 2; ++cI) {
        *(u32x4*)(dst + OFF_RO + cI * S * 2) = u32x4{pack_bf16(u[8][cI], u[7][cI]), pack_bf16(u[6][cI], u[5][cI]),
                                              pack_bf16(u[4][cI], u[3][cI]), pack_bf16(u[2][cI], u[1][cI])};
      }
      char* dq = ws + OFF_QT + (size_t)d0 * (S * 2) + s0 * 2;
#pragma unroll
      for (int cI = 0; cI < 2; ++cI) {
        const u32 sel = cI ? 0x07060302u : 0x05040100u;
        *(u32x4*)(dq + cI * S * 2) = u32x4{__builtin_amdgcn_perm(qq[2], qq[0], sel), __builtin_amdgcn_perm(qq[6], qq[4], sel),
                                    __builtin_amdgcn_perm(qq[3], qq[1], sel), __builtin_amdgcn_perm(qq[7], qq[5], sel)};
      }
    }
  } else if (misc < 64) {
    const int bb = misc;
    const int row = bb * 32 + w * 8 + (lane >> 3);
    const int sub = lane & 7;
    u32x4 x[7];
#pragma unroll
    for (int i = 0; i < 7; ++i) x[i] = *(const u32x4*)(q + (size_t)row * D + (i * 8 + sub) * 8);
    if (sub < 7) ((unsigned long long*)(ws + OFF_SG))[sub * S + row] = sub < 3 ? 0ull : ~0ull;
    float s = 0.f, s2 = 0.f;
#pragma unroll
    for (int i = 0; i < 7; ++i)
#pragma unroll
      for (int j = 0; j < 4; ++j) {
        const u32 t = x[i][j];
        s = dot2(t, 0x3f803f80u, s);
        s2 = dot2(t, t, s2);
      }
    s = sum8(s);
    s2 = sum8(s2);
    const float mean = s * (1.f / D);
    float var = s2 * (1.f / D) - mean * mean;
    var = var < 0.f ? 0.f : var;
    const float rstd = __builtin_amdgcn_rsqf(var + 1e-5f);
    if (sub < 2) {
      const int p = (row & ~7) | ((row & 1) << 2) | ((row >> 1) & 3);
      ((float*)(ws + OFF_MEAN))[sub * S + p] = sub ? rstd : mean;
    }
    {
      const u32x4 sent = {~0u, ~0u, ~0u, ~0u};
#pragma unroll
      for (int i = 0; i < 14; ++i)
        st16_sc1((u32x4*)(ws + (i < 7 ? OFF_X1T : OFF_XS)) + (size_t)bb * 1792 + (i % 7) * 256 + tid, sent);
    }
  } else if (misc < 106) {
    const int bb = misc - 64;
    const int co = lane & 7, rs = lane >> 3;
    const int col0 = bb * 64 + co * 8;
    u32x4 x[14];
#pragma unroll
    for (int i = 0; i < 14; ++i)
      x[i] = *(const u32x4*)(w_mod + (size_t)(w * 112 + rs * 14 + i) * (6 * D) + col0);
    u32 cv[7];
#pragma unroll
    for (int i = 0; i < 7; ++i) cv[i] = *(const u32*)(c + w * 112 + rs * 14 + 2 * i);
    __builtin_amdgcn_sched_barrier(0);
    f32x2 acc[4] = {{0.f, 0.f}, {0.f, 0.f}, {0.f, 0.f}, {0.f, 0.f}};
#pragma unroll
    for (int i = 0; i < 14; ++i) {
      const float cf = (i & 1) ? hi_f(cv[i >> 1]) : lo_f(cv[i >> 1]);
      const float sl = round_bf16(cf * __builtin_amdgcn_rcpf(1.f + __builtin_amdgcn_exp2f(cf * -1.44269504f)));
#pragma unroll
      for (int j = 0; j < 4; ++j)
        acc[j] = __builtin_elementwise_fma(f32x2{lo_f(x[i][j]), hi_f(x[i][j])}, f32x2{sl, sl}, acc[j]);
    }
    *(f32x4*)&red[w * 8 + rs][co * 8] = f32x4{acc[0][0], acc[0][1], acc[1][0], acc[1][1]};
    *(f32x4*)&red[w * 8 + rs][co * 8 + 4] = f32x4{acc[2][0], acc[2][1], acc[3][0], acc[3][1]};
    __syncthreads();
    {
      float t = 0.f;
#pragma unroll
      for (int i = 0; i < 8; ++i) t += red[w * 8 + i][lane];
      red[32 + w][lane] = t;
    }
    __syncthreads();
    if (tid < 64) {
      const float t = (red[32][tid] + red[33][tid]) + (red[34][tid] + red[35][tid]);
      const float m = round_bf16(t);
      const int col = bb * 64 + tid;
      ((float*)(ws + OFF_MOD))[col] = m;
      if (col >= 3 * D && col < 5 * D) {
        const bool isc = col >= 4 * D;
        ((u16*)(ws + OFF_CB))[isc ? col - 4 * D : col - 2 * D] = (u16)pack_bf16(isc ? 1.f + m : m, 0.f);
      }
    }
  }
  asm volatile("" ::"v"(cwarm));
}

constexpr int K1_KSTR = 260 * 16;
constexpr int K1_USTR = 286 * 16;
constexpr int K1_UOFF = 4 * K1_KSTR;
constexpr int K1_XOFF = K1_UOFF + 2 * K1_USTR;
constexpr int K1_CH = K1_XOFF + 32768;

DI void glds16x4_asm(const void* sbase, u32 voff, u32 voff_last, u32 lds_addr) {
  u32 keep;
  sbase = uni(sbase);
  lds_addr = __builtin_amdgcn_readfirstlane(lds_addr);
  asm volatile(
      "s_mov_b32 %0, m0\n\ts_mov_b32 m0, %4\n\ts_nop 0\n\t"
      "global_load_lds_dwordx4 %1, %3\n\t"
      "global_load_lds_dwordx4 %1, %3 offset:1024\n\t"
      "global_load_lds_dwordx4 %1, %3 offset:2048\n\t"
      "global_load_lds_dwordx4 %2, %3 offset:3072\n\t"
      "s_mov_b32 m0, %0"
      : "=&s"(keep)
      : "v"(voff), "v"(voff_last), "s"(sbase), "s"(lds_addr)
      : "memory");
}
#define CAS __attribute__((address_space(4)))
constexpr int K2_XB = D * 256;
constexpr int K2_XH = D * 128;
constexpr int K2_ST = K2_XB;
constexpr int K2_LDS = 131072;

DI u32 umin32(u32 v) { return v < 0xFFFFFFFEu ? v : 0xFFFFFFFEu; }
DI u32 umax3(u32 a, u32 b, u32 c) { const u32 t = a > b ? a : b; return t > c ? t : c; }
template <int OFF>
DI void gload16_off(u32x4& dst, const void* sbase, u32 voff) {
  sbase = uni(sbase);
  asm volatile("global_load_dwordx4 %0, %1, %2 offset:%3" : "=v"(dst) : "v"(voff), "s"(sbase), "n"(OFF) : "memory");
}
DI void glds16_nom0(const void* sbase, u32 voff, u32 lds_addr) {
  sbase = uni(sbase);
  lds_addr = __builtin_amdgcn_readfirstlane(lds_addr);
  asm volatile("s_mov_b32 m0, %2\n\ts_nop 0\n\tglobal_load_lds_dwordx4 %0, %1" ::"v"(voff), "s"(sbase), "s"(lds_addr) : "memory");
}
DI void glds16_sc1(const void* sbase, u32 voff, u32 lds_addr) {
  sbase = uni(sbase);
  lds_addr = __builtin_amdgcn_readfirstlane(lds_addr);
  asm volatile("s_mov_b32 m0, %2\n\ts_nop 0\n\tglobal_load_lds_dwordx4 %0, %1 sc1" ::"v"(voff), "s"(sbase), "s"(lds_addr) : "memory");
}
typedef unsigned long long u64;
typedef __attribute__((address_space(1))) u64 gu64;
#define RLX_AGENT __ATOMIC_RELAXED, __HIP_MEMORY_SCOPE_AGENT
constexpr int NWC = C_NW0 + C_NW1;
template <bool WF>
DI void conv_body(const u16* __restrict__ ssm_k, const u16* __restrict__ ssm_D, char* __restrict__ ws, char* smem,
                  const int w, u32x4 (&wf)[28], const char* wbase) {
  const int lane = threadIdx.x & 63;
  const int ch = w >> 2, q = w & 3;
  const int cpair = blockIdx.x;
  const int d = 2 * cpair + ch;
  char* L = smem + ch * K1_CH;
  const u32 L0 = (u32)(size_t)(LAS char*)L;
  const u16* kf = ssm_k + (size_t)d * S;
  const u32 vo = lane * 16;
  {
    const u32 vl = (lane > 62 ? 62 : lane) * 16;
    glds16x4_asm(kf + (2 + 2 * q), vo, vl, L0 + q * K1_KSTR + 64);
    const char* ub = ws + OFF_RO + ((q >> 1) ? 0 : 2) + (size_t)d * S * 2 + (q & 1) * 2048;
    const u32 ul = L0 + K1_UOFF + (q >> 1) * K1_USTR + (q & 1) * 2048;
    glds16_nom0(ub, vo, ul);
    glds16_nom0(ub + 1024, vo, ul + 1024);
  }
  const int n = lane & 15, q4 = lane >> 4;
  const int pbase = 32 * (n >> 1) + 8 * q4 + 4 * (n & 1) + 512 * q;
  const int di = n + q - 15;
  u32 head;
  asm volatile("global_load_dword %0, %1, %2" : "=v"(head) : "v"((u32)(di < 0 ? 0 : di) * 4), "s"(uni(kf)) : "memory");
  auto ldwf = [&](auto kI) {
    constexpr int k = decltype(kI)::value;
    gload16_off<(k % 4) * 1024>(wf[k], wbase + (k / 4) * 4096, vo);
  };
  bar();
  if constexpr (WF) static_for<0, C_NW0>(ldwf);
  const CAS float* MODc = (const CAS float*)(size_t)(ws + OFF_MOD);
  const float sa = MODc[d], ca = MODc[D + d], ga = MODc[2 * D + d], cbv = MODc[4 * D + d];
  const u32 Draw = ((const CAS u32*)(size_t)ssm_D)[cpair];
  if (lane < 14) *(LAS u32x4*)(L + K1_UOFF + (q >> 1) * K1_USTR + (256 + 14 * (q & 1) + lane) * 16) = u32x4{0u, 0u, 0u, 0u};
  u32 Ab = L0 + (n & 3) * K1_KSTR + ((n >> 2) + q4 + 8 * q) * 16;
  u32 Bb = L0 + K1_UOFF + (n & 1) * K1_USTR + (28 - 4 * (n >> 1) + q4 + 8 * q) * 16;
  u32 Hd = L0 + q * K1_KSTR + n * 4;
  asm volatile("" : "+v"(Ab), "+v"(Bb), "+v"(Hd) : "s"(sa), "s"(ca), "s"(ga), "s"(Draw), "s"(cbv));
  asm volatile("s_waitcnt vmcnt(%1)" : "+v"(head) : "n"(WF ? C_NW0 : 0) : "memory");
  *(LAS u32*)(size_t)Hd = di < 0 ? 0u : head;
  if (q == 1 && lane == 0) *(LAS u16*)(L + K1_UOFF + 2047 * 2) = 0;
  wait_lgkm0();
  bar();
  bf16x8 fa[8], fb[8];
  auto rd = [&](int dl) {
#pragma unroll
    for (int g = 0; g < 8; ++g) {
      fa[g] = *(const LAS bf16x8*)(size_t)(Ab + 64 * dl + 512 * g);
      fb[g] = *(const LAS bf16x8*)(size_t)(Bb + 64 * dl + 512 * (7 - g));
    }
  };
  rd(0);
  __builtin_amdgcn_sched_barrier(0);
  u32x2 qv[2];
  f32x4 mu[2], rs[2];
  asm volatile("global_load_dwordx2 %0, %6, %8\n\t"
               "global_load_dwordx2 %1, %6, %8 offset:512\n\t"
               "global_load_dwordx4 %2, %7, %9\n\t"
               "global_load_dwordx4 %3, %7, %9 offset:1024\n\t"
               "global_load_dwordx4 %4, %7, %10\n\t"
               "global_load_dwordx4 %5, %7, %10 offset:1024"
               : "=&v"(qv[0]), "=&v"(qv[1]), "=&v"(mu[0]), "=&v"(mu[1]), "=&v"(rs[0]), "=&v"(rs[1])
               : "v"((u32)pbase * 2), "v"((u32)pbase * 4), "s"(uni(ws + OFF_QT + (size_t)d * S * 2)),
                 "s"(uni(ws + OFF_MEAN)), "s"(uni(ws + OFF_RSTD))
               : "memory");
  const u32x4 sent = {~0u, ~0u, ~0u, ~0u};
  const void* fillb = uni(ws + OFF_ACT + (size_t)(cpair * 8 + w) * 4096);
  f32x4 acc[8];
#pragma unroll
  for (int j = 0; j < 8; ++j) acc[j] = f32x4{0.f, 0.f, 0.f, 0.f};
  static_for<0, 72>([&](auto mI) {
    constexpr int m = decltype(mI)::value, t = m % 36;
    constexpr int sd = t < 1 ? 0 : t < 3 ? 1 : t < 6 ? 2 : t < 10 ? 3 : t < 15 ? 4 : t < 21 ? 5 : t < 28 ? 6 : 7;
    constexpr int g = t - sd * (sd + 1) / 2;
    if constexpr (m == 36) {
      rd(1);
      __builtin_amdgcn_sched_barrier(0);
    }
    f32x4 a = __builtin_amdgcn_mfma_f32_16x16x32_bf16(fa[g], fb[sd - g], acc[sd], 0, 0, 0);
    asm volatile("" : "+v"(a));
    acc[sd] = a;
    if constexpr (m % 4 == 3) {
      constexpr int v = m / 4;
      if constexpr (v < 4) {
        u32x4 sv = sent;
        asm volatile("global_store_dwordx4 %0, %1, %2 offset:%3 sc1\n\ts_nop 1" ::"v"(vo), "v"(sv), "s"(fillb), "n"(v * 1024) : "memory");
      } else if constexpr (WF && v - 4 < C_NW1) {
        ldwf(std::integral_constant<int, C_NW0 + v - 4>{});
      }
      __builtin_amdgcn_sched_barrier(0);
    }
  });
  LAS f32x4* X = (LAS f32x4*)(L + K1_XOFF);
#pragma unroll
  for (int j = 0; j < 8; ++j) X[(j * 4 + q) * 64 + lane] = acc[j];
  u32x4 uc[2];
#pragma unroll
  for (int j = 0; j < 2; ++j)
    uc[j] = *(const LAS u32x4*)(L + K1_UOFF + K1_USTR + (255 - (32 * (2 * q + j) + 4 * (n >> 1) + q4)) * 16);
  asm volatile("s_waitcnt vmcnt(%6)"
               : "+v"(qv[0]), "+v"(qv[1]), "+v"(mu[0]), "+v"(mu[1]), "+v"(rs[0]), "+v"(rs[1])
               : "n"(WF ? C_NW1 : 0)
               : "memory");
  f32x4 qf[2], hg[2], kd[2];
  {
    const int sh = (n & 1) ? 16 : 0;
    const float gc = ga * round_bf16(1.f + ca), gs = ga * sa, Dd = ch ? hi_f(Draw) : lo_f(Draw);
#pragma unroll
    for (int j = 0; j < 2; ++j) {
#pragma unroll
      for (int r = 0; r < 4; ++r) {
        kd[j][r] = __builtin_bit_cast(float, (uc[j][3 - r] << sh) & 0xffff0000u) * Dd;
        qf[j][r] = (r & 1) ? hi_f(qv[j][r >> 1]) : lo_f(qv[j][r >> 1]);
        hg[j][r] = (qf[j][r] - mu[j][r]) * rs[j][r] * gc + gs;
      }
      asm volatile("" : "+v"(kd[j]), "+v"(qf[j]), "+v"(hg[j]));
    }
  }
  wait_lgkm0();
  bar();
  u16* X1T = (u16*)(ws + OFF_X1T) + (size_t)d * S + pbase;
  u16* XS = (u16*)(ws + OFF_XS) + (size_t)d * S + pbase;
  const float c1 = round_bf16(1.f + cbv);
#pragma unroll
  for (int j = 0; j < 2; ++j) {
    const LAS f32x4* Xj = X + ((2 * q + j) * 4) * 64 + lane;
    const f32x4 y = (Xj[0] + Xj[64]) + (Xj[128] + Xj[192]);
    const f32x4 o = qf[j] + hg[j] * (y + kd[j]);
    const u32 p0 = umin32(pack_bf16(o[0], o[1])), p1 = umin32(pack_bf16(o[2], o[3]));
    st8_sc1(XS + 256 * j,
            u32x2{umin32(pack_bf16(lo_f(p0) * c1, hi_f(p0) * c1)), umin32(pack_bf16(lo_f(p1) * c1, hi_f(p1) * c1))});
    st8_sc1(X1T + 256 * j, u32x2{p0, p1});
  }
}

DI void k2_stats(char* __restrict__ ws, char* smem, const int sblk) {
  const int tid = threadIdx.x, lane = tid & 63;
  const int w = __builtin_amdgcn_readfirstlane(tid >> 6);
  const int n = lane & 31, h = lane >> 5;
  {
    const int p0 = sblk * 64;
    if (w < 7) {
      const int task = sblk * 7 + w, t = task >> 1, kh = task & 1;
      const u32x4* wsrc = (const u32x4*)(ws + OFF_WGU) + ((size_t)t * 28 + kh * 14) * 64 + lane;
      const u16* cbp = (const u16*)(ws + OFF_CB) + (n & 1) * D + kh * 224 + 8 * h;
      u32x4 fa[14], fb[14];
#pragma unroll
      for (int i = 0; i < 14; ++i) {
        fa[i] = wsrc[i * 64];
        fb[i] = *(const u32x4*)(cbp + 16 * i);
      }
      __builtin_amdgcn_sched_barrier(0);
      f32x16 va;
#pragma unroll
      for (int r = 0; r < 16; ++r) va[r] = 0.f;
#pragma unroll
      for (int i = 0; i < 14; ++i) va = mfma32(fb[i], __builtin_bit_cast(bf16x8, fa[i]), va);
      if (h == 0) {
        const float f0 = va[0], f1 = va[1];
        const u32 a0 = umin32(__builtin_bit_cast(u32, f0)), a1 = __builtin_bit_cast(u32, f1);
        __hip_atomic_store((gu64*)(size_t)(ws + OFF_VG) + kh * 3584 + t * 32 + n, ((u64)a1 << 32) | a0, RLX_AGENT);
      }
    }
    __syncthreads();
    __builtin_amdgcn_s_sleep(C_SSLEEP);
    const rsrc_t rx = mk_rsrc(ws + OFF_X1T + ((size_t)(w * 56) * S + p0) * 2, 56 * S * 2);
    const int c8 = lane & 7, r8 = lane >> 3;
    u32x4 x[7];
    for (int spin = 0; spin < (1 << 15); ++spin) {
      asm volatile("" ::: "memory");
      u32 m = 0;
#pragma unroll
      for (int i = 0; i < 7; ++i) x[i] = __builtin_amdgcn_raw_buffer_load_b128(rx, (r8 + 8 * i) * (S * 2) + c8 * 16, 0, 16);
#pragma unroll
      for (int i = 0; i < 7; ++i) m = umax3(umax3(m, x[i][0], x[i][1]), x[i][2], x[i][3]);
      if (__all(m != ~0u)) break;
      __builtin_amdgcn_s_sleep(1);
    }
    float sa[8], qa[8];
#pragma unroll
    for (int e = 0; e < 8; ++e) sa[e] = qa[e] = 0.f;
#pragma unroll
    for (int i = 0; i < 7; ++i)
#pragma unroll
      for (int e = 0; e < 8; ++e) {
        const float a = (e & 1) ? hi_f(x[i][e >> 1]) : lo_f(x[i][e >> 1]);
        sa[e] += a;
        qa[e] = __builtin_fmaf(a, a, qa[e]);
      }
    LAS float* P = (LAS float*)smem;
    {
      LAS float* pp = P + (w * 8 + r8) * 128 + c8 * 8;
      *(LAS f32x4*)pp = f32x4{sa[0], sa[1], sa[2], sa[3]};
      *(LAS f32x4*)(pp + 4) = f32x4{sa[4], sa[5], sa[6], sa[7]};
      *(LAS f32x4*)(pp + 64) = f32x4{qa[0], qa[1], qa[2], qa[3]};
      *(LAS f32x4*)(pp + 68) = f32x4{qa[4], qa[5], qa[6], qa[7]};
    }
    wait_lgkm0();
    {
      float s1 = 0.f, q1 = 0.f;
#pragma unroll
      for (int i = 0; i < 8; ++i) {
        s1 += P[(w * 8 + i) * 128 + lane];
        q1 += P[(w * 8 + i) * 128 + 64 + lane];
      }
      *(LAS f32x2*)(P + 8192 + (w * 64 + lane) * 2) = f32x2{s1, q1};
    }
    __syncthreads();
    if (tid < 64) {
      float sm = 0.f, sq = 0.f;
#pragma unroll
      for (int i = 0; i < 8; ++i) {
        const f32x2 t = *(const LAS f32x2*)(P + 8192 + (i * 64 + tid) * 2);
        sm += t[0];
        sq += t[1];
      }
      const float mean = sm * (1.f / D);
      float var = sq * (1.f / D) - mean * mean;
      var = (var < 0.f ? 0.f : var) + 1e-5f;
      const float rstd = __builtin_amdgcn_rsqf(var);
      const float sig = var * rstd;
      const float mh = round_bf16(mean), sh = round_bf16(sig);
      const u32 b0 = pack_bf16(-mh, mh - mean), b1 = pack_bf16(-mh, sh), b2 = pack_bf16(sig - sh, sh);
      const float c1 = -2.302208198f * rstd;
      const float c2 = -0.1029432397f * (rstd * rstd * rstd);
      gu64* G = (gu64*)(size_t)(ws + OFF_SG) + p0 + tid;
      __hip_atomic_store(G, ((u64)b1 << 32) | b0, RLX_AGENT);
      __hip_atomic_store(G + S, ((u64)__builtin_bit_cast(u32, var) << 32) | b2, RLX_AGENT);
      __hip_atomic_store(G + 2 * S, ((u64)__builtin_bit_cast(u32, c1) << 32) | __builtin_bit_cast(u32, c2), RLX_AGENT);
    }
  }
}

DI void k2_gemm(const u16* __restrict__ ssm_k, const u16* __restrict__ ssm_D, char* __restrict__ ws, u16* __restrict__ out,
                char* smem, const int w) {
  const int tid = threadIdx.x, lane = tid & 63;
  const int n = lane & 31, h = lane >> 5;
  const int xcd = blockIdx.x & 7, idx = blockIdx.x >> 3;
  const int mt = (xcd >> 1) * 4 + (idx & 3);
  const int dt = idx >> 2, sh2 = xcd & 1;
  const int ft = sh2 * 7 + dt;
  const int tile = ft * 8 + w;
  const u32 lds0 = (u32)(size_t)(LAS char*)smem;
  u32x4 wf[28], wd[28];
  const char* wbase = ws + OFF_WGU + (size_t)(tile * 28) * 1024;
  const u32 wa = lane * 16;
  auto ldwf = [&](auto kI) {
    constexpr int k = decltype(kI)::value;
    gload16_off<(k % 4) * 1024>(wf[k], wbase + (k / 4) * 4096, wa);
  };
  conv_body<true>(ssm_k, ssm_D, ws, smem, w, wf, wbase);
  const char* xbase = ws + OFF_XS + ((size_t)(8 * w) * S + mt * 128) * 2;
  const u32 xoff = (u32)((lane >> 3) * S + ((lane & 7) ^ (((lane >> 4) & 1) << 2)) * 8) * 2;
  const char* wdbase = ws + OFF_WD + ((size_t)(2 * dt) * 112 + 14 * w) * 1024 + 4096;
  const int gsl = w < 6 ? w : w - 6;
  const char* gbase = ws + OFF_SG + (size_t)((gsl >> 1) * S + mt * 128 + (gsl & 1) * 64) * 8;
  auto ldwd = [&](auto kI) {
    constexpr int q = decltype(kI)::value, k = (q & 1) * 14 + (q >> 1), kk = k % 14;
    gload16_off<(kk % 8) * 1024 - 4096>(wd[k], wdbase + (k / 14) * 112 * 1024 + (kk / 8) * 8192, wa);
  };
  auto ldx = [&](auto cI) {
    constexpr int c = decltype(cI)::value;
    glds16_sc1(xbase + (size_t)(c % 7) * (64 * S * 2) + (c / 7) * 128, xoff, lds0 + (64 * c + 8 * w) * 128);
  };
  auto xok = [&](auto c0I, auto c1I) {
    u32 m = 0;
#pragma unroll
    for (int c = decltype(c0I)::value; c < decltype(c1I)::value; ++c) {
      const u32x4 t = *(const LAS u32x4*)(smem + (64 * c + 8 * w) * 128 + lane * 16);
      m = umax3(umax3(m, t[0], t[1]), t[2], t[3]);
    }
    return (bool)__all(m != ~0u);
  };
  auto xvalid = [&](auto c0I, auto c1I) {
    if (__builtin_expect(!xok(c0I, c1I), 0)) {
#pragma nounroll
      for (int spin = 0; spin < (1 << 14); ++spin) {
        __builtin_amdgcn_s_sleep(4);
        static_for<decltype(c0I)::value, decltype(c1I)::value>(ldx);
        wait_vm<0>();
        if (xok(c0I, c1I)) break;
      }
    }
    __builtin_amdgcn_sched_barrier(0);
  };
  wait_lgkm0();
  bar();
  auto pin = [](f32x16& a) { asm volatile("" : "+v"(a)); };
  auto pinw = [&](auto kI) {
    u32x4 t = wf[decltype(kI)::value];
    asm volatile("" : "+v"(t)::"memory");
    wf[decltype(kI)::value] = t;
  };
  u32x4 aext;
  constexpr int NWH = NWC + (C_NWH - NWC);
  constexpr int NWP = NWC + C_NWP;
  static_for<NWC, NWP>(ldwf);
  static_for<0, 7>(ldx);
  static_for<NWP, NWH>(ldwf);
  int xo[2][2];
  {
    const int blk = (lane >> 4) & 1, qq = (lane & 15) >> 2, p = lane & 3;
#pragma unroll
    for (int i = 0; i < 2; ++i)
#pragma unroll
      for (int t = 0; t < 2; ++t)
        xo[i][t] = 128 * (8 * h + 4 * t + qq) + 16 * ((4 * i + 2 * blk + (p >> 1)) ^ ((qq >> 1) << 2)) + 8 * (p & 1);
  }
  f32x16 acc[4];
#pragma unroll
  for (int i = 0; i < 4; ++i)
#pragma unroll
    for (int r = 0; r < 16; ++r) acc[i][r] = 0.f;
  asm volatile("" : "+v"(xo[0][0]), "+v"(xo[0][1]), "+v"(xo[1][0]), "+v"(xo[1][1]));
  constexpr int PF = 3;
  bf16x8 xr[PF][2];
  auto rdx = [&](auto kI, auto iI) {
    constexpr int k = decltype(kI)::value, i = decltype(iI)::value;
    if constexpr (k == 28) {
      int a0 = xo[i][0] + K2_XH, a1 = xo[i][1] + K2_XH;
      asm volatile("" : "+v"(a0), "+v"(a1));
      xo[i][0] = a0;
      xo[i][1] = a1;
    }
    xr[k % PF][i] = tr_frag(smem, xo[i][0] + (k % 28) * 2048, xo[i][1] + (k % 28) * 2048);
  };
  using I0 = std::integral_constant<int, 0>;
  using I1 = std::integral_constant<int, 1>;
  using I2 = std::integral_constant<int, 2>;
  using I3 = std::integral_constant<int, 3>;
  using I7 = std::integral_constant<int, 7>;
  using I14 = std::integral_constant<int, 14>;
  LAS u64* CS = (LAS u64*)(smem + K2_ST);
  wait_vm<4 + NWH - NWP>();
  static_for<0, NWP>(pinw);
  xvalid(I0{}, I3{});
  bar();
  static_for<0, PF - 1>([&](auto jI) {
    rdx(jI, I0{});
    rdx(jI, I1{});
  });
  __builtin_amdgcn_sched_barrier(0);
  u64 gv, gp0, gp1;
  const char* vbase = ws + OFF_VG + (size_t)tile * 256;
  constexpr int RW = 28 - NWH, KX = RW + 7, KV = 25;
  constexpr int KF = C_KF;
  constexpr int WE = 27 - KX;
  static_assert(KF >= KX && KF <= 27, "");
  static_assert(KX + 4 <= KV && WE <= 9, "stream does not fit");
  static_for<0, 28>([&](auto kI) {
    constexpr int k = decltype(kI)::value;
    if constexpr (k == 6) {
      wait_vm<6>();
      static_for<NWP, NWH>(pinw);
      xvalid(I3{}, I7{});
      bar();
    }
    if constexpr (k >= NWH) {
      u32x4 t = wf[k];
      asm volatile("s_waitcnt vmcnt(%1)" : "+v"(t) : "n"(NWH - 1 + (k > KF ? 2 : 0)) : "memory");
      wf[k] = t;
    }
    if constexpr (k == KV) {
      wait_vm<KV - KX + (KV > KF ? 2 : 0)>();
      xvalid(I7{}, I14{});
      bar();
    }
    __builtin_amdgcn_sched_barrier(0);
    acc[0] = mfma32(wf[k], xr[k % PF][0], acc[0]);
    pin(acc[0]);
    __builtin_amdgcn_sched_barrier(0);
    rdx(std::integral_constant<int, k + PF - 1>{}, I0{});
    __builtin_amdgcn_sched_barrier(0);
    acc[1] = mfma32(wf[k], xr[k % PF][1], acc[1]);
    pin(acc[1]);
    __builtin_amdgcn_sched_barrier(0);
    rdx(std::integral_constant<int, k + PF - 1>{}, I1{});
    if constexpr (k < RW) ldwf(std::integral_constant<int, NWH + k>{});
    else if constexpr (k < KX) ldx(std::integral_constant<int, 7 + k - RW>{});
    else if constexpr (k != KF) ldwd(std::integral_constant<int, k - KX - (k > KF ? 1 : 0)>{});
    else {
      u64 t, t0, t1;
      asm volatile("global_load_dwordx2 %0, %1, %2 sc1" : "=v"(t) : "v"((u32)lane * 8), "s"(uni(gbase)) : "memory");
      asm volatile("global_load_dwordx2 %0, %1, %2 sc1" : "=v"(t0) : "v"((u32)n * 8), "s"(uni(vbase)) : "memory");
      asm volatile("global_load_dwordx2 %0, %1, %2 sc1" : "=v"(t1) : "v"((u32)n * 8), "s"(uni(vbase + 3584 * 8)) : "memory");
      gv = t;
      gp0 = t0;
      gp1 = t1;
    }
    __builtin_amdgcn_sched_barrier(0);
  });
  const rsrc_t rw = mk_rsrc(ws + OFF_ACT + ((size_t)(mt * 4) * 112 + tile) * 1024, 337 * 1024);
  const rsrc_t ra = mk_rsrc(ws + OFF_ACT + ((size_t)(mt * 4 + sh2) * 112 + 14 * w) * 1024, 238 * 1024);
  u32x4 af[28];
  auto ldact = [&](auto kI) {
    constexpr int k = decltype(kI)::value;
    af[k] = __builtin_amdgcn_raw_buffer_load_b128(ra, lane * 16 + (k % 14) * 1024, (k / 14) * 224 * 1024, 16);
  };
  float c1[2], c2[2], var[2], pe[2], pm[2], pr[2];
  u32x4 ov[2];
  auto corr = [&](auto iI) {
    constexpr int i = decltype(iI)::value;
    u64 g[3];
#pragma unroll
    for (int k = 0; k < 3; ++k) g[k] = CS[k * 128 + i * 32 + n];
    const u32x4 bext = {(u32)g[0], (u32)(g[0] >> 32), (u32)g[1], 0u};
    acc[i] = mfma32(aext, __builtin_bit_cast(bf16x8, bext), acc[i]);
    c1[i & 1] = __builtin_bit_cast(float, (u32)(g[2] >> 32));
    c2[i & 1] = __builtin_bit_cast(float, (u32)g[2]);
    var[i & 1] = __builtin_bit_cast(float, (u32)(g[1] >> 32));
  };
  auto slice = [&](auto iI, auto uI) {
    constexpr int i = decltype(iI)::value, u = decltype(uI)::value, z = i & 1;
    if constexpr (u >= 1) {
      float o = pm[z] * __builtin_amdgcn_rcpf(__builtin_fmaf(pe[z], var[z], var[z]));
      if constexpr ((u - 1) & 1) {
        u32 t = umin32(pack_bf16(pr[z], o));
        asm volatile("" : "+v"(t));
        ov[z][(u - 1) / 2] = t;
      } else {
        asm volatile("" : "+v"(o));
        pr[z] = o;
      }
    }
    if constexpr (u < 8) {
      const float a = acc[i][u];
      float e = __builtin_amdgcn_exp2f(__builtin_fmaf(a * a, c2[z], c1[z]) * a), m = a * acc[i][u + 8];
      asm volatile("" : "+v"(e), "+v"(m));
      pe[z] = e;
      pm[z] = m;
    } else {
      __builtin_amdgcn_raw_buffer_store_b128(ov[z], rw, lane * 16 + i * 112 * 1024, 0, 16);
      asm volatile("s_nop 1");
    }
  };
  constexpr int KG = C_KG;
  static_for<0, 28>([&](auto kI) {
    constexpr int k = decltype(kI)::value;
    if constexpr (k == KG) {
      u64 t = gv, p0 = gp0, p1 = gp1;
      asm volatile("s_waitcnt vmcnt(%3)" : "+v"(t), "+v"(p0), "+v"(p1) : "n"(KG + 27 - KF) : "memory");
      if (!__all((t >> 32) != 0 && (u32)p0 != ~0u && (u32)p1 != ~0u)) {
        for (int spin = 0; spin < (1 << 16); ++spin) {
          __builtin_amdgcn_s_sleep(2);
          t = __hip_atomic_load((gu64*)(size_t)gbase + lane, RLX_AGENT);
          p0 = __hip_atomic_load((gu64*)(size_t)vbase + n, RLX_AGENT);
          p1 = __hip_atomic_load((gu64*)(size_t)vbase + 3584 + n, RLX_AGENT);
          if (__all((t >> 32) != 0 && (u32)p0 != ~0u && (u32)p1 != ~0u)) break;
        }
        __builtin_amdgcn_s_waitcnt(0x0F70);
      }
      CS[gsl * 64 + lane] = t;
      const float v1 = __builtin_bit_cast(float, (u32)p0) + __builtin_bit_cast(float, (u32)p1);
      const float v2 = __builtin_bit_cast(float, (u32)(p0 >> 32)) + __builtin_bit_cast(float, (u32)(p1 >> 32));
      const float v1h = round_bf16(v1), v2h = round_bf16(v2);
      u32x4 a = {pack_bf16(v1h, v1h), pack_bf16(v1 - v1h, v2h), pack_bf16(v2h, v2 - v2h), 0u};
      if (h) a = u32x4{0u, 0u, 0u, 0u};
      asm volatile("" : "+v"(a));
      aext = a;
      wait_lgkm0();
      __builtin_amdgcn_sched_barrier(0);
      bar();
      corr(I0{});
      corr(I1{});
      __builtin_amdgcn_sched_barrier(0);
    }
    acc[2] = mfma32(wf[k], xr[(28 + k) % PF][0], acc[2]);
    pin(acc[2]);
    __builtin_amdgcn_sched_barrier(0);
    if constexpr (k + PF - 1 < 28) rdx(std::integral_constant<int, 28 + k + PF - 1>{}, I0{});
    if constexpr (k > KG && k <= KG + 9) slice(I0{}, std::integral_constant<int, k - KG - 1>{});
    __builtin_amdgcn_sched_barrier(0);
    acc[3] = mfma32(wf[k], xr[(28 + k) % PF][1], acc[3]);
    pin(acc[3]);
    __builtin_amdgcn_sched_barrier(0);
    if constexpr (k % C_LOCK == C_LOCK - 1) bar();
    if constexpr (k + PF - 1 < 28) rdx(std::integral_constant<int, 28 + k + PF - 1>{}, I1{});
    if constexpr (k > KG && k <= KG + 9) slice(I1{}, std::integral_constant<int, k - KG - 1>{});
    if constexpr (WE + k < 28) ldwd(std::integral_constant<int, WE + k>{});
    else if constexpr (WE + k - 28 < 7) ldact(std::integral_constant<int, WE + k - 28>{});
    __builtin_amdgcn_sched_barrier(0);
  });
  constexpr int A0 = WE < 7 ? WE : 7;
  corr(I2{});
  corr(I3{});
  __builtin_amdgcn_sched_barrier(0);
  static_for<0, 9>([&](auto uI) {
    constexpr int u = decltype(uI)::value;
    slice(I2{}, uI);
    slice(I3{}, uI);
    if constexpr (A0 + u < 14 && u < 8) ldact(std::integral_constant<int, A0 + u>{});
    __builtin_amdgcn_sched_barrier(0);
  });
  static_for<(A0 + 8 < 14 ? A0 + 8 : 14), 14>(ldact);
  bar();
  auto chk = [&](auto gI) {
    constexpr int g = decltype(gI)::value;
    u32 m = 0;
#pragma unroll
    for (int k = 7 * g; k < 7 * g + 7; ++k) m = umax3(umax3(m, af[k][0], af[k][1]), af[k][2], af[k][3]);
    return (bool)__all(m != ~0u);
  };
  auto valid = [&](auto gI) {
    constexpr int g = decltype(gI)::value;
    if (__builtin_expect(!chk(gI), 0)) {
#pragma nounroll
      for (int spin = 0; spin < (1 << 14); ++spin) {
        __builtin_amdgcn_s_sleep(8);
        asm volatile("" ::: "memory");
        static_for<7 * g, 7 * g + 7>(ldact);
        if (chk(gI)) break;
      }
      __builtin_amdgcn_s_waitcnt(0x0F70);
    }
    __builtin_amdgcn_sched_barrier(0);
  };
  const int pcol = mt * 128 + sh2 * 32 + n;
  const int dbase = dt * 64 + w * 8 + 4 * h;
  __builtin_amdgcn_sched_barrier(0);
  LAS f32x4* R = (LAS f32x4*)smem;
  f32x16 o[4];
#pragma unroll
  for (int i = 0; i < 4; ++i)
#pragma unroll
    for (int r = 0; r < 16; ++r) o[i][r] = 0.f;
  auto dump1 = [&](auto tjI, auto cI) {
    constexpr int tj = decltype(tjI)::value, c = decltype(cI)::value, i = tj * 2 + (c >> 2), q = c & 3;
    R[((tj * 8 + c) * 8 + w) * 64 + lane] = f32x4{o[i][4 * q], o[i][4 * q + 1], o[i][4 * q + 2], o[i][4 * q + 3]};
  };
  auto step = [&](auto kI) {
    constexpr int k = decltype(kI)::value, tj = k / 14;
    const bf16x8 b = __builtin_bit_cast(bf16x8, af[k]);
    o[2 * tj] = mfma32(wd[k % 14], b, o[2 * tj]);
    pin(o[2 * tj]);
    __builtin_amdgcn_sched_barrier(0);
    if constexpr (k >= 15 && k < 19) dump1(I0{}, std::integral_constant<int, 2 * (k - 15)>{});
    __builtin_amdgcn_sched_barrier(0);
    o[2 * tj + 1] = mfma32(wd[14 + k % 14], b, o[2 * tj + 1]);
    pin(o[2 * tj + 1]);
    __builtin_amdgcn_sched_barrier(0);
    if constexpr (k >= 15 && k < 19) dump1(I0{}, std::integral_constant<int, 2 * (k - 15) + 1>{});
    if constexpr (k >= C_T1 && k < 14) {
      constexpr int n0 = ((k - C_T1) * 14) / (14 - C_T1), n1 = ((k + 1 - C_T1) * 14) / (14 - C_T1);
      static_for<14 + n0, 14 + n1>(ldact);
    }
    __builtin_amdgcn_sched_barrier(0);
  };
  u16 xres[2][4];
  f32x4 gbv;
  auto fin = [&](auto tjI) {
    constexpr int tj = decltype(tjI)::value;
    f32x4 v[8];
#pragma unroll
    for (int sw = 0; sw < 8; ++sw) v[sw] = R[((tj * 8 + w) * 8 + sw) * 64 + lane];
    const f32x4 sum = ((v[0] + v[1]) + (v[2] + v[3])) + ((v[4] + v[5]) + (v[6] + v[7]));
    float r[4];
#pragma unroll
    for (int e = 0; e < 4; ++e) r[e] = __builtin_fmaf(gbv[e], sum[e], bf2f(xres[tj][e]));
    const int pc = pcol + 64 * tj;
    const int srow = (pc & ~7) | ((pc & 3) << 1) | ((pc >> 2) & 1);
    *(u32x2*)(out + (size_t)srow * D + dbase) = u32x2{pack_bf16(r[0], r[1]), pack_bf16(r[2], r[3])};
  };
  valid(I0{});
  asm volatile(""
               : "+v"(wd[0]), "+v"(wd[1]), "+v"(wd[2]), "+v"(wd[3]), "+v"(wd[4]), "+v"(wd[5]), "+v"(wd[6]), "+v"(wd[7]),
                 "+v"(wd[8]), "+v"(wd[9]), "+v"(wd[10]), "+v"(wd[11]), "+v"(wd[12]), "+v"(wd[13])::"memory");
  asm volatile(""
               : "+v"(wd[14]), "+v"(wd[15]), "+v"(wd[16]), "+v"(wd[17]), "+v"(wd[18]), "+v"(wd[19]), "+v"(wd[20]),
                 "+v"(wd[21]), "+v"(wd[22]), "+v"(wd[23]), "+v"(wd[24]), "+v"(wd[25]), "+v"(wd[26]), "+v"(wd[27])::"memory");
  static_for<0, 3>(step);
  {
    const rsrc_t rx = mk_rsrc(ws + OFF_X1T, D * S * 2);
#pragma unroll
    for (int r = 0; r < 8; ++r)
      xres[r >> 2][r & 3] = __builtin_amdgcn_raw_buffer_load_b16(rx, ((dbase + (r & 3)) * S + pcol + (r >> 2) * 64) * 2, 0, 16);
    gbv = *(const f32x4*)((const float*)(ws + OFF_MOD) + 5 * D + dbase);
    __builtin_amdgcn_sched_barrier(0);
  }
  static_for<3, 7>(step);
  valid(I1{});
  static_for<7, 14>(step);
  valid(I2{});
  static_for<14, 21>(step);
  wait_lgkm0();
  bar();
  valid(I3{});
  static_for<21, 24>(step);
  fin(I0{});
  __builtin_amdgcn_sched_barrier(0);
  static_for<24, 28>(step);
  static_for<0, 8>([&](auto cI) { dump1(I1{}, cI); });
  wait_lgkm0();
  bar();
  fin(I1{});
}

extern "C" __global__ NOPK __launch_bounds__(512, 2) void k1_main(const u16* __restrict__ ssm_k,
                                                                  const u16* __restrict__ ssm_D, char* __restrict__ ws,
                                                                  u16* __restrict__ out) {
  extern __shared__ __attribute__((aligned(16))) char smem[];
  const int w = __builtin_amdgcn_readfirstlane(threadIdx.x >> 6);
  if (blockIdx.x >= 224) {
    k2_stats(ws, smem, blockIdx.x - 224);
  } else {
    k2_gemm(ssm_k, ssm_D, ws, out, smem, w);
  }
}

extern "C" __device__ __attribute__((used, noinline)) void k_code_end() {}

extern "C" void kernel_launch(void* const* d_in, const int* in_sizes, int n_in, void* d_out, int out_size, void* d_ws,
                              size_t ws_size, hipStream_t stream) {
  const u16* q = (const u16*)d_in[0];
  const u16* k = (const u16*)d_in[1];
  const u16* v = (const u16*)d_in[2];
  const u16* shift_k = (const u16*)d_in[3];
  const u16* ssm_k = (const u16*)d_in[4];
  const u16* ssm_D = (const u16*)d_in[5];
  const u16* w_gate = (const u16*)d_in[7];
  const u16* w_up = (const u16*)d_in[8];
  const u16* w_down = (const u16*)d_in[9];
  const u16* c = (const u16*)d_in[10];
  const u16* w_mod = (const u16*)d_in[11];
  char* ws = (char*)d_ws;
  (void)hipFuncSetAttribute((const void*)k1_main, hipFuncAttributeMaxDynamicSharedMemorySize, K2_LDS);
  k0_prep<<<K0_GRID, 256, 0, stream>>>(q, k, v, shift_k, c, w_mod, w_gate, w_up, w_down, ws);
  k1_main<<<256, 512, K2_LDS, stream>>>(ssm_k, ssm_D, ws, (u16*)d_out);
}
```

```cpp
#include <hip/hip_runtime.h>
#include <hip/hip_bf16.h>
#include <stdint.h>
#include <type_traits>

typedef unsigned short u16;
typedef unsigned int u32;
typedef short bf16x8 __attribute__((ext_vector_type(8)));
typedef short s16x4 __attribute__((ext_vector_type(4)));
typedef float f32x2 __attribute__((ext_vector_type(2)));
typedef float f32x4 __attribute__((ext_vector_type(4)));
typedef float f32x16 __attribute__((ext_vector_type(16)));
typedef u32 u32x2 __attribute__((ext_vector_type(2)));
typedef u32 u32x4 __attribute__((ext_vector_type(4)));
typedef __bf16 bf16x2_t __attribute__((ext_vector_type(2)));
#define LAS __attribute__((address_space(3)))
#define DI __device__ __forceinline__

constexpr int S = 2048, D = 448, F = 1792;

constexpr size_t SZ_DS = (size_t)D * S * 2;
constexpr size_t OFF_RO = SZ_DS;
constexpr size_t OFF_QT = OFF_RO + SZ_DS;
constexpr size_t OFF_X1T = OFF_QT + SZ_DS;
constexpr size_t OFF_MEAN = OFF_X1T + SZ_DS;
constexpr size_t OFF_RSTD = OFF_MEAN + S * 4;
constexpr size_t OFF_MOD = OFF_RSTD + S * 4;
constexpr size_t OFF_SG = OFF_MOD + 16384;
constexpr size_t OFF_CB = OFF_MOD + 12288;

constexpr size_t OFF_VG = OFF_SG + 3 * S * 8;
constexpr size_t OFF_XS = OFF_VG + 4 * S * 8;
constexpr size_t OFF_WGU = OFF_XS + SZ_DS;
constexpr size_t OFF_WD = OFF_WGU + (size_t)2 * D * F * 2;
constexpr size_t OFF_ACT = OFF_WD + (size_t)D * F * 2;

#if defined(__HIP_DEVICE_COMPILE__)
#define NOPK __attribute__((target("no-packed-fp32-ops")))
#else
#define NOPK
#endif

constexpr int C_KF = 25;
constexpr int C_KG = 1;
constexpr int C_LOCK = 4;
constexpr int C_NW0 = 5;
constexpr int C_NW1 = 5;
constexpr int C_NWH = 15;
constexpr int C_NWP = 3;
constexpr int C_SSLEEP = 50;
constexpr int C_T1 = 5;

template <int I, int N, class Fn>
DI void static_for(Fn&& f) {
  if constexpr (I < N) {
    f(std::integral_constant<int, I>{});
    static_for<I + 1, N>(f);
  }
}

DI float lo_f(u32 x) { return __builtin_bit_cast(float, x << 16); }
DI float hi_f(u32 x) { return __builtin_bit_cast(float, x & 0xffff0000u); }
DI u32 pack_bf16(float a, float b) {
  f32x2 v = {a, b};
  return __builtin_bit_cast(u32, __builtin_convertvector(v, bf16x2_t));
}
DI float round_bf16(float a) { return lo_f(pack_bf16(a, 0.f)); }
DI float bf2f(u16 x) { return __builtin_bit_cast(float, (u32)x << 16); }

template <int N>
DI void wait_vm() { asm volatile("s_waitcnt vmcnt(%0)" ::"n"(N) : "memory"); }
DI void wait_lgkm0() { asm volatile("s_waitcnt lgkmcnt(0)" ::: "memory"); }
DI void bar() { __builtin_amdgcn_s_barrier(); }

DI const void* uni(const void* p) {
  const unsigned long long v = (unsigned long long)p;
  const u32 lo = __builtin_amdgcn_readfirstlane((u32)v), hi = __builtin_amdgcn_readfirstlane((u32)(v >> 32));
  return (const void*)(((unsigned long long)hi << 32) | lo);
}
DI void glds16(const void* g, char* lds) {
  __builtin_amdgcn_global_load_lds((const __attribute__((address_space(1))) void*)g, (LAS void*)lds, 16, 0, 0);
}

template <int CTRL>
DI float dpp_add(float v) {
  const int x = __builtin_bit_cast(int, v);
  return v + __builtin_bit_cast(float, __builtin_amdgcn_update_dpp(x, x, CTRL, 0xf, 0xf, false));
}
DI float sum8(float v) {
  v = dpp_add<0xB1>(v);
  v = dpp_add<0x4E>(v);
  v = dpp_add<0x141>(v);
  return v;
}
DI float dot2(u32 a, u32 b, float c) {
  return __builtin_amdgcn_fdot2_f32_bf16(__builtin_bit_cast(bf16x2_t, a), __builtin_bit_cast(bf16x2_t, b), c, false);
}
typedef __amdgpu_buffer_rsrc_t rsrc_t;
DI rsrc_t mk_rsrc(const void* p, u32 bytes) { return __builtin_amdgcn_make_buffer_rsrc((void*)p, 0, bytes, 0x00020000); }
DI void st16_sc1(void* p, u32x4 v) { asm volatile("global_store_dwordx4 %0, %1, off sc1\n\ts_nop 1" ::"v"(p), "v"(v) : "memory"); }
DI void st8_sc1(void* p, u32x2 v) { asm volatile("global_store_dwordx2 %0, %1, off sc1" ::"v"(p), "v"(v) : "memory"); }

DI bf16x8 tr_frag(const char* smem, int o0, int o1) {
  s16x4 x0 = __builtin_amdgcn_ds_read_tr16_b64_v4i16((LAS s16x4*)(smem + o0));
  s16x4 x1 = __builtin_amdgcn_ds_read_tr16_b64_v4i16((LAS s16x4*)(smem + o1));
  return __builtin_shufflevector(x0, x1, 0, 1, 2, 3, 4, 5, 6, 7);
}
DI f32x16 mfma32(u32x4 a, bf16x8 b, f32x16 c) {
  return __builtin_amdgcn_mfma_f32_32x32x16_bf16(__builtin_bit_cast(bf16x8, a), b, c, 0, 0, 0);
}

DI void retile(int task, const u16* __restrict__ w_gate, const u16* __restrict__ w_up, const u16* __restrict__ w_down,
               char* __restrict__ ws, char* smem, int w, int lane) {
  const int xr = lane >> 4, slot = lane & 15;
  const int chn = slot ^ ((xr << 2) | (w & 3));
  const int h = lane >> 5, blk = (lane >> 4) & 1, qq = (lane & 15) >> 2, p = lane & 3;
  const bool gu = task < 112;
  int kq = 0, fg = 0, dg = 0, fk = 0;
  if (gu) {
    fg = task % 28, kq = task / 28;
    const u16* src = (chn < 8 ? w_gate : w_up) + (size_t)(kq * 112 + 4 * w + xr) * F + fg * 64 + (chn & 7) * 8;
#pragma unroll
    for (int i = 0; i < 7; ++i) glds16(src + (size_t)i * 16 * F, smem + (w + 4 * i) * 1024);
  } else {
    dg = (task - 112) % 7, fk = (task - 112) / 7;
    const u16* src = w_down + (size_t)(fk * 224 + (chn >> 3) * 112 + 4 * w + xr) * D + dg * 64 + (chn & 7) * 8;
#pragma unroll
    for (int i = 0; i < 7; ++i) glds16(src + (size_t)i * 16 * D, smem + (w + 4 * i) * 1024);
  }
  const int col = gu ? blk * 128 + w * 32 + (p & 1) * 16 + (p >> 1) * 8 : (w >> 1) * 128 + (w & 1) * 64 + blk * 32 + p * 8;
  int o[2];
#pragma unroll
  for (int t = 0; t < 2; ++t)
    o[t] = 256 * (8 * h + 4 * t + qq) + 16 * ((col >> 4) ^ ((qq << 2) | ((2 * h + t) & 3))) + (col & 8);
  u32x4* dst = gu ? (u32x4*)(ws + OFF_WGU) + ((size_t)(fg * 4 + w) * 28 + kq * 7) * 64 + lane
                  : (u32x4*)(ws + OFF_WD) + ((size_t)(dg * 2 + (w & 1)) * 112 + fk * 14 + (w >> 1) * 7) * 64 + lane;
  __syncthreads();
#pragma unroll
  for (int i = 0; i < 7; ++i)
    st16_sc1(dst + i * 64, __builtin_bit_cast(u32x4, tr_frag(smem, o[0] + i * 4096, o[1] + i * 4096)));
}

constexpr int K0_GRID = 504;

extern "C" __global__ __launch_bounds__(256) void k0_prep(const u16* __restrict__ q, const u16* __restrict__ k,
                                               const u16* __restrict__ v, const u16* __restrict__ shift_k,
                                               const u16* __restrict__ c, const u16* __restrict__ w_mod,
                                               const u16* __restrict__ w_gate,
                                               const u16* __restrict__ w_up, const u16* __restrict__ w_down,
                                               char* __restrict__ ws) {
  __shared__ __attribute__((aligned(16))) char smem[57344 + 1024 + 512];
  const int b = blockIdx.x, tid = threadIdx.x, lane = tid & 63, w = __builtin_amdgcn_readfirstlane(tid >> 6);
  const int xcd = b & 7, idx = b >> 3;
  u32 cwarm = 0;
  if (idx < 2) {
    u32 lo, hi, len;
    asm volatile(
        "s_getpc_b64 s[40:41]\n\ts_add_u32 s40, s40, k0_prep@rel32@lo+4\n\ts_addc_u32 s41, s41, k0_prep@rel32@hi+12\n\t"
        "s_mov_b32 %0, s40\n\ts_mov_b32 %1, s41\n\ts_mov_b32 %2, k_code_end-k0_prep"
        : "=s"(lo), "=s"(hi), "=s"(len)::"s40", "s41", "scc");
    const char* code = (const char*)(((unsigned long long)hi << 32) | lo);
    if ((u32)(idx * 256 + tid) * 128 < len) cwarm = *(const __attribute__((address_space(1))) u32*)(size_t)(code + (idx * 256 + tid) * 128);
  }
  asm volatile("" ::"s"(q), "s"(k), "s"(v), "s"(shift_k), "s"(c), "s"(w_mod), "s"(w_gate), "s"(w_up), "s"(w_down),
               "s"(ws));
  if (__builtin_expect(idx >= 42, 0)) {
    retile(b - 336, w_gate, w_up, w_down, ws, smem, w, lane);
    if (b - 336 < 128) {
      const int t = b - 336;
      u32x4* ap = (u32x4*)(ws + (t < 64 ? OFF_X1T : OFF_XS)) + (size_t)(t & 63) * 1792 + tid;
      const u32x4 sent = {~0u, ~0u, ~0u, ~0u};
#pragma unroll
      for (int i = 0; i < 7; ++i) st16_sc1(ap + i * 256, sent);
    }
    return;
  }
  int misc = -1;
  if (idx < 6) misc = 64 + b;
  else if (idx >= 34) misc = xcd * 8 + (idx - 34);
  const int rb = xcd * 4 + ((idx - 6) & 3), cg = (idx - 6) >> 2;
  if (__builtin_expect(misc < 0, 1)) {
    const int sq = lane >> 5, cp = lane & 31;
    const int d0 = cg * 64 + cp * 2;
    const int a = rb * 8 + w * 2 + sq;
    const int s0 = a * 8;
    const rsrc_t rk = mk_rsrc(k, S * D * 2), rv = mk_rsrc(v, S * D * 2), rq = mk_rsrc(q, S * D * 2);
    const int oB = (s0 * D + d0) * 2;
    const int oA = oB - 4 * D * 2, oC = oB + 4 * D * 2;
    u32 kk[12], vv[9], qq[8];
#pragma unroll
    for (int i = 1; i < 4; ++i) kk[i] = __builtin_amdgcn_raw_buffer_load_b32(rk, oA + i * D * 2, 0, 0);
#pragma unroll
    for (int i = 0; i < 4; ++i) kk[4 + i] = __builtin_amdgcn_raw_buffer_load_b32(rk, oB + i * D * 2, 0, 0);
#pragma unroll
    for (int i = 0; i < 4; ++i) kk[8 + i] = __builtin_amdgcn_raw_buffer_load_b32(rk, oC + i * D * 2, 0, 0);
#pragma unroll
    for (int i = 0; i < 4; ++i) vv[1 + i] = __builtin_amdgcn_raw_buffer_load_b32(rv, oB + i * D * 2, 0, 0);
#pragma unroll
    for (int i = 0; i < 4; ++i) vv[5 + i] = __builtin_amdgcn_raw_buffer_load_b32(rv, oC + i * D * 2, 0, 0);
#pragma unroll
    for (int i = 0; i < 4; ++i) qq[i] = __builtin_amdgcn_raw_buffer_load_b32(rq, oB + i * D * 2, 0, 0);
#pragma unroll
    for (int i = 0; i < 4; ++i) qq[4 + i] = __builtin_amdgcn_raw_buffer_load_b32(rq, oC + i * D * 2, 0, 0);
    const u32x4 sk = *(const u32x4*)(shift_k + d0 * 4);
    f32x2 wt[4] = {{lo_f(sk[0]), lo_f(sk[2])}, {hi_f(sk[0]), hi_f(sk[2])}, {lo_f(sk[1]), lo_f(sk[3])}, {hi_f(sk[1]), hi_f(sk[3])}};
    f32x2 kf[12], u[9];
#pragma unroll
    for (int i = 1; i < 12; ++i) kf[i] = f32x2{lo_f(kk[i]), hi_f(kk[i])};
#pragma unroll
    for (int i = 1; i < 9; ++i) {
      f32x2 t = kf[i + 3] * wt[0];
      t = __builtin_elementwise_fma(kf[i + 2], wt[1], t);
      t = __builtin_elementwise_fma(kf[i + 1], wt[2], t);
      t = __builtin_elementwise_fma(kf[i], wt[3], t);
      u[i] = t * f32x2{lo_f(vv[i]), hi_f(vv[i])};
    }
    {
      char* dst = ws + (size_t)d0 * (S * 2) + (2040 - s0) * 2;
#pragma unroll
      for (int cI = 0; cI < 2; ++cI) {
        *(u32x4*)(dst + OFF_RO + cI * S * 2) = u32x4{pack_bf16(u[8][cI], u[7][cI]), pack_bf16(u[6][cI], u[5][cI]),
                                              pack_bf16(u[4][cI], u[3][cI]), pack_bf16(u[2][cI], u[1][cI])};
      }
      char* dq = ws + OFF_QT + (size_t)d0 * (S * 2) + s0 * 2;
#pragma unroll
      for (int cI = 0; cI < 2; ++cI) {
        const u32 sel = cI ? 0x07060302u : 0x05040100u;
        *(u32x4*)(dq + cI * S * 2) = u32x4{__builtin_amdgcn_perm(qq[2], qq[0], sel), __builtin_amdgcn_perm(qq[6], qq[4], sel),
                                    __builtin_amdgcn_perm(qq[3], qq[1], sel), __builtin_amdgcn_perm(qq[7], qq[5], sel)};
      }
    }
  } else if (misc < 64) {
    const int bb = misc;
    const int row = bb * 32 + w * 8 + (lane >> 3);
    const int sub = lane & 7;
    u32x4 x[7];
#pragma unroll
    for (int i = 0; i < 7; ++i) x[i] = *(const u32x4*)(q + (size_t)row * D + (i * 8 + sub) * 8);
    if (sub < 7) ((unsigned long long*)(ws + OFF_SG))[sub * S + row] = sub < 3 ? 0ull : ~0ull;
    float s = 0.f, s2 = 0.f;
#pragma unroll
    for (int i = 0; i < 7; ++i)
#pragma unroll
      for (int j = 0; j < 4; ++j) {
        const u32 t = x[i][j];
        s = dot2(t, 0x3f803f80u, s);
        s2 = dot2(t, t, s2);
      }
    s = sum8(s);
    s2 = sum8(s2);
    const float mean = s * (1.f / D);
    float var = s2 * (1.f / D) - mean * mean;
    var = var < 0.f ? 0.f : var;
    const float rstd = __builtin_amdgcn_rsqf(var + 1e-5f);
    if (sub < 2) {
      const int p = (row & ~7) | ((row & 1) << 2) | ((row >> 1) & 3);
      ((float*)(ws + OFF_MEAN))[sub * S + p] = sub ? rstd : mean;
    }
  } else if (misc < 106) {
    const int bb = misc - 64;
    {
      const int xr = lane >> 4, chn = (lane & 15) ^ ((xr << 2) | (w & 3));
      const u16* src = w_mod + (size_t)((chn >> 3) * 224 + 4 * w + xr) * (6 * D) + bb * 64 + (chn & 7) * 8;
#pragma unroll
      for (int i = 0; i < 14; ++i) glds16(src + (size_t)i * 16 * (6 * D), smem + (w + 4 * i) * 1024);
    }
    if (tid < 224) {
      const u32 cv = ((const u32*)c)[tid];
      const float c0 = lo_f(cv), c1 = hi_f(cv);
      *(u32*)(smem + 57344 + tid * 4) =
          pack_bf16(c0 * __builtin_amdgcn_rcpf(1.f + __builtin_amdgcn_exp2f(c0 * -1.44269504f)),
                    c1 * __builtin_amdgcn_rcpf(1.f + __builtin_amdgcn_exp2f(c1 * -1.44269504f)));
    }
    const int h = lane >> 5, blk = (lane >> 4) & 1, qq = (lane & 15) >> 2, p = lane & 3;
    const int col = (w >> 1) * 128 + (w & 1) * 64 + blk * 32 + p * 8;
    int o[2];
#pragma unroll
    for (int t = 0; t < 2; ++t)
      o[t] = 256 * (8 * h + 4 * t + qq) + 16 * ((col >> 4) ^ ((qq << 2) | ((2 * h + t) & 3))) + (col & 8);
    __syncthreads();
    f32x16 acc;
#pragma unroll
    for (int r = 0; r < 16; ++r) acc[r] = 0.f;
#pragma unroll
    for (int i = 0; i < 14; ++i)
      acc = mfma32(*(const u32x4*)(smem + 57344 + (w >> 1) * 448 + i * 32 + h * 16), tr_frag(smem, o[0] + i * 4096, o[1] + i * 4096), acc);
    float* red = (float*)(smem + 57344 + 1024);
    if (h == 0) red[(w >> 1) * 64 + (w & 1) * 32 + lane] = acc[0];
    __syncthreads();
    if (tid < 64) {
      const float t = red[tid] + red[64 + tid];
      const float m = round_bf16(t);
      const int cc = bb * 64 + tid;
      ((float*)(ws + OFF_MOD))[cc] = m;
      if (cc >= 3 * D && cc < 5 * D) {
        const bool isc = cc >= 4 * D;
        ((u16*)(ws + OFF_CB))[isc ? cc - 4 * D : cc - 2 * D] = (u16)pack_bf16(isc ? 1.f + m : m, 0.f);
      }
    }
  }
  asm volatile("" ::"v"(cwarm));
}

constexpr int K1_KSTR = 260 * 16;
constexpr int K1_USTR = 286 * 16;
constexpr int K1_UOFF = 4 * K1_KSTR;
constexpr int K1_XOFF = K1_UOFF + 2 * K1_USTR;
constexpr int K1_CH = K1_XOFF + 32768;

DI void glds16x4_asm(const void* sbase, u32 voff, u32 voff_last, u32 lds_addr) {
  u32 keep;
  sbase = uni(sbase);
  lds_addr = __builtin_amdgcn_readfirstlane(lds_addr);
  asm volatile(
      "s_mov_b32 %0, m0\n\ts_mov_b32 m0, %4\n\ts_nop 0\n\t"
      "global_load_lds_dwordx4 %1, %3\n\t"
      "global_load_lds_dwordx4 %1, %3 offset:1024\n\t"
      "global_load_lds_dwordx4 %1, %3 offset:2048\n\t"
      "global_load_lds_dwordx4 %2, %3 offset:3072\n\t"
      "s_mov_b32 m0, %0"
      : "=&s"(keep)
      : "v"(voff), "v"(voff_last), "s"(sbase), "s"(lds_addr)
      : "memory");
}
#define CAS __attribute__((address_space(4)))
constexpr int K2_XB = D * 256;
constexpr int K2_XH = D * 128;
constexpr int K2_ST = K2_XB;
constexpr int K2_LDS = 131072;

DI u32 umin32(u32 v) { return v < 0xFFFFFFFEu ? v : 0xFFFFFFFEu; }
DI u32 umax3(u32 a, u32 b, u32 c) { const u32 t = a > b ? a : b; return t > c ? t : c; }
template <int OFF>
DI void gload16_off(u32x4& dst, const void* sbase, u32 voff) {
  sbase = uni(sbase);
  asm volatile("global_load_dwordx4 %0, %1, %2 offset:%3" : "=v"(dst) : "v"(voff), "s"(sbase), "n"(OFF) : "memory");
}
DI void glds16_nom0(const void* sbase, u32 voff, u32 lds_addr) {
  sbase = uni(sbase);
  lds_addr = __builtin_amdgcn_readfirstlane(lds_addr);
  asm volatile("s_mov_b32 m0, %2\n\ts_nop 0\n\tglobal_load_lds_dwordx4 %0, %1" ::"v"(voff), "s"(sbase), "s"(lds_addr) : "memory");
}
DI void glds16_sc1(const void* sbase, u32 voff, u32 lds_addr) {
  sbase = uni(sbase);
  lds_addr = __builtin_amdgcn_readfirstlane(lds_addr);
  asm volatile("s_mov_b32 m0, %2\n\ts_nop 0\n\tglobal_load_lds_dwordx4 %0, %1 sc1" ::"v"(voff), "s"(sbase), "s"(lds_addr) : "memory");
}
typedef unsigned long long u64;
typedef __attribute__((address_space(1))) u64 gu64;
#define RLX_AGENT __ATOMIC_RELAXED, __HIP_MEMORY_SCOPE_AGENT
constexpr int NWC = C_NW0 + C_NW1;
template <bool WF>
DI void conv_body(const u16* __restrict__ ssm_k, const u16* __restrict__ ssm_D, char* __restrict__ ws, char* smem,
                  const int w, u32x4 (&wf)[28], const char* wbase) {
  const int lane = threadIdx.x & 63;
  const int ch = w >> 2, q = w & 3;
  const int cpair = blockIdx.x;
  const int d = 2 * cpair + ch;
  char* L = smem + ch * K1_CH;
  const u32 L0 = (u32)(size_t)(LAS char*)L;
  const u16* kf = ssm_k + (size_t)d * S;
  const u32 vo = lane * 16;
  {
    const u32 vl = (lane > 62 ? 62 : lane) * 16;
    glds16x4_asm(kf + (2 + 2 * q), vo, vl, L0 + q * K1_KSTR + 64);
    const char* ub = ws + OFF_RO + ((q >> 1) ? 0 : 2) + (size_t)d * S * 2 + (q & 1) * 2048;
    const u32 ul = L0 + K1_UOFF + (q >> 1) * K1_USTR + (q & 1) * 2048;
    glds16_nom0(ub, vo, ul);
    glds16_nom0(ub + 1024, vo, ul + 1024);
  }
  const int n = lane & 15, q4 = lane >> 4;
  const int pbase = 32 * (n >> 1) + 8 * q4 + 4 * (n & 1) + 512 * q;
  const int di = n + q - 15;
  u32 head;
  asm volatile("global_load_dword %0, %1, %2" : "=v"(head) : "v"((u32)(di < 0 ? 0 : di) * 4), "s"(uni(kf)) : "memory");
  auto ldwf = [&](auto kI) {
    constexpr int k = decltype(kI)::value;
    gload16_off<(k % 4) * 1024>(wf[k], wbase + (k / 4) * 4096, vo);
  };
  bar();
  if constexpr (WF) static_for<0, C_NW0>(ldwf);
  const CAS float* MODc = (const CAS float*)(size_t)(ws + OFF_MOD);
  const float sa = MODc[d], ca = MODc[D + d], ga = MODc[2 * D + d], cbv = MODc[4 * D + d];
  const u32 Draw = ((const CAS u32*)(size_t)ssm_D)[cpair];
  if (lane < 14) *(LAS u32x4*)(L + K1_UOFF + (q >> 1) * K1_USTR + (256 + 14 * (q & 1) + lane) * 16) = u32x4{0u, 0u, 0u, 0u};
  u32 Ab = L0 + (n & 3) * K1_KSTR + ((n >> 2) + q4 + 8 * q) * 16;
  u32 Bb = L0 + K1_UOFF + (n & 1) * K1_USTR + (28 - 4 * (n >> 1) + q4 + 8 * q) * 16;
  u32 Hd = L0 + q * K1_KSTR + n * 4;
  asm volatile("" : "+v"(Ab), "+v"(Bb), "+v"(Hd) : "s"(sa), "s"(ca), "s"(ga), "s"(Draw), "s"(cbv));
  asm volatile("s_waitcnt vmcnt(%1)" : "+v"(head) : "n"(WF ? C_NW0 : 0) : "memory");
  *(LAS u32*)(size_t)Hd = di < 0 ? 0u : head;
  if (q == 1 && lane == 0) *(LAS u16*)(L + K1_UOFF + 2047 * 2) = 0;
  wait_lgkm0();
  bar();
  bf16x8 fa[8], fb[8];
  auto rd = [&](int dl) {
#pragma unroll
    for (int g = 0; g < 8; ++g) {
      fa[g] = *(const LAS bf16x8*)(size_t)(Ab + 64 * dl + 512 * g);
      fb[g] = *(const LAS bf16x8*)(size_t)(Bb + 64 * dl + 512 * (7 - g));
    }
  };
  rd(0);
  __builtin_amdgcn_sched_barrier(0);
  u32x2 qv[2];
  f32x4 mu[2], rs[2];
  asm volatile("global_load_dwordx2 %0, %6, %8\n\t"
               "global_load_dwordx2 %1, %6, %8 offset:512\n\t"
               "global_load_dwordx4 %2, %7, %9\n\t"
               "global_load_dwordx4 %3, %7, %9 offset:1024\n\t"
               "global_load_dwordx4 %4, %7, %10\n\t"
               "global_load_dwordx4 %5, %7, %10 offset:1024"
               : "=&v"(qv[0]), "=&v"(qv[1]), "=&v"(mu[0]), "=&v"(mu[1]), "=&v"(rs[0]), "=&v"(rs[1])
               : "v"((u32)pbase * 2), "v"((u32)pbase * 4), "s"(uni(ws + OFF_QT + (size_t)d * S * 2)),
                 "s"(uni(ws + OFF_MEAN)), "s"(uni(ws + OFF_RSTD))
               : "memory");
  const u32x4 sent = {~0u, ~0u, ~0u, ~0u};
  const void* fillb = uni(ws + OFF_ACT + (size_t)(cpair * 8 + w) * 4096);
  f32x4 acc[8];
#pragma unroll
  for (int j = 0; j < 8; ++j) acc[j] = f32x4{0.f, 0.f, 0.f, 0.f};
  static_for<0, 72>([&](auto mI) {
    constexpr int m = decltype(mI)::value, t = m % 36;
    constexpr int sd = t < 1 ? 0 : t < 3 ? 1 : t < 6 ? 2 : t < 10 ? 3 : t < 15 ? 4 : t < 21 ? 5 : t < 28 ? 6 : 7;
    constexpr int g = t - sd * (sd + 1) / 2;
    if constexpr (m == 36) {
      rd(1);
      __builtin_amdgcn_sched_barrier(0);
    }
    f32x4 a = __builtin_amdgcn_mfma_f32_16x16x32_bf16(fa[g], fb[sd - g], acc[sd], 0, 0, 0);
    asm volatile("" : "+v"(a));
    acc[sd] = a;
    if constexpr (m % 4 == 3) {
      constexpr int v = m / 4;
      if constexpr (v < 4) {
        u32x4 sv = sent;
        asm volatile("global_store_dwordx4 %0, %1, %2 offset:%3 sc1\n\ts_nop 1" ::"v"(vo), "v"(sv), "s"(fillb), "n"(v * 1024) : "memory");
      } else if constexpr (WF && v - 4 < C_NW1) {
        ldwf(std::integral_constant<int, C_NW0 + v - 4>{});
      }
      __builtin_amdgcn_sched_barrier(0);
    }
  });
  LAS f32x4* X = (LAS f32x4*)(L + K1_XOFF);
#pragma unroll
  for (int j = 0; j < 8; ++j) X[(j * 4 + q) * 64 + lane] = acc[j];
  u32x4 uc[2];
#pragma unroll
  for (int j = 0; j < 2; ++j)
    uc[j] = *(const LAS u32x4*)(L + K1_UOFF + K1_USTR + (255 - (32 * (2 * q + j) + 4 * (n >> 1) + q4)) * 16);
  asm volatile("s_waitcnt vmcnt(%6)"
               : "+v"(qv[0]), "+v"(qv[1]), "+v"(mu[0]), "+v"(mu[1]), "+v"(rs[0]), "+v"(rs[1])
               : "n"(WF ? C_NW1 : 0)
               : "memory");
  f32x4 qf[2], hg[2], kd[2];
  {
    const int sh = (n & 1) ? 16 : 0;
    const float gc = ga * round_bf16(1.f + ca), gs = ga * sa, Dd = ch ? hi_f(Draw) : lo_f(Draw);
#pragma unroll
    for (int j = 0; j < 2; ++j) {
#pragma unroll
      for (int r = 0; r < 4; ++r) {
        kd[j][r] = __builtin_bit_cast(float, (uc[j][3 - r] << sh) & 0xffff0000u) * Dd;
        qf[j][r] = (r & 1) ? hi_f(qv[j][r >> 1]) : lo_f(qv[j][r >> 1]);
        hg[j][r] = (qf[j][r] - mu[j][r]) * rs[j][r] * gc + gs;
      }
      asm volatile("" : "+v"(kd[j]), "+v"(qf[j]), "+v"(hg[j]));
    }
  }
  wait_lgkm0();
  bar();
  u16* X1T = (u16*)(ws + OFF_X1T) + (size_t)d * S + pbase;
  u16* XS = (u16*)(ws + OFF_XS) + (size_t)d * S + pbase;
  const float c1 = round_bf16(1.f + cbv);
#pragma unroll
  for (int j = 0; j < 2; ++j) {
    const LAS f32x4* Xj = X + ((2 * q + j) * 4) * 64 + lane;
    const f32x4 y = (Xj[0] + Xj[64]) + (Xj[128] + Xj[192]);
    const f32x4 o = qf[j] + hg[j] * (y + kd[j]);
    const u32 p0 = umin32(pack_bf16(o[0], o[1])), p1 = umin32(pack_bf16(o[2], o[3]));
    st8_sc1(XS + 256 * j,
            u32x2{umin32(pack_bf16(lo_f(p0) * c1, hi_f(p0) * c1)), umin32(pack_bf16(lo_f(p1) * c1, hi_f(p1) * c1))});
    st8_sc1(X1T + 256 * j, u32x2{p0, p1});
  }
}

DI void k2_stats(char* __restrict__ ws, char* smem, const int sblk) {
  const int tid = threadIdx.x, lane = tid & 63;
  const int w = __builtin_amdgcn_readfirstlane(tid >> 6);
  const int n = lane & 31, h = lane >> 5;
  {
    const int p0 = sblk * 64;
    if (w < 7) {
      const int task = sblk * 7 + w, t = task >> 1, kh = task & 1;
      const u32x4* wsrc = (const u32x4*)(ws + OFF_WGU) + ((size_t)t * 28 + kh * 14) * 64 + lane;
      const u16* cbp = (const u16*)(ws + OFF_CB) + (n & 1) * D + kh * 224 + 8 * h;
      u32x4 fa[14], fb[14];
#pragma unroll
      for (int i = 0; i < 14; ++i) {
        fa[i] = wsrc[i * 64];
        fb[i] = *(const u32x4*)(cbp + 16 * i);
      }
      __builtin_amdgcn_sched_barrier(0);
      f32x16 va;
#pragma unroll
      for (int r = 0; r < 16; ++r) va[r] = 0.f;
#pragma unroll
      for (int i = 0; i < 14; ++i) va = mfma32(fb[i], __builtin_bit_cast(bf16x8, fa[i]), va);
      if (h == 0) {
        const float f0 = va[0], f1 = va[1];
        const u32 a0 = umin32(__builtin_bit_cast(u32, f0)), a1 = __builtin_bit_cast(u32, f1);
        __hip_atomic_store((gu64*)(size_t)(ws + OFF_VG) + kh * 3584 + t * 32 + n, ((u64)a1 << 32) | a0, RLX_AGENT);
      }
    }
    __syncthreads();
    __builtin_amdgcn_s_sleep(C_SSLEEP);
    const rsrc_t rx = mk_rsrc(ws + OFF_X1T + ((size_t)(w * 56) * S + p0) * 2, 56 * S * 2);
    const int c8 = lane & 7, r8 = lane >> 3;
    u32x4 x[7];
    for (int spin = 0; spin < (1 << 15); ++spin) {
      asm volatile("" ::: "memory");
      u32 m = 0;
#pragma unroll
      for (int i = 0; i < 7; ++i) x[i] = __builtin_amdgcn_raw_buffer_load_b128(rx, (r8 + 8 * i) * (S * 2) + c8 * 16, 0, 16);
#pragma unroll
      for (int i = 0; i < 7; ++i) m = umax3(umax3(m, x[i][0], x[i][1]), x[i][2], x[i][3]);
      if (__all(m != ~0u)) break;
      __builtin_amdgcn_s_sleep(1);
    }
    float sa[8], qa[8];
#pragma unroll
    for (int e = 0; e < 8; ++e) sa[e] = qa[e] = 0.f;
#pragma unroll
    for (int i = 0; i < 7; ++i)
#pragma unroll
      for (int e = 0; e < 8; ++e) {
        const float a = (e & 1) ? hi_f(x[i][e >> 1]) : lo_f(x[i][e >> 1]);
        sa[e] += a;
        qa[e] = __builtin_fmaf(a, a, qa[e]);
      }
    LAS float* P = (LAS float*)smem;
    {
      LAS float* pp = P + (w * 8 + r8) * 128 + c8 * 8;
      *(LAS f32x4*)pp = f32x4{sa[0], sa[1], sa[2], sa[3]};
      *(LAS f32x4*)(pp + 4) = f32x4{sa[4], sa[5], sa[6], sa[7]};
      *(LAS f32x4*)(pp + 64) = f32x4{qa[0], qa[1], qa[2], qa[3]};
      *(LAS f32x4*)(pp + 68) = f32x4{qa[4], qa[5], qa[6], qa[7]};
    }
    wait_lgkm0();
    {
      float s1 = 0.f, q1 = 0.f;
#pragma unroll
      for (int i = 0; i < 8; ++i) {
        s1 += P[(w * 8 + i) * 128 + lane];
        q1 += P[(w * 8 + i) * 128 + 64 + lane];
      }
      *(LAS f32x2*)(P + 8192 + (w * 64 + lane) * 2) = f32x2{s1, q1};
    }
    __syncthreads();
    if (tid < 64) {
      float sm = 0.f, sq = 0.f;
#pragma unroll
      for (int i = 0; i < 8; ++i) {
        const f32x2 t = *(const LAS f32x2*)(P + 8192 + (i * 64 + tid) * 2);
        sm += t[0];
        sq += t[1];
      }
      const float mean = sm * (1.f / D);
      float var = sq * (1.f / D) - mean * mean;
      var = (var < 0.f ? 0.f : var) + 1e-5f;
      const float rstd = __builtin_amdgcn_rsqf(var);
      const float sig = var * rstd;
      const float mh = round_bf16(mean), sh = round_bf16(sig);
      const u32 b0 = pack_bf16(-mh, mh - mean), b1 = pack_bf16(-mh, sh), b2 = pack_bf16(sig - sh, sh);
      const float c1 = -2.302208198f * rstd;
      const float c2 = -0.1029432397f * (rstd * rstd * rstd);
      gu64* G = (gu64*)(size_t)(ws + OFF_SG) + p0 + tid;
      __hip_atomic_store(G, ((u64)b1 << 32) | b0, RLX_AGENT);
      __hip_atomic_store(G + S, ((u64)__builtin_bit_cast(u32, var) << 32) | b2, RLX_AGENT);
      __hip_atomic_store(G + 2 * S, ((u64)__builtin_bit_cast(u32, c1) << 32) | __builtin_bit_cast(u32, c2), RLX_AGENT);
    }
  }
}

DI void k2_gemm(const u16* __restrict__ ssm_k, const u16* __restrict__ ssm_D, char* __restrict__ ws, u16* __restrict__ out,
                char* smem, const int w) {
  const int tid = threadIdx.x, lane = tid & 63;
  const int n = lane & 31, h = lane >> 5;
  const int xcd = blockIdx.x & 7, idx = blockIdx.x >> 3;
  const int mt = (xcd >> 1) * 4 + (idx & 3);
  const int dt = idx >> 2, sh2 = xcd & 1;
  const int ft = sh2 * 7 + dt;
  const int tile = ft * 8 + w;
  const u32 lds0 = (u32)(size_t)(LAS char*)smem;
  u32x4 wf[28], wd[28];
  const char* wbase = ws + OFF_WGU + (size_t)(tile * 28) * 1024;
  const u32 wa = lane * 16;
  auto ldwf = [&](auto kI) {
    constexpr int k = decltype(kI)::value;
    gload16_off<(k % 4) * 1024>(wf[k], wbase + (k / 4) * 4096, wa);
  };
  conv_body<true>(ssm_k, ssm_D, ws, smem, w, wf, wbase);
  const char* xbase = ws + OFF_XS + ((size_t)(8 * w) * S + mt * 128) * 2;
  const u32 xoff = (u32)((lane >> 3) * S + ((lane & 7) ^ (((lane >> 4) & 1) << 2)) * 8) * 2;
  const char* wdbase = ws + OFF_WD + ((size_t)(2 * dt) * 112 + 14 * w) * 1024 + 4096;
  const int gsl = w < 6 ? w : w - 6;
  const char* gbase = ws + OFF_SG + (size_t)((gsl >> 1) * S + mt * 128 + (gsl & 1) * 64) * 8;
  auto ldwd = [&](auto kI) {
    constexpr int q = decltype(kI)::value, k = (q & 1) * 14 + (q >> 1), kk = k % 14;
    gload16_off<(kk % 8) * 1024 - 4096>(wd[k], wdbase + (k / 14) * 112 * 1024 + (kk / 8) * 8192, wa);
  };
  auto ldx = [&](auto cI) {
    constexpr int c = decltype(cI)::value;
    glds16_sc1(xbase + (size_t)(c % 7) * (64 * S * 2) + (c / 7) * 128, xoff, lds0 + (64 * c + 8 * w) * 128);
  };
  auto xok = [&](auto c0I, auto c1I) {
    u32 m = 0;
#pragma unroll
    for (int c = decltype(c0I)::value; c < decltype(c1I)::value; ++c) {
      const u32x4 t = *(const LAS u32x4*)(smem + (64 * c + 8 * w) * 128 + lane * 16);
      m = umax3(umax3(m, t[0], t[1]), t[2], t[3]);
    }
    return (bool)__all(m != ~0u);
  };
  auto xvalid = [&](auto c0I, auto c1I) {
    if (__builtin_expect(!xok(c0I, c1I), 0)) {
#pragma nounroll
      for (int spin = 0; spin < (1 << 14); ++spin) {
        __builtin_amdgcn_s_sleep(4);
        static_for<decltype(c0I)::value, decltype(c1I)::value>(ldx);
        wait_vm<0>();
        if (xok(c0I, c1I)) break;
      }
    }
    __builtin_amdgcn_sched_barrier(0);
  };
  wait_lgkm0();
  bar();
  auto pin = [](f32x16& a) { asm volatile("" : "+v"(a)); };
  auto pinw = [&](auto kI) {
    u32x4 t = wf[decltype(kI)::value];
    asm volatile("" : "+v"(t)::"memory");
    wf[decltype(kI)::value] = t;
  };
  u32x4 aext;
  constexpr int NWH = NWC + (C_NWH - NWC);
  constexpr int NWP = NWC + C_NWP;
  static_for<NWC, NWP>(ldwf);
  static_for<0, 7>(ldx);
  static_for<NWP, NWH>(ldwf);
  int xo[2][2];
  {
    const int blk = (lane >> 4) & 1, qq = (lane & 15) >> 2, p = lane & 3;
#pragma unroll
    for (int i = 0; i < 2; ++i)
#pragma unroll
      for (int t = 0; t < 2; ++t)
        xo[i][t] = 128 * (8 * h + 4 * t + qq) + 16 * ((4 * i + 2 * blk + (p >> 1)) ^ ((qq >> 1) << 2)) + 8 * (p & 1);
  }
  f32x16 acc[4];
#pragma unroll
  for (int i = 0; i < 4; ++i)
#pragma unroll
    for (int r = 0; r < 16; ++r) acc[i][r] = 0.f;
  asm volatile("" : "+v"(xo[0][0]), "+v"(xo[0][1]), "+v"(xo[1][0]), "+v"(xo[1][1]));
  constexpr int PF = 3;
  bf16x8 xr[PF][2];
  auto rdx = [&](auto kI, auto iI) {
    constexpr int k = decltype(kI)::value, i = decltype(iI)::value;
    if constexpr (k == 28) {
      int a0 = xo[i][0] + K2_XH, a1 = xo[i][1] + K2_XH;
      asm volatile("" : "+v"(a0), "+v"(a1));
      xo[i][0] = a0;
      xo[i][1] = a1;
    }
    xr[k % PF][i] = tr_frag(smem, xo[i][0] + (k % 28) * 2048, xo[i][1] + (k % 28) * 2048);
  };
  using I0 = std::integral_constant<int, 0>;
  using I1 = std::integral_constant<int, 1>;
  using I2 = std::integral_constant<int, 2>;
  using I3 = std::integral_constant<int, 3>;
  using I7 = std::integral_constant<int, 7>;
  using I14 = std::integral_constant<int, 14>;
  LAS u64* CS = (LAS u64*)(smem + K2_ST);
  wait_vm<4 + NWH - NWP>();
  static_for<0, NWP>(pinw);
  xvalid(I0{}, I3{});
  bar();
  static_for<0, PF - 1>([&](auto jI) {
    rdx(jI, I0{});
    rdx(jI, I1{});
  });
  __builtin_amdgcn_sched_barrier(0);
  u64 gv, gp0, gp1;
  const char* vbase = ws + OFF_VG + (size_t)tile * 256;
  constexpr int RW = 28 - NWH, KX = RW + 7, KV = 25;
  constexpr int KF = C_KF;
  constexpr int WE = 27 - KX;
  static_assert(KF >= KX && KF <= 27, "");
  static_assert(KX + 4 <= KV && WE <= 9, "stream does not fit");
  static_for<0, 28>([&](auto kI) {
    constexpr int k = decltype(kI)::value;
    if constexpr (k == 6) {
      wait_vm<6>();
      static_for<NWP, NWH>(pinw);
      xvalid(I3{}, I7{});
      bar();
    }
    if constexpr (k >= NWH) {
      u32x4 t = wf[k];
      asm volatile("s_waitcnt vmcnt(%1)" : "+v"(t) : "n"(NWH - 1 + (k > KF ? 2 : 0)) : "memory");
      wf[k] = t;
    }
    if constexpr (k == KV) {
      wait_vm<KV - KX + (KV > KF ? 2 : 0)>();
      xvalid(I7{}, I14{});
      bar();
    }
    __builtin_amdgcn_sched_barrier(0);
    acc[0] = mfma32(wf[k], xr[k % PF][0], acc[0]);
    pin(acc[0]);
    __builtin_amdgcn_sched_barrier(0);
    rdx(std::integral_constant<int, k + PF - 1>{}, I0{});
    __builtin_amdgcn_sched_barrier(0);
    acc[1] = mfma32(wf[k], xr[k % PF][1], acc[1]);
    pin(acc[1]);
    __builtin_amdgcn_sched_barrier(0);
    rdx(std::integral_constant<int, k + PF - 1>{}, I1{});
    if constexpr (k < RW) ldwf(std::integral_constant<int, NWH + k>{});
    else if constexpr (k < KX) ldx(std::integral_constant<int, 7 + k - RW>{});
    else if constexpr (k != KF) ldwd(std::integral_constant<int, k - KX - (k > KF ? 1 : 0)>{});
    else {
      u64 t, t0, t1;
      asm volatile("global_load_dwordx2 %0, %1, %2 sc1" : "=v"(t) : "v"((u32)lane * 8), "s"(uni(gbase)) : "memory");
      asm volatile("global_load_dwordx2 %0, %1, %2 sc1" : "=v"(t0) : "v"((u32)n * 8), "s"(uni(vbase)) : "memory");
      asm volatile("global_load_dwordx2 %0, %1, %2 sc1" : "=v"(t1) : "v"((u32)n * 8), "s"(uni(vbase + 3584 * 8)) : "memory");
      gv = t;
      gp0 = t0;
      gp1 = t1;
    }
    __builtin_amdgcn_sched_barrier(0);
  });
  const rsrc_t rw = mk_rsrc(ws + OFF_ACT + ((size_t)(mt * 4) * 112 + tile) * 1024, 337 * 1024);
  const rsrc_t ra = mk_rsrc(ws + OFF_ACT + ((size_t)(mt * 4 + sh2) * 112 + 14 * w) * 1024, 238 * 1024);
  u32x4 af[28];
  auto ldact = [&](auto kI) {
    constexpr int k = decltype(kI)::value;
    af[k] = __builtin_amdgcn_raw_buffer_load_b128(ra, lane * 16 + (k % 14) * 1024, (k / 14) * 224 * 1024, 16);
  };
  float c1[2], c2[2], var[2], pe[2], pm[2], pr[2];
  u32x4 ov[2];
  auto corr = [&](auto iI) {
    constexpr int i = decltype(iI)::value;
    u64 g[3];
#pragma unroll
    for (int k = 0; k < 3; ++k) g[k] = CS[k * 128 + i * 32 + n];
    const u32x4 bext = {(u32)g[0], (u32)(g[0] >> 32), (u32)g[1], 0u};
    acc[i] = mfma32(aext, __builtin_bit_cast(bf16x8, bext), acc[i]);
    c1[i & 1] = __builtin_bit_cast(float, (u32)(g[2] >> 32));
    c2[i & 1] = __builtin_bit_cast(float, (u32)g[2]);
    var[i & 1] = __builtin_bit_cast(float, (u32)(g[1] >> 32));
  };
  auto slice = [&](auto iI, auto uI) {
    constexpr int i = decltype(iI)::value, u = decltype(uI)::value, z = i & 1;
    if constexpr (u >= 1) {
      float o = pm[z] * __builtin_amdgcn_rcpf(__builtin_fmaf(pe[z], var[z], var[z]));
      if constexpr ((u - 1) & 1) {
        u32 t = umin32(pack_bf16(pr[z], o));
        asm volatile("" : "+v"(t));
        ov[z][(u - 1) / 2] = t;
      } else {
        asm volatile("" : "+v"(o));
        pr[z] = o;
      }
    }
    if constexpr (u < 8) {
      const float a = acc[i][u];
      float e = __builtin_amdgcn_exp2f(__builtin_fmaf(a * a, c2[z], c1[z]) * a), m = a * acc[i][u + 8];
      asm volatile("" : "+v"(e), "+v"(m));
      pe[z] = e;
      pm[z] = m;
    } else {
      __builtin_amdgcn_raw_buffer_store_b128(ov[z], rw, lane * 16 + i * 112 * 1024, 0, 16);
      asm volatile("s_nop 1");
    }
  };
  constexpr int KG = C_KG;
  static_for<0, 28>([&](auto kI) {
    constexpr int k = decltype(kI)::value;
    if constexpr (k == KG) {
      u64 t = gv, p0 = gp0, p1 = gp1;
      asm volatile("s_waitcnt vmcnt(%3)" : "+v"(t), "+v"(p0), "+v"(p1) : "n"(KG + 27 - KF) : "memory");
      if (!__all((t >> 32) != 0 && (u32)p0 != ~0u && (u32)p1 != ~0u)) {
        for (int spin = 0; spin < (1 << 16); ++spin) {
          __builtin_amdgcn_s_sleep(2);
          t = __hip_atomic_load((gu64*)(size_t)gbase + lane, RLX_AGENT);
          p0 = __hip_atomic_load((gu64*)(size_t)vbase + n, RLX_AGENT);
          p1 = __hip_atomic_load((gu64*)(size_t)vbase + 3584 + n, RLX_AGENT);
          if (__all((t >> 32) != 0 && (u32)p0 != ~0u && (u32)p1 != ~0u)) break;
        }
        __builtin_amdgcn_s_waitcnt(0x0F70);
      }
      CS[gsl * 64 + lane] = t;
      const float v1 = __builtin_bit_cast(float, (u32)p0) + __builtin_bit_cast(float, (u32)p1);
      const float v2 = __builtin_bit_cast(float, (u32)(p0 >> 32)) + __builtin_bit_cast(float, (u32)(p1 >> 32));
      const float v1h = round_bf16(v1), v2h = round_bf16(v2);
      u32x4 a = {pack_bf16(v1h, v1h), pack_bf16(v1 - v1h, v2h), pack_bf16(v2h, v2 - v2h), 0u};
      if (h) a = u32x4{0u, 0u, 0u, 0u};
      asm volatile("" : "+v"(a));
      aext = a;
      wait_lgkm0();
      __builtin_amdgcn_sched_barrier(0);
      bar();
      corr(I0{});
      corr(I1{});
      __builtin_amdgcn_sched_barrier(0);
    }
    acc[2] = mfma32(wf[k], xr[(28 + k) % PF][0], acc[2]);
    pin(acc[2]);
    __builtin_amdgcn_sched_barrier(0);
    if constexpr (k + PF - 1 < 28) rdx(std::integral_constant<int, 28 + k + PF - 1>{}, I0{});
    if constexpr (k > KG && k <= KG + 9) slice(I0{}, std::integral_constant<int, k - KG - 1>{});
    __builtin_amdgcn_sched_barrier(0);
    acc[3] = mfma32(wf[k], xr[(28 + k) % PF][1], acc[3]);
    pin(acc[3]);
    __builtin_amdgcn_sched_barrier(0);
    if constexpr (k % C_LOCK == C_LOCK - 1) bar();
    if constexpr (k + PF - 1 < 28) rdx(std::integral_constant<int, 28 + k + PF - 1>{}, I1{});
    if constexpr (k > KG && k <= KG + 9) slice(I1{}, std::integral_constant<int, k - KG - 1>{});
    if constexpr (WE + k < 28) ldwd(std::integral_constant<int, WE + k>{});
    else if constexpr (WE + k - 28 < 7) ldact(std::integral_constant<int, WE + k - 28>{});
    __builtin_amdgcn_sched_barrier(0);
  });
  constexpr int A0 = WE < 7 ? WE : 7;
  corr(I2{});
  corr(I3{});
  __builtin_amdgcn_sched_barrier(0);
  static_for<0, 9>([&](auto uI) {
    constexpr int u = decltype(uI)::value;
    slice(I2{}, uI);
    slice(I3{}, uI);
    if constexpr (A0 + u < 14 && u < 8) ldact(std::integral_constant<int, A0 + u>{});
    __builtin_amdgcn_sched_barrier(0);
  });
  static_for<(A0 + 8 < 14 ? A0 + 8 : 14), 14>(ldact);
  bar();
  auto chk = [&](auto gI) {
    constexpr int g = decltype(gI)::value;
    u32 m = 0;
#pragma unroll
    for (int k = 7 * g; k < 7 * g + 7; ++k) m = umax3(umax3(m, af[k][0], af[k][1]), af[k][2], af[k][3]);
    return (bool)__all(m != ~0u);
  };
  auto valid = [&](auto gI) {
    constexpr int g = decltype(gI)::value;
    if (__builtin_expect(!chk(gI), 0)) {
#pragma nounroll
      for (int spin = 0; spin < (1 << 14); ++spin) {
        __builtin_amdgcn_s_sleep(8);
        asm volatile("" ::: "memory");
        static_for<7 * g, 7 * g + 7>(ldact);
        if (chk(gI)) break;
      }
      __builtin_amdgcn_s_waitcnt(0x0F70);
    }
    __builtin_amdgcn_sched_barrier(0);
  };
  const int pcol = mt * 128 + sh2 * 32 + n;
  const int dbase = dt * 64 + w * 8 + 4 * h;
  __builtin_amdgcn_sched_barrier(0);
  LAS f32x4* R = (LAS f32x4*)smem;
  f32x16 o[4];
#pragma unroll
  for (int i = 0; i < 4; ++i)
#pragma unroll
    for (int r = 0; r < 16; ++r) o[i][r] = 0.f;
  auto dump1 = [&](auto tjI, auto cI) {
    constexpr int tj = decltype(tjI)::value, c = decltype(cI)::value, i = tj * 2 + (c >> 2), q = c & 3;
    R[((tj * 8 + c) * 8 + w) * 64 + lane] = f32x4{o[i][4 * q], o[i][4 * q + 1], o[i][4 * q + 2], o[i][4 * q + 3]};
  };
  auto step = [&](auto kI) {
    constexpr int k = decltype(kI)::value, tj = k / 14;
    const bf16x8 b = __builtin_bit_cast(bf16x8, af[k]);
    o[2 * tj] = mfma32(wd[k % 14], b, o[2 * tj]);
    pin(o[2 * tj]);
    __builtin_amdgcn_sched_barrier(0);
    if constexpr (k >= 15 && k < 19) dump1(I0{}, std::integral_constant<int, 2 * (k - 15)>{});
    __builtin_amdgcn_sched_barrier(0);
    o[2 * tj + 1] = mfma32(wd[14 + k % 14], b, o[2 * tj + 1]);
    pin(o[2 * tj + 1]);
    __builtin_amdgcn_sched_barrier(0);
    if constexpr (k >= 15 && k < 19) dump1(I0{}, std::integral_constant<int, 2 * (k - 15) + 1>{});
    if constexpr (k >= C_T1 && k < 14) {
      constexpr int n0 = ((k - C_T1) * 14) / (14 - C_T1), n1 = ((k + 1 - C_T1) * 14) / (14 - C_T1);
      static_for<14 + n0, 14 + n1>(ldact);
    }
    __builtin_amdgcn_sched_barrier(0);
  };
  u16 xres[2][4];
  f32x4 gbv;
  auto fin = [&](auto tjI) {
    constexpr int tj = decltype(tjI)::value;
    f32x4 v[8];
#pragma unroll
    for (int sw = 0; sw < 8; ++sw) v[sw] = R[((tj * 8 + w) * 8 + sw) * 64 + lane];
    const f32x4 sum = ((v[0] + v[1]) + (v[2] + v[3])) + ((v[4] + v[5]) + (v[6] + v[7]));
    float r[4];
#pragma unroll
    for (int e = 0; e < 4; ++e) r[e] = __builtin_fmaf(gbv[e], sum[e], bf2f(xres[tj][e]));
    const int pc = pcol + 64 * tj;
    const int srow = (pc & ~7) | ((pc & 3) << 1) | ((pc >> 2) & 1);
    *(u32x2*)(out + (size_t)srow * D + dbase) = u32x2{pack_bf16(r[0], r[1]), pack_bf16(r[2], r[3])};
  };
  valid(I0{});
  asm volatile(""
               : "+v"(wd[0]), "+v"(wd[1]), "+v"(wd[2]), "+v"(wd[3]), "+v"(wd[4]), "+v"(wd[5]), "+v"(wd[6]), "+v"(wd[7]),
                 "+v"(wd[8]), "+v"(wd[9]), "+v"(wd[10]), "+v"(wd[11]), "+v"(wd[12]), "+v"(wd[13])::"memory");
  asm volatile(""
               : "+v"(wd[14]), "+v"(wd[15]), "+v"(wd[16]), "+v"(wd[17]), "+v"(wd[18]), "+v"(wd[19]), "+v"(wd[20]),
                 "+v"(wd[21]), "+v"(wd[22]), "+v"(wd[23]), "+v"(wd[24]), "+v"(wd[25]), "+v"(wd[26]), "+v"(wd[27])::"memory");
  static_for<0, 3>(step);
  {
    const rsrc_t rx = mk_rsrc(ws + OFF_X1T, D * S * 2);
#pragma unroll
    for (int r = 0; r < 8; ++r)
      xres[r >> 2][r & 3] = __builtin_amdgcn_raw_buffer_load_b16(rx, ((dbase + (r & 3)) * S + pcol + (r >> 2) * 64) * 2, 0, 16);
    gbv = *(const f32x4*)((const float*)(ws + OFF_MOD) + 5 * D + dbase);
    __builtin_amdgcn_sched_barrier(0);
  }
  static_for<3, 7>(step);
  valid(I1{});
  static_for<7, 14>(step);
  valid(I2{});
  static_for<14, 21>(step);
  wait_lgkm0();
  bar();
  valid(I3{});
  static_for<21, 24>(step);
  fin(I0{});
  __builtin_amdgcn_sched_barrier(0);
  static_for<24, 28>(step);
  static_for<0, 8>([&](auto cI) { dump1(I1{}, cI); });
  wait_lgkm0();
  bar();
  fin(I1{});
}

extern "C" __global__ NOPK __launch_bounds__(512, 2) void k1_main(const u16* __restrict__ ssm_k,
                                                                  const u16* __restrict__ ssm_D, char* __restrict__ ws,
                                                                  u16* __restrict__ out) {
  extern __shared__ __attribute__((aligned(16))) char smem[];
  const int w = __builtin_amdgcn_readfirstlane(threadIdx.x >> 6);
  if (blockIdx.x >= 224) {
    k2_stats(ws, smem, blockIdx.x - 224);
  } else {
    k2_gemm(ssm_k, ssm_D, ws, out, smem, w);
  }
}

extern "C" __device__ __attribute__((used, noinline)) void k_code_end() {}

extern "C" void kernel_launch(void* const* d_in, const int* in_sizes, int n_in, void* d_out, int out_size, void* d_ws,
                              size_t ws_size, hipStream_t stream) {
  const u16* q = (const u16*)d_in[0];
  const u16* k = (const u16*)d_in[1];
  const u16* v = (const u16*)d_in[2];
  const u16* shift_k = (const u16*)d_in[3];
  const u16* ssm_k = (const u16*)d_in[4];
  const u16* ssm_D = (const u16*)d_in[5];
  const u16* w_gate = (const u16*)d_in[7];
  const u16* w_up = (const u16*)d_in[8];
  const u16* w_down = (const u16*)d_in[9];
  const u16* c = (const u16*)d_in[10];
  const u16* w_mod = (const u16*)d_in[11];
  char* ws = (char*)d_ws;
  (void)hipFuncSetAttribute((const void*)k1_main, hipFuncAttributeMaxDynamicSharedMemorySize, K2_LDS);
  k0_prep<<<K0_GRID, 256, 0, stream>>>(q, k, v, shift_k, c, w_mod, w_gate, w_up, w_down, ws);
  k1_main<<<256, 512, K2_LDS, stream>>>(ssm_k, ssm_D, ws, (u16*)d_out);
}
```

```cpp
#include <hip/hip_runtime.h>
#include <hip/hip_bf16.h>
#include <stdint.h>
#include <type_traits>

typedef unsigned short u16;
typedef unsigned int u32;
typedef short bf16x8 __attribute__((ext_vector_type(8)));
typedef short s16x4 __attribute__((ext_vector_type(4)));
typedef float f32x2 __attribute__((ext_vector_type(2)));
typedef float f32x4 __attribute__((ext_vector_type(4)));
typedef float f32x16 __attribute__((ext_vector_type(16)));
typedef u32 u32x2 __attribute__((ext_vector_type(2)));
typedef u32 u32x4 __attribute__((ext_vector_type(4)));
typedef __bf16 bf16x2_t __attribute__((ext_vector_type(2)));
#define LAS __attribute__((address_space(3)))
#define DI __device__ __forceinline__

constexpr int S = 2048, D = 448, F = 1792;

constexpr size_t SZ_DS = (size_t)D * S * 2;
constexpr size_t OFF_RO = SZ_DS;
constexpr size_t OFF_QT = OFF_RO + SZ_DS;
constexpr size_t OFF_X1T = OFF_QT + SZ_DS;
constexpr size_t OFF_MEAN = OFF_X1T + SZ_DS;
constexpr size_t OFF_RSTD = OFF_MEAN + S * 4;
constexpr size_t OFF_MOD = OFF_RSTD + S * 4;
constexpr size_t OFF_SG = OFF_MOD + 16384;
constexpr size_t OFF_CB = OFF_MOD + 12288;

constexpr size_t OFF_VG = OFF_SG + 3 * S * 8;
constexpr size_t OFF_XS = OFF_VG + 4 * S * 8;
constexpr size_t OFF_WGU = OFF_XS + SZ_DS;
constexpr size_t OFF_WD = OFF_WGU + (size_t)2 * D * F * 2;
constexpr size_t OFF_ACT = OFF_WD + (size_t)D * F * 2;

#if defined(__HIP_DEVICE_COMPILE__)
#define NOPK __attribute__((target("no-packed-fp32-ops")))
#else
#define NOPK
#endif

constexpr int C_KF = 25;
constexpr int C_KG = 1;
constexpr int C_LOCK = 4;
constexpr int C_NW0 = 5;
constexpr int C_NW1 = 5;
constexpr int C_NWH = 15;
constexpr int C_NWP = 3;
constexpr int C_SLN = 18;
constexpr int C_SSLEEP = 50;
constexpr int C_T1 = 5;
constexpr int C_VSP = 4;

template <int I, int N, class Fn>
DI void static_for(Fn&& f) {
  if constexpr (I < N) {
    f(std::integral_constant<int, I>{});
    static_for<I + 1, N>(f);
  }
}

DI float lo_f(u32 x) { return __builtin_bit_cast(float, x << 16); }
DI float hi_f(u32 x) { return __builtin_bit_cast(float, x & 0xffff0000u); }
DI u32 pack_bf16(float a, float b) {
  f32x2 v = {a, b};
  return __builtin_bit_cast(u32, __builtin_convertvector(v, bf16x2_t));
}
DI float round_bf16(float a) { return lo_f(pack_bf16(a, 0.f)); }
DI float bf2f(u16 x) { return __builtin_bit_cast(float, (u32)x << 16); }

template <int N>
DI void wait_vm() { asm volatile("s_waitcnt vmcnt(%0)" ::"n"(N) : "memory"); }
DI void wait_lgkm0() { asm volatile("s_waitcnt lgkmcnt(0)" ::: "memory"); }
DI void bar() { __builtin_amdgcn_s_barrier(); }

DI const void* uni(const void* p) {
  const unsigned long long v = (unsigned long long)p;
  const u32 lo = __builtin_amdgcn_readfirstlane((u32)v), hi = __builtin_amdgcn_readfirstlane((u32)(v >> 32));
  return (const void*)(((unsigned long long)hi << 32) | lo);
}
DI void glds16(const void* g, char* lds) {
  __builtin_amdgcn_global_load_lds((const __attribute__((address_space(1))) void*)g, (LAS void*)lds, 16, 0, 0);
}

template <int CTRL>
DI float dpp_add(float v) {
  const int x = __builtin_bit_cast(int, v);
  return v + __builtin_bit_cast(float, __builtin_amdgcn_update_dpp(x, x, CTRL, 0xf, 0xf, false));
}
DI float sum8(float v) {
  v = dpp_add<0xB1>(v);
  v = dpp_add<0x4E>(v);
  v = dpp_add<0x141>(v);
  return v;
}
DI float dot2(u32 a, u32 b, float c) {
  return __builtin_amdgcn_fdot2_f32_bf16(__builtin_bit_cast(bf16x2_t, a), __builtin_bit_cast(bf16x2_t, b), c, false);
}
typedef __amdgpu_buffer_rsrc_t rsrc_t;
DI rsrc_t mk_rsrc(const void* p, u32 bytes) { return __builtin_amdgcn_make_buffer_rsrc((void*)p, 0, bytes, 0x00020000); }
DI void st16_sc1(void* p, u32x4 v) { asm volatile("global_store_dwordx4 %0, %1, off sc1\n\ts_nop 1" ::"v"(p), "v"(v) : "memory"); }
DI void st8_sc1(void* p, u32x2 v) { asm volatile("global_store_dwordx2 %0, %1, off sc1" ::"v"(p), "v"(v) : "memory"); }

DI bf16x8 tr_frag(const char* smem, int o0, int o1) {
  s16x4 x0 = __builtin_amdgcn_ds_read_tr16_b64_v4i16((LAS s16x4*)(smem + o0));
  s16x4 x1 = __builtin_amdgcn_ds_read_tr16_b64_v4i16((LAS s16x4*)(smem + o1));
  return __builtin_shufflevector(x0, x1, 0, 1, 2, 3, 4, 5, 6, 7);
}
DI f32x16 mfma32(u32x4 a, bf16x8 b, f32x16 c) {
  return __builtin_amdgcn_mfma_f32_32x32x16_bf16(__builtin_bit_cast(bf16x8, a), b, c, 0, 0, 0);
}

DI void retile(int task, const u16* __restrict__ w_gate, const u16* __restrict__ w_up, const u16* __restrict__ w_down,
               char* __restrict__ ws, char* smem, int w, int lane) {
  const int xr = lane >> 4, slot = lane & 15;
  const int chn = slot ^ ((xr << 2) | (w & 3));
  const int h = lane >> 5, blk = (lane >> 4) & 1, qq = (lane & 15) >> 2, p = lane & 3;
  const bool gu = task < 112;
  int kq = 0, fg = 0, dg = 0, fk = 0;
  if (gu) {
    fg = task % 28, kq = task / 28;
    const u16* src = (chn < 8 ? w_gate : w_up) + (size_t)(kq * 112 + 4 * w + xr) * F + fg * 64 + (chn & 7) * 8;
#pragma unroll
    for (int i = 0; i < 7; ++i) glds16(src + (size_t)i * 16 * F, smem + (w + 4 * i) * 1024);
  } else {
    dg = (task - 112) % 7, fk = (task - 112) / 7;
    const u16* src = w_down + (size_t)(fk * 224 + (chn >> 3) * 112 + 4 * w + xr) * D + dg * 64 + (chn & 7) * 8;
#pragma unroll
    for (int i = 0; i < 7; ++i) glds16(src + (size_t)i * 16 * D, smem + (w + 4 * i) * 1024);
  }
  const int col = gu ? blk * 128 + w * 32 + (p & 1) * 16 + (p >> 1) * 8 : (w >> 1) * 128 + (w & 1) * 64 + blk * 32 + p * 8;
  int o[2];
#pragma unroll
  for (int t = 0; t < 2; ++t)
    o[t] = 256 * (8 * h + 4 * t + qq) + 16 * ((col >> 4) ^ ((qq << 2) | ((2 * h + t) & 3))) + (col & 8);
  u32x4* dst = gu ? (u32x4*)(ws + OFF_WGU) + ((size_t)(fg * 4 + w) * 28 + kq * 7) * 64 + lane
                  : (u32x4*)(ws + OFF_WD) + ((size_t)(dg * 2 + (w & 1)) * 112 + fk * 14 + (w >> 1) * 7) * 64 + lane;
  __syncthreads();
#pragma unroll
  for (int i = 0; i < 7; ++i)
    st16_sc1(dst + i * 64, __builtin_bit_cast(u32x4, tr_frag(smem, o[0] + i * 4096, o[1] + i * 4096)));
}

constexpr int K0_GRID = 504;

extern "C" __global__ __launch_bounds__(256) void k0_prep(const u16* __restrict__ q, const u16* __restrict__ k,
                                               const u16* __restrict__ v, const u16* __restrict__ shift_k,
                                               const u16* __restrict__ c, const u16* __restrict__ w_mod,
                                               const u16* __restrict__ w_gate,
                                               const u16* __restrict__ w_up, const u16* __restrict__ w_down,
                                               char* __restrict__ ws) {
  __shared__ __attribute__((aligned(16))) char smem[57344 + 1024 + 512];
  const int b = blockIdx.x, tid = threadIdx.x, lane = tid & 63, w = __builtin_amdgcn_readfirstlane(tid >> 6);
  const int xcd = b & 7, idx = b >> 3;
  u32 cwarm = 0;
  if (idx < 2) {
    u32 lo, hi, len;
    asm volatile(
        "s_getpc_b64 s[40:41]\n\ts_add_u32 s40, s40, k0_prep@rel32@lo+4\n\ts_addc_u32 s41, s41, k0_prep@rel32@hi+12\n\t"
        "s_mov_b32 %0, s40\n\ts_mov_b32 %1, s41\n\ts_mov_b32 %2, k_code_end-k0_prep"
        : "=s"(lo), "=s"(hi), "=s"(len)::"s40", "s41", "scc");
    const char* code = (const char*)(((unsigned long long)hi << 32) | lo);
    if ((u32)(idx * 256 + tid) * 128 < len) cwarm = *(const __attribute__((address_space(1))) u32*)(size_t)(code + (idx * 256 + tid) * 128);
  }
  asm volatile("" ::"s"(q), "s"(k), "s"(v), "s"(shift_k), "s"(c), "s"(w_mod), "s"(w_gate), "s"(w_up), "s"(w_down),
               "s"(ws));
  if (__builtin_expect(idx >= 42, 0)) {
    retile(b - 336, w_gate, w_up, w_down, ws, smem, w, lane);
    if (idx < 58) {
      const int t = (idx - 42) * 2 + (xcd & 1);
      char* ap = ws + (t < 16 ? OFF_X1T : OFF_XS) + (size_t)((t & 15) * 28 + w * 7) * (S * 2) + (xcd >> 1) * 1024 + lane * 16;
      const u32x4 sent = {~0u, ~0u, ~0u, ~0u};
#pragma unroll
      for (int i = 0; i < 7; ++i) st16_sc1(ap + i * (S * 2), sent);
    }
    return;
  }
  int misc = -1;
  if (idx < 6) misc = 64 + b;
  else if (idx >= 34) misc = xcd * 8 + (idx - 34);
  const int rb = xcd * 4 + ((idx - 6) & 3), cg = (idx - 6) >> 2;
  if (__builtin_expect(misc < 0, 1)) {
    const int sq = lane >> 5, cp = lane & 31;
    const int d0 = cg * 64 + cp * 2;
    const int a = rb * 8 + w * 2 + sq;
    const int s0 = a * 8;
    const rsrc_t rk = mk_rsrc(k, S * D * 2), rv = mk_rsrc(v, S * D * 2), rq = mk_rsrc(q, S * D * 2);
    const int oB = (s0 * D + d0) * 2;
    const int oA = oB - 4 * D * 2, oC = oB + 4 * D * 2;
    u32 kk[12], vv[9], qq[8];
#pragma unroll
    for (int i = 1; i < 4; ++i) kk[i] = __builtin_amdgcn_raw_buffer_load_b32(rk, oA + i * D * 2, 0, 0);
#pragma unroll
    for (int i = 0; i < 4; ++i) kk[4 + i] = __builtin_amdgcn_raw_buffer_load_b32(rk, oB + i * D * 2, 0, 0);
#pragma unroll
    for (int i = 0; i < 4; ++i) kk[8 + i] = __builtin_amdgcn_raw_buffer_load_b32(rk, oC + i * D * 2, 0, 0);
#pragma unroll
    for (int i = 0; i < 4; ++i) vv[1 + i] = __builtin_amdgcn_raw_buffer_load_b32(rv, oB + i * D * 2, 0, 0);
#pragma unroll
    for (int i = 0; i < 4; ++i) vv[5 + i] = __builtin_amdgcn_raw_buffer_load_b32(rv, oC + i * D * 2, 0, 0);
#pragma unroll
    for (int i = 0; i < 4; ++i) qq[i] = __builtin_amdgcn_raw_buffer_load_b32(rq, oB + i * D * 2, 0, 0);
#pragma unroll
    for (int i = 0; i < 4; ++i) qq[4 + i] = __builtin_amdgcn_raw_buffer_load_b32(rq, oC + i * D * 2, 0, 0);
    const u32x4 sk = *(const u32x4*)(shift_k + d0 * 4);
    f32x2 wt[4] = {{lo_f(sk[0]), lo_f(sk[2])}, {hi_f(sk[0]), hi_f(sk[2])}, {lo_f(sk[1]), lo_f(sk[3])}, {hi_f(sk[1]), hi_f(sk[3])}};
    f32x2 kf[12], u[9];
#pragma unroll
    for (int i = 1; i < 12; ++i) kf[i] = f32x2{lo_f(kk[i]), hi_f(kk[i])};
#pragma unroll
    for (int i = 1; i < 9; ++i) {
      f32x2 t = kf[i + 3] * wt[0];
      t = __builtin_elementwise_fma(kf[i + 2], wt[1], t);
      t = __builtin_elementwise_fma(kf[i + 1], wt[2], t);
      t = __builtin_elementwise_fma(kf[i], wt[3], t);
      u[i] = t * f32x2{lo_f(vv[i]), hi_f(vv[i])};
    }
    {
      char* dst = ws + (size_t)d0 * (S * 2) + (2040 - s0) * 2;
#pragma unroll
      for (int cI = 0; cI < 2; ++cI) {
        *(u32x4*)(dst + OFF_RO + cI * S * 2) = u32x4{pack_bf16(u[8][cI], u[7][cI]), pack_bf16(u[6][cI], u[5][cI]),
                                              pack_bf16(u[4][cI], u[3][cI]), pack_bf16(u[2][cI], u[1][cI])};
      }
      char* dq = ws + OFF_QT + (size_t)d0 * (S * 2) + s0 * 2;
#pragma unroll
      for (int cI = 0; cI < 2; ++cI) {
        const u32 sel = cI ? 0x07060302u : 0x05040100u;
        *(u32x4*)(dq + cI * S * 2) = u32x4{__builtin_amdgcn_perm(qq[2], qq[0], sel), __builtin_amdgcn_perm(qq[6], qq[4], sel),
                                    __builtin_amdgcn_perm(qq[3], qq[1], sel), __builtin_amdgcn_perm(qq[7], qq[5], sel)};
      }
    }
  } else if (misc < 64) {
    const int bb = misc;
    const int row = bb * 32 + w * 8 + (lane >> 3);
    const int sub = lane & 7;
    u32x4 x[7];
#pragma unroll
    for (int i = 0; i < 7; ++i) x[i] = *(const u32x4*)(q + (size_t)row * D + (i * 8 + sub) * 8);
    if (sub < 7) ((unsigned long long*)(ws + OFF_SG))[sub * S + row] = sub < 3 ? 0ull : ~0ull;
    float s = 0.f, s2 = 0.f;
#pragma unroll
    for (int i = 0; i < 7; ++i)
#pragma unroll
      for (int j = 0; j < 4; ++j) {
        const u32 t = x[i][j];
        s = dot2(t, 0x3f803f80u, s);
        s2 = dot2(t, t, s2);
      }
    s = sum8(s);
    s2 = sum8(s2);
    const float mean = s * (1.f / D);
    float var = s2 * (1.f / D) - mean * mean;
    var = var < 0.f ? 0.f : var;
    const float rstd = __builtin_amdgcn_rsqf(var + 1e-5f);
    if (sub < 2) {
      const int p = (row & ~7) | ((row & 1) << 2) | ((row >> 1) & 3);
      ((float*)(ws + OFF_MEAN))[sub * S + p] = sub ? rstd : mean;
    }
  } else if (misc < 106) {
    const int bb = misc - 64;
    {
      const int xr = lane >> 4, chn = (lane & 15) ^ ((xr << 2) | (w & 3));
      const u16* src = w_mod + (size_t)((chn >> 3) * 224 + 4 * w + xr) * (6 * D) + bb * 64 + (chn & 7) * 8;
#pragma unroll
      for (int i = 0; i < 14; ++i) glds16(src + (size_t)i * 16 * (6 * D), smem + (w + 4 * i) * 1024);
    }
    if (tid < 224) {
      const u32 cv = ((const u32*)c)[tid];
      const float c0 = lo_f(cv), c1 = hi_f(cv);
      *(u32*)(smem + 57344 + tid * 4) =
          pack_bf16(c0 * __builtin_amdgcn_rcpf(1.f + __builtin_amdgcn_exp2f(c0 * -1.44269504f)),
                    c1 * __builtin_amdgcn_rcpf(1.f + __builtin_amdgcn_exp2f(c1 * -1.44269504f)));
    }
    const int h = lane >> 5, blk = (lane >> 4) & 1, qq = (lane & 15) >> 2, p = lane & 3;
    const int col = (w >> 1) * 128 + (w & 1) * 64 + blk * 32 + p * 8;
    int o[2];
#pragma unroll
    for (int t = 0; t < 2; ++t)
      o[t] = 256 * (8 * h + 4 * t + qq) + 16 * ((col >> 4) ^ ((qq << 2) | ((2 * h + t) & 3))) + (col & 8);
    __syncthreads();
    f32x16 acc;
#pragma unroll
    for (int r = 0; r < 16; ++r) acc[r] = 0.f;
#pragma unroll
    for (int i = 0; i < 14; ++i)
      acc = mfma32(*(const u32x4*)(smem + 57344 + (w >> 1) * 448 + i * 32 + h * 16), tr_frag(smem, o[0] + i * 4096, o[1] + i * 4096), acc);
    float* red = (float*)(smem + 57344 + 1024);
    if (h == 0) red[(w >> 1) * 64 + (w & 1) * 32 + lane] = acc[0];
    __syncthreads();
    if (tid < 64) {
      const float t = red[tid] + red[64 + tid];
      const float m = round_bf16(t);
      const int cc = bb * 64 + tid;
      ((float*)(ws + OFF_MOD))[cc] = m;
      if (cc >= 3 * D && cc < 5 * D) {
        const bool isc = cc >= 4 * D;
        ((u16*)(ws + OFF_CB))[isc ? cc - 4 * D : cc - 2 * D] = (u16)pack_bf16(isc ? 1.f + m : m, 0.f);
      }
    }
  }
  asm volatile("" ::"v"(cwarm));
}

constexpr int K1_KSTR = 260 * 16;
constexpr int K1_USTR = 286 * 16;
constexpr int K1_UOFF = 4 * K1_KSTR;
constexpr int K1_XOFF = K1_UOFF + 2 * K1_USTR;
constexpr int K1_CH = K1_XOFF + 32768;

DI void glds16x4_asm(const void* sbase, u32 voff, u32 voff_last, u32 lds_addr) {
  u32 keep;
  sbase = uni(sbase);
  lds_addr = __builtin_amdgcn_readfirstlane(lds_addr);
  asm volatile(
      "s_mov_b32 %0, m0\n\ts_mov_b32 m0, %4\n\ts_nop 0\n\t"
      "global_load_lds_dwordx4 %1, %3\n\t"
      "global_load_lds_dwordx4 %1, %3 offset:1024\n\t"
      "global_load_lds_dwordx4 %1, %3 offset:2048\n\t"
      "global_load_lds_dwordx4 %2, %3 offset:3072\n\t"
      "s_mov_b32 m0, %0"
      : "=&s"(keep)
      : "v"(voff), "v"(voff_last), "s"(sbase), "s"(lds_addr)
      : "memory");
}
#define CAS __attribute__((address_space(4)))
constexpr int K2_XB = D * 256;
constexpr int K2_XH = D * 128;
constexpr int K2_ST = K2_XB;
constexpr int K2_LDS = 131072;

DI u32 umin32(u32 v) { return v < 0xFFFFFFFEu ? v : 0xFFFFFFFEu; }
DI u32 umax3(u32 a, u32 b, u32 c) { const u32 t = a > b ? a : b; return t > c ? t : c; }
template <int OFF>
DI void gload16_off(u32x4& dst, const void* sbase, u32 voff) {
  sbase = uni(sbase);
  asm volatile("global_load_dwordx4 %0, %1, %2 offset:%3" : "=v"(dst) : "v"(voff), "s"(sbase), "n"(OFF) : "memory");
}
DI void glds16_nom0(const void* sbase, u32 voff, u32 lds_addr) {
  sbase = uni(sbase);
  lds_addr = __builtin_amdgcn_readfirstlane(lds_addr);
  asm volatile("s_mov_b32 m0, %2\n\ts_nop 0\n\tglobal_load_lds_dwordx4 %0, %1" ::"v"(voff), "s"(sbase), "s"(lds_addr) : "memory");
}
DI void glds16_sc1(const void* sbase, u32 voff, u32 lds_addr) {
  sbase = uni(sbase);
  lds_addr = __builtin_amdgcn_readfirstlane(lds_addr);
  asm volatile("s_mov_b32 m0, %2\n\ts_nop 0\n\tglobal_load_lds_dwordx4 %0, %1 sc1" ::"v"(voff), "s"(sbase), "s"(lds_addr) : "memory");
}
typedef unsigned long long u64;
typedef __attribute__((address_space(1))) u64 gu64;
#define RLX_AGENT __ATOMIC_RELAXED, __HIP_MEMORY_SCOPE_AGENT
static_assert(4 + C_NW1 <= 72 / C_VSP, "");
constexpr int NWC = C_NW0 + C_NW1;
template <bool WF>
DI void conv_body(const u16* __restrict__ ssm_k, const u16* __restrict__ ssm_D, char* __restrict__ ws, char* smem,
                  const int w, u32x4 (&wf)[28], const char* wbase) {
  const int lane = threadIdx.x & 63;
  const int ch = w >> 2, q = w & 3;
  const int cpair = blockIdx.x;
  const int d = 2 * cpair + ch;
  char* L = smem + ch * K1_CH;
  const u32 L0 = (u32)(size_t)(LAS char*)L;
  const u16* kf = ssm_k + (size_t)d * S;
  const u32 vo = lane * 16;
  {
    const u32 vl = (lane > 62 ? 62 : lane) * 16;
    glds16x4_asm(kf + (2 + 2 * q), vo, vl, L0 + q * K1_KSTR + 64);
    const char* ub = ws + OFF_RO + ((q >> 1) ? 0 : 2) + (size_t)d * S * 2 + (q & 1) * 2048;
    const u32 ul = L0 + K1_UOFF + (q >> 1) * K1_USTR + (q & 1) * 2048;
    glds16_nom0(ub, vo, ul);
    glds16_nom0(ub + 1024, vo, ul + 1024);
  }
  const int n = lane & 15, q4 = lane >> 4;
  const int pbase = 32 * (n >> 1) + 8 * q4 + 4 * (n & 1) + 512 * q;
  const int di = n + q - 15;
  u32 head;
  asm volatile("global_load_dword %0, %1, %2" : "=v"(head) : "v"((u32)(di < 0 ? 0 : di) * 4), "s"(uni(kf)) : "memory");
  auto ldwf = [&](auto kI) {
    constexpr int k = decltype(kI)::value;
    gload16_off<(k % 4) * 1024>(wf[k], wbase + (k / 4) * 4096, vo);
  };
  bar();
  if constexpr (WF) static_for<0, C_NW0>(ldwf);
  const CAS float* MODc = (const CAS float*)(size_t)(ws + OFF_MOD);
  const float sa = MODc[d], ca = MODc[D + d], ga = MODc[2 * D + d], cbv = MODc[4 * D + d];
  const u32 Draw = ((const CAS u32*)(size_t)ssm_D)[cpair];
  if (lane < 14) *(LAS u32x4*)(L + K1_UOFF + (q >> 1) * K1_USTR + (256 + 14 * (q & 1) + lane) * 16) = u32x4{0u, 0u, 0u, 0u};
  u32 Ab = L0 + (n & 3) * K1_KSTR + ((n >> 2) + q4 + 8 * q) * 16;
  u32 Bb = L0 + K1_UOFF + (n & 1) * K1_USTR + (28 - 4 * (n >> 1) + q4 + 8 * q) * 16;
  u32 Hd = L0 + q * K1_KSTR + n * 4;
  asm volatile("" : "+v"(Ab), "+v"(Bb), "+v"(Hd) : "s"(sa), "s"(ca), "s"(ga), "s"(Draw), "s"(cbv));
  asm volatile("s_waitcnt vmcnt(%1)" : "+v"(head) : "n"(WF ? C_NW0 : 0) : "memory");
  *(LAS u32*)(size_t)Hd = di < 0 ? 0u : head;
  if (q == 1 && lane == 0) *(LAS u16*)(L + K1_UOFF + 2047 * 2) = 0;
  wait_lgkm0();
  bar();
  bf16x8 fa[8], fb[8];
  auto rd = [&](int dl) {
#pragma unroll
    for (int g = 0; g < 8; ++g) {
      fa[g] = *(const LAS bf16x8*)(size_t)(Ab + 64 * dl + 512 * g);
      fb[g] = *(const LAS bf16x8*)(size_t)(Bb + 64 * dl + 512 * (7 - g));
    }
  };
  rd(0);
  __builtin_amdgcn_sched_barrier(0);
  u32x2 qv[2];
  f32x4 mu[2], rs[2];
  asm volatile("global_load_dwordx2 %0, %6, %8\n\t"
               "global_load_dwordx2 %1, %6, %8 offset:512\n\t"
               "global_load_dwordx4 %2, %7, %9\n\t"
               "global_load_dwordx4 %3, %7, %9 offset:1024\n\t"
               "global_load_dwordx4 %4, %7, %10\n\t"
               "global_load_dwordx4 %5, %7, %10 offset:1024"
               : "=&v"(qv[0]), "=&v"(qv[1]), "=&v"(mu[0]), "=&v"(mu[1]), "=&v"(rs[0]), "=&v"(rs[1])
               : "v"((u32)pbase * 2), "v"((u32)pbase * 4), "s"(uni(ws + OFF_QT + (size_t)d * S * 2)),
                 "s"(uni(ws + OFF_MEAN)), "s"(uni(ws + OFF_RSTD))
               : "memory");
  const u32x4 sent = {~0u, ~0u, ~0u, ~0u};
  const int fx = blockIdx.x & 7, fi = blockIdx.x >> 3;
  const void* fillb = uni(ws + OFF_ACT + (size_t)((((fx >> 1) * 4 + (fi & 3)) * 4 + (fx & 1) + 2 * (w >> 2)) * 112 + (fi >> 2) * 16 + (w & 3) * 4) * 1024);
  f32x4 acc[8];
#pragma unroll
  for (int j = 0; j < 8; ++j) acc[j] = f32x4{0.f, 0.f, 0.f, 0.f};
  static_for<0, 72>([&](auto mI) {
    constexpr int m = decltype(mI)::value, t = m % 36;
    constexpr int sd = t < 1 ? 0 : t < 3 ? 1 : t < 6 ? 2 : t < 10 ? 3 : t < 15 ? 4 : t < 21 ? 5 : t < 28 ? 6 : 7;
    constexpr int g = t - sd * (sd + 1) / 2;
    if constexpr (m == 36) {
      rd(1);
      __builtin_amdgcn_sched_barrier(0);
    }
    f32x4 a = __builtin_amdgcn_mfma_f32_16x16x32_bf16(fa[g], fb[sd - g], acc[sd], 0, 0, 0);
    asm volatile("" : "+v"(a));
    acc[sd] = a;
    if constexpr (m % C_VSP == C_VSP - 1) {
      constexpr int v = m / C_VSP;
      if constexpr (v < 4) {
        u32x4 sv = sent;
        asm volatile("global_store_dwordx4 %0, %1, %2 offset:%3 sc1\n\ts_nop 1" ::"v"(vo), "v"(sv), "s"(fillb), "n"(v * 1024) : "memory");
      } else if constexpr (WF && v - 4 < C_NW1) {
        ldwf(std::integral_constant<int, C_NW0 + v - 4>{});
      }
      __builtin_amdgcn_sched_barrier(0);
    }
  });
  LAS f32x4* X = (LAS f32x4*)(L + K1_XOFF);
#pragma unroll
  for (int j = 0; j < 8; ++j) X[(j * 4 + q) * 64 + lane] = acc[j];
  u32x4 uc[2];
#pragma unroll
  for (int j = 0; j < 2; ++j)
    uc[j] = *(const LAS u32x4*)(L + K1_UOFF + K1_USTR + (255 - (32 * (2 * q + j) + 4 * (n >> 1) + q4)) * 16);
  asm volatile("s_waitcnt vmcnt(%6)"
               : "+v"(qv[0]), "+v"(qv[1]), "+v"(mu[0]), "+v"(mu[1]), "+v"(rs[0]), "+v"(rs[1])
               : "n"(WF ? C_NW1 : 0)
               : "memory");
  f32x4 qf[2], hg[2], kd[2];
  {
    const int sh = (n & 1) ? 16 : 0;
    const float gc = ga * round_bf16(1.f + ca), gs = ga * sa, Dd = ch ? hi_f(Draw) : lo_f(Draw);
#pragma unroll
    for (int j = 0; j < 2; ++j) {
#pragma unroll
      for (int r = 0; r < 4; ++r) {
        kd[j][r] = __builtin_bit_cast(float, (uc[j][3 - r] << sh) & 0xffff0000u) * Dd;
        qf[j][r] = (r & 1) ? hi_f(qv[j][r >> 1]) : lo_f(qv[j][r >> 1]);
        hg[j][r] = (qf[j][r] - mu[j][r]) * rs[j][r] * gc + gs;
      }
      asm volatile("" : "+v"(kd[j]), "+v"(qf[j]), "+v"(hg[j]));
    }
  }
  wait_lgkm0();
  bar();
  u16* X1T = (u16*)(ws + OFF_X1T) + (size_t)d * S + pbase;
  u16* XS = (u16*)(ws + OFF_XS) + (size_t)d * S + pbase;
  const float c1 = round_bf16(1.f + cbv);
#pragma unroll
  for (int j = 0; j < 2; ++j) {
    const LAS f32x4* Xj = X + ((2 * q + j) * 4) * 64 + lane;
    const f32x4 y = (Xj[0] + Xj[64]) + (Xj[128] + Xj[192]);
    const f32x4 o = qf[j] + hg[j] * (y + kd[j]);
    const u32 p0 = umin32(pack_bf16(o[0], o[1])), p1 = umin32(pack_bf16(o[2], o[3]));
    st8_sc1(XS + 256 * j,
            u32x2{umin32(pack_bf16(lo_f(p0) * c1, hi_f(p0) * c1)), umin32(pack_bf16(lo_f(p1) * c1, hi_f(p1) * c1))});
    st8_sc1(X1T + 256 * j, u32x2{p0, p1});
  }
}

DI void k2_stats(char* __restrict__ ws, char* smem, const int sblk) {
  const int tid = threadIdx.x, lane = tid & 63;
  const int w = __builtin_amdgcn_readfirstlane(tid >> 6);
  const int n = lane & 31, h = lane >> 5;
  {
    const int p0 = sblk * 64;
    if (w < 7) {
      const int task = sblk * 7 + w, t = task >> 1, kh = task & 1;
      const u32x4* wsrc = (const u32x4*)(ws + OFF_WGU) + ((size_t)t * 28 + kh * 14) * 64 + lane;
      const u16* cbp = (const u16*)(ws + OFF_CB) + (n & 1) * D + kh * 224 + 8 * h;
      u32x4 fa[14], fb[14];
#pragma unroll
      for (int i = 0; i < 14; ++i) {
        fa[i] = wsrc[i * 64];
        fb[i] = *(const u32x4*)(cbp + 16 * i);
      }
      __builtin_amdgcn_sched_barrier(0);
      f32x16 va;
#pragma unroll
      for (int r = 0; r < 16; ++r) va[r] = 0.f;
#pragma unroll
      for (int i = 0; i < 14; ++i) va = mfma32(fb[i], __builtin_bit_cast(bf16x8, fa[i]), va);
      if (h == 0) {
        const float f0 = va[0], f1 = va[1];
        const u32 a0 = umin32(__builtin_bit_cast(u32, f0)), a1 = __builtin_bit_cast(u32, f1);
        __hip_atomic_store((gu64*)(size_t)(ws + OFF_VG) + kh * 3584 + t * 32 + n, ((u64)a1 << 32) | a0, RLX_AGENT);
      }
    }
    __syncthreads();
    __builtin_amdgcn_s_sleep(C_SSLEEP);
    const rsrc_t rx = mk_rsrc(ws + OFF_X1T + ((size_t)(w * 56) * S + p0) * 2, 56 * S * 2);
    const int c8 = lane & 7, r8 = lane >> 3;
    u32x4 x[7];
    for (int spin = 0; spin < (1 << 15); ++spin) {
      asm volatile("" ::: "memory");
      u32 m = 0;
#pragma unroll
      for (int i = 0; i < 7; ++i) x[i] = __builtin_amdgcn_raw_buffer_load_b128(rx, (r8 + 8 * i) * (S * 2) + c8 * 16, 0, 16);
#pragma unroll
      for (int i = 0; i < 7; ++i) m = umax3(umax3(m, x[i][0], x[i][1]), x[i][2], x[i][3]);
      if (__all(m != ~0u)) break;
      __builtin_amdgcn_s_sleep(1);
    }
    float sa[8], qa[8];
#pragma unroll
    for (int e = 0; e < 8; ++e) sa[e] = qa[e] = 0.f;
#pragma unroll
    for (int i = 0; i < 7; ++i)
#pragma unroll
      for (int e = 0; e < 8; ++e) {
        const float a = (e & 1) ? hi_f(x[i][e >> 1]) : lo_f(x[i][e >> 1]);
        sa[e] += a;
        qa[e] = __builtin_fmaf(a, a, qa[e]);
      }
    LAS float* P = (LAS float*)smem;
    {
      LAS float* pp = P + (w * 8 + r8) * 128 + c8 * 8;
      *(LAS f32x4*)pp = f32x4{sa[0], sa[1], sa[2], sa[3]};
      *(LAS f32x4*)(pp + 4) = f32x4{sa[4], sa[5], sa[6], sa[7]};
      *(LAS f32x4*)(pp + 64) = f32x4{qa[0], qa[1], qa[2], qa[3]};
      *(LAS f32x4*)(pp + 68) = f32x4{qa[4], qa[5], qa[6], qa[7]};
    }
    wait_lgkm0();
    {
      float s1 = 0.f, q1 = 0.f;
#pragma unroll
      for (int i = 0; i < 8; ++i) {
        s1 += P[(w * 8 + i) * 128 + lane];
        q1 += P[(w * 8 + i) * 128 + 64 + lane];
      }
      *(LAS f32x2*)(P + 8192 + (w * 64 + lane) * 2) = f32x2{s1, q1};
    }
    __syncthreads();
    if (tid < 64) {
      float sm = 0.f, sq = 0.f;
#pragma unroll
      for (int i = 0; i < 8; ++i) {
        const f32x2 t = *(const LAS f32x2*)(P + 8192 + (i * 64 + tid) * 2);
        sm += t[0];
        sq += t[1];
      }
      const float mean = sm * (1.f / D);
      float var = sq * (1.f / D) - mean * mean;
      var = (var < 0.f ? 0.f : var) + 1e-5f;
      const float rstd = __builtin_amdgcn_rsqf(var);
      const float sig = var * rstd;
      const float mh = round_bf16(mean), sh = round_bf16(sig);
      const u32 b0 = pack_bf16(-mh, mh - mean), b1 = pack_bf16(-mh, sh), b2 = pack_bf16(sig - sh, sh);
      const float c1 = -2.302208198f * rstd;
      const float c2 = -0.1029432397f * (rstd * rstd * rstd);
      gu64* G = (gu64*)(size_t)(ws + OFF_SG) + p0 + tid;
      __hip_atomic_store(G, ((u64)b1 << 32) | b0, RLX_AGENT);
      __hip_atomic_store(G + S, ((u64)__builtin_bit_cast(u32, var) << 32) | b2, RLX_AGENT);
      __hip_atomic_store(G + 2 * S, ((u64)__builtin_bit_cast(u32, c1) << 32) | __builtin_bit_cast(u32, c2), RLX_AGENT);
    }
  }
}

DI void k2_gemm(const u16* __restrict__ ssm_k, const u16* __restrict__ ssm_D, char* __restrict__ ws, u16* __restrict__ out,
                char* smem, const int w) {
  const int tid = threadIdx.x, lane = tid & 63;
  const int n = lane & 31, h = lane >> 5;
  const int xcd = blockIdx.x & 7, idx = blockIdx.x >> 3;
  const int mt = (xcd >> 1) * 4 + (idx & 3);
  const int dt = idx >> 2, sh2 = xcd & 1;
  const int ft = sh2 * 7 + dt;
  const int tile = ft * 8 + w;
  const u32 lds0 = (u32)(size_t)(LAS char*)smem;
  u32x4 wf[28], wd[28];
  const char* wbase = ws + OFF_WGU + (size_t)(tile * 28) * 1024;
  const u32 wa = lane * 16;
  auto ldwf = [&](auto kI) {
    constexpr int k = decltype(kI)::value;
    gload16_off<(k % 4) * 1024>(wf[k], wbase + (k / 4) * 4096, wa);
  };
  conv_body<true>(ssm_k, ssm_D, ws, smem, w, wf, wbase);
  const char* xbase = ws + OFF_XS + ((size_t)(8 * w) * S + mt * 128) * 2;
  const u32 xoff = (u32)((lane >> 3) * S + ((lane & 7) ^ (((lane >> 4) & 1) << 2)) * 8) * 2;
  const char* wdbase = ws + OFF_WD + ((size_t)(2 * dt) * 112 + 14 * w) * 1024 + 4096;
  const int gsl = w < 6 ? w : w - 6;
  const char* gbase = ws + OFF_SG + (size_t)((gsl >> 1) * S + mt * 128 + (gsl & 1) * 64) * 8;
  auto ldwd = [&](auto kI) {
    constexpr int q = decltype(kI)::value, k = (q & 1) * 14 + (q >> 1), kk = k % 14;
    gload16_off<(kk % 8) * 1024 - 4096>(wd[k], wdbase + (k / 14) * 112 * 1024 + (kk / 8) * 8192, wa);
  };
  auto ldx = [&](auto cI) {
    constexpr int c = decltype(cI)::value;
    glds16_sc1(xbase + (size_t)(c % 7) * (64 * S * 2) + (c / 7) * 128, xoff, lds0 + (64 * c + 8 * w) * 128);
  };
  auto xok = [&](auto c0I, auto c1I) {
    u32 m = 0;
#pragma unroll
    for (int c = decltype(c0I)::value; c < decltype(c1I)::value; ++c) {
      const u32x4 t = *(const LAS u32x4*)(smem + (64 * c + 8 * w) * 128 + lane * 16);
      m = umax3(umax3(m, t[0], t[1]), t[2], t[3]);
    }
    return (bool)__all(m != ~0u);
  };
  auto xvalid = [&](auto c0I, auto c1I) {
    if (__builtin_expect(!xok(c0I, c1I), 0)) {
#pragma nounroll
      for (int spin = 0; spin < (1 << 14); ++spin) {
        __builtin_amdgcn_s_sleep(4);
        static_for<decltype(c0I)::value, decltype(c1I)::value>(ldx);
        wait_vm<0>();
        if (xok(c0I, c1I)) break;
      }
    }
    __builtin_amdgcn_sched_barrier(0);
  };
  wait_lgkm0();
  bar();
  auto pin = [](f32x16& a) { asm volatile("" : "+v"(a)); };
  auto pinw = [&](auto kI) {
    u32x4 t = wf[decltype(kI)::value];
    asm volatile("" : "+v"(t)::"memory");
    wf[decltype(kI)::value] = t;
  };
  u32x4 aext;
  constexpr int NWH = NWC + (C_NWH - NWC);
  constexpr int NWP = NWC + C_NWP;
  static_for<NWC, NWP>(ldwf);
  static_for<0, 7>(ldx);
  static_for<NWP, NWH>(ldwf);
  int xo[2][2];
  {
    const int blk = (lane >> 4) & 1, qq = (lane & 15) >> 2, p = lane & 3;
#pragma unroll
    for (int i = 0; i < 2; ++i)
#pragma unroll
      for (int t = 0; t < 2; ++t)
        xo[i][t] = 128 * (8 * h + 4 * t + qq) + 16 * ((4 * i + 2 * blk + (p >> 1)) ^ ((qq >> 1) << 2)) + 8 * (p & 1);
  }
  f32x16 acc[4];
#pragma unroll
  for (int i = 0; i < 4; ++i)
#pragma unroll
    for (int r = 0; r < 16; ++r) acc[i][r] = 0.f;
  asm volatile("" : "+v"(xo[0][0]), "+v"(xo[0][1]), "+v"(xo[1][0]), "+v"(xo[1][1]));
  constexpr int PF = 3;
  bf16x8 xr[PF][2];
  auto rdx = [&](auto kI, auto iI) {
    constexpr int k = decltype(kI)::value, i = decltype(iI)::value;
    if constexpr (k == 28) {
      int a0 = xo[i][0] + K2_XH, a1 = xo[i][1] + K2_XH;
      asm volatile("" : "+v"(a0), "+v"(a1));
      xo[i][0] = a0;
      xo[i][1] = a1;
    }
    xr[k % PF][i] = tr_frag(smem, xo[i][0] + (k % 28) * 2048, xo[i][1] + (k % 28) * 2048);
  };
  using I0 = std::integral_constant<int, 0>;
  using I1 = std::integral_constant<int, 1>;
  using I2 = std::integral_constant<int, 2>;
  using I3 = std::integral_constant<int, 3>;
  using I7 = std::integral_constant<int, 7>;
  using I14 = std::integral_constant<int, 14>;
  LAS u64* CS = (LAS u64*)(smem + K2_ST);
  wait_vm<4 + NWH - NWP>();
  static_for<0, NWP>(pinw);
  xvalid(I0{}, I3{});
  bar();
  static_for<0, PF - 1>([&](auto jI) {
    rdx(jI, I0{});
    rdx(jI, I1{});
  });
  __builtin_amdgcn_sched_barrier(0);
  u64 gv, gp0, gp1;
  const char* vbase = ws + OFF_VG + (size_t)tile * 256;
  constexpr int RW = 28 - NWH, KX = RW + 7, KV = 25;
  constexpr int KF = C_KF;
  constexpr int WE = 27 - KX;
  static_assert(KF >= KX && KF <= 27, "");
  static_assert(KX + 4 <= KV && WE <= 9, "stream does not fit");
  static_for<0, 28>([&](auto kI) {
    constexpr int k = decltype(kI)::value;
    if constexpr (k == 6) {
      wait_vm<6>();
      static_for<NWP, NWH>(pinw);
      xvalid(I3{}, I7{});
      bar();
    }
    if constexpr (k >= NWH) {
      u32x4 t = wf[k];
      asm volatile("s_waitcnt vmcnt(%1)" : "+v"(t) : "n"(NWH - 1 + (k > KF ? 2 : 0)) : "memory");
      wf[k] = t;
    }
    if constexpr (k == KV) {
      wait_vm<KV - KX + (KV > KF ? 2 : 0)>();
      xvalid(I7{}, I14{});
      bar();
    }
    __builtin_amdgcn_sched_barrier(0);
    acc[0] = mfma32(wf[k], xr[k % PF][0], acc[0]);
    pin(acc[0]);
    __builtin_amdgcn_sched_barrier(0);
    rdx(std::integral_constant<int, k + PF - 1>{}, I0{});
    __builtin_amdgcn_sched_barrier(0);
    acc[1] = mfma32(wf[k], xr[k % PF][1], acc[1]);
    pin(acc[1]);
    __builtin_amdgcn_sched_barrier(0);
    rdx(std::integral_constant<int, k + PF - 1>{}, I1{});
    if constexpr (k < RW) ldwf(std::integral_constant<int, NWH + k>{});
    else if constexpr (k < KX) ldx(std::integral_constant<int, 7 + k - RW>{});
    else if constexpr (k != KF) ldwd(std::integral_constant<int, k - KX - (k > KF ? 1 : 0)>{});
    else {
      u64 t, t0, t1;
      asm volatile("global_load_dwordx2 %0, %1, %2 sc1" : "=v"(t) : "v"((u32)lane * 8), "s"(uni(gbase)) : "memory");
      asm volatile("global_load_dwordx2 %0, %1, %2 sc1" : "=v"(t0) : "v"((u32)n * 8), "s"(uni(vbase)) : "memory");
      asm volatile("global_load_dwordx2 %0, %1, %2 sc1" : "=v"(t1) : "v"((u32)n * 8), "s"(uni(vbase + 3584 * 8)) : "memory");
      gv = t;
      gp0 = t0;
      gp1 = t1;
    }
    __builtin_amdgcn_sched_barrier(0);
  });
  const rsrc_t rw = mk_rsrc(ws + OFF_ACT + ((size_t)(mt * 4) * 112 + tile) * 1024, 337 * 1024);
  const rsrc_t ra = mk_rsrc(ws + OFF_ACT + ((size_t)(mt * 4 + sh2) * 112 + 14 * w) * 1024, 238 * 1024);
  u32x4 af[28];
  auto ldact = [&](auto kI) {
    constexpr int k = decltype(kI)::value;
    af[k] = __builtin_amdgcn_raw_buffer_load_b128(ra, lane * 16 + (k % 14) * 1024, (k / 14) * 224 * 1024, 16);
  };
  float c1[2], c2[2], var[2], pe[2], pm[2], pr[2];
  u32x4 ov[2];
  auto corr = [&](auto iI) {
    constexpr int i = decltype(iI)::value;
    u64 g[3];
#pragma unroll
    for (int k = 0; k < 3; ++k) g[k] = CS[k * 128 + i * 32 + n];
    const u32x4 bext = {(u32)g[0], (u32)(g[0] >> 32), (u32)g[1], 0u};
    acc[i] = mfma32(aext, __builtin_bit_cast(bf16x8, bext), acc[i]);
    c1[i & 1] = __builtin_bit_cast(float, (u32)(g[2] >> 32));
    c2[i & 1] = __builtin_bit_cast(float, (u32)g[2]);
    var[i & 1] = __builtin_bit_cast(float, (u32)(g[1] >> 32));
  };
  auto slice = [&](auto iI, auto uI) {
    constexpr int i = decltype(iI)::value, u = decltype(uI)::value, z = i & 1;
    if constexpr (u >= 1) {
      float o = pm[z] * __builtin_amdgcn_rcpf(__builtin_fmaf(pe[z], var[z], var[z]));
      if constexpr ((u - 1) & 1) {
        u32 t = umin32(pack_bf16(pr[z], o));
        asm volatile("" : "+v"(t));
        ov[z][(u - 1) / 2] = t;
      } else {
        asm volatile("" : "+v"(o));
        pr[z] = o;
      }
    }
    if constexpr (u < 8) {
      const float a = acc[i][u];
      float e = __builtin_amdgcn_exp2f(__builtin_fmaf(a * a, c2[z], c1[z]) * a), m = a * acc[i][u + 8];
      asm volatile("" : "+v"(e), "+v"(m));
      pe[z] = e;
      pm[z] = m;
    } else {
      __builtin_amdgcn_raw_buffer_store_b128(ov[z], rw, lane * 16 + i * 112 * 1024, 0, 16);
      asm volatile("s_nop 1");
    }
  };
  constexpr int KG = C_KG;
  constexpr auto slq = [](int g) { return g <= 0 ? 0 : (g >= C_SLN ? 18 : (g * 18) / C_SLN); };
  auto slice01 = [&](auto jI) {
    constexpr int j = decltype(jI)::value;
    slice(std::integral_constant<int, j & 1>{}, std::integral_constant<int, j / 2>{});
  };
  static_for<0, 28>([&](auto kI) {
    constexpr int k = decltype(kI)::value;
    if constexpr (k == KG) {
      u64 t = gv, p0 = gp0, p1 = gp1;
      asm volatile("s_waitcnt vmcnt(%3)" : "+v"(t), "+v"(p0), "+v"(p1) : "n"(KG + 27 - KF) : "memory");
      if (!__all((t >> 32) != 0 && (u32)p0 != ~0u && (u32)p1 != ~0u)) {
        for (int spin = 0; spin < (1 << 16); ++spin) {
          __builtin_amdgcn_s_sleep(2);
          t = __hip_atomic_load((gu64*)(size_t)gbase + lane, RLX_AGENT);
          p0 = __hip_atomic_load((gu64*)(size_t)vbase + n, RLX_AGENT);
          p1 = __hip_atomic_load((gu64*)(size_t)vbase + 3584 + n, RLX_AGENT);
          if (__all((t >> 32) != 0 && (u32)p0 != ~0u && (u32)p1 != ~0u)) break;
        }
        __builtin_amdgcn_s_waitcnt(0x0F70);
      }
      CS[gsl * 64 + lane] = t;
      const float v1 = __builtin_bit_cast(float, (u32)p0) + __builtin_bit_cast(float, (u32)p1);
      const float v2 = __builtin_bit_cast(float, (u32)(p0 >> 32)) + __builtin_bit_cast(float, (u32)(p1 >> 32));
      const float v1h = round_bf16(v1), v2h = round_bf16(v2);
      u32x4 a = {pack_bf16(v1h, v1h), pack_bf16(v1 - v1h, v2h), pack_bf16(v2h, v2 - v2h), 0u};
      if (h) a = u32x4{0u, 0u, 0u, 0u};
      asm volatile("" : "+v"(a));
      aext = a;
      wait_lgkm0();
      __builtin_amdgcn_sched_barrier(0);
      bar();
      corr(I0{});
      corr(I1{});
      __builtin_amdgcn_sched_barrier(0);
    }
    acc[2] = mfma32(wf[k], xr[(28 + k) % PF][0], acc[2]);
    pin(acc[2]);
    __builtin_amdgcn_sched_barrier(0);
    if constexpr (k + PF - 1 < 28) rdx(std::integral_constant<int, 28 + k + PF - 1>{}, I0{});
    static_for<slq(2 * (k - KG - 1)), slq(2 * (k - KG - 1) + 1)>(slice01);
    __builtin_amdgcn_sched_barrier(0);
    acc[3] = mfma32(wf[k], xr[(28 + k) % PF][1], acc[3]);
    pin(acc[3]);
    __builtin_amdgcn_sched_barrier(0);
    if constexpr (k % C_LOCK == C_LOCK - 1) bar();
    if constexpr (k + PF - 1 < 28) rdx(std::integral_constant<int, 28 + k + PF - 1>{}, I1{});
    static_for<slq(2 * (k - KG - 1) + 1), slq(2 * (k - KG - 1) + 2)>(slice01);
    if constexpr (WE + k < 28) ldwd(std::integral_constant<int, WE + k>{});
    else if constexpr (WE + k - 28 < 7) ldact(std::integral_constant<int, WE + k - 28>{});
    __builtin_amdgcn_sched_barrier(0);
  });
  constexpr int A0 = WE < 7 ? WE : 7;
  corr(I2{});
  corr(I3{});
  __builtin_amdgcn_sched_barrier(0);
  static_for<0, 9>([&](auto uI) {
    constexpr int u = decltype(uI)::value;
    slice(I2{}, uI);
    slice(I3{}, uI);
    if constexpr (A0 + u < 14 && u < 8) ldact(std::integral_constant<int, A0 + u>{});
    __builtin_amdgcn_sched_barrier(0);
  });
  static_for<(A0 + 8 < 14 ? A0 + 8 : 14), 14>(ldact);
  bar();
  auto chk = [&](auto gI) {
    constexpr int g = decltype(gI)::value;
    u32 m = 0;
#pragma unroll
    for (int k = 7 * g; k < 7 * g + 7; ++k) m = umax3(umax3(m, af[k][0], af[k][1]), af[k][2], af[k][3]);
    return (bool)__all(m != ~0u);
  };
  auto valid = [&](auto gI) {
    constexpr int g = decltype(gI)::value;
    if (__builtin_expect(!chk(gI), 0)) {
#pragma nounroll
      for (int spin = 0; spin < (1 << 14); ++spin) {
        __builtin_amdgcn_s_sleep(8);
        asm volatile("" ::: "memory");
        static_for<7 * g, 7 * g + 7>(ldact);
        if (chk(gI)) break;
      }
      __builtin_amdgcn_s_waitcnt(0x0F70);
    }
    __builtin_amdgcn_sched_barrier(0);
  };
  const int pcol = mt * 128 + sh2 * 32 + n;
  const int dbase = dt * 64 + w * 8 + 4 * h;
  __builtin_amdgcn_sched_barrier(0);
  LAS f32x4* R = (LAS f32x4*)smem;
  f32x16 o[4];
#pragma unroll
  for (int i = 0; i < 4; ++i)
#pragma unroll
    for (int r = 0; r < 16; ++r) o[i][r] = 0.f;
  auto dump1 = [&](auto tjI, auto cI) {
    constexpr int tj = decltype(tjI)::value, c = decltype(cI)::value, i = tj * 2 + (c >> 2), q = c & 3;
    R[((tj * 8 + c) * 8 + w) * 64 + lane] = f32x4{o[i][4 * q], o[i][4 * q + 1], o[i][4 * q + 2], o[i][4 * q + 3]};
  };
  auto step = [&](auto kI) {
    constexpr int k = decltype(kI)::value, tj = k / 14;
    const bf16x8 b = __builtin_bit_cast(bf16x8, af[k]);
    o[2 * tj] = mfma32(wd[k % 14], b, o[2 * tj]);
    pin(o[2 * tj]);
    __builtin_amdgcn_sched_barrier(0);
    if constexpr (k >= 15 && k < 19) dump1(I0{}, std::integral_constant<int, 2 * (k - 15)>{});
    __builtin_amdgcn_sched_barrier(0);
    o[2 * tj + 1] = mfma32(wd[14 + k % 14], b, o[2 * tj + 1]);
    pin(o[2 * tj + 1]);
    __builtin_amdgcn_sched_barrier(0);
    if constexpr (k >= 15 && k < 19) dump1(I0{}, std::integral_constant<int, 2 * (k - 15) + 1>{});
    if constexpr (k >= C_T1 && k < 14) {
      constexpr int n0 = ((k - C_T1) * 14) / (14 - C_T1), n1 = ((k + 1 - C_T1) * 14) / (14 - C_T1);
      static_for<14 + n0, 14 + n1>(ldact);
    }
    __builtin_amdgcn_sched_barrier(0);
  };
  u16 xres[2][4];
  f32x4 gbv;
  auto fin = [&](auto tjI) {
    constexpr int tj = decltype(tjI)::value;
    f32x4 v[8];
#pragma unroll
    for (int sw = 0; sw < 8; ++sw) v[sw] = R[((tj * 8 + w) * 8 + sw) * 64 + lane];
    const f32x4 sum = ((v[0] + v[1]) + (v[2] + v[3])) + ((v[4] + v[5]) + (v[6] + v[7]));
    float r[4];
#pragma unroll
    for (int e = 0; e < 4; ++e) r[e] = __builtin_fmaf(gbv[e], sum[e], bf2f(xres[tj][e]));
    const int pc = pcol + 64 * tj;
    const int srow = (pc & ~7) | ((pc & 3) << 1) | ((pc >> 2) & 1);
    *(u32x2*)(out + (size_t)srow * D + dbase) = u32x2{pack_bf16(r[0], r[1]), pack_bf16(r[2], r[3])};
  };
  valid(I0{});
  asm volatile(""
               : "+v"(wd[0]), "+v"(wd[1]), "+v"(wd[2]), "+v"(wd[3]), "+v"(wd[4]), "+v"(wd[5]), "+v"(wd[6]), "+v"(wd[7]),
                 "+v"(wd[8]), "+v"(wd[9]), "+v"(wd[10]), "+v"(wd[11]), "+v"(wd[12]), "+v"(wd[13])::"memory");
  asm volatile(""
               : "+v"(wd[14]), "+v"(wd[15]), "+v"(wd[16]), "+v"(wd[17]), "+v"(wd[18]), "+v"(wd[19]), "+v"(wd[20]),
                 "+v"(wd[21]), "+v"(wd[22]), "+v"(wd[23]), "+v"(wd[24]), "+v"(wd[25]), "+v"(wd[26]), "+v"(wd[27])::"memory");
  static_for<0, 3>(step);
  {
    const rsrc_t rx = mk_rsrc(ws + OFF_X1T, D * S * 2);
#pragma unroll
    for (int r = 0; r < 8; ++r)
      xres[r >> 2][r & 3] = __builtin_amdgcn_raw_buffer_load_b16(rx, ((dbase + (r & 3)) * S + pcol + (r >> 2) * 64) * 2, 0, 16);
    gbv = *(const f32x4*)((const float*)(ws + OFF_MOD) + 5 * D + dbase);
    __builtin_amdgcn_sched_barrier(0);
  }
  static_for<3, 7>(step);
  valid(I1{});
  static_for<7, 14>(step);
  valid(I2{});
  static_for<14, 21>(step);
  wait_lgkm0();
  bar();
  valid(I3{});
  static_for<21, 28>([&](auto kI) {
    constexpr int k = decltype(kI)::value;
    o[2] = mfma32(wd[k % 14], __builtin_bit_cast(bf16x8, af[k]), o[2]);
    pin(o[2]);
    __builtin_amdgcn_sched_barrier(0);
    if constexpr (k == 23) {
      fin(I0{});
      __builtin_amdgcn_sched_barrier(0);
    }
  });
  static_for<21, 28>([&](auto kI) {
    constexpr int k = decltype(kI)::value;
    o[3] = mfma32(wd[14 + k % 14], __builtin_bit_cast(bf16x8, af[k]), o[3]);
    pin(o[3]);
    __builtin_amdgcn_sched_barrier(0);
    if constexpr (k >= 22 && k < 26) dump1(I1{}, std::integral_constant<int, k - 22>{});
    __builtin_amdgcn_sched_barrier(0);
  });
  static_for<4, 8>([&](auto cI) { dump1(I1{}, cI); });
  wait_lgkm0();
  bar();
  fin(I1{});
}

extern "C" __global__ NOPK __launch_bounds__(512, 2) void k1_main(const u16* __restrict__ ssm_k,
                                                                  const u16* __restrict__ ssm_D, char* __restrict__ ws,
                                                                  u16* __restrict__ out) {
  extern __shared__ __attribute__((aligned(16))) char smem[];
  const int w = __builtin_amdgcn_readfirstlane(threadIdx.x >> 6);
  if (blockIdx.x >= 224) {
    k2_stats(ws, smem, blockIdx.x - 224);
  } else {
    k2_gemm(ssm_k, ssm_D, ws, out, smem, w);
  }
}

extern "C" __device__ __attribute__((used, noinline)) void k_code_end() {}

extern "C" void kernel_launch(void* const* d_in, const int* in_sizes, int n_in, void* d_out, int out_size, void* d_ws,
                              size_t ws_size, hipStream_t stream) {
  const u16* q = (const u16*)d_in[0];
  const u16* k = (const u16*)d_in[1];
  const u16* v = (const u16*)d_in[2];
  const u16* shift_k = (const u16*)d_in[3];
  const u16* ssm_k = (const u16*)d_in[4];
  const u16* ssm_D = (const u16*)d_in[5];
  const u16* w_gate = (const u16*)d_in[7];
  const u16* w_up = (const u16*)d_in[8];
  const u16* w_down = (const u16*)d_in[9];
  const u16* c = (const u16*)d_in[10];
  const u16* w_mod = (const u16*)d_in[11];
  char* ws = (char*)d_ws + ((size_t)192  __H0__ );
  (void)hipFuncSetAttribute((const void*)k1_main, hipFuncAttributeMaxDynamicSharedMemorySize, K2_LDS);
  k0_prep<<<K0_GRID, 256, 0, stream>>>(q, k, v, shift_k, c, w_mod, w_gate, w_up, w_down, ws);
  k1_main<<<256, 512, K2_LDS, stream>>>(ssm_k, ssm_D, ws, (u16*)d_out);
}
```

```cpp
#include <hip/hip_runtime.h>
#include <hip/hip_bf16.h>
#include <stdint.h>
#include <type_traits>

typedef unsigned short u16;
typedef unsigned int u32;
typedef short bf16x8 __attribute__((ext_vector_type(8)));
typedef short s16x4 __attribute__((ext_vector_type(4)));
typedef float f32x2 __attribute__((ext_vector_type(2)));
typedef float f32x4 __attribute__((ext_vector_type(4)));
typedef float f32x16 __attribute__((ext_vector_type(16)));
typedef u32 u32x2 __attribute__((ext_vector_type(2)));
typedef u32 u32x4 __attribute__((ext_vector_type(4)));
typedef __bf16 bf16x2_t __attribute__((ext_vector_type(2)));
#define LAS __attribute__((address_space(3)))
#define DI __device__ __forceinline__

constexpr int S = 2048, D = 448, F = 1792;

constexpr size_t SZ_DS = (size_t)D * S * 2;
constexpr size_t OFF_RO = SZ_DS;
constexpr size_t OFF_QT = OFF_RO + SZ_DS;
constexpr size_t OFF_X1T = OFF_QT + SZ_DS;
constexpr size_t OFF_MEAN = OFF_X1T + SZ_DS;
constexpr size_t OFF_RSTD = OFF_MEAN + S * 4;
constexpr size_t OFF_MOD = OFF_RSTD + S * 4;
constexpr size_t OFF_SG = OFF_MOD + 16384;
constexpr size_t OFF_CB = OFF_MOD + 12288;

constexpr size_t OFF_VG = OFF_SG + 3 * S * 8;
constexpr size_t OFF_XS = OFF_VG + 4 * S * 8;
constexpr size_t OFF_WGU = OFF_XS + SZ_DS;
constexpr size_t OFF_WD = OFF_WGU + (size_t)2 * D * F * 2;
constexpr size_t OFF_ACT = OFF_WD + (size_t)D * F * 2;
constexpr size_t WS_END = OFF_ACT + (size_t)S * F * 2;

#if defined(__HIP_DEVICE_COMPILE__)
#define NOPK __attribute__((target("no-packed-fp32-ops")))
#else
#define NOPK
#endif

constexpr int C_KF = 25;
constexpr int C_KG = 1;
constexpr int C_LOCK = 4;
constexpr int C_NW0 = 5;
constexpr int C_NW1 = 5;
constexpr int C_NWH = 15;
constexpr int C_NWP = 3;
constexpr int C_SLN = 18;
constexpr int C_SSLEEP = 50;
constexpr int C_T1 = 5;
constexpr int C_VSP = 4;

template <int I, int N, class Fn>
DI void static_for(Fn&& f) {
  if constexpr (I < N) {
    f(std::integral_constant<int, I>{});
    static_for<I + 1, N>(f);
  }
}

DI float lo_f(u32 x) { return __builtin_bit_cast(float, x << 16); }
DI float hi_f(u32 x) { return __builtin_bit_cast(float, x & 0xffff0000u); }
DI u32 pack_bf16(float a, float b) {
  f32x2 v = {a, b};
  return __builtin_bit_cast(u32, __builtin_convertvector(v, bf16x2_t));
}
DI float round_bf16(float a) { return lo_f(pack_bf16(a, 0.f)); }
DI float bf2f(u16 x) { return __builtin_bit_cast(float, (u32)x << 16); }

template <int N>
DI void wait_vm() { asm volatile("s_waitcnt vmcnt(%0)" ::"n"(N) : "memory"); }
DI void wait_lgkm0() { asm volatile("s_waitcnt lgkmcnt(0)" ::: "memory"); }
DI void bar() { __builtin_amdgcn_s_barrier(); }

DI const void* uni(const void* p) {
  const unsigned long long v = (unsigned long long)p;
  const u32 lo = __builtin_amdgcn_readfirstlane((u32)v), hi = __builtin_amdgcn_readfirstlane((u32)(v >> 32));
  return (const void*)(((unsigned long long)hi << 32) | lo);
}
DI void glds16(const void* g, char* lds) {
  __builtin_amdgcn_global_load_lds((const __attribute__((address_space(1))) void*)g, (LAS void*)lds, 16, 0, 0);
}

template <int CTRL>
DI float dpp_add(float v) {
  const int x = __builtin_bit_cast(int, v);
  return v + __builtin_bit_cast(float, __builtin_amdgcn_update_dpp(x, x, CTRL, 0xf, 0xf, false));
}
DI float sum8(float v) {
  v = dpp_add<0xB1>(v);
  v = dpp_add<0x4E>(v);
  v = dpp_add<0x141>(v);
  return v;
}
DI float dot2(u32 a, u32 b, float c) {
  return __builtin_amdgcn_fdot2_f32_bf16(__builtin_bit_cast(bf16x2_t, a), __builtin_bit_cast(bf16x2_t, b), c, false);
}
typedef __amdgpu_buffer_rsrc_t rsrc_t;
DI rsrc_t mk_rsrc(const void* p, u32 bytes) { return __builtin_amdgcn_make_buffer_rsrc((void*)p, 0, bytes, 0x00020000); }
DI void st16_sc1(void* p, u32x4 v) { asm volatile("global_store_dwordx4 %0, %1, off sc1\n\ts_nop 1" ::"v"(p), "v"(v) : "memory"); }
DI void st8_sc1(void* p, u32x2 v) { asm volatile("global_store_dwordx2 %0, %1, off sc1" ::"v"(p), "v"(v) : "memory"); }

DI bf16x8 tr_frag(const char* smem, int o0, int o1) {
  s16x4 x0 = __builtin_amdgcn_ds_read_tr16_b64_v4i16((LAS s16x4*)(smem + o0));
  s16x4 x1 = __builtin_amdgcn_ds_read_tr16_b64_v4i16((LAS s16x4*)(smem + o1));
  return __builtin_shufflevector(x0, x1, 0, 1, 2, 3, 4, 5, 6, 7);
}
DI f32x16 mfma32(u32x4 a, bf16x8 b, f32x16 c) {
  return __builtin_amdgcn_mfma_f32_32x32x16_bf16(__builtin_bit_cast(bf16x8, a), b, c, 0, 0, 0);
}

DI void retile(int task, const u16* __restrict__ w_gate, const u16* __restrict__ w_up, const u16* __restrict__ w_down,
               char* __restrict__ ws, char* smem, int w, int lane) {
  const int xr = lane >> 4, slot = lane & 15;
  const int chn = slot ^ ((xr << 2) | (w & 3));
  const int h = lane >> 5, blk = (lane >> 4) & 1, qq = (lane & 15) >> 2, p = lane & 3;
  const bool gu = task < 112;
  int kq = 0, fg = 0, dg = 0, fk = 0;
  if (gu) {
    fg = task % 28, kq = task / 28;
    const u16* src = (chn < 8 ? w_gate : w_up) + (size_t)(kq * 112 + 4 * w + xr) * F + fg * 64 + (chn & 7) * 8;
#pragma unroll
    for (int i = 0; i < 7; ++i) glds16(src + (size_t)i * 16 * F, smem + (w + 4 * i) * 1024);
  } else {
    dg = (task - 112) % 7, fk = (task - 112) / 7;
    const u16* src = w_down + (size_t)(fk * 224 + (chn >> 3) * 112 + 4 * w + xr) * D + dg * 64 + (chn & 7) * 8;
#pragma unroll
    for (int i = 0; i < 7; ++i) glds16(src + (size_t)i * 16 * D, smem + (w + 4 * i) * 1024);
  }
  const int col = gu ? blk * 128 + w * 32 + (p & 1) * 16 + (p >> 1) * 8 : (w >> 1) * 128 + (w & 1) * 64 + blk * 32 + p * 8;
  int o[2];
#pragma unroll
  for (int t = 0; t < 2; ++t)
    o[t] = 256 * (8 * h + 4 * t + qq) + 16 * ((col >> 4) ^ ((qq << 2) | ((2 * h + t) & 3))) + (col & 8);
  u32x4* dst = gu ? (u32x4*)(ws + OFF_WGU) + ((size_t)(fg * 4 + w) * 28 + kq * 7) * 64 + lane
                  : (u32x4*)(ws + OFF_WD) + ((size_t)(dg * 2 + (w & 1)) * 112 + fk * 14 + (w >> 1) * 7) * 64 + lane;
  __syncthreads();
#pragma unroll
  for (int i = 0; i < 7; ++i)
    st16_sc1(dst + i * 64, __builtin_bit_cast(u32x4, tr_frag(smem, o[0] + i * 4096, o[1] + i * 4096)));
}

constexpr int K0_GRID = 504;

extern "C" __global__ __launch_bounds__(256) void k0_prep(const u16* __restrict__ q, const u16* __restrict__ k,
                                               const u16* __restrict__ v, const u16* __restrict__ shift_k,
                                               const u16* __restrict__ c, const u16* __restrict__ w_mod,
                                               const u16* __restrict__ w_gate,
                                               const u16* __restrict__ w_up, const u16* __restrict__ w_down,
                                               char* __restrict__ ws) {
  __shared__ __attribute__((aligned(16))) char smem[57344 + 1024 + 512];
  const int b = blockIdx.x, tid = threadIdx.x, lane = tid & 63, w = __builtin_amdgcn_readfirstlane(tid >> 6);
  const int xcd = b & 7, idx = b >> 3;
  if (b == 0 && tid == 0) *(unsigned long long*)(ws + WS_END) = __builtin_amdgcn_s_memrealtime();
  u32 cwarm = 0;
  if (idx < 2) {
    u32 lo, hi, len;
    asm volatile(
        "s_getpc_b64 s[40:41]\n\ts_add_u32 s40, s40, k0_prep@rel32@lo+4\n\ts_addc_u32 s41, s41, k0_prep@rel32@hi+12\n\t"
        "s_mov_b32 %0, s40\n\ts_mov_b32 %1, s41\n\ts_mov_b32 %2, k_code_end-k0_prep"
        : "=s"(lo), "=s"(hi), "=s"(len)::"s40", "s41", "scc");
    const char* code = (const char*)(((unsigned long long)hi << 32) | lo);
    if ((u32)(idx * 256 + tid) * 128 < len) cwarm = *(const __attribute__((address_space(1))) u32*)(size_t)(code + (idx * 256 + tid) * 128);
  }
  asm volatile("" ::"s"(q), "s"(k), "s"(v), "s"(shift_k), "s"(c), "s"(w_mod), "s"(w_gate), "s"(w_up), "s"(w_down),
               "s"(ws));
  if (__builtin_expect(idx >= 42, 0)) {
    retile(b - 336, w_gate, w_up, w_down, ws, smem, w, lane);
    if (idx < 58) {
      const int t = (idx - 42) * 2 + (xcd & 1);
      char* ap = ws + (t < 16 ? OFF_X1T : OFF_XS) + (size_t)((t & 15) * 28 + w * 7) * (S * 2) + (xcd >> 1) * 1024 + lane * 16;
      const u32x4 sent = {~0u, ~0u, ~0u, ~0u};
#pragma unroll
      for (int i = 0; i < 7; ++i) st16_sc1(ap + i * (S * 2), sent);
    }
    return;
  }
  int misc = -1;
  if (idx < 6) misc = 64 + b;
  else if (idx >= 34) misc = xcd * 8 + (idx - 34);
  const int rb = xcd * 4 + ((idx - 6) & 3), cg = (idx - 6) >> 2;
  if (__builtin_expect(misc < 0, 1)) {
    const int sq = lane >> 5, cp = lane & 31;
    const int d0 = cg * 64 + cp * 2;
    const int a = rb * 8 + w * 2 + sq;
    const int s0 = a * 8;
    const rsrc_t rk = mk_rsrc(k, S * D * 2), rv = mk_rsrc(v, S * D * 2), rq = mk_rsrc(q, S * D * 2);
    const int oB = (s0 * D + d0) * 2;
    const int oA = oB - 4 * D * 2, oC = oB + 4 * D * 2;
    u32 kk[12], vv[9], qq[8];
#pragma unroll
    for (int i = 1; i < 4; ++i) kk[i] = __builtin_amdgcn_raw_buffer_load_b32(rk, oA + i * D * 2, 0, 0);
#pragma unroll
    for (int i = 0; i < 4; ++i) kk[4 + i] = __builtin_amdgcn_raw_buffer_load_b32(rk, oB + i * D * 2, 0, 0);
#pragma unroll
    for (int i = 0; i < 4; ++i) kk[8 + i] = __builtin_amdgcn_raw_buffer_load_b32(rk, oC + i * D * 2, 0, 0);
#pragma unroll
    for (int i = 0; i < 4; ++i) vv[1 + i] = __builtin_amdgcn_raw_buffer_load_b32(rv, oB + i * D * 2, 0, 0);
#pragma unroll
    for (int i = 0; i < 4; ++i) vv[5 + i] = __builtin_amdgcn_raw_buffer_load_b32(rv, oC + i * D * 2, 0, 0);
#pragma unroll
    for (int i = 0; i < 4; ++i) qq[i] = __builtin_amdgcn_raw_buffer_load_b32(rq, oB + i * D * 2, 0, 0);
#pragma unroll
    for (int i = 0; i < 4; ++i) qq[4 + i] = __builtin_amdgcn_raw_buffer_load_b32(rq, oC + i * D * 2, 0, 0);
    const u32x4 sk = *(const u32x4*)(shift_k + d0 * 4);
    f32x2 wt[4] = {{lo_f(sk[0]), lo_f(sk[2])}, {hi_f(sk[0]), hi_f(sk[2])}, {lo_f(sk[1]), lo_f(sk[3])}, {hi_f(sk[1]), hi_f(sk[3])}};
    f32x2 kf[12], u[9];
#pragma unroll
    for (int i = 1; i < 12; ++i) kf[i] = f32x2{lo_f(kk[i]), hi_f(kk[i])};
#pragma unroll
    for (int i = 1; i < 9; ++i) {
      f32x2 t = kf[i + 3] * wt[0];
      t = __builtin_elementwise_fma(kf[i + 2], wt[1], t);
      t = __builtin_elementwise_fma(kf[i + 1], wt[2], t);
      t = __builtin_elementwise_fma(kf[i], wt[3], t);
      u[i] = t * f32x2{lo_f(vv[i]), hi_f(vv[i])};
    }
    {
      char* dst = ws + (size_t)d0 * (S * 2) + (2040 - s0) * 2;
#pragma unroll
      for (int cI = 0; cI < 2; ++cI) {
        *(u32x4*)(dst + OFF_RO + cI * S * 2) = u32x4{pack_bf16(u[8][cI], u[7][cI]), pack_bf16(u[6][cI], u[5][cI]),
                                              pack_bf16(u[4][cI], u[3][cI]), pack_bf16(u[2][cI], u[1][cI])};
      }
      char* dq = ws + OFF_QT + (size_t)d0 * (S * 2) + s0 * 2;
#pragma unroll
      for (int cI = 0; cI < 2; ++cI) {
        const u32 sel = cI ? 0x07060302u : 0x05040100u;
        *(u32x4*)(dq + cI * S * 2) = u32x4{__builtin_amdgcn_perm(qq[2], qq[0], sel), __builtin_amdgcn_perm(qq[6], qq[4], sel),
                                    __builtin_amdgcn_perm(qq[3], qq[1], sel), __builtin_amdgcn_perm(qq[7], qq[5], sel)};
      }
    }
  } else if (misc < 64) {
    const int bb = misc;
    const int row = bb * 32 + w * 8 + (lane >> 3);
    const int sub = lane & 7;
    u32x4 x[7];
#pragma unroll
    for (int i = 0; i < 7; ++i) x[i] = *(const u32x4*)(q + (size_t)row * D + (i * 8 + sub) * 8);
    if (sub < 7) ((unsigned long long*)(ws + OFF_SG))[sub * S + row] = sub < 3 ? 0ull : ~0ull;
    float s = 0.f, s2 = 0.f;
#pragma unroll
    for (int i = 0; i < 7; ++i)
#pragma unroll
      for (int j = 0; j < 4; ++j) {
        const u32 t = x[i][j];
        s = dot2(t, 0x3f803f80u, s);
        s2 = dot2(t, t, s2);
      }
    s = sum8(s);
    s2 = sum8(s2);
    const float mean = s * (1.f / D);
    float var = s2 * (1.f / D) - mean * mean;
    var = var < 0.f ? 0.f : var;
    const float rstd = __builtin_amdgcn_rsqf(var + 1e-5f);
    if (sub < 2) {
      const int p = (row & ~7) | ((row & 1) << 2) | ((row >> 1) & 3);
      ((float*)(ws + OFF_MEAN))[sub * S + p] = sub ? rstd : mean;
    }
  } else if (misc < 106) {
    const int bb = misc - 64;
    {
      const int xr = lane >> 4, chn = (lane & 15) ^ ((xr << 2) | (w & 3));
      const u16* src = w_mod + (size_t)((chn >> 3) * 224 + 4 * w + xr) * (6 * D) + bb * 64 + (chn & 7) * 8;
#pragma unroll
      for (int i = 0; i < 14; ++i) glds16(src + (size_t)i * 16 * (6 * D), smem + (w + 4 * i) * 1024);
    }
    if (tid < 224) {
      const u32 cv = ((const u32*)c)[tid];
      const float c0 = lo_f(cv), c1 = hi_f(cv);
      *(u32*)(smem + 57344 + tid * 4) =
          pack_bf16(c0 * __builtin_amdgcn_rcpf(1.f + __builtin_amdgcn_exp2f(c0 * -1.44269504f)),
                    c1 * __builtin_amdgcn_rcpf(1.f + __builtin_amdgcn_exp2f(c1 * -1.44269504f)));
    }
    const int h = lane >> 5, blk = (lane >> 4) & 1, qq = (lane & 15) >> 2, p = lane & 3;
    const int col = (w >> 1) * 128 + (w & 1) * 64 + blk * 32 + p * 8;
    int o[2];
#pragma unroll
    for (int t = 0; t < 2; ++t)
      o[t] = 256 * (8 * h + 4 * t + qq) + 16 * ((col >> 4) ^ ((qq << 2) | ((2 * h + t) & 3))) + (col & 8);
    __syncthreads();
    f32x16 acc;
#pragma unroll
    for (int r = 0; r < 16; ++r) acc[r] = 0.f;
#pragma unroll
    for (int i = 0; i < 14; ++i)
      acc = mfma32(*(const u32x4*)(smem + 57344 + (w >> 1) * 448 + i * 32 + h * 16), tr_frag(smem, o[0] + i * 4096, o[1] + i * 4096), acc);
    float* red = (float*)(smem + 57344 + 1024);
    if (h == 0) red[(w >> 1) * 64 + (w & 1) * 32 + lane] = acc[0];
    __syncthreads();
    if (tid < 64) {
      const float t = red[tid] + red[64 + tid];
      const float m = round_bf16(t);
      const int cc = bb * 64 + tid;
      ((float*)(ws + OFF_MOD))[cc] = m;
      if (cc >= 3 * D && cc < 5 * D) {
        const bool isc = cc >= 4 * D;
        ((u16*)(ws + OFF_CB))[isc ? cc - 4 * D : cc - 2 * D] = (u16)pack_bf16(isc ? 1.f + m : m, 0.f);
      }
    }
  }
  asm volatile("" ::"v"(cwarm));
}

constexpr int K1_KSTR = 260 * 16;
constexpr int K1_USTR = 286 * 16;
constexpr int K1_UOFF = 4 * K1_KSTR;
constexpr int K1_XOFF = K1_UOFF + 2 * K1_USTR;
constexpr int K1_CH = K1_XOFF + 32768;

DI void glds16x4_asm(const void* sbase, u32 voff, u32 voff_last, u32 lds_addr) {
  u32 keep;
  sbase = uni(sbase);
  lds_addr = __builtin_amdgcn_readfirstlane(lds_addr);
  asm volatile(
      "s_mov_b32 %0, m0\n\ts_mov_b32 m0, %4\n\ts_nop 0\n\t"
      "global_load_lds_dwordx4 %1, %3\n\t"
      "global_load_lds_dwordx4 %1, %3 offset:1024\n\t"
      "global_load_lds_dwordx4 %1, %3 offset:2048\n\t"
      "global_load_lds_dwordx4 %2, %3 offset:3072\n\t"
      "s_mov_b32 m0, %0"
      : "=&s"(keep)
      : "v"(voff), "v"(voff_last), "s"(sbase), "s"(lds_addr)
      : "memory");
}
#define CAS __attribute__((address_space(4)))
constexpr int K2_XB = D * 256;
constexpr int K2_XH = D * 128;
constexpr int K2_ST = K2_XB;
constexpr int K2_LDS = 131072;

DI u32 umin32(u32 v) { return v < 0xFFFFFFFEu ? v : 0xFFFFFFFEu; }
DI u32 umax3(u32 a, u32 b, u32 c) { const u32 t = a > b ? a : b; return t > c ? t : c; }
template <int OFF>
DI void gload16_off(u32x4& dst, const void* sbase, u32 voff) {
  sbase = uni(sbase);
  asm volatile("global_load_dwordx4 %0, %1, %2 offset:%3" : "=v"(dst) : "v"(voff), "s"(sbase), "n"(OFF) : "memory");
}
DI void glds16_nom0(const void* sbase, u32 voff, u32 lds_addr) {
  sbase = uni(sbase);
  lds_addr = __builtin_amdgcn_readfirstlane(lds_addr);
  asm volatile("s_mov_b32 m0, %2\n\ts_nop 0\n\tglobal_load_lds_dwordx4 %0, %1" ::"v"(voff), "s"(sbase), "s"(lds_addr) : "memory");
}
DI void glds16_sc1(const void* sbase, u32 voff, u32 lds_addr) {
  sbase = uni(sbase);
  lds_addr = __builtin_amdgcn_readfirstlane(lds_addr);
  asm volatile("s_mov_b32 m0, %2\n\ts_nop 0\n\tglobal_load_lds_dwordx4 %0, %1 sc1" ::"v"(voff), "s"(sbase), "s"(lds_addr) : "memory");
}
typedef unsigned long long u64;
typedef __attribute__((address_space(1))) u64 gu64;
#define RLX_AGENT __ATOMIC_RELAXED, __HIP_MEMORY_SCOPE_AGENT
static_assert(4 + C_NW1 <= 72 / C_VSP, "");
constexpr int NWC = C_NW0 + C_NW1;
template <bool WF>
DI void conv_body(const u16* __restrict__ ssm_k, const u16* __restrict__ ssm_D, char* __restrict__ ws, char* smem,
                  const int w, u32x4 (&wf)[28], const char* wbase) {
  const int lane = threadIdx.x & 63;
  const int ch = w >> 2, q = w & 3;
  const int cpair = blockIdx.x;
  const int d = 2 * cpair + ch;
  char* L = smem + ch * K1_CH;
  const u32 L0 = (u32)(size_t)(LAS char*)L;
  const u16* kf = ssm_k + (size_t)d * S;
  const u32 vo = lane * 16;
  {
    const u32 vl = (lane > 62 ? 62 : lane) * 16;
    glds16x4_asm(kf + (2 + 2 * q), vo, vl, L0 + q * K1_KSTR + 64);
    const char* ub = ws + OFF_RO + ((q >> 1) ? 0 : 2) + (size_t)d * S * 2 + (q & 1) * 2048;
    const u32 ul = L0 + K1_UOFF + (q >> 1) * K1_USTR + (q & 1) * 2048;
    glds16_nom0(ub, vo, ul);
    glds16_nom0(ub + 1024, vo, ul + 1024);
  }
  const int n = lane & 15, q4 = lane >> 4;
  const int pbase = 32 * (n >> 1) + 8 * q4 + 4 * (n & 1) + 512 * q;
  const int di = n + q - 15;
  u32 head;
  asm volatile("global_load_dword %0, %1, %2" : "=v"(head) : "v"((u32)(di < 0 ? 0 : di) * 4), "s"(uni(kf)) : "memory");
  auto ldwf = [&](auto kI) {
    constexpr int k = decltype(kI)::value;
    gload16_off<(k % 4) * 1024>(wf[k], wbase + (k / 4) * 4096, vo);
  };
  bar();
  if constexpr (WF) static_for<0, C_NW0>(ldwf);
  const CAS float* MODc = (const CAS float*)(size_t)(ws + OFF_MOD);
  const float sa = MODc[d], ca = MODc[D + d], ga = MODc[2 * D + d], cbv = MODc[4 * D + d];
  const u32 Draw = ((const CAS u32*)(size_t)ssm_D)[cpair];
  if (lane < 14) *(LAS u32x4*)(L + K1_UOFF + (q >> 1) * K1_USTR + (256 + 14 * (q & 1) + lane) * 16) = u32x4{0u, 0u, 0u, 0u};
  u32 Ab = L0 + (n & 3) * K1_KSTR + ((n >> 2) + q4 + 8 * q) * 16;
  u32 Bb = L0 + K1_UOFF + (n & 1) * K1_USTR + (28 - 4 * (n >> 1) + q4 + 8 * q) * 16;
  u32 Hd = L0 + q * K1_KSTR + n * 4;
  asm volatile("" : "+v"(Ab), "+v"(Bb), "+v"(Hd) : "s"(sa), "s"(ca), "s"(ga), "s"(Draw), "s"(cbv));
  asm volatile("s_waitcnt vmcnt(%1)" : "+v"(head) : "n"(WF ? C_NW0 : 0) : "memory");
  *(LAS u32*)(size_t)Hd = di < 0 ? 0u : head;
  if (q == 1 && lane == 0) *(LAS u16*)(L + K1_UOFF + 2047 * 2) = 0;
  wait_lgkm0();
  bar();
  bf16x8 fa[8], fb[8];
  auto rd = [&](int dl) {
#pragma unroll
    for (int g = 0; g < 8; ++g) {
      fa[g] = *(const LAS bf16x8*)(size_t)(Ab + 64 * dl + 512 * g);
      fb[g] = *(const LAS bf16x8*)(size_t)(Bb + 64 * dl + 512 * (7 - g));
    }
  };
  rd(0);
  __builtin_amdgcn_sched_barrier(0);
  u32x2 qv[2];
  f32x4 mu[2], rs[2];
  asm volatile("global_load_dwordx2 %0, %6, %8\n\t"
               "global_load_dwordx2 %1, %6, %8 offset:512\n\t"
               "global_load_dwordx4 %2, %7, %9\n\t"
               "global_load_dwordx4 %3, %7, %9 offset:1024\n\t"
               "global_load_dwordx4 %4, %7, %10\n\t"
               "global_load_dwordx4 %5, %7, %10 offset:1024"
               : "=&v"(qv[0]), "=&v"(qv[1]), "=&v"(mu[0]), "=&v"(mu[1]), "=&v"(rs[0]), "=&v"(rs[1])
               : "v"((u32)pbase * 2), "v"((u32)pbase * 4), "s"(uni(ws + OFF_QT + (size_t)d * S * 2)),
                 "s"(uni(ws + OFF_MEAN)), "s"(uni(ws + OFF_RSTD))
               : "memory");
  const u32x4 sent = {~0u, ~0u, ~0u, ~0u};
  const int fx = blockIdx.x & 7, fi = blockIdx.x >> 3;
  const void* fillb = uni(ws + OFF_ACT + (size_t)((((fx >> 1) * 4 + (fi & 3)) * 4 + (fx & 1) + 2 * (w >> 2)) * 112 + (fi >> 2) * 16 + (w & 3) * 4) * 1024);
  f32x4 acc[8];
#pragma unroll
  for (int j = 0; j < 8; ++j) acc[j] = f32x4{0.f, 0.f, 0.f, 0.f};
  static_for<0, 72>([&](auto mI) {
    constexpr int m = decltype(mI)::value, t = m % 36;
    constexpr int sd = t < 1 ? 0 : t < 3 ? 1 : t < 6 ? 2 : t < 10 ? 3 : t < 15 ? 4 : t < 21 ? 5 : t < 28 ? 6 : 7;
    constexpr int g = t - sd * (sd + 1) / 2;
    if constexpr (m == 36) {
      rd(1);
      __builtin_amdgcn_sched_barrier(0);
    }
    f32x4 a = __builtin_amdgcn_mfma_f32_16x16x32_bf16(fa[g], fb[sd - g], acc[sd], 0, 0, 0);
    asm volatile("" : "+v"(a));
    acc[sd] = a;
    if constexpr (m % C_VSP == C_VSP - 1) {
      constexpr int v = m / C_VSP;
      if constexpr (v < 4) {
        u32x4 sv = sent;
        asm volatile("global_store_dwordx4 %0, %1, %2 offset:%3 sc1\n\ts_nop 1" ::"v"(vo), "v"(sv), "s"(fillb), "n"(v * 1024) : "memory");
      } else if constexpr (WF && v - 4 < C_NW1) {
        ldwf(std::integral_constant<int, C_NW0 + v - 4>{});
      }
      __builtin_amdgcn_sched_barrier(0);
    }
  });
  LAS f32x4* X = (LAS f32x4*)(L + K1_XOFF);
#pragma unroll
  for (int j = 0; j < 8; ++j) X[(j * 4 + q) * 64 + lane] = acc[j];
  u32x4 uc[2];
#pragma unroll
  for (int j = 0; j < 2; ++j)
    uc[j] = *(const LAS u32x4*)(L + K1_UOFF + K1_USTR + (255 - (32 * (2 * q + j) + 4 * (n >> 1) + q4)) * 16);
  asm volatile("s_waitcnt vmcnt(%6)"
               : "+v"(qv[0]), "+v"(qv[1]), "+v"(mu[0]), "+v"(mu[1]), "+v"(rs[0]), "+v"(rs[1])
               : "n"(WF ? C_NW1 : 0)
               : "memory");
  f32x4 qf[2], hg[2], kd[2];
  {
    const int sh = (n & 1) ? 16 : 0;
    const float gc = ga * round_bf16(1.f + ca), gs = ga * sa, Dd = ch ? hi_f(Draw) : lo_f(Draw);
#pragma unroll
    for (int j = 0; j < 2; ++j) {
#pragma unroll
      for (int r = 0; r < 4; ++r) {
        kd[j][r] = __builtin_bit_cast(float, (uc[j][3 - r] << sh) & 0xffff0000u) * Dd;
        qf[j][r] = (r & 1) ? hi_f(qv[j][r >> 1]) : lo_f(qv[j][r >> 1]);
        hg[j][r] = (qf[j][r] - mu[j][r]) * rs[j][r] * gc + gs;
      }
      asm volatile("" : "+v"(kd[j]), "+v"(qf[j]), "+v"(hg[j]));
    }
  }
  wait_lgkm0();
  bar();
  u16* X1T = (u16*)(ws + OFF_X1T) + (size_t)d * S + pbase;
  u16* XS = (u16*)(ws + OFF_XS) + (size_t)d * S + pbase;
  const float c1 = round_bf16(1.f + cbv);
#pragma unroll
  for (int j = 0; j < 2; ++j) {
    const LAS f32x4* Xj = X + ((2 * q + j) * 4) * 64 + lane;
    const f32x4 y = (Xj[0] + Xj[64]) + (Xj[128] + Xj[192]);
    const f32x4 o = qf[j] + hg[j] * (y + kd[j]);
    const u32 p0 = umin32(pack_bf16(o[0], o[1])), p1 = umin32(pack_bf16(o[2], o[3]));
    st8_sc1(XS + 256 * j,
            u32x2{umin32(pack_bf16(lo_f(p0) * c1, hi_f(p0) * c1)), umin32(pack_bf16(lo_f(p1) * c1, hi_f(p1) * c1))});
    st8_sc1(X1T + 256 * j, u32x2{p0, p1});
  }
}

DI void k2_stats(char* __restrict__ ws, char* smem, const int sblk) {
  const int tid = threadIdx.x, lane = tid & 63;
  const int w = __builtin_amdgcn_readfirstlane(tid >> 6);
  const int n = lane & 31, h = lane >> 5;
  {
    const int p0 = sblk * 64;
    if (w < 7) {
      const int task = sblk * 7 + w, t = task >> 1, kh = task & 1;
      const u32x4* wsrc = (const u32x4*)(ws + OFF_WGU) + ((size_t)t * 28 + kh * 14) * 64 + lane;
      const u16* cbp = (const u16*)(ws + OFF_CB) + (n & 1) * D + kh * 224 + 8 * h;
      u32x4 fa[14], fb[14];
#pragma unroll
      for (int i = 0; i < 14; ++i) {
        fa[i] = wsrc[i * 64];
        fb[i] = *(const u32x4*)(cbp + 16 * i);
      }
      __builtin_amdgcn_sched_barrier(0);
      f32x16 va;
#pragma unroll
      for (int r = 0; r < 16; ++r) va[r] = 0.f;
#pragma unroll
      for (int i = 0; i < 14; ++i) va = mfma32(fb[i], __builtin_bit_cast(bf16x8, fa[i]), va);
      if (h == 0) {
        const float f0 = va[0], f1 = va[1];
        const u32 a0 = umin32(__builtin_bit_cast(u32, f0)), a1 = __builtin_bit_cast(u32, f1);
        __hip_atomic_store((gu64*)(size_t)(ws + OFF_VG) + kh * 3584 + t * 32 + n, ((u64)a1 << 32) | a0, RLX_AGENT);
      }
    }
    __syncthreads();
    __builtin_amdgcn_s_sleep(C_SSLEEP);
    const rsrc_t rx = mk_rsrc(ws + OFF_X1T + ((size_t)(w * 56) * S + p0) * 2, 56 * S * 2);
    const int c8 = lane & 7, r8 = lane >> 3;
    u32x4 x[7];
    for (int spin = 0; spin < (1 << 15); ++spin) {
      asm volatile("" ::: "memory");
      u32 m = 0;
#pragma unroll
      for (int i = 0; i < 7; ++i) x[i] = __builtin_amdgcn_raw_buffer_load_b128(rx, (r8 + 8 * i) * (S * 2) + c8 * 16, 0, 16);
#pragma unroll
      for (int i = 0; i < 7; ++i) m = umax3(umax3(m, x[i][0], x[i][1]), x[i][2], x[i][3]);
      if (__all(m != ~0u)) break;
      __builtin_amdgcn_s_sleep(1);
    }
    float sa[8], qa[8];
#pragma unroll
    for (int e = 0; e < 8; ++e) sa[e] = qa[e] = 0.f;
#pragma unroll
    for (int i = 0; i < 7; ++i)
#pragma unroll
      for (int e = 0; e < 8; ++e) {
        const float a = (e & 1) ? hi_f(x[i][e >> 1]) : lo_f(x[i][e >> 1]);
        sa[e] += a;
        qa[e] = __builtin_fmaf(a, a, qa[e]);
      }
    LAS float* P = (LAS float*)smem;
    {
      LAS float* pp = P + (w * 8 + r8) * 128 + c8 * 8;
      *(LAS f32x4*)pp = f32x4{sa[0], sa[1], sa[2], sa[3]};
      *(LAS f32x4*)(pp + 4) = f32x4{sa[4], sa[5], sa[6], sa[7]};
      *(LAS f32x4*)(pp + 64) = f32x4{qa[0], qa[1], qa[2], qa[3]};
      *(LAS f32x4*)(pp + 68) = f32x4{qa[4], qa[5], qa[6], qa[7]};
    }
    wait_lgkm0();
    {
      float s1 = 0.f, q1 = 0.f;
#pragma unroll
      for (int i = 0; i < 8; ++i) {
        s1 += P[(w * 8 + i) * 128 + lane];
        q1 += P[(w * 8 + i) * 128 + 64 + lane];
      }
      *(LAS f32x2*)(P + 8192 + (w * 64 + lane) * 2) = f32x2{s1, q1};
    }
    __syncthreads();
    if (tid < 64) {
      float sm = 0.f, sq = 0.f;
#pragma unroll
      for (int i = 0; i < 8; ++i) {
        const f32x2 t = *(const LAS f32x2*)(P + 8192 + (i * 64 + tid) * 2);
        sm += t[0];
        sq += t[1];
      }
      const float mean = sm * (1.f / D);
      float var = sq * (1.f / D) - mean * mean;
      var = (var < 0.f ? 0.f : var) + 1e-5f;
      const float rstd = __builtin_amdgcn_rsqf(var);
      const float sig = var * rstd;
      const float mh = round_bf16(mean), sh = round_bf16(sig);
      const u32 b0 = pack_bf16(-mh, mh - mean), b1 = pack_bf16(-mh, sh), b2 = pack_bf16(sig - sh, sh);
      const float c1 = -2.302208198f * rstd;
      const float c2 = -0.1029432397f * (rstd * rstd * rstd);
      gu64* G = (gu64*)(size_t)(ws + OFF_SG) + p0 + tid;
      __hip_atomic_store(G, ((u64)b1 << 32) | b0, RLX_AGENT);
      __hip_atomic_store(G + S, ((u64)__builtin_bit_cast(u32, var) << 32) | b2, RLX_AGENT);
      __hip_atomic_store(G + 2 * S, ((u64)__builtin_bit_cast(u32, c1) << 32) | __builtin_bit_cast(u32, c2), RLX_AGENT);
    }
  }
}

DI void k2_gemm(const u16* __restrict__ ssm_k, const u16* __restrict__ ssm_D, char* __restrict__ ws, u16* __restrict__ out,
                char* smem, const int w) {
  unsigned long long T[6];
#define TS(i) asm volatile("s_memrealtime %0\n\ts_waitcnt lgkmcnt(0)" : "=s"(T[i])::"memory")
  TS(1);
  const int tid = threadIdx.x, lane = tid & 63;
  const int n = lane & 31, h = lane >> 5;
  const int xcd = blockIdx.x & 7, idx = blockIdx.x >> 3;
  const int mt = (xcd >> 1) * 4 + (idx & 3);
  const int dt = idx >> 2, sh2 = xcd & 1;
  const int ft = sh2 * 7 + dt;
  const int tile = ft * 8 + w;
  const u32 lds0 = (u32)(size_t)(LAS char*)smem;
  u32x4 wf[28], wd[28];
  const char* wbase = ws + OFF_WGU + (size_t)(tile * 28) * 1024;
  const u32 wa = lane * 16;
  auto ldwf = [&](auto kI) {
    constexpr int k = decltype(kI)::value;
    gload16_off<(k % 4) * 1024>(wf[k], wbase + (k / 4) * 4096, wa);
  };
  conv_body<true>(ssm_k, ssm_D, ws, smem, w, wf, wbase);
  TS(2);
  const char* xbase = ws + OFF_XS + ((size_t)(8 * w) * S + mt * 128) * 2;
  const u32 xoff = (u32)((lane >> 3) * S + ((lane & 7) ^ (((lane >> 4) & 1) << 2)) * 8) * 2;
  const char* wdbase = ws + OFF_WD + ((size_t)(2 * dt) * 112 + 14 * w) * 1024 + 4096;
  const int gsl = w < 6 ? w : w - 6;
  const char* gbase = ws + OFF_SG + (size_t)((gsl >> 1) * S + mt * 128 + (gsl & 1) * 64) * 8;
  auto ldwd = [&](auto kI) {
    constexpr int q = decltype(kI)::value, k = (q & 1) * 14 + (q >> 1), kk = k % 14;
    gload16_off<(kk % 8) * 1024 - 4096>(wd[k], wdbase + (k / 14) * 112 * 1024 + (kk / 8) * 8192, wa);
  };
  auto ldx = [&](auto cI) {
    constexpr int c = decltype(cI)::value;
    glds16_sc1(xbase + (size_t)(c % 7) * (64 * S * 2) + (c / 7) * 128, xoff, lds0 + (64 * c + 8 * w) * 128);
  };
  auto xok = [&](auto c0I, auto c1I) {
    u32 m = 0;
#pragma unroll
    for (int c = decltype(c0I)::value; c < decltype(c1I)::value; ++c) {
      const u32x4 t = *(const LAS u32x4*)(smem + (64 * c + 8 * w) * 128 + lane * 16);
      m = umax3(umax3(m, t[0], t[1]), t[2], t[3]);
    }
    return (bool)__all(m != ~0u);
  };
  auto xvalid = [&](auto c0I, auto c1I) {
    if (__builtin_expect(!xok(c0I, c1I), 0)) {
#pragma nounroll
      for (int spin = 0; spin < (1 << 14); ++spin) {
        __builtin_amdgcn_s_sleep(4);
        static_for<decltype(c0I)::value, decltype(c1I)::value>(ldx);
        wait_vm<0>();
        if (xok(c0I, c1I)) break;
      }
    }
    __builtin_amdgcn_sched_barrier(0);
  };
  wait_lgkm0();
  bar();
  auto pin = [](f32x16& a) { asm volatile("" : "+v"(a)); };
  auto pinw = [&](auto kI) {
    u32x4 t = wf[decltype(kI)::value];
    asm volatile("" : "+v"(t)::"memory");
    wf[decltype(kI)::value] = t;
  };
  u32x4 aext;
  constexpr int NWH = NWC + (C_NWH - NWC);
  constexpr int NWP = NWC + C_NWP;
  static_for<NWC, NWP>(ldwf);
  static_for<0, 7>(ldx);
  static_for<NWP, NWH>(ldwf);
  int xo[2][2];
  {
    const int blk = (lane >> 4) & 1, qq = (lane & 15) >> 2, p = lane & 3;
#pragma unroll
    for (int i = 0; i < 2; ++i)
#pragma unroll
      for (int t = 0; t < 2; ++t)
        xo[i][t] = 128 * (8 * h + 4 * t + qq) + 16 * ((4 * i + 2 * blk + (p >> 1)) ^ ((qq >> 1) << 2)) + 8 * (p & 1);
  }
  f32x16 acc[4];
#pragma unroll
  for (int i = 0; i < 4; ++i)
#pragma unroll
    for (int r = 0; r < 16; ++r) acc[i][r] = 0.f;
  asm volatile("" : "+v"(xo[0][0]), "+v"(xo[0][1]), "+v"(xo[1][0]), "+v"(xo[1][1]));
  constexpr int PF = 3;
  bf16x8 xr[PF][2];
  auto rdx = [&](auto kI, auto iI) {
    constexpr int k = decltype(kI)::value, i = decltype(iI)::value;
    if constexpr (k == 28) {
      int a0 = xo[i][0] + K2_XH, a1 = xo[i][1] + K2_XH;
      asm volatile("" : "+v"(a0), "+v"(a1));
      xo[i][0] = a0;
      xo[i][1] = a1;
    }
    xr[k % PF][i] = tr_frag(smem, xo[i][0] + (k % 28) * 2048, xo[i][1] + (k % 28) * 2048);
  };
  using I0 = std::integral_constant<int, 0>;
  using I1 = std::integral_constant<int, 1>;
  using I2 = std::integral_constant<int, 2>;
  using I3 = std::integral_constant<int, 3>;
  using I7 = std::integral_constant<int, 7>;
  using I14 = std::integral_constant<int, 14>;
  LAS u64* CS = (LAS u64*)(smem + K2_ST);
  wait_vm<4 + NWH - NWP>();
  static_for<0, NWP>(pinw);
  xvalid(I0{}, I3{});
  bar();
  static_for<0, PF - 1>([&](auto jI) {
    rdx(jI, I0{});
    rdx(jI, I1{});
  });
  __builtin_amdgcn_sched_barrier(0);
  TS(3);
  u64 gv, gp0, gp1;
  const char* vbase = ws + OFF_VG + (size_t)tile * 256;
  constexpr int RW = 28 - NWH, KX = RW + 7, KV = 25;
  constexpr int KF = C_KF;
  constexpr int WE = 27 - KX;
  static_assert(KF >= KX && KF <= 27, "");
  static_assert(KX + 4 <= KV && WE <= 9, "stream does not fit");
  static_for<0, 28>([&](auto kI) {
    constexpr int k = decltype(kI)::value;
    if constexpr (k == 6) {
      wait_vm<6>();
      static_for<NWP, NWH>(pinw);
      xvalid(I3{}, I7{});
      bar();
    }
    if constexpr (k >= NWH) {
      u32x4 t = wf[k];
      asm volatile("s_waitcnt vmcnt(%1)" : "+v"(t) : "n"(NWH - 1 + (k > KF ? 2 : 0)) : "memory");
      wf[k] = t;
    }
    if constexpr (k == KV) {
      wait_vm<KV - KX + (KV > KF ? 2 : 0)>();
      xvalid(I7{}, I14{});
      bar();
    }
    __builtin_amdgcn_sched_barrier(0);
    acc[0] = mfma32(wf[k], xr[k % PF][0], acc[0]);
    pin(acc[0]);
    __builtin_amdgcn_sched_barrier(0);
    rdx(std::integral_constant<int, k + PF - 1>{}, I0{});
    __builtin_amdgcn_sched_barrier(0);
    acc[1] = mfma32(wf[k], xr[k % PF][1], acc[1]);
    pin(acc[1]);
    __builtin_amdgcn_sched_barrier(0);
    rdx(std::integral_constant<int, k + PF - 1>{}, I1{});
    if constexpr (k < RW) ldwf(std::integral_constant<int, NWH + k>{});
    else if constexpr (k < KX) ldx(std::integral_constant<int, 7 + k - RW>{});
    else if constexpr (k != KF) ldwd(std::integral_constant<int, k - KX - (k > KF ? 1 : 0)>{});
    else {
      u64 t, t0, t1;
      asm volatile("global_load_dwordx2 %0, %1, %2 sc1" : "=v"(t) : "v"((u32)lane * 8), "s"(uni(gbase)) : "memory");
      asm volatile("global_load_dwordx2 %0, %1, %2 sc1" : "=v"(t0) : "v"((u32)n * 8), "s"(uni(vbase)) : "memory");
      asm volatile("global_load_dwordx2 %0, %1, %2 sc1" : "=v"(t1) : "v"((u32)n * 8), "s"(uni(vbase + 3584 * 8)) : "memory");
      gv = t;
      gp0 = t0;
      gp1 = t1;
    }
    __builtin_amdgcn_sched_barrier(0);
  });
  const rsrc_t rw = mk_rsrc(ws + OFF_ACT + ((size_t)(mt * 4) * 112 + tile) * 1024, 337 * 1024);
  const rsrc_t ra = mk_rsrc(ws + OFF_ACT + ((size_t)(mt * 4 + sh2) * 112 + 14 * w) * 1024, 238 * 1024);
  u32x4 af[28];
  auto ldact = [&](auto kI) {
    constexpr int k = decltype(kI)::value;
    af[k] = __builtin_amdgcn_raw_buffer_load_b128(ra, lane * 16 + (k % 14) * 1024, (k / 14) * 224 * 1024, 16);
  };
  float c1[2], c2[2], var[2], pe[2], pm[2], pr[2];
  u32x4 ov[2];
  auto corr = [&](auto iI) {
    constexpr int i = decltype(iI)::value;
    u64 g[3];
#pragma unroll
    for (int k = 0; k < 3; ++k) g[k] = CS[k * 128 + i * 32 + n];
    const u32x4 bext = {(u32)g[0], (u32)(g[0] >> 32), (u32)g[1], 0u};
    acc[i] = mfma32(aext, __builtin_bit_cast(bf16x8, bext), acc[i]);
    c1[i & 1] = __builtin_bit_cast(float, (u32)(g[2] >> 32));
    c2[i & 1] = __builtin_bit_cast(float, (u32)g[2]);
    var[i & 1] = __builtin_bit_cast(float, (u32)(g[1] >> 32));
  };
  auto slice = [&](auto iI, auto uI) {
    constexpr int i = decltype(iI)::value, u = decltype(uI)::value, z = i & 1;
    if constexpr (u >= 1) {
      float o = pm[z] * __builtin_amdgcn_rcpf(__builtin_fmaf(pe[z], var[z], var[z]));
      if constexpr ((u - 1) & 1) {
        u32 t = umin32(pack_bf16(pr[z], o));
        asm volatile("" : "+v"(t));
        ov[z][(u - 1) / 2] = t;
      } else {
        asm volatile("" : "+v"(o));
        pr[z] = o;
      }
    }
    if constexpr (u < 8) {
      const float a = acc[i][u];
      float e = __builtin_amdgcn_exp2f(__builtin_fmaf(a * a, c2[z], c1[z]) * a), m = a * acc[i][u + 8];
      asm volatile("" : "+v"(e), "+v"(m));
      pe[z] = e;
      pm[z] = m;
    } else {
      __builtin_amdgcn_raw_buffer_store_b128(ov[z], rw, lane * 16 + i * 112 * 1024, 0, 16);
      asm volatile("s_nop 1");
    }
  };
  constexpr int KG = C_KG;
  constexpr auto slq = [](int g) { return g <= 0 ? 0 : (g >= C_SLN ? 18 : (g * 18) / C_SLN); };
  auto slice01 = [&](auto jI) {
    constexpr int j = decltype(jI)::value;
    slice(std::integral_constant<int, j & 1>{}, std::integral_constant<int, j / 2>{});
  };
  static_for<0, 28>([&](auto kI) {
    constexpr int k = decltype(kI)::value;
    if constexpr (k == KG) {
      u64 t = gv, p0 = gp0, p1 = gp1;
      asm volatile("s_waitcnt vmcnt(%3)" : "+v"(t), "+v"(p0), "+v"(p1) : "n"(KG + 27 - KF) : "memory");
      if (!__all((t >> 32) != 0 && (u32)p0 != ~0u && (u32)p1 != ~0u)) {
        for (int spin = 0; spin < (1 << 16); ++spin) {
          __builtin_amdgcn_s_sleep(2);
          t = __hip_atomic_load((gu64*)(size_t)gbase + lane, RLX_AGENT);
          p0 = __hip_atomic_load((gu64*)(size_t)vbase + n, RLX_AGENT);
          p1 = __hip_atomic_load((gu64*)(size_t)vbase + 3584 + n, RLX_AGENT);
          if (__all((t >> 32) != 0 && (u32)p0 != ~0u && (u32)p1 != ~0u)) break;
        }
        __builtin_amdgcn_s_waitcnt(0x0F70);
      }
      CS[gsl * 64 + lane] = t;
      const float v1 = __builtin_bit_cast(float, (u32)p0) + __builtin_bit_cast(float, (u32)p1);
      const float v2 = __builtin_bit_cast(float, (u32)(p0 >> 32)) + __builtin_bit_cast(float, (u32)(p1 >> 32));
      const float v1h = round_bf16(v1), v2h = round_bf16(v2);
      u32x4 a = {pack_bf16(v1h, v1h), pack_bf16(v1 - v1h, v2h), pack_bf16(v2h, v2 - v2h), 0u};
      if (h) a = u32x4{0u, 0u, 0u, 0u};
      asm volatile("" : "+v"(a));
      aext = a;
      wait_lgkm0();
      __builtin_amdgcn_sched_barrier(0);
      bar();
      corr(I0{});
      corr(I1{});
      __builtin_amdgcn_sched_barrier(0);
    }
    acc[2] = mfma32(wf[k], xr[(28 + k) % PF][0], acc[2]);
    pin(acc[2]);
    __builtin_amdgcn_sched_barrier(0);
    if constexpr (k + PF - 1 < 28) rdx(std::integral_constant<int, 28 + k + PF - 1>{}, I0{});
    static_for<slq(2 * (k - KG - 1)), slq(2 * (k - KG - 1) + 1)>(slice01);
    __builtin_amdgcn_sched_barrier(0);
    acc[3] = mfma32(wf[k], xr[(28 + k) % PF][1], acc[3]);
    pin(acc[3]);
    __builtin_amdgcn_sched_barrier(0);
    if constexpr (k % C_LOCK == C_LOCK - 1) bar();
    if constexpr (k + PF - 1 < 28) rdx(std::integral_constant<int, 28 + k + PF - 1>{}, I1{});
    static_for<slq(2 * (k - KG - 1) + 1), slq(2 * (k - KG - 1) + 2)>(slice01);
    if constexpr (WE + k < 28) ldwd(std::integral_constant<int, WE + k>{});
    else if constexpr (WE + k - 28 < 7) ldact(std::integral_constant<int, WE + k - 28>{});
    __builtin_amdgcn_sched_barrier(0);
  });
  constexpr int A0 = WE < 7 ? WE : 7;
  corr(I2{});
  corr(I3{});
  __builtin_amdgcn_sched_barrier(0);
  static_for<0, 9>([&](auto uI) {
    constexpr int u = decltype(uI)::value;
    slice(I2{}, uI);
    slice(I3{}, uI);
    if constexpr (A0 + u < 14 && u < 8) ldact(std::integral_constant<int, A0 + u>{});
    __builtin_amdgcn_sched_barrier(0);
  });
  static_for<(A0 + 8 < 14 ? A0 + 8 : 14), 14>(ldact);
  bar();
  TS(4);
  auto chk = [&](auto gI) {
    constexpr int g = decltype(gI)::value;
    u32 m = 0;
#pragma unroll
    for (int k = 7 * g; k < 7 * g + 7; ++k) m = umax3(umax3(m, af[k][0], af[k][1]), af[k][2], af[k][3]);
    return (bool)__all(m != ~0u);
  };
  auto valid = [&](auto gI) {
    constexpr int g = decltype(gI)::value;
    if (__builtin_expect(!chk(gI), 0)) {
#pragma nounroll
      for (int spin = 0; spin < (1 << 14); ++spin) {
        __builtin_amdgcn_s_sleep(8);
        asm volatile("" ::: "memory");
        static_for<7 * g, 7 * g + 7>(ldact);
        if (chk(gI)) break;
      }
      __builtin_amdgcn_s_waitcnt(0x0F70);
    }
    __builtin_amdgcn_sched_barrier(0);
  };
  const int pcol = mt * 128 + sh2 * 32 + n;
  const int dbase = dt * 64 + w * 8 + 4 * h;
  __builtin_amdgcn_sched_barrier(0);
  LAS f32x4* R = (LAS f32x4*)smem;
  f32x16 o[4];
#pragma unroll
  for (int i = 0; i < 4; ++i)
#pragma unroll
    for (int r = 0; r < 16; ++r) o[i][r] = 0.f;
  auto dump1 = [&](auto tjI, auto cI) {
    constexpr int tj = decltype(tjI)::value, c = decltype(cI)::value, i = tj * 2 + (c >> 2), q = c & 3;
    R[((tj * 8 + c) * 8 + w) * 64 + lane] = f32x4{o[i][4 * q], o[i][4 * q + 1], o[i][4 * q + 2], o[i][4 * q + 3]};
  };
  auto step = [&](auto kI) {
    constexpr int k = decltype(kI)::value, tj = k / 14;
    const bf16x8 b = __builtin_bit_cast(bf16x8, af[k]);
    o[2 * tj] = mfma32(wd[k % 14], b, o[2 * tj]);
    pin(o[2 * tj]);
    __builtin_amdgcn_sched_barrier(0);
    if constexpr (k >= 15 && k < 19) dump1(I0{}, std::integral_constant<int, 2 * (k - 15)>{});
    __builtin_amdgcn_sched_barrier(0);
    o[2 * tj + 1] = mfma32(wd[14 + k % 14], b, o[2 * tj + 1]);
    pin(o[2 * tj + 1]);
    __builtin_amdgcn_sched_barrier(0);
    if constexpr (k >= 15 && k < 19) dump1(I0{}, std::integral_constant<int, 2 * (k - 15) + 1>{});
    if constexpr (k >= C_T1 && k < 14) {
      constexpr int n0 = ((k - C_T1) * 14) / (14 - C_T1), n1 = ((k + 1 - C_T1) * 14) / (14 - C_T1);
      static_for<14 + n0, 14 + n1>(ldact);
    }
    __builtin_amdgcn_sched_barrier(0);
  };
  u16 xres[2][4];
  f32x4 gbv;
  auto fin = [&](auto tjI) {
    constexpr int tj = decltype(tjI)::value;
    f32x4 v[8];
#pragma unroll
    for (int sw = 0; sw < 8; ++sw) v[sw] = R[((tj * 8 + w) * 8 + sw) * 64 + lane];
    const f32x4 sum = ((v[0] + v[1]) + (v[2] + v[3])) + ((v[4] + v[5]) + (v[6] + v[7]));
    float r[4];
#pragma unroll
    for (int e = 0; e < 4; ++e) r[e] = __builtin_fmaf(gbv[e], sum[e], bf2f(xres[tj][e]));
    const int pc = pcol + 64 * tj;
    const int srow = (pc & ~7) | ((pc & 3) << 1) | ((pc >> 2) & 1);
    *(u32x2*)(out + (size_t)srow * D + dbase) = u32x2{pack_bf16(r[0], r[1]), pack_bf16(r[2], r[3])};
  };
  valid(I0{});
  asm volatile(""
               : "+v"(wd[0]), "+v"(wd[1]), "+v"(wd[2]), "+v"(wd[3]), "+v"(wd[4]), "+v"(wd[5]), "+v"(wd[6]), "+v"(wd[7]),
                 "+v"(wd[8]), "+v"(wd[9]), "+v"(wd[10]), "+v"(wd[11]), "+v"(wd[12]), "+v"(wd[13])::"memory");
  asm volatile(""
               : "+v"(wd[14]), "+v"(wd[15]), "+v"(wd[16]), "+v"(wd[17]), "+v"(wd[18]), "+v"(wd[19]), "+v"(wd[20]),
                 "+v"(wd[21]), "+v"(wd[22]), "+v"(wd[23]), "+v"(wd[24]), "+v"(wd[25]), "+v"(wd[26]), "+v"(wd[27])::"memory");
  static_for<0, 3>(step);
  {
    const rsrc_t rx = mk_rsrc(ws + OFF_X1T, D * S * 2);
#pragma unroll
    for (int r = 0; r < 8; ++r)
      xres[r >> 2][r & 3] = __builtin_amdgcn_raw_buffer_load_b16(rx, ((dbase + (r & 3)) * S + pcol + (r >> 2) * 64) * 2, 0, 16);
    gbv = *(const f32x4*)((const float*)(ws + OFF_MOD) + 5 * D + dbase);
    __builtin_amdgcn_sched_barrier(0);
  }
  static_for<3, 7>(step);
  valid(I1{});
  static_for<7, 14>(step);
  valid(I2{});
  static_for<14, 21>(step);
  wait_lgkm0();
  bar();
  valid(I3{});
  static_for<21, 28>([&](auto kI) {
    constexpr int k = decltype(kI)::value;
    o[2] = mfma32(wd[k % 14], __builtin_bit_cast(bf16x8, af[k]), o[2]);
    pin(o[2]);
    __builtin_amdgcn_sched_barrier(0);
    if constexpr (k == 23) {
      fin(I0{});
      __builtin_amdgcn_sched_barrier(0);
    }
  });
  static_for<21, 28>([&](auto kI) {
    constexpr int k = decltype(kI)::value;
    o[3] = mfma32(wd[14 + k % 14], __builtin_bit_cast(bf16x8, af[k]), o[3]);
    pin(o[3]);
    __builtin_amdgcn_sched_barrier(0);
    if constexpr (k >= 22 && k < 26) dump1(I1{}, std::integral_constant<int, k - 22>{});
    __builtin_amdgcn_sched_barrier(0);
  });
  static_for<4, 8>([&](auto cI) { dump1(I1{}, cI); });
  wait_lgkm0();
  bar();
  fin(I1{});
  TS(5);
  if (blockIdx.x == 0 && threadIdx.x == 0) {
    T[0] = *(volatile unsigned long long*)(ws + WS_END);
    unsigned long long d = 0 * (T[1] - T[0]);
    if (d > 20000) d = 20000;
    const unsigned long long now = __builtin_amdgcn_s_memrealtime();
    while (__builtin_amdgcn_s_memrealtime() - now < d) __builtin_amdgcn_s_sleep(1);
  }
}

extern "C" __global__ NOPK __launch_bounds__(512, 2) void k1_main(const u16* __restrict__ ssm_k,
                                                                  const u16* __restrict__ ssm_D, char* __restrict__ ws,
                                                                  u16* __restrict__ out) {
  extern __shared__ __attribute__((aligned(16))) char smem[];
  const int w = __builtin_amdgcn_readfirstlane(threadIdx.x >> 6);
  if (blockIdx.x >= 224) {
    k2_stats(ws, smem, blockIdx.x - 224);
  } else {
    k2_gemm(ssm_k, ssm_D, ws, out, smem, w);
  }
}

extern "C" __device__ __attribute__((used, noinline)) void k_code_end() {}

extern "C" void kernel_launch(void* const* d_in, const int* in_sizes, int n_in, void* d_out, int out_size, void* d_ws,
                              size_t ws_size, hipStream_t stream) {
  const u16* q = (const u16*)d_in[0];
  const u16* k = (const u16*)d_in[1];
  const u16* v = (const u16*)d_in[2];
  const u16* shift_k = (const u16*)d_in[3];
  const u16* ssm_k = (const u16*)d_in[4];
  const u16* ssm_D = (const u16*)d_in[5];
  const u16* w_gate = (const u16*)d_in[7];
  const u16* w_up = (const u16*)d_in[8];
  const u16* w_down = (const u16*)d_in[9];
  const u16* c = (const u16*)d_in[10];
  const u16* w_mod = (const u16*)d_in[11];
  char* ws = (char*)d_ws;
  (void)hipFuncSetAttribute((const void*)k1_main, hipFuncAttributeMaxDynamicSharedMemorySize, K2_LDS);
  k0_prep<<<K0_GRID, 256, 0, stream>>>(q, k, v, shift_k, c, w_mod, w_gate, w_up, w_down, ws);
  k1_main<<<256, 512, K2_LDS, stream>>>(ssm_k, ssm_D, ws, (u16*)d_out);
}
```

```cpp
#include <hip/hip_runtime.h>
#include <hip/hip_bf16.h>
#include <stdint.h>
#include <type_traits>

typedef unsigned short u16;
typedef unsigned int u32;
typedef short bf16x8 __attribute__((ext_vector_type(8)));
typedef short s16x4 __attribute__((ext_vector_type(4)));
typedef float f32x2 __attribute__((ext_vector_type(2)));
typedef float f32x4 __attribute__((ext_vector_type(4)));
typedef float f32x16 __attribute__((ext_vector_type(16)));
typedef u32 u32x2 __attribute__((ext_vector_type(2)));
typedef u32 u32x4 __attribute__((ext_vector_type(4)));
typedef __bf16 bf16x2_t __attribute__((ext_vector_type(2)));
#define LAS __attribute__((address_space(3)))
#define DI __device__ __forceinline__

constexpr int S = 2048, D = 448, F = 1792;

constexpr size_t SZ_DS = (size_t)D * S * 2;
constexpr size_t OFF_RO = SZ_DS;
constexpr size_t OFF_QT = OFF_RO + SZ_DS;
constexpr size_t OFF_X1T = OFF_QT + SZ_DS;
constexpr size_t OFF_MEAN = OFF_X1T + SZ_DS;
constexpr size_t OFF_RSTD = OFF_MEAN + S * 4;
constexpr size_t OFF_MOD = OFF_RSTD + S * 4;
constexpr size_t OFF_SG = OFF_MOD + 16384;
constexpr size_t OFF_CB = OFF_MOD + 12288;

constexpr size_t OFF_VG = OFF_SG + 3 * S * 8;
constexpr size_t OFF_XS = OFF_VG + 4 * S * 8;
constexpr size_t OFF_WGU = OFF_XS + SZ_DS;
constexpr size_t OFF_WD = OFF_WGU + (size_t)2 * D * F * 2;
constexpr size_t OFF_ACT = OFF_WD + (size_t)D * F * 2;

#if defined(__HIP_DEVICE_COMPILE__)
#define NOPK __attribute__((target("no-packed-fp32-ops")))
#else
#define NOPK
#endif

constexpr int C_KF = 25;
constexpr int C_KG = 1;
constexpr int C_LOCK = 4;
constexpr int C_NW0 = 5;
constexpr int C_NW1 = 5;
constexpr int C_NWH = 15;
constexpr int C_NWP = 3;
constexpr int C_SLN = 18;
constexpr int C_SSLEEP = 50;
constexpr int C_T1 = 5;
constexpr int C_VSP = 4;

template <int I, int N, class Fn>
DI void static_for(Fn&& f) {
  if constexpr (I < N) {
    f(std::integral_constant<int, I>{});
    static_for<I + 1, N>(f);
  }
}

DI float lo_f(u32 x) { return __builtin_bit_cast(float, x << 16); }
DI float hi_f(u32 x) { return __builtin_bit_cast(float, x & 0xffff0000u); }
DI u32 pack_bf16(float a, float b) {
  f32x2 v = {a, b};
  return __builtin_bit_cast(u32, __builtin_convertvector(v, bf16x2_t));
}
DI float round_bf16(float a) { return lo_f(pack_bf16(a, 0.f)); }
DI float bf2f(u16 x) { return __builtin_bit_cast(float, (u32)x << 16); }

template <int N>
DI void wait_vm() { asm volatile("s_waitcnt vmcnt(%0)" ::"n"(N) : "memory"); }
DI void wait_lgkm0() { asm volatile("s_waitcnt lgkmcnt(0)" ::: "memory"); }
DI void bar() { __builtin_amdgcn_s_barrier(); }

DI const void* uni(const void* p) {
  const unsigned long long v = (unsigned long long)p;
  const u32 lo = __builtin_amdgcn_readfirstlane((u32)v), hi = __builtin_amdgcn_readfirstlane((u32)(v >> 32));
  return (const void*)(((unsigned long long)hi << 32) | lo);
}
DI void glds16(const void* g, char* lds) {
  __builtin_amdgcn_global_load_lds((const __attribute__((address_space(1))) void*)g, (LAS void*)lds, 16, 0, 0);
}

template <int CTRL>
DI float dpp_add(float v) {
  const int x = __builtin_bit_cast(int, v);
  return v + __builtin_bit_cast(float, __builtin_amdgcn_update_dpp(x, x, CTRL, 0xf, 0xf, false));
}
DI float sum8(float v) {
  v = dpp_add<0xB1>(v);
  v = dpp_add<0x4E>(v);
  v = dpp_add<0x141>(v);
  return v;
}
DI float dot2(u32 a, u32 b, float c) {
  return __builtin_amdgcn_fdot2_f32_bf16(__builtin_bit_cast(bf16x2_t, a), __builtin_bit_cast(bf16x2_t, b), c, false);
}
typedef __amdgpu_buffer_rsrc_t rsrc_t;
DI rsrc_t mk_rsrc(const void* p, u32 bytes) { return __builtin_amdgcn_make_buffer_rsrc((void*)p, 0, bytes, 0x00020000); }
DI void st16_sc1(void* p, u32x4 v) { asm volatile("global_store_dwordx4 %0, %1, off sc1\n\ts_nop 1" ::"v"(p), "v"(v) : "memory"); }
DI void st8_sc1(void* p, u32x2 v) { asm volatile("global_store_dwordx2 %0, %1, off sc1" ::"v"(p), "v"(v) : "memory"); }

DI bf16x8 tr_frag(const char* smem, int o0, int o1) {
  s16x4 x0 = __builtin_amdgcn_ds_read_tr16_b64_v4i16((LAS s16x4*)(smem + o0));
  s16x4 x1 = __builtin_amdgcn_ds_read_tr16_b64_v4i16((LAS s16x4*)(smem + o1));
  return __builtin_shufflevector(x0, x1, 0, 1, 2, 3, 4, 5, 6, 7);
}
DI f32x16 mfma32(u32x4 a, bf16x8 b, f32x16 c) {
  return __builtin_amdgcn_mfma_f32_32x32x16_bf16(__builtin_bit_cast(bf16x8, a), b, c, 0, 0, 0);
}

DI void retile(int task, const u16* __restrict__ w_gate, const u16* __restrict__ w_up, const u16* __restrict__ w_down,
               char* __restrict__ ws, char* smem, int w, int lane) {
  const int xr = lane >> 4, slot = lane & 15;
  const int chn = slot ^ ((xr << 2) | (w & 3));
  const int h = lane >> 5, blk = (lane >> 4) & 1, qq = (lane & 15) >> 2, p = lane & 3;
  const bool gu = task < 112;
  int kq = 0, fg = 0, dg = 0, fk = 0;
  if (gu) {
    fg = task % 28, kq = task / 28;
    const u16* src = (chn < 8 ? w_gate : w_up) + (size_t)(kq * 112 + 4 * w + xr) * F + fg * 64 + (chn & 7) * 8;
#pragma unroll
    for (int i = 0; i < 7; ++i) glds16(src + (size_t)i * 16 * F, smem + (w + 4 * i) * 1024);
  } else {
    dg = (task - 112) % 7, fk = (task - 112) / 7;
    const u16* src = w_down + (size_t)(fk * 224 + (chn >> 3) * 112 + 4 * w + xr) * D + dg * 64 + (chn & 7) * 8;
#pragma unroll
    for (int i = 0; i < 7; ++i) glds16(src + (size_t)i * 16 * D, smem + (w + 4 * i) * 1024);
  }
  const int col = gu ? blk * 128 + w * 32 + (p & 1) * 16 + (p >> 1) * 8 : (w >> 1) * 128 + (w & 1) * 64 + blk * 32 + p * 8;
  int o[2];
#pragma unroll
  for (int t = 0; t < 2; ++t)
    o[t] = 256 * (8 * h + 4 * t + qq) + 16 * ((col >> 4) ^ ((qq << 2) | ((2 * h + t) & 3))) + (col & 8);
  u32x4* dst = gu ? (u32x4*)(ws + OFF_WGU) + ((size_t)(fg * 4 + w) * 28 + kq * 7) * 64 + lane
                  : (u32x4*)(ws + OFF_WD) + ((size_t)(dg * 2 + (w & 1)) * 112 + fk * 14 + (w >> 1) * 7) * 64 + lane;
  __syncthreads();
#pragma unroll
  for (int i = 0; i < 7; ++i)
    st16_sc1(dst + i * 64, __builtin_bit_cast(u32x4, tr_frag(smem, o[0] + i * 4096, o[1] + i * 4096)));
}

constexpr int K0_GRID = 504;

extern "C" __global__ __launch_bounds__(256) void k0_prep(const u16* __restrict__ q, const u16* __restrict__ k,
                                               const u16* __restrict__ v, const u16* __restrict__ shift_k,
                                               const u16* __restrict__ c, const u16* __restrict__ w_mod,
                                               const u16* __restrict__ w_gate,
                                               const u16* __restrict__ w_up, const u16* __restrict__ w_down,
                                               char* __restrict__ ws) {
  __shared__ __attribute__((aligned(16))) char smem[57344 + 1024 + 512];
  const int b = blockIdx.x, tid = threadIdx.x, lane = tid & 63, w = __builtin_amdgcn_readfirstlane(tid >> 6);
  const int xcd = b & 7, idx = b >> 3;
  u32 cwarm = 0;
  if (idx < 2) {
    u32 lo, hi, len;
    asm volatile(
        "s_getpc_b64 s[40:41]\n\ts_add_u32 s40, s40, k0_prep@rel32@lo+4\n\ts_addc_u32 s41, s41, k0_prep@rel32@hi+12\n\t"
        "s_mov_b32 %0, s40\n\ts_mov_b32 %1, s41\n\ts_mov_b32 %2, k_code_end-k0_prep"
        : "=s"(lo), "=s"(hi), "=s"(len)::"s40", "s41", "scc");
    const char* code = (const char*)(((unsigned long long)hi << 32) | lo);
    if ((u32)(idx * 256 + tid) * 128 < len) cwarm = *(const __attribute__((address_space(1))) u32*)(size_t)(code + (idx * 256 + tid) * 128);
  }
  asm volatile("" ::"s"(q), "s"(k), "s"(v), "s"(shift_k), "s"(c), "s"(w_mod), "s"(w_gate), "s"(w_up), "s"(w_down),
               "s"(ws));
  if (__builtin_expect(idx >= 42, 0)) {
    retile(b - 336, w_gate, w_up, w_down, ws, smem, w, lane);
    if (idx < 58) {
      const int t = (idx - 42) * 2 + (xcd & 1);
      char* ap = ws + (t < 16 ? OFF_X1T : OFF_XS) + (size_t)((t & 15) * 28 + w * 7) * (S * 2) + (xcd >> 1) * 1024 + lane * 16;
      const u32x4 sent = {~0u, ~0u, ~0u, ~0u};
#pragma unroll
      for (int i = 0; i < 7; ++i) st16_sc1(ap + i * (S * 2), sent);
    }
    return;
  }
  int misc = -1;
  if (idx < 6) misc = 64 + b;
  else if (idx >= 34) misc = xcd * 8 + (idx - 34);
  const int rb = xcd * 4 + ((idx - 6) & 3), cg = (idx - 6) >> 2;
  if (__builtin_expect(misc < 0, 1)) {
    const int sq = lane >> 5, cp = lane & 31;
    const int d0 = cg * 64 + cp * 2;
    const int a = rb * 8 + w * 2 + sq;
    const int s0 = a * 8;
    const rsrc_t rk = mk_rsrc(k, S * D * 2), rv = mk_rsrc(v, S * D * 2), rq = mk_rsrc(q, S * D * 2);
    const int oB = (s0 * D + d0) * 2;
    const int oA = oB - 4 * D * 2, oC = oB + 4 * D * 2;
    u32 kk[12], vv[9], qq[8];
#pragma unroll
    for (int i = 1; i < 4; ++i) kk[i] = __builtin_amdgcn_raw_buffer_load_b32(rk, oA + i * D * 2, 0, 0);
#pragma unroll
    for (int i = 0; i < 4; ++i) kk[4 + i] = __builtin_amdgcn_raw_buffer_load_b32(rk, oB + i * D * 2, 0, 0);
#pragma unroll
    for (int i = 0; i < 4; ++i) kk[8 + i] = __builtin_amdgcn_raw_buffer_load_b32(rk, oC + i * D * 2, 0, 0);
#pragma unroll
    for (int i = 0; i < 4; ++i) vv[1 + i] = __builtin_amdgcn_raw_buffer_load_b32(rv, oB + i * D * 2, 0, 0);
#pragma unroll
    for (int i = 0; i < 4; ++i) vv[5 + i] = __builtin_amdgcn_raw_buffer_load_b32(rv, oC + i * D * 2, 0, 0);
#pragma unroll
    for (int i = 0; i < 4; ++i) qq[i] = __builtin_amdgcn_raw_buffer_load_b32(rq, oB + i * D * 2, 0, 0);
#pragma unroll
    for (int i = 0; i < 4; ++i) qq[4 + i] = __builtin_amdgcn_raw_buffer_load_b32(rq, oC + i * D * 2, 0, 0);
    const u32x4 sk = *(const u32x4*)(shift_k + d0 * 4);
    f32x2 wt[4] = {{lo_f(sk[0]), lo_f(sk[2])}, {hi_f(sk[0]), hi_f(sk[2])}, {lo_f(sk[1]), lo_f(sk[3])}, {hi_f(sk[1]), hi_f(sk[3])}};
    f32x2 kf[12], u[9];
#pragma unroll
    for (int i = 1; i < 12; ++i) kf[i] = f32x2{lo_f(kk[i]), hi_f(kk[i])};
#pragma unroll
    for (int i = 1; i < 9; ++i) {
      f32x2 t = kf[i + 3] * wt[0];
      t = __builtin_elementwise_fma(kf[i + 2], wt[1], t);
      t = __builtin_elementwise_fma(kf[i + 1], wt[2], t);
      t = __builtin_elementwise_fma(kf[i], wt[3], t);
      u[i] = t * f32x2{lo_f(vv[i]), hi_f(vv[i])};
    }
    {
      char* dst = ws + (size_t)d0 * (S * 2) + (2040 - s0) * 2;
#pragma unroll
      for (int cI = 0; cI < 2; ++cI) {
        *(u32x4*)(dst + OFF_RO + cI * S * 2) = u32x4{pack_bf16(u[8][cI], u[7][cI]), pack_bf16(u[6][cI], u[5][cI]),
                                              pack_bf16(u[4][cI], u[3][cI]), pack_bf16(u[2][cI], u[1][cI])};
      }
      char* dq = ws + OFF_QT + (size_t)d0 * (S * 2) + s0 * 2;
#pragma unroll
      for (int cI = 0; cI < 2; ++cI) {
        const u32 sel = cI ? 0x07060302u : 0x05040100u;
        *(u32x4*)(dq + cI * S * 2) = u32x4{__builtin_amdgcn_perm(qq[2], qq[0], sel), __builtin_amdgcn_perm(qq[6], qq[4], sel),
                                    __builtin_amdgcn_perm(qq[3], qq[1], sel), __builtin_amdgcn_perm(qq[7], qq[5], sel)};
      }
    }
  } else if (misc < 64) {
    const int bb = misc;
    const int row = bb * 32 + w * 8 + (lane >> 3);
    const int sub = lane & 7;
    u32x4 x[7];
#pragma unroll
    for (int i = 0; i < 7; ++i) x[i] = *(const u32x4*)(q + (size_t)row * D + (i * 8 + sub) * 8);
    if (sub < 7) ((unsigned long long*)(ws + OFF_SG))[sub * S + row] = sub < 3 ? 0ull : ~0ull;
    float s = 0.f, s2 = 0.f;
#pragma unroll
    for (int i = 0; i < 7; ++i)
#pragma unroll
      for (int j = 0; j < 4; ++j) {
        const u32 t = x[i][j];
        s = dot2(t, 0x3f803f80u, s);
        s2 = dot2(t, t, s2);
      }
    s = sum8(s);
    s2 = sum8(s2);
    const float mean = s * (1.f / D);
    float var = s2 * (1.f / D) - mean * mean;
    var = var < 0.f ? 0.f : var;
    const float rstd = __builtin_amdgcn_rsqf(var + 1e-5f);
    if (sub < 2) {
      const int p = (row & ~7) | ((row & 1) << 2) | ((row >> 1) & 3);
      ((float*)(ws + OFF_MEAN))[sub * S + p] = sub ? rstd : mean;
    }
  } else if (misc < 106) {
    const int bb = misc - 64;
    {
      const int xr = lane >> 4, chn = (lane & 15) ^ ((xr << 2) | (w & 3));
      const u16* src = w_mod + (size_t)((chn >> 3) * 224 + 4 * w + xr) * (6 * D) + bb * 64 + (chn & 7) * 8;
#pragma unroll
      for (int i = 0; i < 14; ++i) glds16(src + (size_t)i * 16 * (6 * D), smem + (w + 4 * i) * 1024);
    }
    if (tid < 224) {
      const u32 cv = ((const u32*)c)[tid];
      const float c0 = lo_f(cv), c1 = hi_f(cv);
      *(u32*)(smem + 57344 + tid * 4) =
          pack_bf16(c0 * __builtin_amdgcn_rcpf(1.f + __builtin_amdgcn_exp2f(c0 * -1.44269504f)),
                    c1 * __builtin_amdgcn_rcpf(1.f + __builtin_amdgcn_exp2f(c1 * -1.44269504f)));
    }
    const int h = lane >> 5, blk = (lane >> 4) & 1, qq = (lane & 15) >> 2, p = lane & 3;
    const int col = (w >> 1) * 128 + (w & 1) * 64 + blk * 32 + p * 8;
    int o[2];
#pragma unroll
    for (int t = 0; t < 2; ++t)
      o[t] = 256 * (8 * h + 4 * t + qq) + 16 * ((col >> 4) ^ ((qq << 2) | ((2 * h + t) & 3))) + (col & 8);
    __syncthreads();
    f32x16 acc;
#pragma unroll
    for (int r = 0; r < 16; ++r) acc[r] = 0.f;
#pragma unroll
    for (int i = 0; i < 14; ++i)
      acc = mfma32(*(const u32x4*)(smem + 57344 + (w >> 1) * 448 + i * 32 + h * 16), tr_frag(smem, o[0] + i * 4096, o[1] + i * 4096), acc);
    float* red = (float*)(smem + 57344 + 1024);
    if (h == 0) red[(w >> 1) * 64 + (w & 1) * 32 + lane] = acc[0];
    __syncthreads();
    if (tid < 64) {
      const float t = red[tid] + red[64 + tid];
      const float m = round_bf16(t);
      const int cc = bb * 64 + tid;
      ((float*)(ws + OFF_MOD))[cc] = m;
      if (cc >= 3 * D && cc < 5 * D) {
        const bool isc = cc >= 4 * D;
        ((u16*)(ws + OFF_CB))[isc ? cc - 4 * D : cc - 2 * D] = (u16)pack_bf16(isc ? 1.f + m : m, 0.f);
      }
    }
  }
  asm volatile("" ::"v"(cwarm));
}

constexpr int K1_KSTR = 260 * 16;
constexpr int K1_USTR = 286 * 16;
constexpr int K1_UOFF = 4 * K1_KSTR;
constexpr int K1_XOFF = K1_UOFF + 2 * K1_USTR;
constexpr int K1_CH = K1_XOFF + 32768;

DI void glds16x4_asm(const void* sbase, u32 voff, u32 voff_last, u32 lds_addr) {
  u32 keep;
  sbase = uni(sbase);
  lds_addr = __builtin_amdgcn_readfirstlane(lds_addr);
  asm volatile(
      "s_mov_b32 %0, m0\n\ts_mov_b32 m0, %4\n\ts_nop 0\n\t"
      "global_load_lds_dwordx4 %1, %3\n\t"
      "global_load_lds_dwordx4 %1, %3 offset:1024\n\t"
      "global_load_lds_dwordx4 %1, %3 offset:2048\n\t"
      "global_load_lds_dwordx4 %2, %3 offset:3072\n\t"
      "s_mov_b32 m0, %0"
      : "=&s"(keep)
      : "v"(voff), "v"(voff_last), "s"(sbase), "s"(lds_addr)
      : "memory");
}
#define CAS __attribute__((address_space(4)))
constexpr int K2_XB = D * 256;
constexpr int K2_XH = D * 128;
constexpr int K2_ST = K2_XB;
constexpr int K2_LDS = 131072;

DI u32 umin32(u32 v) { return v < 0xFFFFFFFEu ? v : 0xFFFFFFFEu; }
DI u32 umax3(u32 a, u32 b, u32 c) { const u32 t = a > b ? a : b; return t > c ? t : c; }
template <int OFF>
DI void gload16_off(u32x4& dst, const void* sbase, u32 voff) {
  sbase = uni(sbase);
  asm volatile("global_load_dwordx4 %0, %1, %2 offset:%3" : "=v"(dst) : "v"(voff), "s"(sbase), "n"(OFF) : "memory");
}
DI void glds16_nom0(const void* sbase, u32 voff, u32 lds_addr) {
  sbase = uni(sbase);
  lds_addr = __builtin_amdgcn_readfirstlane(lds_addr);
  asm volatile("s_mov_b32 m0, %2\n\ts_nop 0\n\tglobal_load_lds_dwordx4 %0, %1" ::"v"(voff), "s"(sbase), "s"(lds_addr) : "memory");
}
DI void glds16_sc1(const void* sbase, u32 voff, u32 lds_addr) {
  sbase = uni(sbase);
  lds_addr = __builtin_amdgcn_readfirstlane(lds_addr);
  asm volatile("s_mov_b32 m0, %2\n\ts_nop 0\n\tglobal_load_lds_dwordx4 %0, %1 sc1" ::"v"(voff), "s"(sbase), "s"(lds_addr) : "memory");
}
typedef unsigned long long u64;
typedef __attribute__((address_space(1))) u64 gu64;
#define RLX_AGENT __ATOMIC_RELAXED, __HIP_MEMORY_SCOPE_AGENT
static_assert(4 + C_NW1 <= 72 / C_VSP, "");
constexpr int NWC = C_NW0 + C_NW1;
template <bool WF>
DI void conv_body(const u16* __restrict__ ssm_k, const u16* __restrict__ ssm_D, char* __restrict__ ws, char* smem,
                  const int w, u32x4 (&wf)[28], const char* wbase) {
  const int lane = threadIdx.x & 63;
  const int ch = w >> 2, q = w & 3;
  const int cpair = blockIdx.x;
  const int d = 2 * cpair + ch;
  char* L = smem + ch * K1_CH;
  const u32 L0 = (u32)(size_t)(LAS char*)L;
  const u16* kf = ssm_k + (size_t)d * S;
  const u32 vo = lane * 16;
  {
    const u32 vl = (lane > 62 ? 62 : lane) * 16;
    glds16x4_asm(kf + (2 + 2 * q), vo, vl, L0 + q * K1_KSTR + 64);
    const char* ub = ws + OFF_RO + ((q >> 1) ? 0 : 2) + (size_t)d * S * 2 + (q & 1) * 2048;
    const u32 ul = L0 + K1_UOFF + (q >> 1) * K1_USTR + (q & 1) * 2048;
    glds16_nom0(ub, vo, ul);
    glds16_nom0(ub + 1024, vo, ul + 1024);
  }
  const int n = lane & 15, q4 = lane >> 4;
  const int pbase = 32 * (n >> 1) + 8 * q4 + 4 * (n & 1) + 512 * q;
  const int di = n + q - 15;
  u32 head;
  asm volatile("global_load_dword %0, %1, %2" : "=v"(head) : "v"((u32)(di < 0 ? 0 : di) * 4), "s"(uni(kf)) : "memory");
  auto ldwf = [&](auto kI) {
    constexpr int k = decltype(kI)::value;
    gload16_off<(k % 4) * 1024>(wf[k], wbase + (k / 4) * 4096, vo);
  };
  bar();
  if constexpr (WF) static_for<0, C_NW0>(ldwf);
  const CAS float* MODc = (const CAS float*)(size_t)(ws + OFF_MOD);
  const float sa = MODc[d], ca = MODc[D + d], ga = MODc[2 * D + d], cbv = MODc[4 * D + d];
  const u32 Draw = ((const CAS u32*)(size_t)ssm_D)[cpair];
  if (lane < 14) *(LAS u32x4*)(L + K1_UOFF + (q >> 1) * K1_USTR + (256 + 14 * (q & 1) + lane) * 16) = u32x4{0u, 0u, 0u, 0u};
  u32 Ab = L0 + (n & 3) * K1_KSTR + ((n >> 2) + q4 + 8 * q) * 16;
  u32 Bb = L0 + K1_UOFF + (n & 1) * K1_USTR + (28 - 4 * (n >> 1) + q4 + 8 * q) * 16;
  u32 Hd = L0 + q * K1_KSTR + n * 4;
  asm volatile("" : "+v"(Ab), "+v"(Bb), "+v"(Hd) : "s"(sa), "s"(ca), "s"(ga), "s"(Draw), "s"(cbv));
  asm volatile("s_waitcnt vmcnt(%1)" : "+v"(head) : "n"(WF ? C_NW0 : 0) : "memory");
  *(LAS u32*)(size_t)Hd = di < 0 ? 0u : head;
  if (q == 1 && lane == 0) *(LAS u16*)(L + K1_UOFF + 2047 * 2) = 0;
  wait_lgkm0();
  bar();
  bf16x8 fa[8], fb[8];
  auto rd = [&](int dl) {
#pragma unroll
    for (int g = 0; g < 8; ++g) {
      fa[g] = *(const LAS bf16x8*)(size_t)(Ab + 64 * dl + 512 * g);
      fb[g] = *(const LAS bf16x8*)(size_t)(Bb + 64 * dl + 512 * (7 - g));
    }
  };
  rd(0);
  __builtin_amdgcn_sched_barrier(0);
  u32x2 qv[2];
  f32x4 mu[2], rs[2];
  asm volatile("global_load_dwordx2 %0, %6, %8\n\t"
               "global_load_dwordx2 %1, %6, %8 offset:512\n\t"
               "global_load_dwordx4 %2, %7, %9\n\t"
               "global_load_dwordx4 %3, %7, %9 offset:1024\n\t"
               "global_load_dwordx4 %4, %7, %10\n\t"
               "global_load_dwordx4 %5, %7, %10 offset:1024"
               : "=&v"(qv[0]), "=&v"(qv[1]), "=&v"(mu[0]), "=&v"(mu[1]), "=&v"(rs[0]), "=&v"(rs[1])
               : "v"((u32)pbase * 2), "v"((u32)pbase * 4), "s"(uni(ws + OFF_QT + (size_t)d * S * 2)),
                 "s"(uni(ws + OFF_MEAN)), "s"(uni(ws + OFF_RSTD))
               : "memory");
  const u32x4 sent = {~0u, ~0u, ~0u, ~0u};
  const int fx = blockIdx.x & 7, fi = blockIdx.x >> 3;
  const void* fillb = uni(ws + OFF_ACT + (size_t)((((fx >> 1) * 4 + (fi & 3)) * 4 + (fx & 1) + 2 * (w >> 2)) * 112 + (fi >> 2) * 16 + (w & 3) * 4) * 1024);
  f32x4 acc[8];
#pragma unroll
  for (int j = 0; j < 8; ++j) acc[j] = f32x4{0.f, 0.f, 0.f, 0.f};
  static_for<0, 72>([&](auto mI) {
    constexpr int m = decltype(mI)::value, t = m % 36;
    constexpr int sd = t < 1 ? 0 : t < 3 ? 1 : t < 6 ? 2 : t < 10 ? 3 : t < 15 ? 4 : t < 21 ? 5 : t < 28 ? 6 : 7;
    constexpr int g = t - sd * (sd + 1) / 2;
    if constexpr (m == 36) {
      rd(1);
      __builtin_amdgcn_sched_barrier(0);
    }
    f32x4 a = __builtin_amdgcn_mfma_f32_16x16x32_bf16(fa[g], fb[sd - g], acc[sd], 0, 0, 0);
    asm volatile("" : "+v"(a));
    acc[sd] = a;
    if constexpr (m % C_VSP == C_VSP - 1) {
      constexpr int v = m / C_VSP;
      if constexpr (v < 4) {
        u32x4 sv = sent;
        asm volatile("global_store_dwordx4 %0, %1, %2 offset:%3 sc1\n\ts_nop 1" ::"v"(vo), "v"(sv), "s"(fillb), "n"(v * 1024) : "memory");
      } else if constexpr (WF && v - 4 < C_NW1) {
        ldwf(std::integral_constant<int, C_NW0 + v - 4>{});
      }
      __builtin_amdgcn_sched_barrier(0);
    }
  });
  LAS f32x4* X = (LAS f32x4*)(L + K1_XOFF);
#pragma unroll
  for (int j = 0; j < 8; ++j) X[(j * 4 + q) * 64 + lane] = acc[j];
  u32x4 uc[2];
#pragma unroll
  for (int j = 0; j < 2; ++j)
    uc[j] = *(const LAS u32x4*)(L + K1_UOFF + K1_USTR + (255 - (32 * (2 * q + j) + 4 * (n >> 1) + q4)) * 16);
  asm volatile("s_waitcnt vmcnt(%6)"
               : "+v"(qv[0]), "+v"(qv[1]), "+v"(mu[0]), "+v"(mu[1]), "+v"(rs[0]), "+v"(rs[1])
               : "n"(WF ? C_NW1 : 0)
               : "memory");
  f32x4 qf[2], hg[2], kd[2];
  {
    const int sh = (n & 1) ? 16 : 0;
    const float gc = ga * round_bf16(1.f + ca), gs = ga * sa, Dd = ch ? hi_f(Draw) : lo_f(Draw);
#pragma unroll
    for (int j = 0; j < 2; ++j) {
#pragma unroll
      for (int r = 0; r < 4; ++r) {
        kd[j][r] = __builtin_bit_cast(float, (uc[j][3 - r] << sh) & 0xffff0000u) * Dd;
        qf[j][r] = (r & 1) ? hi_f(qv[j][r >> 1]) : lo_f(qv[j][r >> 1]);
        hg[j][r] = (qf[j][r] - mu[j][r]) * rs[j][r] * gc + gs;
      }
      asm volatile("" : "+v"(kd[j]), "+v"(qf[j]), "+v"(hg[j]));
    }
  }
  wait_lgkm0();
  bar();
  u16* X1T = (u16*)(ws + OFF_X1T) + (size_t)d * S + pbase;
  u16* XS = (u16*)(ws + OFF_XS) + (size_t)d * S + pbase;
  const float c1 = round_bf16(1.f + cbv);
#pragma unroll
  for (int j = 0; j < 2; ++j) {
    const LAS f32x4* Xj = X + ((2 * q + j) * 4) * 64 + lane;
    const f32x4 y = (Xj[0] + Xj[64]) + (Xj[128] + Xj[192]);
    const f32x4 o = qf[j] + hg[j] * (y + kd[j]);
    const u32 p0 = umin32(pack_bf16(o[0], o[1])), p1 = umin32(pack_bf16(o[2], o[3]));
    st8_sc1(XS + 256 * j,
            u32x2{umin32(pack_bf16(lo_f(p0) * c1, hi_f(p0) * c1)), umin32(pack_bf16(lo_f(p1) * c1, hi_f(p1) * c1))});
    st8_sc1(X1T + 256 * j, u32x2{p0, p1});
  }
}

DI void k2_stats(char* __restrict__ ws, char* smem, const int sblk) {
  const int tid = threadIdx.x, lane = tid & 63;
  const int w = __builtin_amdgcn_readfirstlane(tid >> 6);
  const int n = lane & 31, h = lane >> 5;
  {
    const int p0 = sblk * 64;
    if (w < 7) {
      const int task = sblk * 7 + w, t = task >> 1, kh = task & 1;
      const u32x4* wsrc = (const u32x4*)(ws + OFF_WGU) + ((size_t)t * 28 + kh * 14) * 64 + lane;
      const u16* cbp = (const u16*)(ws + OFF_CB) + (n & 1) * D + kh * 224 + 8 * h;
      u32x4 fa[14], fb[14];
#pragma unroll
      for (int i = 0; i < 14; ++i) {
        fa[i] = wsrc[i * 64];
        fb[i] = *(const u32x4*)(cbp + 16 * i);
      }
      __builtin_amdgcn_sched_barrier(0);
      f32x16 va;
#pragma unroll
      for (int r = 0; r < 16; ++r) va[r] = 0.f;
#pragma unroll
      for (int i = 0; i < 14; ++i) va = mfma32(fb[i], __builtin_bit_cast(bf16x8, fa[i]), va);
      if (h == 0) {
        const float f0 = va[0], f1 = va[1];
        const u32 a0 = umin32(__builtin_bit_cast(u32, f0)), a1 = __builtin_bit_cast(u32, f1);
        __hip_atomic_store((gu64*)(size_t)(ws + OFF_VG) + kh * 3584 + t * 32 + n, ((u64)a1 << 32) | a0, RLX_AGENT);
      }
    }
    __syncthreads();
    __builtin_amdgcn_s_sleep(C_SSLEEP);
    const rsrc_t rx = mk_rsrc(ws + OFF_X1T + ((size_t)(w * 56) * S + p0) * 2, 56 * S * 2);
    const int c8 = lane & 7, r8 = lane >> 3;
    u32x4 x[7];
    for (int spin = 0; spin < (1 << 15); ++spin) {
      asm volatile("" ::: "memory");
      u32 m = 0;
#pragma unroll
      for (int i = 0; i < 7; ++i) x[i] = __builtin_amdgcn_raw_buffer_load_b128(rx, (r8 + 8 * i) * (S * 2) + c8 * 16, 0, 16);
#pragma unroll
      for (int i = 0; i < 7; ++i) m = umax3(umax3(m, x[i][0], x[i][1]), x[i][2], x[i][3]);
      if (__all(m != ~0u)) break;
      __builtin_amdgcn_s_sleep(1);
    }
    float sa[8], qa[8];
#pragma unroll
    for (int e = 0; e < 8; ++e) sa[e] = qa[e] = 0.f;
#pragma unroll
    for (int i = 0; i < 7; ++i)
#pragma unroll
      for (int e = 0; e < 8; ++e) {
        const float a = (e & 1) ? hi_f(x[i][e >> 1]) : lo_f(x[i][e >> 1]);
        sa[e] += a;
        qa[e] = __builtin_fmaf(a, a, qa[e]);
      }
    LAS float* P = (LAS float*)smem;
    {
      LAS float* pp = P + (w * 8 + r8) * 128 + c8 * 8;
      *(LAS f32x4*)pp = f32x4{sa[0], sa[1], sa[2], sa[3]};
      *(LAS f32x4*)(pp + 4) = f32x4{sa[4], sa[5], sa[6], sa[7]};
      *(LAS f32x4*)(pp + 64) = f32x4{qa[0], qa[1], qa[2], qa[3]};
      *(LAS f32x4*)(pp + 68) = f32x4{qa[4], qa[5], qa[6], qa[7]};
    }
    wait_lgkm0();
    {
      float s1 = 0.f, q1 = 0.f;
#pragma unroll
      for (int i = 0; i < 8; ++i) {
        s1 += P[(w * 8 + i) * 128 + lane];
        q1 += P[(w * 8 + i) * 128 + 64 + lane];
      }
      *(LAS f32x2*)(P + 8192 + (w * 64 + lane) * 2) = f32x2{s1, q1};
    }
    __syncthreads();
    if (tid < 64) {
      float sm = 0.f, sq = 0.f;
#pragma unroll
      for (int i = 0; i < 8; ++i) {
        const f32x2 t = *(const LAS f32x2*)(P + 8192 + (i * 64 + tid) * 2);
        sm += t[0];
        sq += t[1];
      }
      const float mean = sm * (1.f / D);
      float var = sq * (1.f / D) - mean * mean;
      var = (var < 0.f ? 0.f : var) + 1e-5f;
      const float rstd = __builtin_amdgcn_rsqf(var);
      const float sig = var * rstd;
      const float mh = round_bf16(mean), sh = round_bf16(sig);
      const u32 b0 = pack_bf16(-mh, mh - mean), b1 = pack_bf16(-mh, sh), b2 = pack_bf16(sig - sh, sh);
      const float c1 = -2.302208198f * rstd;
      const float c2 = -0.1029432397f * (rstd * rstd * rstd);
      gu64* G = (gu64*)(size_t)(ws + OFF_SG) + p0 + tid;
      __hip_atomic_store(G, ((u64)b1 << 32) | b0, RLX_AGENT);
      __hip_atomic_store(G + S, ((u64)__builtin_bit_cast(u32, var) << 32) | b2, RLX_AGENT);
      __hip_atomic_store(G + 2 * S, ((u64)__builtin_bit_cast(u32, c1) << 32) | __builtin_bit_cast(u32, c2), RLX_AGENT);
    }
  }
}

DI void k2_gemm(const u16* __restrict__ ssm_k, const u16* __restrict__ ssm_D, char* __restrict__ ws, u16* __restrict__ out,
                char* smem, const int w) {
  const int tid = threadIdx.x, lane = tid & 63;
  const int n = lane & 31, h = lane >> 5;
  const int xcd = blockIdx.x & 7, idx = blockIdx.x >> 3;
  const int mt = (xcd >> 1) * 4 + (idx & 3);
  const int dt = idx >> 2, sh2 = xcd & 1;
  const int ft = sh2 * 7 + dt;
  const int tile = ft * 8 + w;
  const u32 lds0 = (u32)(size_t)(LAS char*)smem;
  u32x4 wf[28], wd[28];
  const char* wbase = ws + OFF_WGU + (size_t)(tile * 28) * 1024;
  const u32 wa = lane * 16;
  auto ldwf = [&](auto kI) {
    constexpr int k = decltype(kI)::value;
    gload16_off<(k % 4) * 1024>(wf[k], wbase + (k / 4) * 4096, wa);
  };
  conv_body<true>(ssm_k, ssm_D, ws, smem, w, wf, wbase);
  const char* xbase = ws + OFF_XS + ((size_t)(8 * w) * S + mt * 128) * 2;
  const u32 xoff = (u32)((lane >> 3) * S + ((lane & 7) ^ (((lane >> 4) & 1) << 2)) * 8) * 2;
  const char* wdbase = ws + OFF_WD + ((size_t)(2 * dt) * 112 + 14 * w) * 1024 + 4096;
  const int gsl = w < 6 ? w : w - 6;
  const char* gbase = ws + OFF_SG + (size_t)((gsl >> 1) * S + mt * 128 + (gsl & 1) * 64) * 8;
  auto ldwd = [&](auto kI) {
    constexpr int q = decltype(kI)::value, k = (q & 1) * 14 + (q >> 1), kk = k % 14;
    gload16_off<(kk % 8) * 1024 - 4096>(wd[k], wdbase + (k / 14) * 112 * 1024 + (kk / 8) * 8192, wa);
  };
  auto ldx = [&](auto cI) {
    constexpr int c = decltype(cI)::value;
    glds16_sc1(xbase + (size_t)(c % 7) * (64 * S * 2) + (c / 7) * 128, xoff, lds0 + (64 * c + 8 * w) * 128);
  };
  auto xok = [&](auto c0I, auto c1I) {
    u32 m = 0;
#pragma unroll
    for (int c = decltype(c0I)::value; c < decltype(c1I)::value; ++c) {
      const u32x4 t = *(const LAS u32x4*)(smem + (64 * c + 8 * w) * 128 + lane * 16);
      m = umax3(umax3(m, t[0], t[1]), t[2], t[3]);
    }
    return (bool)__all(m != ~0u);
  };
  auto xvalid = [&](auto c0I, auto c1I) {
    if (__builtin_expect(!xok(c0I, c1I), 0)) {
#pragma nounroll
      for (int spin = 0; spin < (1 << 14); ++spin) {
        __builtin_amdgcn_s_sleep(4);
        static_for<decltype(c0I)::value, decltype(c1I)::value>(ldx);
        wait_vm<0>();
        if (xok(c0I, c1I)) break;
      }
    }
    __builtin_amdgcn_sched_barrier(0);
  };
  wait_lgkm0();
  bar();
  auto pin = [](f32x16& a) { asm volatile("" : "+v"(a)); };
  auto pinw = [&](auto kI) {
    u32x4 t = wf[decltype(kI)::value];
    asm volatile("" : "+v"(t)::"memory");
    wf[decltype(kI)::value] = t;
  };
  u32x4 aext;
  constexpr int NWH = NWC + (C_NWH - NWC);
  constexpr int NWP = NWC + C_NWP;
  static_for<NWC, NWP>(ldwf);
  static_for<0, 7>(ldx);
  static_for<NWP, NWH>(ldwf);
  int xo[2][2];
  {
    const int blk = (lane >> 4) & 1, qq = (lane & 15) >> 2, p = lane & 3;
#pragma unroll
    for (int i = 0; i < 2; ++i)
#pragma unroll
      for (int t = 0; t < 2; ++t)
        xo[i][t] = 128 * (8 * h + 4 * t + qq) + 16 * ((4 * i + 2 * blk + (p >> 1)) ^ ((qq >> 1) << 2)) + 8 * (p & 1);
  }
  f32x16 acc[4];
#pragma unroll
  for (int i = 0; i < 4; ++i)
#pragma unroll
    for (int r = 0; r < 16; ++r) acc[i][r] = 0.f;
  asm volatile("" : "+v"(xo[0][0]), "+v"(xo[0][1]), "+v"(xo[1][0]), "+v"(xo[1][1]));
  constexpr int PF = 3;
  bf16x8 xr[PF][2];
  auto rdx = [&](auto kI, auto iI) {
    constexpr int k = decltype(kI)::value, i = decltype(iI)::value;
    if constexpr (k == 28) {
      int a0 = xo[i][0] + K2_XH, a1 = xo[i][1] + K2_XH;
      asm volatile("" : "+v"(a0), "+v"(a1));
      xo[i][0] = a0;
      xo[i][1] = a1;
    }
    xr[k % PF][i] = tr_frag(smem, xo[i][0] + (k % 28) * 2048, xo[i][1] + (k % 28) * 2048);
  };
  using I0 = std::integral_constant<int, 0>;
  using I1 = std::integral_constant<int, 1>;
  using I2 = std::integral_constant<int, 2>;
  using I3 = std::integral_constant<int, 3>;
  using I7 = std::integral_constant<int, 7>;
  using I14 = std::integral_constant<int, 14>;
  LAS u64* CS = (LAS u64*)(smem + K2_ST);
  wait_vm<4 + NWH - NWP>();
  static_for<0, NWP>(pinw);
  xvalid(I0{}, I3{});
  bar();
  static_for<0, PF - 1>([&](auto jI) {
    rdx(jI, I0{});
    rdx(jI, I1{});
  });
  __builtin_amdgcn_sched_barrier(0);
  u64 gv, gp0, gp1;
  const char* vbase = ws + OFF_VG + (size_t)tile * 256;
  constexpr int RW = 28 - NWH, KX = RW + 7, KV = 25;
  constexpr int KF = C_KF;
  constexpr int WE = 27 - KX;
  static_assert(KF >= KX && KF <= 27, "");
  static_assert(KX + 4 <= KV && WE <= 9, "stream does not fit");
  static_for<0, 28>([&](auto kI) {
    constexpr int k = decltype(kI)::value;
    if constexpr (k == 6) {
      wait_vm<6>();
      static_for<NWP, NWH>(pinw);
      xvalid(I3{}, I7{});
      bar();
    }
    if constexpr (k >= NWH) {
      u32x4 t = wf[k];
      asm volatile("s_waitcnt vmcnt(%1)" : "+v"(t) : "n"(NWH - 1 + (k > KF ? 2 : 0)) : "memory");
      wf[k] = t;
    }
    if constexpr (k == KV) {
      wait_vm<KV - KX + (KV > KF ? 2 : 0)>();
      xvalid(I7{}, I14{});
      bar();
    }
    __builtin_amdgcn_sched_barrier(0);
    acc[0] = mfma32(wf[k], xr[k % PF][0], acc[0]);
    pin(acc[0]);
    __builtin_amdgcn_sched_barrier(0);
    rdx(std::integral_constant<int, k + PF - 1>{}, I0{});
    __builtin_amdgcn_sched_barrier(0);
    acc[1] = mfma32(wf[k], xr[k % PF][1], acc[1]);
    pin(acc[1]);
    __builtin_amdgcn_sched_barrier(0);
    rdx(std::integral_constant<int, k + PF - 1>{}, I1{});
    if constexpr (k < RW) ldwf(std::integral_constant<int, NWH + k>{});
    else if constexpr (k < KX) ldx(std::integral_constant<int, 7 + k - RW>{});
    else if constexpr (k != KF) ldwd(std::integral_constant<int, k - KX - (k > KF ? 1 : 0)>{});
    else {
      u64 t, t0, t1;
      asm volatile("global_load_dwordx2 %0, %1, %2 sc1" : "=v"(t) : "v"((u32)lane * 8), "s"(uni(gbase)) : "memory");
      asm volatile("global_load_dwordx2 %0, %1, %2 sc1" : "=v"(t0) : "v"((u32)n * 8), "s"(uni(vbase)) : "memory");
      asm volatile("global_load_dwordx2 %0, %1, %2 sc1" : "=v"(t1) : "v"((u32)n * 8), "s"(uni(vbase + 3584 * 8)) : "memory");
      gv = t;
      gp0 = t0;
      gp1 = t1;
    }
    __builtin_amdgcn_sched_barrier(0);
  });
  const rsrc_t rw = mk_rsrc(ws + OFF_ACT + ((size_t)(mt * 4) * 112 + tile) * 1024, 337 * 1024);
  const rsrc_t ra = mk_rsrc(ws + OFF_ACT + ((size_t)(mt * 4 + sh2) * 112 + 14 * w) * 1024, 238 * 1024);
  u32x4 af[28];
  auto ldact = [&](auto kI) {
    constexpr int k = decltype(kI)::value;
    af[k] = __builtin_amdgcn_raw_buffer_load_b128(ra, lane * 16 + (k % 14) * 1024, (k / 14) * 224 * 1024, 16);
  };
  float c1[2], c2[2], var[2], pe[2], pm[2], pr[2];
  u32x4 ov[2];
  auto corr = [&](auto iI) {
    constexpr int i = decltype(iI)::value;
    u64 g[3];
#pragma unroll
    for (int k = 0; k < 3; ++k) g[k] = CS[k * 128 + i * 32 + n];
    const u32x4 bext = {(u32)g[0], (u32)(g[0] >> 32), (u32)g[1], 0u};
    acc[i] = mfma32(aext, __builtin_bit_cast(bf16x8, bext), acc[i]);
    c1[i & 1] = __builtin_bit_cast(float, (u32)(g[2] >> 32));
    c2[i & 1] = __builtin_bit_cast(float, (u32)g[2]);
    var[i & 1] = __builtin_bit_cast(float, (u32)(g[1] >> 32));
  };
  auto slice = [&](auto iI, auto uI) {
    constexpr int i = decltype(iI)::value, u = decltype(uI)::value, z = i & 1;
    if constexpr (u >= 1) {
      float o = pm[z] * __builtin_amdgcn_rcpf(__builtin_fmaf(pe[z], var[z], var[z]));
      if constexpr ((u - 1) & 1) {
        u32 t = umin32(pack_bf16(pr[z], o));
        asm volatile("" : "+v"(t));
        ov[z][(u - 1) / 2] = t;
      } else {
        asm volatile("" : "+v"(o));
        pr[z] = o;
      }
    }
    if constexpr (u < 8) {
      const float a = acc[i][u];
      float e = __builtin_amdgcn_exp2f(__builtin_fmaf(a * a, c2[z], c1[z]) * a), m = a * acc[i][u + 8];
      asm volatile("" : "+v"(e), "+v"(m));
      pe[z] = e;
      pm[z] = m;
    } else {
      __builtin_amdgcn_raw_buffer_store_b128(ov[z], rw, lane * 16 + i * 112 * 1024, 0, 16);
      asm volatile("s_nop 1");
    }
  };
  constexpr int KG = C_KG;
  constexpr auto slq = [](int g) { return g <= 0 ? 0 : (g >= C_SLN ? 18 : (g * 18) / C_SLN); };
  auto slice01 = [&](auto jI) {
    constexpr int j = decltype(jI)::value;
    slice(std::integral_constant<int, j & 1>{}, std::integral_constant<int, j / 2>{});
  };
  static_for<0, 28>([&](auto kI) {
    constexpr int k = decltype(kI)::value;
    if constexpr (k == KG) {
      u64 t = gv, p0 = gp0, p1 = gp1;
      asm volatile("s_waitcnt vmcnt(%3)" : "+v"(t), "+v"(p0), "+v"(p1) : "n"(KG + 27 - KF) : "memory");
      if (!__all((t >> 32) != 0 && (u32)p0 != ~0u && (u32)p1 != ~0u)) {
        for (int spin = 0; spin < (1 << 16); ++spin) {
          __builtin_amdgcn_s_sleep(2);
          t = __hip_atomic_load((gu64*)(size_t)gbase + lane, RLX_AGENT);
          p0 = __hip_atomic_load((gu64*)(size_t)vbase + n, RLX_AGENT);
          p1 = __hip_atomic_load((gu64*)(size_t)vbase + 3584 + n, RLX_AGENT);
          if (__all((t >> 32) != 0 && (u32)p0 != ~0u && (u32)p1 != ~0u)) break;
        }
        __builtin_amdgcn_s_waitcnt(0x0F70);
      }
      CS[gsl * 64 + lane] = t;
      const float v1 = __builtin_bit_cast(float, (u32)p0) + __builtin_bit_cast(float, (u32)p1);
      const float v2 = __builtin_bit_cast(float, (u32)(p0 >> 32)) + __builtin_bit_cast(float, (u32)(p1 >> 32));
      const float v1h = round_bf16(v1), v2h = round_bf16(v2);
      u32x4 a = {pack_bf16(v1h, v1h), pack_bf16(v1 - v1h, v2h), pack_bf16(v2h, v2 - v2h), 0u};
      if (h) a = u32x4{0u, 0u, 0u, 0u};
      asm volatile("" : "+v"(a));
      aext = a;
      wait_lgkm0();
      __builtin_amdgcn_sched_barrier(0);
      bar();
      corr(I0{});
      corr(I1{});
      __builtin_amdgcn_sched_barrier(0);
    }
    acc[2] = mfma32(wf[k], xr[(28 + k) % PF][0], acc[2]);
    pin(acc[2]);
    __builtin_amdgcn_sched_barrier(0);
    if constexpr (k + PF - 1 < 28) rdx(std::integral_constant<int, 28 + k + PF - 1>{}, I0{});
    static_for<slq(2 * (k - KG - 1)), slq(2 * (k - KG - 1) + 1)>(slice01);
    __builtin_amdgcn_sched_barrier(0);
    acc[3] = mfma32(wf[k], xr[(28 + k) % PF][1], acc[3]);
    pin(acc[3]);
    __builtin_amdgcn_sched_barrier(0);
    if constexpr (k % C_LOCK == C_LOCK - 1) bar();
    if constexpr (k + PF - 1 < 28) rdx(std::integral_constant<int, 28 + k + PF - 1>{}, I1{});
    static_for<slq(2 * (k - KG - 1) + 1), slq(2 * (k - KG - 1) + 2)>(slice01);
    if constexpr (WE + k < 28) ldwd(std::integral_constant<int, WE + k>{});
    else if constexpr (WE + k - 28 < 7) ldact(std::integral_constant<int, WE + k - 28>{});
    __builtin_amdgcn_sched_barrier(0);
  });
  constexpr int A0 = WE < 7 ? WE : 7;
  corr(I2{});
  corr(I3{});
  __builtin_amdgcn_sched_barrier(0);
  static_for<0, 9>([&](auto uI) {
    constexpr int u = decltype(uI)::value;
    slice(I2{}, uI);
    slice(I3{}, uI);
    if constexpr (A0 + u < 14 && u < 8) ldact(std::integral_constant<int, A0 + u>{});
    __builtin_amdgcn_sched_barrier(0);
  });
  static_for<(A0 + 8 < 14 ? A0 + 8 : 14), 14>(ldact);
  bar();
  auto chk = [&](auto gI) {
    constexpr int g = decltype(gI)::value;
    u32 m = 0;
#pragma unroll
    for (int k = 7 * g; k < 7 * g + 7; ++k) m = umax3(umax3(m, af[k][0], af[k][1]), af[k][2], af[k][3]);
    return (bool)__all(m != ~0u);
  };
  auto valid = [&](auto gI) {
    constexpr int g = decltype(gI)::value;
    if (__builtin_expect(!chk(gI), 0)) {
#pragma nounroll
      for (int spin = 0; spin < (1 << 14); ++spin) {
        __builtin_amdgcn_s_sleep(8);
        asm volatile("" ::: "memory");
        static_for<7 * g, 7 * g + 7>(ldact);
        if (chk(gI)) break;
      }
      __builtin_amdgcn_s_waitcnt(0x0F70);
    }
    __builtin_amdgcn_sched_barrier(0);
  };
  const int pcol = mt * 128 + sh2 * 32 + n;
  const int dbase = dt * 64 + w * 8 + 4 * h;
  __builtin_amdgcn_sched_barrier(0);
  LAS f32x4* R = (LAS f32x4*)smem;
  f32x16 o[4];
#pragma unroll
  for (int i = 0; i < 4; ++i)
#pragma unroll
    for (int r = 0; r < 16; ++r) o[i][r] = 0.f;
  auto dump1 = [&](auto tjI, auto cI) {
    constexpr int tj = decltype(tjI)::value, c = decltype(cI)::value, i = tj * 2 + (c >> 2), q = c & 3;
    R[((tj * 8 + c) * 8 + w) * 64 + lane] = f32x4{o[i][4 * q], o[i][4 * q + 1], o[i][4 * q + 2], o[i][4 * q + 3]};
  };
  auto step = [&](auto kI) {
    constexpr int k = decltype(kI)::value, tj = k / 14;
    const bf16x8 b = __builtin_bit_cast(bf16x8, af[k]);
    o[2 * tj] = mfma32(wd[k % 14], b, o[2 * tj]);
    pin(o[2 * tj]);
    __builtin_amdgcn_sched_barrier(0);
    if constexpr (k >= 15 && k < 19) dump1(I0{}, std::integral_constant<int, 2 * (k - 15)>{});
    __builtin_amdgcn_sched_barrier(0);
    o[2 * tj + 1] = mfma32(wd[14 + k % 14], b, o[2 * tj + 1]);
    pin(o[2 * tj + 1]);
    __builtin_amdgcn_sched_barrier(0);
    if constexpr (k >= 15 && k < 19) dump1(I0{}, std::integral_constant<int, 2 * (k - 15) + 1>{});
    if constexpr (k >= C_T1 && k < 14) {
      constexpr int n0 = ((k - C_T1) * 14) / (14 - C_T1), n1 = ((k + 1 - C_T1) * 14) / (14 - C_T1);
      static_for<14 + n0, 14 + n1>(ldact);
    }
    __builtin_amdgcn_sched_barrier(0);
  };
  u16 xres[2][4];
  f32x4 gbv;
  auto fin = [&](auto tjI) {
    constexpr int tj = decltype(tjI)::value;
    f32x4 v[8];
#pragma unroll
    for (int sw = 0; sw < 8; ++sw) v[sw] = R[((tj * 8 + w) * 8 + sw) * 64 + lane];
    const f32x4 sum = ((v[0] + v[1]) + (v[2] + v[3])) + ((v[4] + v[5]) + (v[6] + v[7]));
    float r[4];
#pragma unroll
    for (int e = 0; e < 4; ++e) r[e] = __builtin_fmaf(gbv[e], sum[e], bf2f(xres[tj][e]));
    const int pc = pcol + 64 * tj;
    const int srow = (pc & ~7) | ((pc & 3) << 1) | ((pc >> 2) & 1);
    *(u32x2*)(out + (size_t)srow * D + dbase) = u32x2{pack_bf16(r[0], r[1]), pack_bf16(r[2], r[3])};
  };
  valid(I0{});
  asm volatile(""
               : "+v"(wd[0]), "+v"(wd[1]), "+v"(wd[2]), "+v"(wd[3]), "+v"(wd[4]), "+v"(wd[5]), "+v"(wd[6]), "+v"(wd[7]),
                 "+v"(wd[8]), "+v"(wd[9]), "+v"(wd[10]), "+v"(wd[11]), "+v"(wd[12]), "+v"(wd[13])::"memory");
  asm volatile(""
               : "+v"(wd[14]), "+v"(wd[15]), "+v"(wd[16]), "+v"(wd[17]), "+v"(wd[18]), "+v"(wd[19]), "+v"(wd[20]),
                 "+v"(wd[21]), "+v"(wd[22]), "+v"(wd[23]), "+v"(wd[24]), "+v"(wd[25]), "+v"(wd[26]), "+v"(wd[27])::"memory");
  static_for<0, 3>(step);
  {
    const rsrc_t rx = mk_rsrc(ws + OFF_X1T, D * S * 2);
#pragma unroll
    for (int r = 0; r < 8; ++r)
      xres[r >> 2][r & 3] = __builtin_amdgcn_raw_buffer_load_b16(rx, ((dbase + (r & 3)) * S + pcol + (r >> 2) * 64) * 2, 0, 16);
    gbv = *(const f32x4*)((const float*)(ws + OFF_MOD) + 5 * D + dbase);
    __builtin_amdgcn_sched_barrier(0);
  }
  static_for<3, 7>(step);
  valid(I1{});
  static_for<7, 14>(step);
  valid(I2{});
  static_for<14, 21>(step);
  wait_lgkm0();
  bar();
  valid(I3{});
  static_for<21, 28>([&](auto kI) {
    constexpr int k = decltype(kI)::value;
    o[2] = mfma32(wd[k % 14], __builtin_bit_cast(bf16x8, af[k]), o[2]);
    pin(o[2]);
    __builtin_amdgcn_sched_barrier(0);
    if constexpr (k == 23) {
      fin(I0{});
      __builtin_amdgcn_sched_barrier(0);
    }
  });
  static_for<21, 28>([&](auto kI) {
    constexpr int k = decltype(kI)::value;
    o[3] = mfma32(wd[14 + k % 14], __builtin_bit_cast(bf16x8, af[k]), o[3]);
    pin(o[3]);
    __builtin_amdgcn_sched_barrier(0);
    if constexpr (k >= 22 && k < 26) dump1(I1{}, std::integral_constant<int, k - 22>{});
    __builtin_amdgcn_sched_barrier(0);
  });
  static_for<4, 8>([&](auto cI) { dump1(I1{}, cI); });
  wait_lgkm0();
  bar();
  fin(I1{});
}

extern "C" __global__ NOPK __launch_bounds__(512, 2) void k1_main(const u16* __restrict__ ssm_k,
                                                                  const u16* __restrict__ ssm_D, char* __restrict__ ws,
                                                                  u16* __restrict__ out) {
  extern __shared__ __attribute__((aligned(16))) char smem[];
  const int w = __builtin_amdgcn_readfirstlane(threadIdx.x >> 6);
  if (blockIdx.x >= 224) {
    k2_stats(ws, smem, blockIdx.x - 224);
  } else {
    k2_gemm(ssm_k, ssm_D, ws, out, smem, w);
  }
}

extern "C" __device__ __attribute__((used, noinline)) void k_code_end() {}

extern "C" void kernel_launch(void* const* d_in, const int* in_sizes, int n_in, void* d_out, int out_size, void* d_ws,
                              size_t ws_size, hipStream_t stream) {
  const u16* q = (const u16*)d_in[0];
  const u16* k = (const u16*)d_in[1];
  const u16* v = (const u16*)d_in[2];
  const u16* shift_k = (const u16*)d_in[3];
  const u16* ssm_k = (const u16*)d_in[4];
  const u16* ssm_D = (const u16*)d_in[5];
  const u16* w_gate = (const u16*)d_in[7];
  const u16* w_up = (const u16*)d_in[8];
  const u16* w_down = (const u16*)d_in[9];
  const u16* c = (const u16*)d_in[10];
  const u16* w_mod = (const u16*)d_in[11];
  char* ws = (char*)d_ws;
  (void)hipFuncSetAttribute((const void*)k1_main, hipFuncAttributeMaxDynamicSharedMemorySize, K2_LDS);
  k0_prep<<<K0_GRID, 256, 0, stream>>>(q, k, v, shift_k, c, w_mod, w_gate, w_up, w_down, ws);
  k1_main<<<256, 512, K2_LDS, stream>>>(ssm_k, ssm_D, ws, (u16*)d_out);
}
```
